```python
import torch

def _softmax(x, axis=-1):
    return torch.softmax(x, dim=axis)

def _gelu(x):
    return torch.nn.functional.gelu(x, approximate='tanh')

def _silu(x):
    return torch.nn.functional.silu(x)

def _sigmoid(x):
    return torch.sigmoid(x)

def _layernorm(x, axis=-1, eps=1e-05):
    m = x.mean(dim=axis, keepdim=True)
    v = x.var(dim=axis, keepdim=True, unbiased=False)
    return (x - m) / torch.sqrt(v + eps)

def _scatter_add(tgt, idx, src, axis=0):
    return tgt.clone().index_add_(axis, idx, src)

def _segment_sum(src, seg, n):
    out = torch.zeros((n,) + src.shape[1:], dtype=src.dtype)
    return out.index_add_(0, seg, src)

def _pad(x, pw):
    flat = tuple((v for pair in reversed(pw) for v in pair))
    return torch.nn.functional.pad(x, flat)
kernel_name = 'adaLN_cond__sliding_block__mixer_channel_mlp__prompt'

def setup_inputs():

    _w = lambda a: (lambda t: t.to(torch.bfloat16) if t.is_floating_point() else t)(torch.as_tensor(a))

    def _A_setup():
        B, S, D = (1, 2048, 512)
        return {'x': _w((lambda *s: torch.randn(*s, dtype=torch.bfloat16) * 0.05)(B, S, D)), 'wqkv': _w((lambda *s: torch.randn(*s, dtype=torch.bfloat16) * 0.05)(D, 3 * D)), 'wo': _w((lambda *s: torch.randn(*s, dtype=torch.bfloat16) * 0.05)(D, D))}
    _A_in = _A_setup()

    def _B_setup():
        B, S, D, F = (1, 2048, 512, 1792)
        return {'x': _w((lambda *s: torch.randn(*s, dtype=torch.bfloat16) * 0.05)(B, S, D)), 'w1': _w((lambda *s: torch.randn(*s, dtype=torch.bfloat16) * 3.2)(D, F) * D ** (-0.5)), 'b1': _w((lambda *s: torch.randn(*s, dtype=torch.bfloat16) * 1.6)(F)), 'w2': _w((lambda *s: torch.randn(*s, dtype=torch.bfloat16) * 0.2)(F, D) * F ** (-0.5)), 'b2': _w((lambda *s: torch.randn(*s, dtype=torch.bfloat16) * 0.4)(D))}
    _B_in = _B_setup()
    return {'A_x': _A_in['x'], 'A_wqkv': _A_in['wqkv'], 'A_wo': _A_in['wo'], 'B_x': _B_in['x'], 'B_w1': _B_in['w1'], 'B_b1': _B_in['b1'], 'B_w2': _B_in['w2'], 'B_b2': _B_in['b2'], 'c': _w((lambda *s: torch.randn(*s, dtype=torch.bfloat16) * 0.1)(1, 512) * 8.0), 'w_mod': _w((lambda *s: torch.randn(*s, dtype=torch.bfloat16) * 0.1)(512, 6 * 512) * 512 ** (-0.5) * 8.0)}

def reference(A_x, A_wqkv, A_wo, B_x, B_w1, B_b1, B_w2, B_b2, c, w_mod):

    def _A_ref(x, wqkv, wo):
        B, S, D = x.shape
        H = 32
        Dh = D // H
        W = 256
        qkv = (lambda x, *d: x.permute(*d))((x @ wqkv).reshape(B, S, 3, H, Dh), 2, 0, 3, 1, 4)
        q, k, v = (qkv[0], qkv[1], qkv[2])
        scores = torch.einsum('bhqd,bhkd->bhqk', q, k) * Dh ** (-0.5)
        idx = torch.arange(S)
        mask = (idx[:, None] - idx[None, :] >= 0) & (idx[:, None] - idx[None, :] < W)
        scores = torch.where(mask[None, None], scores, -1000000000.0)
        p = _softmax(scores, axis=-1)
        o = torch.einsum('bhqk,bhkd->bhqd', p, v)
        return (lambda x, *d: x.permute(*d))(o, 0, 2, 1, 3).reshape(B, S, D) @ wo

    def _B_ref(x, w1, b1, w2, b2):
        y = _layernorm(x, axis=-1)
        h = _gelu(torch.einsum('bsd,df->bsf', y, w1) + b1)
        return torch.einsum('bsf,fd->bsd', h, w2) + b2
    x = A_x
    mod = (_silu(c) @ w_mod).reshape(c.shape[0], 6, 1, -1)
    sa, ca, ga, sb, cb, gb = (mod[:, 0].clone(), mod[:, 1].clone(), mod[:, 2].clone(), mod[:, 3].clone(), mod[:, 4].clone(), mod[:, 5].clone())
    h = _layernorm(x, axis=-1) * (1 + ca) + sa
    x = x + ga * _A_ref(h, A_wqkv, A_wo)
    h = _layernorm(x, axis=-1) * (1 + cb) + sb
    x = x + gb * _B_ref(h, B_w1, B_b1, B_w2, B_b2)
    return x
```

```cpp
#include <hip/hip_runtime.h>
#include <hip/hip_bf16.h>
typedef unsigned short u16;
typedef short bf16x8 __attribute__((ext_vector_type(8)));
typedef float f32x4 __attribute__((ext_vector_type(4)));
__device__ __forceinline__ float bf2f(u16 v) { return __uint_as_float(((unsigned)v) << 16); }
__device__ __forceinline__ u16 f2bf(float x) { unsigned u = __float_as_uint(x); u += 0x7fffu + ((u >> 16) & 1u); return (u16)(u >> 16); }
constexpr int S = 2048, D = 512, F = 1792;

__global__ void k_mod(const u16* c, const u16* wmod, float* mod) {
    int n = blockIdx.x * 64 + (threadIdx.x & 63); int part = threadIdx.x >> 6;
    __shared__ float red[4][64];
    float acc = 0.f;
    for (int k = part * 128; k < part * 128 + 128; ++k) { float cv = bf2f(c[k]); float s = cv / (1.f + __expf(-cv)); s = bf2f(f2bf(s)); acc += s * bf2f(wmod[k * 3072 + n]); }
    red[part][threadIdx.x & 63] = acc; __syncthreads();
    if (part == 0) mod[n] = bf2f(f2bf(red[0][threadIdx.x] + red[1][threadIdx.x] + red[2][threadIdx.x] + red[3][threadIdx.x]));
}
__global__ void k_ln(const u16* x, const float* sh, const float* sc, u16* out, int mode) {
    int row = blockIdx.x * 4 + (threadIdx.x >> 6), lane = threadIdx.x & 63;
    float v[8]; float s = 0.f;
    for (int j = 0; j < 8; ++j) { v[j] = bf2f(x[row * D + lane * 8 + j]); s += v[j]; }
    for (int o = 32; o; o >>= 1) s += __shfl_xor(s, o);
    float m = s / D, q = 0.f;
    for (int j = 0; j < 8; ++j) { v[j] -= m; q += v[j] * v[j]; }
    for (int o = 32; o; o >>= 1) q += __shfl_xor(q, o);
    float r = rsqrtf(q / D + 1e-5f);
    for (int j = 0; j < 8; ++j) v[j] = v[j] * r * (1.f + sc[lane * 8 + j]) + sh[lane * 8 + j];
    if (mode == 1) {
        s = 0.f; for (int j = 0; j < 8; ++j) s += v[j];
        for (int o = 32; o; o >>= 1) s += __shfl_xor(s, o);
        m = s / D; q = 0.f;
        for (int j = 0; j < 8; ++j) { v[j] -= m; q += v[j] * v[j]; }
        for (int o = 32; o; o >>= 1) q += __shfl_xor(q, o);
        r = rsqrtf(q / D + 1e-5f);
        for (int j = 0; j < 8; ++j) v[j] *= r;
    }
    for (int j = 0; j < 8; ++j) out[row * D + lane * 8 + j] = f2bf(v[j]);
}
template <int EPI>
__global__ void k_gemm(const u16* A, const u16* B, u16* C, int N, int K, const u16* resid, const float* gate, const u16* bias) {
    __shared__ u16 As[64][40], Bs[64][40];
    int tm = blockIdx.y * 64, tn = blockIdx.x * 64, t = threadIdx.x, lane = t & 63, w = t >> 6;
    f32x4 acc[4]; for (int i = 0; i < 4; ++i) acc[i] = (f32x4){0, 0, 0, 0};
    for (int k0 = 0; k0 < K; k0 += 32) {
        { int r = t >> 2, c = (t & 3) * 8; *(bf16x8*)&As[r][c] = *(const bf16x8*)&A[(size_t)(tm + r) * K + k0 + c]; }
        { int kr = t >> 3, c = (t & 7) * 8; bf16x8 v = *(const bf16x8*)&B[(size_t)(k0 + kr) * N + tn + c]; for (int j = 0; j < 8; ++j) Bs[c + j][kr] = (u16)v[j]; }
        __syncthreads();
        bf16x8 a = *(const bf16x8*)&As[w * 16 + (lane & 15)][(lane >> 4) * 8];
        for (int i = 0; i < 4; ++i) { bf16x8 b = *(const bf16x8*)&Bs[i * 16 + (lane & 15)][(lane >> 4) * 8]; acc[i] = __builtin_amdgcn_mfma_f32_16x16x32_bf16(a, b, acc[i], 0, 0, 0); }
        __syncthreads();
    }
    for (int i = 0; i < 4; ++i) for (int j = 0; j < 4; ++j) {
        int row = tm + w * 16 + (lane >> 4) * 4 + j, col = tn + i * 16 + (lane & 15);
        float v = acc[i][j];
        if (EPI == 1) v = bf2f(resid[(size_t)row * N + col]) + gate[col] * bf2f(f2bf(v));
        if (EPI == 2) { v += bf2f(bias[col]); float u = 0.7978845608f * (v + 0.044715f * v * v * v); v = v / (1.f + __expf(-2.f * u)); }
        if (EPI == 3) v = bf2f(resid[(size_t)row * N + col]) + gate[col] * (v + bf2f(bias[col]));
        C[(size_t)row * N + col] = f2bf(v);
    }
}
__global__ void k_attn(const u16* qkv, u16* o) {
    int h = blockIdx.y, q = blockIdx.x * 64 + threadIdx.x;
    float qv[16]; for (int d = 0; d < 16; ++d) qv[d] = bf2f(qkv[(size_t)q * 1536 + h * 16 + d]) * 0.25f;
    float m = -1e30f, l = 0.f, acc[16]; for (int d = 0; d < 16; ++d) acc[d] = 0.f;
    int k0 = q - 255 < 0 ? 0 : q - 255;
    for (int k = k0; k <= q; ++k) {
        float s = 0.f; for (int d = 0; d < 16; ++d) s += qv[d] * bf2f(qkv[(size_t)k * 1536 + 512 + h * 16 + d]);
        float mn = fmaxf(m, s), a = __expf(m - mn), p = __expf(s - mn); l = l * a + p;
        for (int d = 0; d < 16; ++d) acc[d] = acc[d] * a + p * bf2f(qkv[(size_t)k * 1536 + 1024 + h * 16 + d]);
        m = mn;
    }
    for (int d = 0; d < 16; ++d) o[(size_t)q * 512 + h * 16 + d] = f2bf(acc[d] / l);
}
extern "C" void kernel_launch(void* const* d_in, const int* in_sizes, int n_in, void* d_out, int out_size, void* d_ws, size_t ws_size, hipStream_t stream) {
    const u16* x = (const u16*)d_in[0]; const u16* wqkv = (const u16*)d_in[1]; const u16* wo = (const u16*)d_in[2];
    const u16* w1 = (const u16*)d_in[4]; const u16* b1 = (const u16*)d_in[5]; const u16* w2 = (const u16*)d_in[6]; const u16* b2 = (const u16*)d_in[7];
    const u16* c = (const u16*)d_in[8]; const u16* wmod = (const u16*)d_in[9];
    char* ws = (char*)d_ws;
    float* mod = (float*)ws; u16* h = (u16*)(ws + (1 << 20)); u16* qkv = (u16*)(ws + (4 << 20)); u16* o = (u16*)(ws + (12 << 20)); u16* x1 = (u16*)(ws + (16 << 20)); u16* hid = (u16*)(ws + (20 << 20));
    k_mod<<<48, 256, 0, stream>>>(c, wmod, mod);
    k_ln<<<S / 4, 256, 0, stream>>>(x, mod, mod + 512, h, 0);
    k_gemm<0><<<dim3(1536 / 64, S / 64), 256, 0, stream>>>(h, wqkv, qkv, 1536, 512, nullptr, nullptr, nullptr);
    k_attn<<<dim3(S / 64, 32), 64, 0, stream>>>(qkv, o);
    k_gemm<1><<<dim3(512 / 64, S / 64), 256, 0, stream>>>(o, wo, x1, 512, 512, x, mod + 1024, nullptr);
    k_ln<<<S / 4, 256, 0, stream>>>(x1, mod + 1536, mod + 2048, h, 1);
    k_gemm<2><<<dim3(F / 64, S / 64), 256, 0, stream>>>(h, w1, hid, F, 512, nullptr, nullptr, b1);
    k_gemm<3><<<dim3(512 / 64, S / 64), 256, 0, stream>>>(hid, w2, (u16*)d_out, 512, F, x1, mod + 2560, b2);
}
```

```cpp
#include <hip/hip_runtime.h>
#include <hip/hip_bf16.h>
#include <stdint.h>

typedef unsigned short u16;
typedef short bf16x8 __attribute__((ext_vector_type(8)));
typedef short s16x4 __attribute__((ext_vector_type(4)));
typedef float f32x4 __attribute__((ext_vector_type(4)));
typedef float f32x16 __attribute__((ext_vector_type(16)));
typedef unsigned u32x4 __attribute__((ext_vector_type(4)));
typedef unsigned u32x2 __attribute__((ext_vector_type(2)));
typedef __bf16 bf2 __attribute__((ext_vector_type(2)));
#define LAS __attribute__((address_space(3)))
#define GAS __attribute__((address_space(1)))
#define DI __device__ __forceinline__

#define TS 0

constexpr unsigned WS_E = 0, WS_HELLO = 4096, WS_FLAG = 8192;
constexpr unsigned WS_MODU = 0x50000, WS_MODN = 0x60000;
constexpr unsigned WS_KV = 0x100000, WS_O = 0x500000, WS_XG = 0x700000, WS_ST = 0x900000, WS_H = 0xA00000, WS_TS = 0x2000000;
constexpr unsigned WS_BYTES = 0x2100000;
constexpr unsigned L_X = 0, L_WQKV = 57344, L_RED = 155648, L_SAG = 157696, L_STAT = 159744;
constexpr unsigned L_EX = 0, L_KIMG = 98304, L_VIMG = 122880, L_WO = 0, L_W1 = 44032;
constexpr unsigned L_GB16 = 158720, L_SB16 = 159744, L_UVW = 160768, L_CONST = 162560, L_ONES = 162592, L_DUMMY = 162816, L_GAO = 163072, L_GBO = 163200;
constexpr unsigned L_SCR = 32768;
constexpr unsigned LDS_BYTES = 163840;
constexpr unsigned SPIN_CAP = 1u << 16;

DI float bf_lo(unsigned u) { return __uint_as_float(u << 16); }
DI float bf_hi(unsigned u) { return __uint_as_float(u & 0xffff0000u); }
DI unsigned pk2(float a, float b) { bf2 v = {(__bf16)a, (__bf16)b}; return __builtin_bit_cast(unsigned, v); }
DI float rbf(float a) { return bf_lo(pk2(a, a)); }
DI void dma16(unsigned voff, const void* sbase, unsigned lds) { asm volatile("s_mov_b32 m0, %1\n\ts_nop 0\n\tglobal_load_lds_dwordx4 %0, %2" ::"v"(voff), "s"(lds), "s"(sbase) : "memory"); }
DI void dma16c(unsigned voff, const void* sbase, unsigned lds) { asm volatile("s_mov_b32 m0, %1\n\ts_nop 0\n\tglobal_load_lds_dwordx4 %0, %2 sc1" ::"v"(voff), "s"(lds), "s"(sbase) : "memory"); }
DI void warm4(unsigned voff, const void* sbase) { asm volatile("s_mov_b32 m0, %1\n\ts_nop 0\n\tglobal_load_lds_dword %0, %2" ::"v"(voff), "s"(L_DUMMY), "s"(sbase) : "memory"); }
#define BAR() asm volatile("s_waitcnt lgkmcnt(0)\n\ts_barrier" ::: "memory")
#define VMWAIT(n) asm volatile("s_waitcnt vmcnt(" #n ")" ::: "memory")
DI unsigned ldg_sc1(const char* ws, unsigned off) { return __hip_atomic_load((GAS unsigned*)(ws + off), __ATOMIC_RELAXED, __HIP_MEMORY_SCOPE_AGENT); }
DI unsigned long long ldg64_sc1(const char* ws, unsigned off) { return __hip_atomic_load((GAS unsigned long long*)(ws + off), __ATOMIC_RELAXED, __HIP_MEMORY_SCOPE_AGENT); }
DI void stg_sc1(char* ws, unsigned off, unsigned v) { __hip_atomic_store((GAS unsigned*)(ws + off), v, __ATOMIC_RELAXED, __HIP_MEMORY_SCOPE_AGENT); }
DI void stg64_sc1(char* ws, unsigned off, unsigned long long v) { __hip_atomic_store((GAS unsigned long long*)(ws + off), v, __ATOMIC_RELAXED, __HIP_MEMORY_SCOPE_AGENT); }
template <class T> DI T lds_ld(unsigned a) { return *(const LAS T*)(size_t)a; }
template <class T> DI void lds_st(unsigned a, T v) { *(LAS T*)(size_t)a = v; }
DI s16x4 lds_tr(unsigned a) { return __builtin_amdgcn_ds_read_tr16_b64_v4i16((LAS s16x4*)(size_t)a); }
DI bf16x8 trfrag(unsigned a0, unsigned a1) { s16x4 lo = lds_tr(a0), hi = lds_tr(a1); return __builtin_shufflevector(lo, hi, 0, 1, 2, 3, 4, 5, 6, 7); }
#define MFMA32(a, b, c) __builtin_amdgcn_mfma_f32_32x32x16_bf16((a), (b), (c), 0, 0, 0)
#define MFMA16(a, b, c) __builtin_amdgcn_mfma_f32_16x16x32_bf16((a), (b), (c), 0, 0, 0)
template <int N> DI float modu(float t, float gv, float sv) {
    float r; asm("v_mov_b32_dpp %0, %1 row_newbcast:%c4 row_mask:0xf bank_mask:0xf\n\tv_fmac_f32_dpp %0, %2, %3 row_newbcast:%c4 row_mask:0xf bank_mask:0xf" : "=&v"(r) : "v"(sv), "v"(gv), "v"(t), "i"(N)); return r; }
template <int C> DI float dppf(float v) { return __builtin_bit_cast(float, __builtin_amdgcn_update_dpp(0, __builtin_bit_cast(int, v), C, 0xf, 0xf, true)); }
DI float rowsum16(float v) { v += dppf<0xB1>(v); v += dppf<0x4E>(v); v += dppf<0x141>(v); v += dppf<0x140>(v); return v; }
DI float xsum32(float v) { auto r = __builtin_amdgcn_permlane32_swap(__float_as_uint(v), __float_as_uint(v), false, false); return __uint_as_float(r[0]) + __uint_as_float(r[1]); }
DI float xmax32(float v) { auto r = __builtin_amdgcn_permlane32_swap(__float_as_uint(v), __float_as_uint(v), false, false); return fmaxf(__uint_as_float(r[0]), __uint_as_float(r[1])); }
DI float xsum16(float v) { auto r = __builtin_amdgcn_permlane16_swap(__float_as_uint(v), __float_as_uint(v), false, false); return __uint_as_float(r[0]) + __uint_as_float(r[1]); }
DI unsigned long long rt() { unsigned long long t; asm volatile("s_memrealtime %0\n\ts_waitcnt lgkmcnt(0)" : "=s"(t)::"memory"); return t; }
#define STAMP(i) do {} while (0)

DI void poll_flags(const char* ws, unsigned off, int n, unsigned epoch, int lane) {
    const unsigned o = off + (unsigned)(lane < n ? lane : 0) * 256u;
    for (unsigned sp = 0; sp < SPIN_CAP; ++sp) { unsigned f = ldg_sc1(ws, o); if (__all(f == epoch)) break; }
}

__global__ __launch_bounds__(512, 2) void fused_block(const u16* __restrict__ x, const u16* __restrict__ wqkv, const u16* __restrict__ wo, const u16* __restrict__ w1, const u16* __restrict__ b1,
                                                      const u16* __restrict__ w2, const u16* __restrict__ b2, const u16* __restrict__ cvec, const u16* __restrict__ wmod, u16* __restrict__ out, char* __restrict__ ws) {
    const unsigned epoch = *(const unsigned*)(ws + WS_E) + 1u;
    const int tid = threadIdx.x, lane = tid & 63, w = __builtin_amdgcn_readfirstlane(tid >> 6);
    const int b = blockIdx.x, xl = b & 7, idx = b >> 3, g = 2 * xl + (idx >> 4), j = idx & 15;
    const __amdgpu_buffer_rsrc_t rws = __builtin_amdgcn_make_buffer_rsrc(ws, 0, WS_BYTES, 0x00020000);
    STAMP(0);
    {
        unsigned long long pc; unsigned len;
        asm volatile("s_getpc_b64 %0\nfb_code_begin:\n\ts_mov_b32 %1, fb_code_end-fb_code_begin" : "=s"(pc), "=s"(len));
        unsigned o = (unsigned)tid * 64u; o = o < len ? o : 0u;
        warm4(o, (const void*)pc);
    }
    const bool urgent = idx < 16;
    const int col0 = urgent ? 128 * xl + 8 * idx : 1024 + 256 * xl + 16 * (idx - 16);
    unsigned cv; u32x4 wm0, wm1;
    asm volatile("global_load_ushort %0, %1, %2" : "=v"(cv) : "v"(tid * 2), "s"(cvec) : "memory");
    { const unsigned o = (unsigned)tid * 6144u + (unsigned)col0 * 2u;
      asm volatile("global_load_dwordx4 %0, %1, %2" : "=v"(wm0) : "v"(o), "s"(wmod) : "memory");
      asm volatile("global_load_dwordx4 %0, %1, %2" : "=v"(wm1) : "v"(o + (urgent ? 0u : 16u)), "s"(wmod) : "memory"); }
#pragma unroll
    for (int i = 0; i < 16; ++i) dma16((unsigned)(128 * g + 16 * w + i) * 1024u + (((unsigned)lane << 4) ^ (unsigned)(i << 4)), x, L_X + (16 * w + i) * 1024);
#pragma unroll
    for (int i = 0; i < 3; ++i) { const unsigned p = 72 + w + 8 * i, u = p * 64 + lane, row = u / 12, c = u % 12; dma16(row * 3072u + (c >> 2) * 1024u + 64u * j + (c & 3) * 16u, wqkv, L_WQKV + p * 1024); }
#pragma unroll
    for (int i = 0; i < 2; ++i) { unsigned s = (w * 2 + i) * 64 + lane; s = s < 576u ? s : 575u; const unsigned row = (idx >> 4) * 192 + s / 3, seg = s % 3; warm4(row * 3072u + seg * 1024u + 64u * j, wqkv); }
    if (tid == 64) stg_sc1(ws, WS_HELLO + b * 4, epoch);
    lds_st<u32x4>(L_ONES, (u32x4){0x3F803F80u, 0x3F803F80u, 0x3F803F80u, 0x3F803F80u});
    asm volatile("s_waitcnt vmcnt(21)" : "+v"(cv), "+v"(wm0), "+v"(wm1)::"memory");
    {
        float cf = bf_lo(cv); float sl = rbf(cf * __builtin_amdgcn_rcpf(1.f + __expf(-cf)));
        float a[16];
#pragma unroll
        for (int i = 0; i < 4; ++i) { a[2 * i] = sl * bf_lo(wm0[i]); a[2 * i + 1] = sl * bf_hi(wm0[i]); a[8 + 2 * i] = sl * bf_lo(wm1[i]); a[8 + 2 * i + 1] = sl * bf_hi(wm1[i]); }
#pragma unroll
        for (int i = 0; i < 8; ++i) a[i] = rowsum16(a[i]);
        if (!urgent) {
#pragma unroll
            for (int i = 8; i < 16; ++i) a[i] = rowsum16(a[i]);
        }
        if ((lane & 15) == 0) { const unsigned o = L_RED + (w * 4 + (lane >> 4)) * 64;
            lds_st<f32x4>(o, (f32x4){a[0], a[1], a[2], a[3]}); lds_st<f32x4>(o + 16, (f32x4){a[4], a[5], a[6], a[7]});
            lds_st<f32x4>(o + 32, (f32x4){a[8], a[9], a[10], a[11]}); lds_st<f32x4>(o + 48, (f32x4){a[12], a[13], a[14], a[15]}); }
    }
    BAR();
    if (w == 0) {
        float s = 0.f;
#pragma unroll
        for (int r = 0; r < 8; ++r) s += lds_ld<float>(L_RED + ((lane >> 4) * 8 + r) * 64 + (lane & 15) * 4);
        s = xsum16(s); s = xsum32(s);
        const float nb = dppf<0xB1>(s);
        const unsigned pr = (lane & 1) ? pk2(nb, s) : pk2(s, nb);
        if (urgent) { const unsigned v = __shfl(pr, 2 * (lane & 3)); stg64_sc1(ws, WS_MODU + (lane >> 2) * 4096 + (col0 / 2 + (lane & 3)) * 8, ((unsigned long long)epoch << 32) | v); }
        else { const unsigned v = __shfl(pr, 2 * (lane & 7));
            stg64_sc1(ws, WS_MODN + (lane >> 3) * 8192 + ((col0 - 1024) / 2 + (lane & 7)) * 8, ((unsigned long long)epoch << 32) | v);
            stg64_sc1(ws, WS_MODN + (8 + (lane >> 3)) * 8192 + ((col0 - 1024) / 2 + (lane & 7)) * 8, ((unsigned long long)epoch << 32) | v); }
    }
    STAMP(1);
    VMWAIT(5);
    BAR();
    const int m = w & 3, kh = w >> 2, h = lane >> 5, gi = lane >> 4, q4 = (lane & 15) >> 2, p4 = lane & 3;
    u32x4 xr[16];
    { const unsigned tok = 32 * m + (lane & 31), rb = L_X + tok * 1024 + kh * 512;
#pragma unroll
      for (int s = 0; s < 16; ++s) xr[s] = lds_ld<u32x4>(rb + (s >> 3) * 256 + ((((2 * (s & 7) + h) ^ (tok & 15))) << 4)); }
#pragma unroll
    for (int s = 0; s < 16; ++s) asm volatile("" : "+v"(xr[s]));
    BAR();
    if (w < 4) {
#pragma unroll
        for (int i = 0; i < 18; ++i) { const unsigned p = w + 4 * i, u = p * 64 + lane, row = u / 12, c = u % 12; dma16(row * 3072u + (c >> 2) * 1024u + 64u * j + (c & 3) * 16u, wqkv, L_WQKV + p * 1024); }
    }
    STAMP(2);
    {
        float s0 = 0.f, s1 = 0.f; const bf2 one = {(__bf16)1.0f, (__bf16)1.0f};
#pragma unroll
        for (int s = 0; s < 16; ++s)
#pragma unroll
            for (int i = 0; i < 4; ++i) { const unsigned uu = xr[s][i]; const bf2 v = __builtin_bit_cast(bf2, uu); s0 = __builtin_amdgcn_fdot2_f32_bf16(v, one, s0, false); s1 = __builtin_amdgcn_fdot2_f32_bf16(v, v, s1, false); }
        s0 = xsum32(s0); s1 = xsum32(s1);
        if (lane < 32) lds_st<u32x2>(L_STAT + (kh * 128 + 32 * m + lane) * 8, (u32x2){__float_as_uint(s0), __float_as_uint(s1)});
    }
    if (w >= 4) {
        const int t = tid - 256; const unsigned o = WS_MODU + g * 4096 + t * 16; u32x4 v;
        for (unsigned sp = 0; sp < SPIN_CAP; ++sp) { asm volatile("" ::: "memory"); v = __builtin_amdgcn_raw_buffer_load_b128(rws, o, 0, 16); if (__all(v[1] == epoch && v[3] == epoch)) break; }
        if (t >= 128) { v[0] = pk2(1.f + bf_lo(v[0]), 1.f + bf_hi(v[0])); v[2] = pk2(1.f + bf_lo(v[2]), 1.f + bf_hi(v[2])); }
        lds_st<u32x2>(L_SAG + t * 8, (u32x2){v[0], v[2]});
    }
    BAR();
    STAMP(3);
    float rstd, nmr, gv[8], sv[8];
    { const u32x2 a = lds_ld<u32x2>(L_STAT + (32 * m + (lane & 31)) * 8), c2 = lds_ld<u32x2>(L_STAT + (128 + 32 * m + (lane & 31)) * 8);
      const float s0 = __uint_as_float(a[0]) + __uint_as_float(c2[0]), s1 = __uint_as_float(a[1]) + __uint_as_float(c2[1]);
      const float mean = s0 * (1.f / 512.f), var = fmaxf(s1 * (1.f / 512.f) - mean * mean, 0.f);
      rstd = __builtin_amdgcn_rsqf(var + 1e-5f); nmr = -mean * rstd;
      const int n = lane & 15;
#pragma unroll
      for (int q = 0; q < 8; ++q) { const unsigned kk = 256 * kh + 16 * (2 * q + (n >> 3)) + 8 * h + (n & 7);
          sv[q] = bf_lo(lds_ld<u16>(L_SAG + kk * 2)); gv[q] = bf_lo(lds_ld<u16>(L_SAG + 1024 + kk * 2)); }
#pragma unroll
      for (int q = 0; q < 8; ++q) asm volatile("" : "+v"(sv[q]), "+v"(gv[q])); }
    VMWAIT(0);
    BAR();
    STAMP(4);
    f32x16 aQ, aK, aV;
#pragma unroll
    for (int i = 0; i < 16; ++i) { aQ[i] = 0.f; aK[i] = 0.f; aV[i] = 0.f; }
    {
        const unsigned rowa = L_WQKV + (256 * kh + 8 * (gi >> 1) + q4) * 192 + p4 * 8;
        const unsigned wqa = rowa + (((gi & 1) ^ kh) * 32), wva = rowa + 128 + (gi & 1) * 32;
#define QKV_STEP(s) { u32x4 hf; \
            _Pragma("unroll") for (int i = 0; i < 4; ++i) { const float t0 = fmaf(bf_lo(xr[s][i]), rstd, nmr), t1 = fmaf(bf_hi(xr[s][i]), rstd, nmr); \
                float h0, h1; \
                if (i == 0) { h0 = modu<8 * ((s) & 1) + 0>(t0, gv[(s) >> 1], sv[(s) >> 1]); h1 = modu<8 * ((s) & 1) + 1>(t1, gv[(s) >> 1], sv[(s) >> 1]); } \
                if (i == 1) { h0 = modu<8 * ((s) & 1) + 2>(t0, gv[(s) >> 1], sv[(s) >> 1]); h1 = modu<8 * ((s) & 1) + 3>(t1, gv[(s) >> 1], sv[(s) >> 1]); } \
                if (i == 2) { h0 = modu<8 * ((s) & 1) + 4>(t0, gv[(s) >> 1], sv[(s) >> 1]); h1 = modu<8 * ((s) & 1) + 5>(t1, gv[(s) >> 1], sv[(s) >> 1]); } \
                if (i == 3) { h0 = modu<8 * ((s) & 1) + 6>(t0, gv[(s) >> 1], sv[(s) >> 1]); h1 = modu<8 * ((s) & 1) + 7>(t1, gv[(s) >> 1], sv[(s) >> 1]); } \
                hf[i] = pk2(h0, h1); } \
            const bf16x8 hb = __builtin_bit_cast(bf16x8, hf); \
            const bf16x8 fk = trfrag(wqa + (s) * 3072 + 64, wqa + (s) * 3072 + 64 + 768); aK = MFMA32(fk, hb, aK); \
            const bf16x8 fv = trfrag(wva + (s) * 3072, wva + (s) * 3072 + 768); aV = MFMA32(hb, fv, aV); \
            const bf16x8 fq = trfrag(wqa + (s) * 3072, wqa + (s) * 3072 + 768); aQ = MFMA32(fq, hb, aQ); }
        QKV_STEP(0) QKV_STEP(1) QKV_STEP(2) QKV_STEP(3) QKV_STEP(4) QKV_STEP(5) QKV_STEP(6) QKV_STEP(7)
        QKV_STEP(8) QKV_STEP(9) QKV_STEP(10) QKV_STEP(11) QKV_STEP(12) QKV_STEP(13) QKV_STEP(14) QKV_STEP(15)
#undef QKV_STEP
    }
    STAMP(5);
    bf16x8 qf;
    {
        const unsigned ex = L_EX + w * 6144 + lane * 16;
        lds_st<f32x4>(ex, (f32x4){aQ[8], aQ[9], aQ[10], aQ[11]}); lds_st<f32x4>(ex + 1024, (f32x4){aQ[12], aQ[13], aQ[14], aQ[15]});
        lds_st<f32x4>(ex + 2048, (f32x4){aK[8], aK[9], aK[10], aK[11]}); lds_st<f32x4>(ex + 3072, (f32x4){aK[12], aK[13], aK[14], aK[15]});
        float vs[8], vk[8];
#pragma unroll
        for (int i = 0; i < 8; ++i) { vs[i] = kh ? aV[i] : aV[8 + i]; vk[i] = kh ? aV[8 + i] : aV[i]; }
        lds_st<f32x4>(ex + 4096, (f32x4){vs[0], vs[1], vs[2], vs[3]}); lds_st<f32x4>(ex + 5120, (f32x4){vs[4], vs[5], vs[6], vs[7]});
        BAR();
        const unsigned ep = L_EX + (w ^ 4) * 6144 + lane * 16;
        const f32x4 q0 = lds_ld<f32x4>(ep), q1 = lds_ld<f32x4>(ep + 1024), k0 = lds_ld<f32x4>(ep + 2048), k1 = lds_ld<f32x4>(ep + 3072), v0 = lds_ld<f32x4>(ep + 4096), v1 = lds_ld<f32x4>(ep + 5120);
        const float qs = 0.25f * 1.44269504089f;
        u32x4 qp, kp, vp;
        qp[0] = pk2((aQ[0] + q0[0]) * qs, (aQ[1] + q0[1]) * qs); qp[1] = pk2((aQ[2] + q0[2]) * qs, (aQ[3] + q0[3]) * qs);
        qp[2] = pk2((aQ[4] + q1[0]) * qs, (aQ[5] + q1[1]) * qs); qp[3] = pk2((aQ[6] + q1[2]) * qs, (aQ[7] + q1[3]) * qs);
        kp[0] = pk2(aK[0] + k0[0], aK[1] + k0[1]); kp[1] = pk2(aK[2] + k0[2], aK[3] + k0[3]); kp[2] = pk2(aK[4] + k1[0], aK[5] + k1[1]); kp[3] = pk2(aK[6] + k1[2], aK[7] + k1[3]);
        vp[0] = pk2(vk[0] + v0[0], vk[1] + v0[1]); vp[1] = pk2(vk[2] + v0[2], vk[3] + v0[3]); vp[2] = pk2(vk[4] + v1[0], vk[5] + v1[1]); vp[3] = pk2(vk[6] + v1[2], vk[7] + v1[3]);
        qf = __builtin_bit_cast(bf16x8, qp);
        const unsigned kvo = WS_KV + (g * 16 + j) * 16384 + lane * 16;
        __builtin_amdgcn_raw_buffer_store_b128(kp, rws, kvo + (kh * 4 + m) * 1024, 0, 16);
        __builtin_amdgcn_raw_buffer_store_b128(vp, rws, kvo + 8192 + (m * 2 + kh) * 1024, 0, 16);
        lds_st<u32x4>(L_KIMG + (kh * 12 + 8 + m) * 1024 + lane * 16, kp);
        lds_st<u32x4>(L_VIMG + (8 + m) * 2048 + kh * 1024 + lane * 16, vp);
    }
    VMWAIT(0);
    BAR();
    if (tid == 0) stg_sc1(ws, WS_FLAG + (0 * 256 + g * 16 + j) * 256, epoch);
    STAMP(6);
    if (w >= 4) {
        const int t = tid - 256; const unsigned o = WS_MODN + g * 8192 + t * 32; u32x4 v, v2;
        for (unsigned sp = 0; sp < SPIN_CAP; ++sp) { asm volatile("" ::: "memory"); v = __builtin_amdgcn_raw_buffer_load_b128(rws, o, 0, 16); v2 = __builtin_amdgcn_raw_buffer_load_b128(rws, o + 16, 0, 16);
            if (__all(v[1] == epoch && v[3] == epoch && v2[1] == epoch && v2[3] == epoch)) break; }
        u32x4 val = {v[0], v[2], v2[0], v2[2]};
        const int vec = t >> 6, c8 = (t & 63) * 8, ct = j & 7;
        if (vec == 1) lds_st<u32x4>(L_SB16 + c8 * 2, val);
        else if (vec == 2) {
#pragma unroll
            for (int i = 0; i < 4; ++i) val[i] = pk2(1.f + bf_lo(val[i]), 1.f + bf_hi(val[i]));
            lds_st<u32x4>(L_GB16 + c8 * 2, val);
        } else if ((c8 >> 6) == ct) lds_st<u32x4>((vec == 0 ? L_GAO : L_GBO) + (c8 & 63) * 2, val);
    } else if (w == 0) {
        if (g >= 2) { const unsigned o = WS_FLAG + (((g - 2 + (lane & 1)) * 16 + j) * 256); for (unsigned sp = 0; sp < SPIN_CAP; ++sp) { unsigned f = ldg_sc1(ws, o); if (__all(f == epoch)) break; } }
        else if (g == 1) { const unsigned o = WS_FLAG + j * 256; for (unsigned sp = 0; sp < SPIN_CAP; ++sp) { unsigned f = ldg_sc1(ws, o); if (__all(f == epoch)) break; } }
    }
    BAR();
    STAMP(7);
#pragma unroll
    for (int i = 0; i < 4; ++i) { const int p = w * 4 + i, src = p >> 4, r = p & 15, gs = g - 2 + src;
        const unsigned dst = r < 8 ? L_KIMG + ((r >> 2) * 12 + 4 * src + (r & 3)) * 1024 : L_VIMG + (4 * src + ((r - 8) >> 1)) * 2048 + (r & 1) * 1024;
        const unsigned so = WS_KV + ((gs < 0 ? g : gs) * 16 + j) * 16384 + r * 1024 + lane * 16;
        dma16c(so, ws, dst); }
    const int ct = j & 7, rh = j >> 3;
#pragma unroll
    for (int i = 0; i < 8; ++i) { const int p = w * 8 + i; const unsigned row = p * 8 + (lane >> 3), c = lane & 7, nt = (c >> 1) ^ ((row >> 1) & 3);
        dma16(row * 1024u + ct * 128u + nt * 32u + (c & 1) * 16u, wo, L_WO + p * 1024); }
#pragma unroll
    for (int i = 0; i < 6; ++i) { int p = w + 8 * i; p = p < 43 ? p : 42; const unsigned ib = (p < 32 ? 21504u + p * 1024u : 103424u + (p - 32) * 1024u), u = ib / 16 + lane, row = u / 14, c = u % 14;
        dma16(row * 3584u + 224u * j + c * 16u, w1, L_W1 + ib); }
    const int tt = w & 3;
    const unsigned tokP = 128 * g + 64 * rh + 16 * tt + (lane & 15), colP = 64 * ct + 32 * kh + 4 * gi;
    u32x2 xres0, xres1;
    asm volatile("global_load_dwordx2 %0, %1, %2" : "=v"(xres0) : "v"(tokP * 1024u + colP * 2u), "s"(x) : "memory");
    asm volatile("global_load_dwordx2 %0, %1, %2" : "=v"(xres1) : "v"(tokP * 1024u + colP * 2u + 32u), "s"(x) : "memory");
    VMWAIT(16);
    BAR();
    STAMP(8);
    {
        const int hh = kh, qq = lane & 31;
        const int i0 = g >= 2 ? 0 : (8 - 4 * g - m > 0 ? 8 - 4 * g - m : 0);
        const unsigned ka = L_KIMG + (hh * 12 + m) * 1024 + lane * 16;
        const bool isd = ((lane >> 4) & 1) == hh;
        const unsigned va = isd ? L_VIMG + m * 2048 + lane * 16 : L_ONES, vst = isd ? 2048u : 0u, vs1 = isd ? 1024u : 0u;
        const float NEG = -__builtin_inff();
        f32x16 c0, c8, z;
#pragma unroll
        for (int r = 0; r < 16; ++r) { const int kk = (r & 3) + 8 * (r >> 2) + 4 * h; c0[r] = kk > qq ? 0.f : NEG; c8[r] = kk <= qq ? 0.f : NEG; z[r] = 0.f; }
        float mx = NEG;
        {
            f32x16 s = MFMA32(lds_ld<bf16x8>(ka + 8 * 1024), qf, c8);
#pragma unroll
            for (int r = 0; r < 16; r += 2) mx = fmaxf(mx, fmaxf(s[r], s[r + 1]));
            if (i0 == 0) { s = MFMA32(lds_ld<bf16x8>(ka), qf, c0);
#pragma unroll
                for (int r = 0; r < 16; r += 2) mx = fmaxf(mx, fmaxf(s[r], s[r + 1])); }
            for (int i = (i0 > 1 ? i0 : 1); i < 8; ++i) { s = MFMA32(lds_ld<bf16x8>(ka + i * 1024), qf, z);
#pragma unroll
                for (int r = 0; r < 16; r += 2) mx = fmaxf(mx, fmaxf(s[r], s[r + 1])); }
            mx = xmax32(mx);
        }
        f32x16 nm, o;
#pragma unroll
        for (int r = 0; r < 16; ++r) { nm[r] = -mx; c0[r] -= mx; c8[r] -= mx; o[r] = 0.f; }
#define ATT_BLOCK(I, C) { const f32x16 s = MFMA32(lds_ld<bf16x8>(ka + (I) * 1024), qf, C); u32x4 pa, pb; \
            _Pragma("unroll") for (int r = 0; r < 4; ++r) { pa[r] = pk2(__builtin_amdgcn_exp2f(s[2 * r]), __builtin_amdgcn_exp2f(s[2 * r + 1])); pb[r] = pk2(__builtin_amdgcn_exp2f(s[8 + 2 * r]), __builtin_amdgcn_exp2f(s[8 + 2 * r + 1])); } \
            o = MFMA32(lds_ld<bf16x8>(va + (I) * vst), __builtin_bit_cast(bf16x8, pa), o); \
            o = MFMA32(lds_ld<bf16x8>(va + (I) * vst + vs1), __builtin_bit_cast(bf16x8, pb), o); }
        ATT_BLOCK(8, c8)
        if (i0 == 0) ATT_BLOCK(0, c0)
        for (int i = (i0 > 1 ? i0 : 1); i < 8; ++i) ATT_BLOCK(i, nm)
#undef ATT_BLOCK
        const float l = hh ? o[0] : o[8], inv = __builtin_amdgcn_rcpf(l);
        u32x4 op;
#pragma unroll
        for (int r = 0; r < 4; ++r) op[r] = pk2((hh ? o[8 + 2 * r] : o[2 * r]) * inv, (hh ? o[9 + 2 * r] : o[2 * r + 1]) * inv);
        __builtin_amdgcn_raw_buffer_store_b128(op, rws, WS_O + ((g * 8 + 2 * m + (qq >> 4)) * 16 + j) * 1024 + ((qq & 15) + 16 * (2 * hh + h)) * 16, 0, 16);
    }
    asm volatile("s_waitcnt vmcnt(0)" : "+v"(xres0), "+v"(xres1)::"memory");
    BAR();
    if (tid == 0) stg_sc1(ws, WS_FLAG + (1 * 256 + g * 16 + j) * 256, epoch);
    STAMP(9);
    if (w > 0) {
#pragma unroll
        for (int i = 0; i < 7; ++i) { int p = (w - 1) + 7 * i; p = p < 48 ? p : 47; const unsigned ib = 54272u + p * 1024u, u = ib / 16 + lane, row = u / 14, c = u % 14;
            dma16(row * 3584u + 224u * j + c * 16u, w1, L_W1 + ib); }
    } else poll_flags(ws, WS_FLAG + (1 * 256 + g * 16) * 256, 16, epoch, lane);
    BAR();
    STAMP(10);
    u32x2 x1p0, x1p1;
    {
        u32x4 of[8];
#pragma unroll
        for (int s = 0; s < 8; ++s) of[s] = __builtin_amdgcn_raw_buffer_load_b128(rws, WS_O + ((g * 8 + 4 * rh + tt) * 16 + 8 * kh + s) * 1024 + lane * 16, 0, 16);
        f32x4 acc[4];
#pragma unroll
        for (int n = 0; n < 4; ++n) acc[n] = (f32x4){0.f, 0.f, 0.f, 0.f};
        const unsigned xv = 2 * (gi & 1) + (q4 >> 1), rowa = L_WO + (256 * kh + 16 * (gi >> 1) + 4 * (gi & 1) + q4) * 128 + p4 * 8;
#pragma unroll
        for (int s = 0; s < 8; ++s)
#pragma unroll
            for (int n = 0; n < 4; ++n) { const unsigned a = rowa + ((n ^ xv) * 32) + s * 4096; acc[n] = MFMA16(trfrag(a, a + 1024), __builtin_bit_cast(bf16x8, of[s]), acc[n]); }
        BAR();
        STAMP(11);
#pragma unroll
        for (int i = 0; i < 3; ++i) { int p = w + 8 * i; p = p < 21 ? p : 20; const unsigned u = p * 64 + lane, row = u / 14, c = u % 14; dma16(row * 3584u + 224u * j + c * 16u, w1, L_W1 + p * 1024); }
        const unsigned ex = w * 2048 + lane * 16;
        lds_st<f32x4>(ex, kh ? acc[0] : acc[2]); lds_st<f32x4>(ex + 1024, kh ? acc[1] : acc[3]);
        BAR();
        const unsigned ep = (w ^ 4) * 2048 + lane * 16;
        const f32x4 a0 = lds_ld<f32x4>(ep) + (kh ? acc[2] : acc[0]), a1 = lds_ld<f32x4>(ep + 1024) + (kh ? acc[3] : acc[1]);
        const unsigned cl = (32 * kh + 4 * gi) * 2, cg = colP * 2;
        const u32x2 ga0 = lds_ld<u32x2>(L_GAO + cl), ga1 = lds_ld<u32x2>(L_GAO + cl + 32);
        const u32x2 gb0 = lds_ld<u32x2>(L_GB16 + cg), gb1 = lds_ld<u32x2>(L_GB16 + cg + 32), sb0 = lds_ld<u32x2>(L_SB16 + cg), sb1 = lds_ld<u32x2>(L_SB16 + cg + 32);
        float xv1[8], gg[8], ss[8];
#pragma unroll
        for (int r = 0; r < 4; ++r) {
            const float ga_0 = (r & 1) ? bf_hi(ga0[r >> 1]) : bf_lo(ga0[r >> 1]), ga_1 = (r & 1) ? bf_hi(ga1[r >> 1]) : bf_lo(ga1[r >> 1]);
            const float x_0 = (r & 1) ? bf_hi(xres0[r >> 1]) : bf_lo(xres0[r >> 1]), x_1 = (r & 1) ? bf_hi(xres1[r >> 1]) : bf_lo(xres1[r >> 1]);
            xv1[r] = rbf(fmaf(ga_0, a0[r], x_0)); xv1[4 + r] = rbf(fmaf(ga_1, a1[r], x_1));
            gg[r] = (r & 1) ? bf_hi(gb0[r >> 1]) : bf_lo(gb0[r >> 1]); gg[4 + r] = (r & 1) ? bf_hi(gb1[r >> 1]) : bf_lo(gb1[r >> 1]);
            ss[r] = (r & 1) ? bf_hi(sb0[r >> 1]) : bf_lo(sb0[r >> 1]); ss[4 + r] = (r & 1) ? bf_hi(sb1[r >> 1]) : bf_lo(sb1[r >> 1]);
        }
        x1p0 = (u32x2){pk2(xv1[0], xv1[1]), pk2(xv1[2], xv1[3])}; x1p1 = (u32x2){pk2(xv1[4], xv1[5]), pk2(xv1[6], xv1[7])};
        float st[6] = {0.f, 0.f, 0.f, 0.f, 0.f, 0.f}, pg[8];
#pragma unroll
        for (int r = 0; r < 8; ++r) { const float p = xv1[r] * gg[r]; pg[r] = p; st[0] += xv1[r]; st[1] = fmaf(xv1[r], xv1[r], st[1]); st[2] += p; st[3] = fmaf(p, p, st[3]); st[4] = fmaf(p, gg[r], st[4]); st[5] = fmaf(p, ss[r], st[5]); }
        const u32x4 xg = {pk2(pg[0], pg[1]), pk2(pg[2], pg[3]), pk2(pg[4], pg[5]), pk2(pg[6], pg[7])};
        __builtin_amdgcn_raw_buffer_store_b128(xg, rws, WS_XG + ((g * 8 + 4 * rh + tt) * 16 + 2 * ct + kh) * 1024 + lane * 16, 0, 16);
#pragma unroll
        for (int i = 0; i < 6; ++i) st[i] = xsum32(xsum16(st[i]));
        if (kh == 1 && lane < 16) { lds_st<f32x4>(L_SCR + (tt * 16 + lane) * 32, (f32x4){st[0], st[1], st[2], st[3]}); lds_st<u32x2>(L_SCR + (tt * 16 + lane) * 32 + 16, (u32x2){__float_as_uint(st[4]), __float_as_uint(st[5])}); }
        BAR();
        if (kh == 0 && lane < 16) {
            const f32x4 o4 = lds_ld<f32x4>(L_SCR + (tt * 16 + lane) * 32); const u32x2 o2 = lds_ld<u32x2>(L_SCR + (tt * 16 + lane) * 32 + 16);
            const unsigned so = WS_ST + g * 24576, T = 4 * rh + tt;
            const u32x4 sa4 = {__float_as_uint(st[0] + o4[0]), __float_as_uint(st[1] + o4[1]), __float_as_uint(st[2] + o4[2]), __float_as_uint(st[3] + o4[3])};
            const u32x2 sb2 = {__float_as_uint(st[4] + __uint_as_float(o2[0])), __float_as_uint(st[5] + __uint_as_float(o2[1]))};
            __builtin_amdgcn_raw_buffer_store_b128(sa4, rws, so + ((T * 8 + ct) * 16 + lane) * 16, 0, 16);
            __builtin_amdgcn_raw_buffer_store_b64(sb2, rws, so + 16384 + ((T * 8 + ct) * 16 + lane) * 8, 0, 16);
        }
    }
    VMWAIT(0);
    BAR();
    if (tid == 0) stg_sc1(ws, WS_FLAG + (2 * 256 + g * 16 + j) * 256, epoch);
    STAMP(12);
    const int odd = j & 1;
    if (w > 0) {
#pragma unroll
        for (int i = 0; i < 5; ++i) { int p = (w - 1) + 7 * i; p = p < 32 ? p : 31; const int c = p >> 4, pp = p & 15; const unsigned lr = (pp & 7) * 8 + (lane >> 3), row = (pp >> 3) * 896 + 64 * c + lr, cc = lane & 7, nt = (cc >> 1) ^ ((lr >> 1) & 3);
            dma16(row * 1024u + ct * 128u + nt * 32u + (cc & 1) * 16u, w2, c * 16384 + pp * 1024); }
    }
    if (w > 0) {
        const int nt = w - 1; f32x4 acc = {0.f, 0.f, 0.f, 0.f};
        const unsigned ra = L_W1 + (4 * gi + q4) * 224 + p4 * 8 + nt * 32; const int tk = lane & 15;
        const unsigned va = (tk == 0 ? L_GB16 : L_SB16) + 4 * gi * 2;
#pragma unroll
        for (int s = 0; s < 16; ++s) {
            const u32x2 lo = lds_ld<u32x2>(va + s * 64), hi = lds_ld<u32x2>(va + s * 64 + 32);
            u32x4 bv = {lo[0], lo[1], hi[0], hi[1]};
            if (tk == 2) bv = (u32x4){0x3F803F80u, 0x3F803F80u, 0x3F803F80u, 0x3F803F80u};
            if (tk > 2) bv = (u32x4){0u, 0u, 0u, 0u};
            acc = MFMA16(trfrag(ra + s * 7168, ra + s * 7168 + 3584), __builtin_bit_cast(bf16x8, bv), acc);
        }
        if (tk < 3) lds_st<f32x4>(L_UVW + (tk * 112 + nt * 16 + 4 * gi) * 4, acc);
        if (tk == 3) { const u32x2 bb = *(const u32x2*)(b1 + 112 * j + nt * 16 + 4 * gi); lds_st<f32x4>(L_UVW + (3 * 112 + nt * 16 + 4 * gi) * 4, (f32x4){bf_lo(bb[0]), bf_hi(bb[0]), bf_lo(bb[1]), bf_hi(bb[1])}); }
    } else {
        const u32x4 gq = lds_ld<u32x4>(L_GB16 + lane * 16), sq = lds_ld<u32x4>(L_SB16 + lane * 16);
        float c5[5] = {0.f, 0.f, 0.f, 0.f, 0.f};
#pragma unroll
        for (int i = 0; i < 4; ++i) { const float g0 = bf_lo(gq[i]), g1 = bf_hi(gq[i]), s0 = bf_lo(sq[i]), s1 = bf_hi(sq[i]);
            c5[0] += g0 + g1; c5[1] += s0 + s1; c5[2] += g0 * g0 + g1 * g1; c5[3] += g0 * s0 + g1 * s1; c5[4] += s0 * s0 + s1 * s1; }
#pragma unroll
        for (int i = 0; i < 5; ++i) c5[i] = xsum32(xsum16(rowsum16(c5[i])));
        if (lane == 0) { lds_st<f32x4>(L_CONST, (f32x4){c5[0], c5[1], c5[2], c5[3]}); lds_st<float>(L_CONST + 16, c5[4]); }
        poll_flags(ws, WS_FLAG + (2 * 256 + g * 16) * 256, 16, epoch, lane);
    }
    BAR();
    STAMP(13);
    {
        u32x4 xf[16];
#pragma unroll
        for (int s = 0; s < 16; ++s) xf[s] = __builtin_amdgcn_raw_buffer_load_b128(rws, WS_XG + ((g * 8 + w) * 16 + s) * 1024 + lane * 16, 0, 16);
        const unsigned so = WS_ST + g * 24576 + w * 2048;
        const u32x4 sA0 = __builtin_amdgcn_raw_buffer_load_b128(rws, so + ((2 * gi) * 16 + (lane & 15)) * 16, 0, 16), sA1 = __builtin_amdgcn_raw_buffer_load_b128(rws, so + ((2 * gi + 1) * 16 + (lane & 15)) * 16, 0, 16);
        const unsigned sob = WS_ST + g * 24576 + 16384 + w * 1024;
        const u32x2 sB0 = __builtin_amdgcn_raw_buffer_load_b64(rws, sob + ((2 * gi) * 16 + (lane & 15)) * 8, 0, 16), sB1 = __builtin_amdgcn_raw_buffer_load_b64(rws, sob + ((2 * gi + 1) * 16 + (lane & 15)) * 8, 0, 16);
        f32x4 acc[7];
#pragma unroll
        for (int n = 0; n < 7; ++n) acc[n] = (f32x4){0.f, 0.f, 0.f, 0.f};
        const unsigned ra = L_W1 + (4 * gi + q4) * 224 + p4 * 8, raA = ra + 32 * odd, raB = ra + (odd ? 0 : 192);
#pragma unroll
        for (int s = 0; s < 16; ++s) {
#pragma unroll
            for (int u = 0; u < 6; ++u) acc[u] = MFMA16(trfrag(raA + s * 7168 + u * 32, raA + s * 7168 + u * 32 + 3584), __builtin_bit_cast(bf16x8, xf[s]), acc[u]);
            acc[6] = MFMA16(trfrag(raB + s * 7168, raB + s * 7168 + 3584), __builtin_bit_cast(bf16x8, xf[s]), acc[6]);
        }
        BAR();
        STAMP(14);
#pragma unroll
        for (int i = 0; i < 14; ++i) { const int p = w + 8 * i, c = 2 + (p >> 4), pp = p & 15; const unsigned lr = (pp & 7) * 8 + (lane >> 3), row = (pp >> 3) * 896 + 64 * c + lr, cc = lane & 7, nt = (cc >> 1) ^ ((lr >> 1) & 3);
            dma16(row * 1024u + ct * 128u + nt * 32u + (cc & 1) * 16u, w2, c * 16384 + pp * 1024); }
        float S[6];
#pragma unroll
        for (int i = 0; i < 4; ++i) S[i] = xsum32(xsum16(__uint_as_float(sA0[i]) + __uint_as_float(sA1[i])));
#pragma unroll
        for (int i = 0; i < 2; ++i) S[4 + i] = xsum32(xsum16(__uint_as_float(sB0[i]) + __uint_as_float(sB1[i])));
        const f32x4 cc4 = lds_ld<f32x4>(L_CONST); const float SB2 = lds_ld<float>(L_CONST + 16);
        const float iD = 1.f / 512.f, m1 = S[0] * iD, r1 = __builtin_amdgcn_rsqf(fmaxf(S[1] * iD - m1 * m1, 0.f) + 1e-5f);
        const float m2 = (r1 * (S[2] - m1 * cc4[0]) + cc4[1]) * iD;
        const float ez2 = (r1 * r1 * (S[3] - 2.f * m1 * S[4] + m1 * m1 * cc4[2]) + 2.f * r1 * (S[5] - m1 * cc4[3]) + SB2) * iD;
        const float r2 = __builtin_amdgcn_rsqf(fmaxf(ez2 - m2 * m2, 0.f) + 1e-5f);
        const float ca = r1 * r2, cb = -ca * m1, cc = -r2 * m2;
        unsigned hp[7][2];
#pragma unroll
        for (int u = 0; u < 7; ++u) {
            const unsigned nt = u < 6 ? u + odd : (odd ? 0 : 6), o = L_UVW + (nt * 16 + 4 * gi) * 4;
            const f32x4 uu = lds_ld<f32x4>(o), vv = lds_ld<f32x4>(o + 448), ww = lds_ld<f32x4>(o + 896), bb = lds_ld<f32x4>(o + 1344);
            float y[4];
#pragma unroll
            for (int r = 0; r < 4; ++r) { const float pre = fmaf(ca, acc[u][r], fmaf(cb, uu[r], fmaf(r2, vv[r], fmaf(cc, ww[r], bb[r]))));
                const float z = pre * fmaf(pre * pre, -0.044715f * 2.302208198f, -2.302208198f);
                y[r] = pre * __builtin_amdgcn_rcpf(1.f + __builtin_amdgcn_exp2f(z)); }
            hp[u][0] = pk2(y[0], y[1]); hp[u][1] = pk2(y[2], y[3]);
        }
        const unsigned hb = WS_H + (g * 8 + w) * 57344, s0 = (7 * j + odd) >> 1;
#pragma unroll
        for (int u = 0; u < 3; ++u) __builtin_amdgcn_raw_buffer_store_b128((u32x4){hp[2 * u][0], hp[2 * u][1], hp[2 * u + 1][0], hp[2 * u + 1][1]}, rws, hb + (s0 + u) * 1024 + lane * 16, 0, 16);
        __builtin_amdgcn_raw_buffer_store_b64((u32x2){hp[6][0], hp[6][1]}, rws, hb + ((7 * j + (odd ? 0 : 6)) >> 1) * 1024 + odd * 512 + lane * 8, 0, 16);
    }
    VMWAIT(0);
    BAR();
    if (tid == 0) stg_sc1(ws, WS_FLAG + (3 * 256 + g * 16 + j) * 256, epoch);
    STAMP(15);
    if (w == 0) poll_flags(ws, WS_FLAG + (3 * 256 + g * 16) * 256, 16, epoch, lane);
    BAR();
    STAMP(16);
    {
        const unsigned hb = WS_H + (g * 8 + 4 * rh + tt) * 57344 + kh * 28 * 1024;
        u32x4 hf[28];
#pragma unroll
        for (int s = 0; s < 28; ++s) {
            if (s % 7 == 3) { const u32x2 lo = __builtin_amdgcn_raw_buffer_load_b64(rws, hb + s * 1024 + lane * 8, 0, 16), hi = __builtin_amdgcn_raw_buffer_load_b64(rws, hb + s * 1024 + 512 + lane * 8, 0, 16); hf[s] = (u32x4){lo[0], lo[1], hi[0], hi[1]}; }
            else hf[s] = __builtin_amdgcn_raw_buffer_load_b128(rws, hb + s * 1024 + lane * 16, 0, 16);
        }
        f32x4 acc[4];
#pragma unroll
        for (int n = 0; n < 4; ++n) acc[n] = (f32x4){0.f, 0.f, 0.f, 0.f};
        const unsigned xv = (2 * gi + (q4 >> 1)) & 3, rowa = kh * 8192 + (4 * gi + q4) * 128 + p4 * 8;
        unsigned an[4];
#pragma unroll
        for (int n = 0; n < 4; ++n) an[n] = rowa + ((n ^ xv) * 32);
#pragma unroll
        for (int c = 0; c < 14; ++c) {
            const unsigned slot = (c % 9) * 16384;
            if (c == 9) { VMWAIT(0); BAR(); }
#pragma unroll
            for (int s2 = 0; s2 < 2; ++s2)
#pragma unroll
                for (int n = 0; n < 4; ++n) { const unsigned a = an[n] + slot + s2 * 4096; acc[n] = MFMA16(trfrag(a, a + 2048), __builtin_bit_cast(bf16x8, hf[2 * c + s2]), acc[n]); }
            if (c == 4) {
                BAR();
#pragma unroll
                for (int i = 0; i < 10; ++i) { const int p = w + 8 * i, cN = 9 + (p >> 4), pp = p & 15; const unsigned lr = (pp & 7) * 8 + (lane >> 3), row = (pp >> 3) * 896 + 64 * cN + lr, cc = lane & 7, nt = (cc >> 1) ^ ((lr >> 1) & 3);
                    dma16(row * 1024u + ct * 128u + nt * 32u + (cc & 1) * 16u, w2, (cN - 9) * 16384 + pp * 1024); }
            }
        }
        BAR();
        STAMP(17);
        const unsigned ex = w * 2048 + lane * 16;
        lds_st<f32x4>(ex, kh ? acc[0] : acc[2]); lds_st<f32x4>(ex + 1024, kh ? acc[1] : acc[3]);
        const u32x2 bb0 = *(const u32x2*)(b2 + colP), bb1 = *(const u32x2*)(b2 + colP + 16);
        BAR();
        const unsigned ep = (w ^ 4) * 2048 + lane * 16;
        const f32x4 a0 = lds_ld<f32x4>(ep) + (kh ? acc[2] : acc[0]), a1 = lds_ld<f32x4>(ep + 1024) + (kh ? acc[3] : acc[1]);
        const unsigned cl = (32 * kh + 4 * gi) * 2;
        const u32x2 g0 = lds_ld<u32x2>(L_GBO + cl), g1 = lds_ld<u32x2>(L_GBO + cl + 32);
        float y0[4], y1[4];
#pragma unroll
        for (int r = 0; r < 4; ++r) {
            const float gg0 = (r & 1) ? bf_hi(g0[r >> 1]) : bf_lo(g0[r >> 1]), gg1 = (r & 1) ? bf_hi(g1[r >> 1]) : bf_lo(g1[r >> 1]);
            const float b_0 = (r & 1) ? bf_hi(bb0[r >> 1]) : bf_lo(bb0[r >> 1]), b_1 = (r & 1) ? bf_hi(bb1[r >> 1]) : bf_lo(bb1[r >> 1]);
            const float x_0 = (r & 1) ? bf_hi(x1p0[r >> 1]) : bf_lo(x1p0[r >> 1]), x_1 = (r & 1) ? bf_hi(x1p1[r >> 1]) : bf_lo(x1p1[r >> 1]);
            y0[r] = fmaf(gg0, a0[r] + b_0, x_0); y1[r] = fmaf(gg1, a1[r] + b_1, x_1);
        }
        *(u32x2*)(out + tokP * 512 + colP) = (u32x2){pk2(y0[0], y0[1]), pk2(y0[2], y0[3])};
        *(u32x2*)(out + tokP * 512 + colP + 16) = (u32x2){pk2(y1[0], y1[1]), pk2(y1[2], y1[3])};
    }
    if (b == 0 && w == 0) {
        for (unsigned sp = 0; sp < SPIN_CAP; ++sp) { asm volatile("" ::: "memory"); const u32x4 v = __builtin_amdgcn_raw_buffer_load_b128(rws, WS_HELLO + lane * 16, 0, 16); if (__all(v[0] == epoch && v[1] == epoch && v[2] == epoch && v[3] == epoch)) break; }
        if (lane == 0) stg_sc1(ws, WS_E, epoch);
    }
    STAMP(18);
    asm volatile("fb_code_end:");
}

extern "C" void kernel_launch(void* const* d_in, const int* in_sizes, int n_in, void* d_out, int out_size, void* d_ws, size_t ws_size, hipStream_t stream) {
    (void)hipFuncSetAttribute((const void*)fused_block, hipFuncAttributeMaxDynamicSharedMemorySize, LDS_BYTES);
    fused_block<<<256, 512, LDS_BYTES, stream>>>((const u16*)d_in[0], (const u16*)d_in[1], (const u16*)d_in[2], (const u16*)d_in[4], (const u16*)d_in[5], (const u16*)d_in[6], (const u16*)d_in[7],
                                                 (const u16*)d_in[8], (const u16*)d_in[9], (u16*)d_out, (char*)d_ws);
}
```

```cpp
#include <hip/hip_runtime.h>
#include <hip/hip_bf16.h>
#include <stdint.h>

typedef unsigned short u16;
typedef short bf16x8 __attribute__((ext_vector_type(8)));
typedef short s16x4 __attribute__((ext_vector_type(4)));
typedef float f32x4 __attribute__((ext_vector_type(4)));
typedef float f32x16 __attribute__((ext_vector_type(16)));
typedef unsigned u32x4 __attribute__((ext_vector_type(4)));
typedef unsigned u32x2 __attribute__((ext_vector_type(2)));
typedef __bf16 bf2 __attribute__((ext_vector_type(2)));
#define LAS __attribute__((address_space(3)))
#define GAS __attribute__((address_space(1)))
#define DI __device__ __forceinline__

#define TS 0

constexpr unsigned WS_E = 0, WS_HELLO = 4096, WS_FLAG = 8192;
constexpr unsigned WS_MODU = 0x50000, WS_MODN = 0x60000;
constexpr unsigned WS_KV = 0x100000, WS_O = 0x500000, WS_XG = 0x700000, WS_ST = 0x900000, WS_H = 0xA00000, WS_TS = 0x2000000;
constexpr unsigned WS_BYTES = 0x2100000;
constexpr unsigned L_X = 0, L_WQKV = 57344, L_RED = 155648, L_SAG = 156672, L_STAT = 160768;
constexpr unsigned L_QS = 65536, L_KIMG = 98304, L_VIMG = 122880, L_WO = 0, L_W1 = 44032;
constexpr unsigned L_GB16 = 158720, L_SB16 = 159744, L_UVW = 160768, L_CONST = 162560, L_GAO = 162592, L_GBO = 162720, L_ONES = 162848, L_DUMMY = 162880;
constexpr unsigned L_SCR = 32768;
constexpr unsigned LDS_BYTES = 163840;
constexpr unsigned SPIN_CAP = 1u << 16;

DI float bf_lo(unsigned u) { return __uint_as_float(u << 16); }
DI float bf_hi(unsigned u) { return __uint_as_float(u & 0xffff0000u); }
DI unsigned pk2(float a, float b) { bf2 v = {(__bf16)a, (__bf16)b}; return __builtin_bit_cast(unsigned, v); }
DI float rbf(float a) { return bf_lo(pk2(a, a)); }
DI void dma16(unsigned voff, const void* sbase, unsigned lds) { asm volatile("s_mov_b32 m0, %1\n\ts_nop 0\n\tglobal_load_lds_dwordx4 %0, %2" ::"v"(voff), "s"(lds), "s"(sbase) : "memory"); }
DI void dma16c(unsigned voff, const void* sbase, unsigned lds) { asm volatile("s_mov_b32 m0, %1\n\ts_nop 0\n\tglobal_load_lds_dwordx4 %0, %2 sc1" ::"v"(voff), "s"(lds), "s"(sbase) : "memory"); }
DI void warm4(unsigned voff, const void* sbase) { asm volatile("s_mov_b32 m0, %1\n\ts_nop 0\n\tglobal_load_lds_dword %0, %2" ::"v"(voff), "s"(L_DUMMY), "s"(sbase) : "memory"); }
#define FRESH(v) int v = threadIdx.x & 63; asm volatile("" : "+v"(v))
#define SBAR() __builtin_amdgcn_sched_barrier(0)
#define PIN(x) asm volatile("" : "+v"(x))
#define BAR() asm volatile("s_waitcnt lgkmcnt(0)\n\ts_barrier" ::: "memory")
#define VMWAIT(n) asm volatile("s_waitcnt vmcnt(" #n ")" ::: "memory")
DI unsigned ldg_sc1(const char* ws, unsigned off) { return __hip_atomic_load((GAS unsigned*)(ws + off), __ATOMIC_RELAXED, __HIP_MEMORY_SCOPE_AGENT); }
DI unsigned long long ldg64_sc1(const char* ws, unsigned off) { return __hip_atomic_load((GAS unsigned long long*)(ws + off), __ATOMIC_RELAXED, __HIP_MEMORY_SCOPE_AGENT); }
DI void stg_sc1(char* ws, unsigned off, unsigned v) { __hip_atomic_store((GAS unsigned*)(ws + off), v, __ATOMIC_RELAXED, __HIP_MEMORY_SCOPE_AGENT); }
DI void stg64_sc1(char* ws, unsigned off, unsigned long long v) { __hip_atomic_store((GAS unsigned long long*)(ws + off), v, __ATOMIC_RELAXED, __HIP_MEMORY_SCOPE_AGENT); }
template <class T> DI T lds_ld(unsigned a) { return *(const LAS T*)(size_t)a; }
template <class T> DI void lds_st(unsigned a, T v) { *(LAS T*)(size_t)a = v; }
DI s16x4 lds_tr(unsigned a) { return __builtin_amdgcn_ds_read_tr16_b64_v4i16((LAS s16x4*)(size_t)a); }
DI bf16x8 trfrag(unsigned a0, unsigned a1) { s16x4 lo = lds_tr(a0), hi = lds_tr(a1); return __builtin_shufflevector(lo, hi, 0, 1, 2, 3, 4, 5, 6, 7); }
#define MFMA32(a, b, c) __builtin_amdgcn_mfma_f32_32x32x16_bf16((a), (b), (c), 0, 0, 0)
#define MFMA16(a, b, c) __builtin_amdgcn_mfma_f32_16x16x32_bf16((a), (b), (c), 0, 0, 0)
template <int N> DI float bcast(float v) { return __builtin_bit_cast(float, __builtin_amdgcn_mov_dpp(__builtin_bit_cast(int, v), 0x150 + N, 0xf, 0xf, true)); }
template <int N> DI float modu(float t, float gv, float sv) {
#pragma clang fp contract(off)
    const float p = bcast<N>(gv) * t; return bcast<N>(sv) + p; }
template <int N> DI unsigned hdw(unsigned x, float r, float n, float gv, float sv) {
    unsigned o; float a, b2, c, d;
    asm("v_lshlrev_b32 %1, 16, %5\n\tv_and_b32 %2, 0xffff0000, %5\n\tv_fma_f32 %1, %1, %6, %7\n\tv_fma_f32 %2, %2, %6, %7\n\t"
        "v_mov_b32_dpp %3, %9 row_newbcast:%c10 row_mask:0xf bank_mask:0xf\n\tv_mov_b32_dpp %4, %9 row_newbcast:%c11 row_mask:0xf bank_mask:0xf\n\t"
        "v_fmac_f32_dpp %3, %8, %1 row_newbcast:%c10 row_mask:0xf bank_mask:0xf\n\tv_fmac_f32_dpp %4, %8, %2 row_newbcast:%c11 row_mask:0xf bank_mask:0xf\n\t"
        "v_cvt_pk_bf16_f32 %0, %3, %4"
        : "=v"(o), "=&v"(a), "=&v"(b2), "=&v"(c), "=&v"(d) : "v"(x), "v"(r), "v"(n), "v"(gv), "v"(sv), "i"(N), "i"(N + 1));
    return o; }
template <int C> DI float dppf(float v) { return __builtin_bit_cast(float, __builtin_amdgcn_update_dpp(0, __builtin_bit_cast(int, v), C, 0xf, 0xf, true)); }
DI float rowsum16(float v) { v += dppf<0xB1>(v); v += dppf<0x4E>(v); v += dppf<0x141>(v); v += dppf<0x140>(v); return v; }
DI float xsum32(float v) { auto r = __builtin_amdgcn_permlane32_swap(__float_as_uint(v), __float_as_uint(v), false, false); return __uint_as_float(r[0]) + __uint_as_float(r[1]); }
DI float xmax32(float v) { auto r = __builtin_amdgcn_permlane32_swap(__float_as_uint(v), __float_as_uint(v), false, false); return fmaxf(__uint_as_float(r[0]), __uint_as_float(r[1])); }
DI float xsum16(float v) { auto r = __builtin_amdgcn_permlane16_swap(__float_as_uint(v), __float_as_uint(v), false, false); return __uint_as_float(r[0]) + __uint_as_float(r[1]); }
DI unsigned long long mt() { unsigned long long t; asm volatile("s_memtime %0\n\ts_waitcnt lgkmcnt(0)" : "=s"(t)::"memory"); return t; }
DI unsigned long long rt() { unsigned long long t; asm volatile("s_memrealtime %0\n\ts_waitcnt lgkmcnt(0)" : "=s"(t)::"memory"); return t; }
#define STAMP(i) do {} while (0)
#define STAMP_OUT() do {} while (0)
#define STAMPC(i) do {} while (0)
#define WSTAMP(i) do {} while (0)

DI void poll_flags(const char* ws, unsigned off, int n, unsigned epoch, int lane) {
    const unsigned o = off + (unsigned)(lane < n ? lane : 0) * 256u;
    for (unsigned sp = 0; sp < SPIN_CAP; ++sp) { unsigned f = ldg_sc1(ws, o); if (__all(f == epoch)) break; }
}

__global__ __launch_bounds__(576, 3) void fused_block(const u16* __restrict__ x, const u16* __restrict__ wqkv, const u16* __restrict__ wo, const u16* __restrict__ w1, const u16* __restrict__ b1,
                                                      const u16* __restrict__ w2, const u16* __restrict__ b2, const u16* __restrict__ cvec, const u16* __restrict__ wmod, u16* __restrict__ out, char* __restrict__ ws) {
    const unsigned epoch = *(const unsigned*)(ws + WS_E) + 1u;
    const int tid = threadIdx.x, lane = tid & 63, w = __builtin_amdgcn_readfirstlane(tid >> 6);
    const int b = blockIdx.x, xl = b & 7, idx = b >> 3, g = 2 * xl + (idx >> 4), j = idx & 15;
    const int ct = j & 7, rh = j >> 3;
    const bool prod = idx < 12;
    unsigned long long code_pc; unsigned code_len;
    asm volatile("s_getpc_b64 %0\nfb_code_begin:\n\ts_mov_b32 %1, fb_code_end-fb_code_begin" : "=s"(code_pc), "=s"(code_len));
    STAMP(0); STAMPC(20);
    if (w == 8) {
        {
            const unsigned o = ((unsigned)idx * 32u + (lane & 31)) * 64u;
            warm4((int)o < (int)code_len ? o : 0u, (const void*)code_pc);
        }
        if (lane == 0) stg_sc1(ws, WS_HELLO + b * 4, epoch);
        const unsigned sw128 = (lane >> 3) * 1024u + ct * 128u + (((lane & 7) >> 1) ^ ((lane >> 4) & 3)) * 32u + (lane & 1) * 16u;
        unsigned w1o[7];
#pragma unroll
        for (int r = 0; r < 7; ++r) { const unsigned u = r * 64 + lane, row = u / 14, c = u % 14; w1o[r] = row * 3584u + 224u * j + c * 16u; }
#define W1GRP(k) { _Pragma("unroll") for (int r = 0; r < 7; ++r) dma16(w1o[r] + (k) * 114688u, w1, L_W1 + (7 * (k) + r) * 1024); }
#define W2CHUNK(c, slot) { _Pragma("unroll 1") for (int pp = 0; pp < 16; ++pp) dma16(sw128 + ((pp >> 3) * 112 + 8 * (c) + (pp & 7)) * 8192u, w2, (slot) * 16384 + pp * 1024); }
        if (prod) __builtin_amdgcn_s_barrier();
        __builtin_amdgcn_s_barrier();
        __builtin_amdgcn_s_barrier();
        __builtin_amdgcn_s_barrier();
#pragma unroll 1
        for (int p = 0; p < 56; ++p) dma16(sw128 + p * 8192u, wo, L_WO + p * 1024);
        __builtin_amdgcn_s_barrier();
#pragma unroll 1
        for (int p = 56; p < 64; ++p) dma16(sw128 + p * 8192u, wo, L_WO + p * 1024);
        __builtin_amdgcn_s_barrier();
        __builtin_amdgcn_s_barrier();
        if (g >= 1) {
            const unsigned fo = WS_FLAG + (((g >= 2 ? g - 2 + (lane & 1) : 0) * 16 + j) * 256);
            for (unsigned sp = 0; sp < SPIN_CAP; ++sp) { unsigned f = ldg_sc1(ws, fo); if (__all(f == epoch)) break; }
#pragma unroll 1
            for (int p = (g >= 2 ? 0 : 16); p < 32; ++p) { const int src = p >> 4, r = p & 15;
                const unsigned dst = r < 8 ? L_KIMG + (4 * src + (r & 3)) * 2048 + (r >> 2) * 1024 : L_VIMG + (4 * src + ((r - 8) >> 1)) * 2048 + (r & 1) * 1024;
                dma16c(WS_KV + ((g - 2 + src) * 16 + j) * 16384 + r * 1024 + lane * 16, ws, dst); }
        }
        VMWAIT(0);
        __builtin_amdgcn_s_barrier();
#pragma unroll 1
        for (int k = 3; k < 7; ++k) W1GRP(k)
        W1GRP(15)
        __builtin_amdgcn_s_barrier();
#pragma unroll 1
        for (int k = 7; k < 15; ++k) W1GRP(k)
        VMWAIT(56);
        __builtin_amdgcn_s_barrier();
        __builtin_amdgcn_s_barrier();
#pragma unroll 1
        for (int k = 0; k < 3; ++k) W1GRP(k)
        W2CHUNK(1, 1)
        __builtin_amdgcn_s_barrier();
        __builtin_amdgcn_s_barrier();
        __builtin_amdgcn_s_barrier();
        W2CHUNK(0, 0)
        VMWAIT(32);
        __builtin_amdgcn_s_barrier();
        __builtin_amdgcn_s_barrier();
        __builtin_amdgcn_s_barrier();
#pragma unroll 1
        for (int c = 2; c < 9; ++c) W2CHUNK(c, c)
        __builtin_amdgcn_s_barrier();
        VMWAIT(0);
        __builtin_amdgcn_s_barrier();
        __builtin_amdgcn_s_barrier();
#pragma unroll 1
        for (int c = 9; c < 14; ++c) W2CHUNK(c, c - 9)
        VMWAIT(0);
        __builtin_amdgcn_s_barrier();
        __builtin_amdgcn_s_barrier();
        __builtin_amdgcn_s_barrier();
#undef W1GRP
#undef W2CHUNK
        return;
    }
    const __amdgpu_buffer_rsrc_t rws = __builtin_amdgcn_make_buffer_rsrc(ws, 0, WS_BYTES, 0x00020000);
    const int mpair = idx >> 1, col0 = 64 * (8 * mpair + ((xl + mpair) & 7)) + 32 * (idx & 1);
    u32x2 cv; u32x4 wm0, wm1, wm2, wm3;
    if (prod) {
        const unsigned o = (unsigned)(tid >> 2) * (4u * 6144u) + (unsigned)col0 * 2u + (tid & 3) * 16u;
        asm volatile("global_load_dwordx2 %0, %1, %2" : "=v"(cv) : "v"((tid >> 2) * 8), "s"(cvec) : "memory");
        asm volatile("global_load_dwordx4 %0, %1, %2" : "=v"(wm0) : "v"(o), "s"(wmod) : "memory");
        asm volatile("global_load_dwordx4 %0, %1, %2" : "=v"(wm1) : "v"(o + 6144u), "s"(wmod) : "memory");
        asm volatile("global_load_dwordx4 %0, %1, %2" : "=v"(wm2) : "v"(o + 12288u), "s"(wmod) : "memory");
        asm volatile("global_load_dwordx4 %0, %1, %2" : "=v"(wm3) : "v"(o + 18432u), "s"(wmod) : "memory");
    }
    { const unsigned xo = (unsigned)(128 * g + 16 * w) * 1024u + ((unsigned)lane << 4);
#pragma unroll
      for (int i = 0; i < 16; ++i) dma16((xo ^ (unsigned)(i << 4)) + i * 1024u, x, L_X + (16 * w + i) * 1024); }
    unsigned wqo[3];
#pragma unroll
    for (int r = 0; r < 3; ++r) { const unsigned u = (w + 8 * r) * 64 + lane, row = u / 12, c = u % 12; wqo[r] = row * 3072u + (c >> 2) * 1024u + 64u * j + (c & 3) * 16u; dma16(wqo[r] + 3u * 393216u, wqkv, L_WQKV + (72 + w + 8 * r) * 1024); }
    lds_st<u32x4>(L_ONES, (u32x4){0x3F803F80u, 0x3F803F80u, 0x3F803F80u, 0x3F803F80u});
    if (prod) {
        asm volatile("s_waitcnt vmcnt(19)" : "+v"(cv), "+v"(wm0), "+v"(wm1), "+v"(wm2), "+v"(wm3)::"memory");
        float a[8] = {0.f, 0.f, 0.f, 0.f, 0.f, 0.f, 0.f, 0.f};
#define MODROW(cf, wm) { const float c_ = (cf), sl = rbf(c_ * __builtin_amdgcn_rcpf(1.f + __expf(-c_))); _Pragma("unroll") for (int i = 0; i < 4; ++i) { const unsigned u_ = wm[i]; a[2 * i] = fmaf(sl, bf_lo(u_), a[2 * i]); a[2 * i + 1] = fmaf(sl, bf_hi(u_), a[2 * i + 1]); } }
        MODROW(bf_lo(cv[0]), wm0) MODROW(bf_hi(cv[0]), wm1) MODROW(bf_lo(cv[1]), wm2) MODROW(bf_hi(cv[1]), wm3)
#undef MODROW
#pragma unroll
        for (int i = 0; i < 8; ++i) { float v = a[i]; v += dppf<0x124>(v); v += dppf<0x128>(v); a[i] = xsum32(xsum16(v)); }
        if (lane < 4) { lds_st<f32x4>(L_RED + w * 128 + lane * 32, (f32x4){a[0], a[1], a[2], a[3]}); lds_st<f32x4>(L_RED + w * 128 + lane * 32 + 16, (f32x4){a[4], a[5], a[6], a[7]}); }
        BAR();
        if (w < 4) {
            float s0 = 0.f, s1 = 0.f;
#pragma unroll
            for (int r = 0; r < 8; ++r) { const u32x2 v = lds_ld<u32x2>(L_RED + r * 128 + (lane & 15) * 8); s0 += __uint_as_float(v[0]); s1 += __uint_as_float(v[1]); }
            const unsigned long long gr = ((unsigned long long)epoch << 32) | pk2(s0, s1);
            const unsigned rep = 4 * w + (lane >> 4);
            if (mpair < 2) stg64_sc1(ws, WS_MODU + rep * 4096 + (col0 / 2 + (lane & 15)) * 8, gr);
            else stg64_sc1(ws, WS_MODN + rep * 8192 + ((col0 - 1024) / 2 + (lane & 15)) * 8, gr);
        }
    }
    STAMP(1);
    VMWAIT(3);
    BAR();
    const int m = w & 3, kh = w >> 2;
    bf16x8 qf;
    {
    const int h = lane >> 5, gi = lane >> 4, q4 = (lane & 15) >> 2, p4 = lane & 3;
    u32x4 xr[16];
    { const unsigned tok = 32 * m + (lane & 31), rb = L_X + tok * 1024 + kh * 512;
#pragma unroll
      for (int s = 0; s < 16; ++s) xr[s] = lds_ld<u32x4>(rb + (s >> 3) * 256 + ((((2 * (s & 7) + h) ^ (tok & 15))) << 4)); }
#pragma unroll
    for (int s = 0; s < 16; ++s) PIN(xr[s]);
    BAR();
#pragma unroll
    for (int k = 0; k < 3; ++k)
#pragma unroll
        for (int r = 0; r < 3; ++r) dma16(wqo[r] + k * 393216u, wqkv, L_WQKV + (w + 8 * r + 24 * k) * 1024);
    STAMP(2);
    {
        float s0 = 0.f, s1 = 0.f, s2 = 0.f, s3 = 0.f; const bf2 one = {(__bf16)1.0f, (__bf16)1.0f};
#pragma unroll
        for (int s = 0; s < 16; ++s)
#pragma unroll
            for (int i = 0; i < 4; i += 2) { const unsigned ua = xr[s][i], ub = xr[s][i + 1]; const bf2 va = __builtin_bit_cast(bf2, ua), vb = __builtin_bit_cast(bf2, ub);
                s0 = __builtin_amdgcn_fdot2_f32_bf16(va, one, s0, false); s1 = __builtin_amdgcn_fdot2_f32_bf16(va, va, s1, false);
                s2 = __builtin_amdgcn_fdot2_f32_bf16(vb, one, s2, false); s3 = __builtin_amdgcn_fdot2_f32_bf16(vb, vb, s3, false); }
        s0 = xsum32(s0 + s2); s1 = xsum32(s1 + s3);
        if (lane < 32) lds_st<u32x2>(L_STAT + (kh * 128 + 32 * m + lane) * 8, (u32x2){__float_as_uint(s0), __float_as_uint(s1)});
    }
    if (w >= 4) {
        const int t = tid - 256; const unsigned o = WS_MODU + g * 4096 + t * 16; u32x4 v;
        for (unsigned sp = 0; sp < SPIN_CAP; ++sp) { asm volatile("" ::: "memory"); v = __builtin_amdgcn_raw_buffer_load_b128(rws, o, 0, 16); if (__all(v[1] == epoch && v[3] == epoch)) break; }
        const unsigned u0 = v[0], u2 = v[2]; f32x4 f = {bf_lo(u0), bf_hi(u0), bf_lo(u2), bf_hi(u2)};
        if (t >= 128) { f[0] = rbf(1.f + f[0]); f[1] = rbf(1.f + f[1]); f[2] = rbf(1.f + f[2]); f[3] = rbf(1.f + f[3]); }
        lds_st<f32x4>(L_SAG + t * 16, f);
    }
    STAMP(3);
    VMWAIT(0);
    BAR();
    STAMP(4); STAMPC(22);
    float rstd, nmr, gv[2], sv[2];
    const unsigned sga = L_SAG + (256 * kh + 16 * ((lane & 15) >> 3) + 8 * h + (lane & 7)) * 4;
    { const u32x2 a = lds_ld<u32x2>(L_STAT + (32 * m + (lane & 31)) * 8), c2 = lds_ld<u32x2>(L_STAT + (128 + 32 * m + (lane & 31)) * 8);
      sv[0] = lds_ld<float>(sga); gv[0] = lds_ld<float>(sga + 2048);
      const float s0 = __uint_as_float(a[0]) + __uint_as_float(c2[0]), s1 = __uint_as_float(a[1]) + __uint_as_float(c2[1]);
      const float mean = s0 * (1.f / 512.f), var = fmaxf(s1 * (1.f / 512.f) - mean * mean, 0.f);
      rstd = __builtin_amdgcn_rsqf(var + 1e-5f); nmr = -mean * rstd;
      asm volatile("s_nop 1" : "+v"(sv[0]), "+v"(gv[0])); }
    f32x16 aQ, aK, aV;
#pragma unroll
    for (int i = 0; i < 16; ++i) { aQ[i] = 0.f; aK[i] = 0.f; aV[i] = 0.f; }
    {
        const unsigned rowa = L_WQKV + (256 * kh + 8 * (gi >> 1) + q4) * 192 + p4 * 8;
        const unsigned wqa = rowa + (((gi & 1) ^ kh) * 32), wva = rowa + 128 + (gi & 1) * 32;
        bf16x8 fk, fv, fq; unsigned h0[2], h1[2], h2[2], h3[2];
#define HDW(s, i) hdw<8 * ((s) & 1) + 2 * (i)>(xr[s][i], rstd, nmr, gv[((s) >> 1) & 1], sv[((s) >> 1) & 1])
        fk = trfrag(wqa + 64, wqa + 64 + 768); fv = trfrag(wva, wva + 768); fq = trfrag(wqa, wqa + 768);
        h0[0] = HDW(0, 0); h1[0] = HDW(0, 1); h2[0] = HDW(0, 2); h3[0] = HDW(0, 3);
        asm volatile("s_nop 1" : "+v"(h0[0]), "+v"(h1[0]), "+v"(h2[0]), "+v"(h3[0])); SBAR();
#define QKV_STEP(s, sn) { const bf16x8 hb = __builtin_bit_cast(bf16x8, ((u32x4){h0[(s) & 1], h1[(s) & 1], h2[(s) & 1], h3[(s) & 1]})); \
            aK = MFMA32(fk, hb, aK); PIN(aK); SBAR(); if ((sn) < 16) { fk = trfrag(wqa + (sn) * 3072 + 64, wqa + (sn) * 3072 + 64 + 768); h0[(sn) & 1] = HDW((sn) & 15, 0); h1[(sn) & 1] = HDW((sn) & 15, 1); PIN(h0[(sn) & 1]); PIN(h1[(sn) & 1]); } SBAR(); \
            aV = MFMA32(hb, fv, aV); PIN(aV); SBAR(); if ((sn) < 16) { fv = trfrag(wva + (sn) * 3072, wva + (sn) * 3072 + 768); h2[(sn) & 1] = HDW((sn) & 15, 2); PIN(h2[(sn) & 1]); } SBAR(); \
            aQ = MFMA32(fq, hb, aQ); PIN(aQ); SBAR(); if ((sn) < 16) { fq = trfrag(wqa + (sn) * 3072, wqa + (sn) * 3072 + 768); h3[(sn) & 1] = HDW((sn) & 15, 3); PIN(h3[(sn) & 1]); } \
            if ((s) % 2 == 0 && (s) + 2 < 16) { sv[(((s) >> 1) + 1) & 1] = lds_ld<float>(sga + (((s) >> 1) + 1) * 128); gv[(((s) >> 1) + 1) & 1] = lds_ld<float>(sga + 2048 + (((s) >> 1) + 1) * 128); } SBAR(); }
        QKV_STEP(0, 1) QKV_STEP(1, 2) QKV_STEP(2, 3) QKV_STEP(3, 4) QKV_STEP(4, 5) QKV_STEP(5, 6) QKV_STEP(6, 7) QKV_STEP(7, 8)
        QKV_STEP(8, 9) QKV_STEP(9, 10) QKV_STEP(10, 11) QKV_STEP(11, 12) QKV_STEP(12, 13) QKV_STEP(13, 14) QKV_STEP(14, 15) QKV_STEP(15, 16)
#undef QKV_STEP
#undef HDW
    }
    STAMP(5); STAMPC(23);
    {
        float vs[8], vk[8];
#pragma unroll
        for (int i = 0; i < 8; ++i) { float lo = aV[i], hi = aV[8 + i]; PIN(lo); PIN(hi); vs[i] = kh ? lo : hi; vk[i] = kh ? hi : lo; }
        BAR();
        const unsigned ex = (kh ? L_VIMG : L_KIMG) + m * 4096 + lane * 16, eq = L_QS + w * 2048 + lane * 16;
        lds_st<f32x4>(ex, (f32x4){aK[8], aK[9], aK[10], aK[11]}); lds_st<f32x4>(ex + 1024, (f32x4){aK[12], aK[13], aK[14], aK[15]});
        lds_st<f32x4>(ex + 2048, (f32x4){vs[0], vs[1], vs[2], vs[3]}); lds_st<f32x4>(ex + 3072, (f32x4){vs[4], vs[5], vs[6], vs[7]});
        lds_st<f32x4>(eq, (f32x4){aQ[8], aQ[9], aQ[10], aQ[11]}); lds_st<f32x4>(eq + 1024, (f32x4){aQ[12], aQ[13], aQ[14], aQ[15]});
        BAR();
        const unsigned ep = (kh ? L_KIMG : L_VIMG) + m * 4096 + lane * 16, eqp = L_QS + (w ^ 4) * 2048 + lane * 16;
        const f32x4 k0 = lds_ld<f32x4>(ep), k1 = lds_ld<f32x4>(ep + 1024), v0 = lds_ld<f32x4>(ep + 2048), v1 = lds_ld<f32x4>(ep + 3072), q0 = lds_ld<f32x4>(eqp), q1 = lds_ld<f32x4>(eqp + 1024);
        const float qs = 0.25f * 1.44269504089f;
        u32x4 qp, kp, vp;
        kp[0] = pk2(aK[0] + k0[0], aK[1] + k0[1]); kp[1] = pk2(aK[2] + k0[2], aK[3] + k0[3]); kp[2] = pk2(aK[4] + k1[0], aK[5] + k1[1]); kp[3] = pk2(aK[6] + k1[2], aK[7] + k1[3]);
        vp[0] = pk2(vk[0] + v0[0], vk[1] + v0[1]); vp[1] = pk2(vk[2] + v0[2], vk[3] + v0[3]); vp[2] = pk2(vk[4] + v1[0], vk[5] + v1[1]); vp[3] = pk2(vk[6] + v1[2], vk[7] + v1[3]);
        const unsigned kvo = WS_KV + (g * 16 + j) * 16384 + lane * 16;
        __builtin_amdgcn_raw_buffer_store_b128(kp, rws, kvo + (kh * 4 + m) * 1024, 0, 16);
        __builtin_amdgcn_raw_buffer_store_b128(vp, rws, kvo + 8192 + (m * 2 + kh) * 1024, 0, 16);
        lds_st<u32x4>(L_KIMG + (8 + m) * 2048 + kh * 1024 + lane * 16, kp);
        lds_st<u32x4>(L_VIMG + (8 + m) * 2048 + kh * 1024 + lane * 16, vp);
        qp[0] = pk2((aQ[0] + q0[0]) * qs, (aQ[1] + q0[1]) * qs); qp[1] = pk2((aQ[2] + q0[2]) * qs, (aQ[3] + q0[3]) * qs);
        qp[2] = pk2((aQ[4] + q1[0]) * qs, (aQ[5] + q1[1]) * qs); qp[3] = pk2((aQ[6] + q1[2]) * qs, (aQ[7] + q1[3]) * qs);
        qf = __builtin_bit_cast(bf16x8, qp); PIN(qf);
    }
    }
    VMWAIT(0);
    BAR();
    if (tid == 0) stg_sc1(ws, WS_FLAG + (0 * 256 + g * 16 + j) * 256, epoch);
    STAMP(6);
    const int tt = w & 3;
    if (w >= 4) {
        const int t = tid - 256; const unsigned o = WS_MODN + g * 8192 + t * 32; u32x4 v, v2;
        for (unsigned sp = 0; sp < SPIN_CAP; ++sp) { asm volatile("" ::: "memory"); v = __builtin_amdgcn_raw_buffer_load_b128(rws, o, 0, 16); v2 = __builtin_amdgcn_raw_buffer_load_b128(rws, o + 16, 0, 16);
            if (__all(v[1] == epoch && v[3] == epoch && v2[1] == epoch && v2[3] == epoch)) break; }
        u32x4 val = {v[0], v[2], v2[0], v2[2]};
        const int vec = t >> 6, c8 = (t & 63) * 8;
        if (vec == 1) lds_st<u32x4>(L_SB16 + c8 * 2, val);
        else if (vec == 2) {
#pragma unroll
            for (int i = 0; i < 4; ++i) { const unsigned u_ = val[i]; val[i] = pk2(1.f + bf_lo(u_), 1.f + bf_hi(u_)); }
            lds_st<u32x4>(L_GB16 + c8 * 2, val);
        } else if ((c8 >> 6) == ct) lds_st<u32x4>((vec == 0 ? L_GAO : L_GBO) + (c8 & 63) * 2, val);
    }
    {
    FRESH(lane);
    const int hh = kh, qq = lane & 31, h = lane >> 5;
    const float NEG = -__builtin_inff();
    f32x16 c0, c8;
#pragma unroll
    for (int r = 0; r < 16; ++r) { const int kk = (r & 3) + 8 * (r >> 2) + 4 * h; c0[r] = kk > qq ? 0.f : NEG; c8[r] = kk <= qq ? 0.f : NEG; }
    PIN(c0); PIN(c8);
    BAR();
    STAMP(8);
    {
        const unsigned ka = L_KIMG + m * 2048 + hh * 1024 + lane * 16;
        const bool isd = ((lane >> 4) & 1) == hh;
        const unsigned va = isd ? L_VIMG + m * 2048 + lane * 16 : L_ONES, vst = isd ? 2048u : 0u, vs1 = isd ? 1024u : 0u;
        f32x16 o;
#pragma unroll
        for (int r = 0; r < 16; ++r) o[r] = 0.f;
#define MAX16(mx, s) { _Pragma("unroll") for (int r = 0; r < 16; r += 2) mx = fmaxf(fmaxf(mx, s[r]), s[r + 1]); }
#define EXPH(P, S, b) { P[0] = pk2(__builtin_amdgcn_exp2f(S[b]), __builtin_amdgcn_exp2f(S[b + 1])); P[1] = pk2(__builtin_amdgcn_exp2f(S[b + 2]), __builtin_amdgcn_exp2f(S[b + 3])); \
                        P[2] = pk2(__builtin_amdgcn_exp2f(S[b + 4]), __builtin_amdgcn_exp2f(S[b + 5])); P[3] = pk2(__builtin_amdgcn_exp2f(S[b + 6]), __builtin_amdgcn_exp2f(S[b + 7])); }
        if (g >= 2) {
            f32x16 z;
#pragma unroll
            for (int r = 0; r < 16; ++r) z[r] = 0.f;
            PIN(z);
            float mx = NEG, mx2 = NEG; f32x16 sA, sB;
#define KF(i) lds_ld<bf16x8>(ka + (i) * 2048)
            bf16x8 k0 = KF(0), k1 = KF(8), k2 = KF(1), k3 = KF(2);
            SBAR();
            sA = MFMA32(k0, qf, c0); k0 = KF(3); SBAR();
            sB = MFMA32(k1, qf, c8); k1 = KF(4); MAX16(mx, sA); PIN(mx); SBAR();
            sA = MFMA32(k2, qf, z); k2 = KF(5); MAX16(mx2, sB); PIN(mx2); SBAR();
            sB = MFMA32(k3, qf, z); k3 = KF(6); MAX16(mx, sA); PIN(mx); SBAR();
            sA = MFMA32(k0, qf, z); k0 = KF(7); MAX16(mx2, sB); PIN(mx2); SBAR();
            sB = MFMA32(k1, qf, z); k1 = KF(0); MAX16(mx, sA); PIN(mx); SBAR();
            sA = MFMA32(k2, qf, z); k2 = KF(1); MAX16(mx2, sB); PIN(mx2); SBAR();
            sB = MFMA32(k3, qf, z); MAX16(mx, sA); PIN(mx); SBAR();
            sA = MFMA32(k0, qf, z); MAX16(mx2, sB); PIN(mx2); SBAR();
            bf16x8 v0 = lds_ld<bf16x8>(va), v1 = lds_ld<bf16x8>(va + vs1);
            MAX16(mx, sA);
            mx = xmax32(fmaxf(mx, mx2));
#pragma unroll
            for (int r = 0; r < 16; ++r) { z[r] = -mx; c0[r] -= mx; c8[r] -= mx; }
            PIN(z); PIN(c0); PIN(c8); SBAR();
            sA = MFMA32(k1, qf, c0); SBAR();
            u32x4 pa, pb; bf16x8 v0n, v1n, kn = k2; unsigned vp = va;
#define ABLK(I, SC, SN, CN) { bf16x8 kc = kn; if ((I) < 8) { vp += vst; v0n = lds_ld<bf16x8>(vp); v1n = lds_ld<bf16x8>(vp + vs1); } if ((I) < 7) kn = KF((I) + 2); SBAR(); \
                if ((I) < 8) SN = MFMA32(kc, qf, CN); EXPH(pa, SC, 0); PIN(pa); SBAR(); \
                o = MFMA32(v0, __builtin_bit_cast(bf16x8, pa), o); EXPH(pb, SC, 8); PIN(pb); SBAR(); \
                o = MFMA32(v1, __builtin_bit_cast(bf16x8, pb), o); SBAR(); if ((I) < 8) { v0 = v0n; v1 = v1n; } }
            ABLK(0, sA, sB, z) ABLK(1, sB, sA, z) ABLK(2, sA, sB, z) ABLK(3, sB, sA, z)
            ABLK(4, sA, sB, z) ABLK(5, sB, sA, z) ABLK(6, sA, sB, z) ABLK(7, sB, sA, c8) ABLK(8, sA, sB, c8)
#undef KF
#undef ABLK
        } else {
            const int i0 = 8 - 4 * g - m;
            f32x16 z;
#pragma unroll
            for (int r = 0; r < 16; ++r) z[r] = 0.f;
            float mx = NEG;
            f32x16 s = MFMA32(lds_ld<bf16x8>(ka + 8 * 2048), qf, c8); MAX16(mx, s);
#pragma unroll 1
            for (int i = i0; i < 8; ++i) { s = MFMA32(lds_ld<bf16x8>(ka + i * 2048), qf, z); MAX16(mx, s); }
            mx = xmax32(mx);
#pragma unroll
            for (int r = 0; r < 16; ++r) { z[r] = -mx; c8[r] -= mx; }
#pragma unroll 1
            for (int i = i0; i <= 8; ++i) { u32x4 pa, pb;
                if (i < 8) s = MFMA32(lds_ld<bf16x8>(ka + i * 2048), qf, z); else s = MFMA32(lds_ld<bf16x8>(ka + i * 2048), qf, c8);
                EXPH(pa, s, 0); EXPH(pb, s, 8);
                o = MFMA32(lds_ld<bf16x8>(va + i * vst), __builtin_bit_cast(bf16x8, pa), o); o = MFMA32(lds_ld<bf16x8>(va + i * vst + vs1), __builtin_bit_cast(bf16x8, pb), o); }
        }
#undef EXPH
#undef MAX16
        float ov[8], l;
        { float lo = o[0], hi = o[8]; PIN(lo); PIN(hi); l = hh ? lo : hi; }
#pragma unroll
        for (int r = 0; r < 8; ++r) { float lo = o[r], hi = o[8 + r]; PIN(lo); PIN(hi); ov[r] = hh ? hi : lo; }
        const float inv = __builtin_amdgcn_rcpf(l);
        const u32x4 op = {pk2(ov[0] * inv, ov[1] * inv), pk2(ov[2] * inv, ov[3] * inv), pk2(ov[4] * inv, ov[5] * inv), pk2(ov[6] * inv, ov[7] * inv)};
        __builtin_amdgcn_raw_buffer_store_b128(op, rws, WS_O + ((g * 8 + 2 * m + (qq >> 4)) * 16 + j) * 1024 + ((qq & 15) + 16 * (2 * hh + h)) * 16, 0, 16);
    }
    }
    VMWAIT(0);
    BAR();
    if (tid == 0) stg_sc1(ws, WS_FLAG + (1 * 256 + g * 16 + j) * 256, epoch);
    STAMP(9);
    if (w == 0) poll_flags(ws, WS_FLAG + (1 * 256 + g * 16) * 256, 16, epoch, lane);
    BAR();
    STAMP(10);
    u32x2 x1p0, x1p1;
    {
        FRESH(lane);
        const int gi = lane >> 4, q4 = (lane & 15) >> 2, p4 = lane & 3;
        const unsigned tokP = 128 * g + 64 * rh + 16 * tt + (lane & 15), colP = 64 * ct + 32 * kh + 4 * gi;
        u32x4 of[8];
#pragma unroll
        for (int s = 0; s < 8; ++s) of[s] = __builtin_amdgcn_raw_buffer_load_b128(rws, WS_O + ((g * 8 + 4 * rh + tt) * 16 + 8 * kh + s) * 1024 + lane * 16, 0, 16);
        const u32x2 xres0 = *(const u32x2*)(x + tokP * 512 + colP), xres1 = *(const u32x2*)(x + tokP * 512 + colP + 16);
        SBAR();
        f32x4 acc[4];
#pragma unroll
        for (int n = 0; n < 4; ++n) acc[n] = (f32x4){0.f, 0.f, 0.f, 0.f};
        const unsigned xv = 2 * (gi & 1) + (q4 >> 1), rowa = L_WO + (256 * kh + 16 * (gi >> 1) + 4 * (gi & 1) + q4) * 128 + p4 * 8;
        unsigned an[4]; bf16x8 wf[4];
#pragma unroll
        for (int n = 0; n < 4; ++n) { an[n] = rowa + ((n ^ xv) * 32); wf[n] = trfrag(an[n], an[n] + 1024); }
        const unsigned cl = (32 * kh + 4 * gi) * 2, cg = colP * 2;
        const u32x2 ga0 = lds_ld<u32x2>(L_GAO + cl), ga1 = lds_ld<u32x2>(L_GAO + cl + 32);
        const u32x2 gb0 = lds_ld<u32x2>(L_GB16 + cg), gb1 = lds_ld<u32x2>(L_GB16 + cg + 32), sb0 = lds_ld<u32x2>(L_SB16 + cg), sb1 = lds_ld<u32x2>(L_SB16 + cg + 32);
        SBAR();
#pragma unroll
        for (int s = 0; s < 8; ++s)
#pragma unroll
            for (int n = 0; n < 4; ++n) { acc[n] = MFMA16(wf[n], __builtin_bit_cast(bf16x8, of[s]), acc[n]); PIN(acc[n]); SBAR(); if (s < 7) wf[n] = trfrag(an[n] + (s + 1) * 4096, an[n] + (s + 1) * 4096 + 1024); SBAR(); }
        BAR();
        STAMP(11);
        const unsigned ex = w * 2048 + lane * 16;
        { f32x4 s0 = acc[0], s1 = acc[1], s2 = acc[2], s3 = acc[3]; PIN(s0); PIN(s1); PIN(s2); PIN(s3);
          lds_st<f32x4>(ex, kh ? s0 : s2); lds_st<f32x4>(ex + 1024, kh ? s1 : s3); acc[0] = kh ? s2 : s0; acc[1] = kh ? s3 : s1; }
        BAR();
        const unsigned ep = (w ^ 4) * 2048 + lane * 16;
        const f32x4 a0 = lds_ld<f32x4>(ep) + acc[0], a1 = lds_ld<f32x4>(ep + 1024) + acc[1];
        float xv1[8], gg[8], ss[8];
#pragma unroll
        for (int r = 0; r < 4; ++r) {
            const unsigned ua0 = ga0[r >> 1], ua1 = ga1[r >> 1], ux0 = xres0[r >> 1], ux1 = xres1[r >> 1], ug0 = gb0[r >> 1], ug1 = gb1[r >> 1], us0 = sb0[r >> 1], us1 = sb1[r >> 1];
            xv1[r] = rbf(fmaf((r & 1) ? bf_hi(ua0) : bf_lo(ua0), a0[r], (r & 1) ? bf_hi(ux0) : bf_lo(ux0))); xv1[4 + r] = rbf(fmaf((r & 1) ? bf_hi(ua1) : bf_lo(ua1), a1[r], (r & 1) ? bf_hi(ux1) : bf_lo(ux1)));
            gg[r] = (r & 1) ? bf_hi(ug0) : bf_lo(ug0); gg[4 + r] = (r & 1) ? bf_hi(ug1) : bf_lo(ug1);
            ss[r] = (r & 1) ? bf_hi(us0) : bf_lo(us0); ss[4 + r] = (r & 1) ? bf_hi(us1) : bf_lo(us1);
        }
        x1p0 = (u32x2){pk2(xv1[0], xv1[1]), pk2(xv1[2], xv1[3])}; x1p1 = (u32x2){pk2(xv1[4], xv1[5]), pk2(xv1[6], xv1[7])};
        float st[6] = {0.f, 0.f, 0.f, 0.f, 0.f, 0.f}, pg[8];
#pragma unroll
        for (int r = 0; r < 8; ++r) pg[r] = xv1[r] * gg[r];
        const u32x4 xg = {pk2(pg[0], pg[1]), pk2(pg[2], pg[3]), pk2(pg[4], pg[5]), pk2(pg[6], pg[7])};
        __builtin_amdgcn_raw_buffer_store_b128(xg, rws, WS_XG + ((g * 8 + 4 * rh + tt) * 16 + 2 * ct + kh) * 1024 + lane * 16, 0, 16);
#pragma unroll
        for (int r = 0; r < 8; ++r) { const float p = pg[r]; st[0] += xv1[r]; st[1] = fmaf(xv1[r], xv1[r], st[1]); st[2] += p; st[3] = fmaf(p, p, st[3]); st[4] = fmaf(p, gg[r], st[4]); st[5] = fmaf(p, ss[r], st[5]); }
#pragma unroll
        for (int i = 0; i < 6; ++i) st[i] = xsum32(xsum16(st[i]));
        if (kh == 1 && lane < 16) { lds_st<f32x4>(L_SCR + (tt * 16 + lane) * 32, (f32x4){st[0], st[1], st[2], st[3]}); lds_st<u32x2>(L_SCR + (tt * 16 + lane) * 32 + 16, (u32x2){__float_as_uint(st[4]), __float_as_uint(st[5])}); }
        BAR();
        if (kh == 0 && lane < 16) {
            const f32x4 o4 = lds_ld<f32x4>(L_SCR + (tt * 16 + lane) * 32); const u32x2 o2 = lds_ld<u32x2>(L_SCR + (tt * 16 + lane) * 32 + 16);
            const unsigned so = WS_ST + g * 24576, T = 4 * rh + tt;
            const u32x4 sa4 = {__float_as_uint(st[0] + o4[0]), __float_as_uint(st[1] + o4[1]), __float_as_uint(st[2] + o4[2]), __float_as_uint(st[3] + o4[3])};
            const u32x2 sb2 = {__float_as_uint(st[4] + __uint_as_float(o2[0])), __float_as_uint(st[5] + __uint_as_float(o2[1]))};
            __builtin_amdgcn_raw_buffer_store_b128(sa4, rws, so + ((T * 8 + ct) * 16 + lane) * 16, 0, 16);
            __builtin_amdgcn_raw_buffer_store_b64(sb2, rws, so + 16384 + ((T * 8 + ct) * 16 + lane) * 8, 0, 16);
        }
    }
    VMWAIT(0);
    BAR();
    if (tid == 0) stg_sc1(ws, WS_FLAG + (2 * 256 + g * 16 + j) * 256, epoch);
    STAMP(12);
    const int odd = j & 1;
    {
    FRESH(lane);
    const int gi = lane >> 4, q4 = (lane & 15) >> 2, p4 = lane & 3;
    const u32x2 b1v = *(const u32x2*)(b1 + 112 * j + (w > 0 ? w - 1 : 0) * 16 + 4 * gi);
    BAR();
    if (w > 0) {
        const int nt = w - 1; f32x4 acc = {0.f, 0.f, 0.f, 0.f};
        const unsigned ra = L_W1 + (4 * gi + q4) * 224 + p4 * 8 + nt * 32; const int tk = lane & 15;
        const unsigned va = (tk == 0 ? L_GB16 : L_SB16) + 8 * gi;
#pragma unroll
        for (int s = 0; s < 16; ++s) {
            const u32x2 lo = lds_ld<u32x2>(va + s * 64), hi = lds_ld<u32x2>(va + s * 64 + 32);
            u32x4 bv = {lo[0], lo[1], hi[0], hi[1]};
            if (tk == 2) bv = (u32x4){0x3F803F80u, 0x3F803F80u, 0x3F803F80u, 0x3F803F80u};
            if (tk > 2) bv = (u32x4){0u, 0u, 0u, 0u};
            acc = MFMA16(trfrag(ra + s * 7168, ra + s * 7168 + 3584), __builtin_bit_cast(bf16x8, bv), acc);
        }
        if (tk < 3) lds_st<f32x4>(L_UVW + (tk * 112 + nt * 16 + 4 * gi) * 4, acc);
        if (tk == 3) lds_st<f32x4>(L_UVW + (3 * 112 + nt * 16 + 4 * gi) * 4, (f32x4){bf_lo(b1v[0]), bf_hi(b1v[0]), bf_lo(b1v[1]), bf_hi(b1v[1])});
    } else {
        const u32x4 gq = lds_ld<u32x4>(L_GB16 + lane * 16), sq = lds_ld<u32x4>(L_SB16 + lane * 16);
        float c5[5] = {0.f, 0.f, 0.f, 0.f, 0.f};
#pragma unroll
        for (int i = 0; i < 4; ++i) { const unsigned ug = gq[i], us = sq[i]; const float g0 = bf_lo(ug), g1 = bf_hi(ug), s0 = bf_lo(us), s1 = bf_hi(us);
            c5[0] += g0 + g1; c5[1] += s0 + s1; c5[2] += g0 * g0 + g1 * g1; c5[3] += g0 * s0 + g1 * s1; c5[4] += s0 * s0 + s1 * s1; }
#pragma unroll
        for (int i = 0; i < 5; ++i) c5[i] = xsum32(xsum16(rowsum16(c5[i])));
        if (lane == 0) { lds_st<f32x4>(L_CONST, (f32x4){c5[0], c5[1], c5[2], c5[3]}); lds_st<float>(L_CONST + 16, c5[4]); }
        poll_flags(ws, WS_FLAG + (2 * 256 + g * 16) * 256, 16, epoch, lane);
    }
    }
    BAR();
    STAMP(13);
    {
        constexpr int WIN = 6;
        FRESH(lane);
        const int gi = lane >> 4, q4 = (lane & 15) >> 2, p4 = lane & 3;
        u32x4 xf[16];
        const unsigned xo = WS_XG + (g * 8 + w) * 16384 + lane * 16;
#pragma unroll
        for (int s = 0; s < WIN; ++s) xf[s] = __builtin_amdgcn_raw_buffer_load_b128(rws, xo + s * 1024, 0, 16);
        SBAR(); WSTAMP(0);
        f32x4 acc[7]; bf16x8 wf[7];
        const unsigned ra = L_W1 + (4 * gi + q4) * 224 + p4 * 8, raA = ra + 32 * odd, raB = ra + (odd ? 0 : 192);
        const unsigned raA2 = raA + 8 * 7168, raB2 = raB + 8 * 7168;
#define W1A(s, u) (((s) < 8 ? ((u) < 6 ? raA : raB) : ((u) < 6 ? raA2 : raB2)) + ((s) & 7) * 7168 + ((u) < 6 ? (u) * 32 : 0))
#pragma unroll
        for (int u = 0; u < 7; ++u) { acc[u] = (f32x4){0.f, 0.f, 0.f, 0.f}; wf[u] = trfrag(W1A(0, u), W1A(0, u) + 3584); }
        SBAR();
        u32x4 sA0, sA1; u32x2 sB0, sB1;
        const unsigned so = WS_ST + g * 24576 + w * 2048, sob = WS_ST + g * 24576 + 16384 + w * 1024;
#pragma unroll
        for (int s = 0; s < 16; ++s) {
            if (s + WIN < 16) xf[s + WIN] = __builtin_amdgcn_raw_buffer_load_b128(rws, xo + (s + WIN) * 1024, 0, 16);
            if (s == 10) { sA0 = __builtin_amdgcn_raw_buffer_load_b128(rws, so + ((2 * gi) * 16 + (lane & 15)) * 16, 0, 16); sA1 = __builtin_amdgcn_raw_buffer_load_b128(rws, so + ((2 * gi + 1) * 16 + (lane & 15)) * 16, 0, 16); }
            if (s == 11) { sB0 = __builtin_amdgcn_raw_buffer_load_b64(rws, sob + ((2 * gi) * 16 + (lane & 15)) * 8, 0, 16); sB1 = __builtin_amdgcn_raw_buffer_load_b64(rws, sob + ((2 * gi + 1) * 16 + (lane & 15)) * 8, 0, 16); }
            SBAR();
#pragma unroll
            for (int u = 0; u < 7; ++u) { acc[u] = MFMA16(wf[u], __builtin_bit_cast(bf16x8, xf[s]), acc[u]); PIN(acc[u]); SBAR(); if (s < 15) wf[u] = trfrag(W1A(s + 1, u), W1A(s + 1, u) + 3584); SBAR(); }
            if (s == 0) WSTAMP(1); if (s == 7) WSTAMP(2); if (s == 15) WSTAMP(3);
        }
#undef W1A
        const unsigned uo = L_UVW + 16 * gi + 64 * odd, uo6 = L_UVW + 16 * gi + (odd ? 0 : 384);
        f32x4 uu = lds_ld<f32x4>(uo), vv = lds_ld<f32x4>(uo + 448), ww = lds_ld<f32x4>(uo + 896), bb = lds_ld<f32x4>(uo + 1344);
        const f32x4 cc4 = lds_ld<f32x4>(L_CONST); const float SB2 = lds_ld<float>(L_CONST + 16);
        float S[6];
#pragma unroll
        for (int i = 0; i < 4; ++i) S[i] = xsum32(xsum16(__uint_as_float(sA0[i]) + __uint_as_float(sA1[i])));
#pragma unroll
        for (int i = 0; i < 2; ++i) S[4 + i] = xsum32(xsum16(__uint_as_float(sB0[i]) + __uint_as_float(sB1[i])));
        const float iD = 1.f / 512.f, m1 = S[0] * iD, r1 = __builtin_amdgcn_rsqf(fmaxf(S[1] * iD - m1 * m1, 0.f) + 1e-5f);
        const float m2 = (r1 * (S[2] - m1 * cc4[0]) + cc4[1]) * iD;
        const float ez2 = (r1 * r1 * (S[3] - 2.f * m1 * S[4] + m1 * m1 * cc4[2]) + 2.f * r1 * (S[5] - m1 * cc4[3]) + SB2) * iD;
        const float r2 = __builtin_amdgcn_rsqf(fmaxf(ez2 - m2 * m2, 0.f) + 1e-5f);
        const float ca = r1 * r2, cb = -ca * m1, cc = -r2 * m2;
        BAR();
        STAMP(14);
        unsigned hp[7][2];
        const unsigned hb = WS_H + (g * 8 + w) * 57344, s0 = (7 * j + odd) >> 1;
#pragma unroll
        for (int u = 0; u < 7; ++u) {
            float pre[4], z[4], e[4];
#pragma unroll
            for (int r = 0; r < 4; ++r) pre[r] = fmaf(ca, acc[u][r], fmaf(cb, uu[r], fmaf(r2, vv[r], fmaf(cc, ww[r], bb[r]))));
            if (u < 6) { const unsigned o = u < 5 ? uo + (u + 1) * 64 : uo6; uu = lds_ld<f32x4>(o); vv = lds_ld<f32x4>(o + 448); ww = lds_ld<f32x4>(o + 896); bb = lds_ld<f32x4>(o + 1344); }
            SBAR();
#pragma unroll
            for (int r = 0; r < 4; ++r) z[r] = pre[r] * fmaf(pre[r] * pre[r], -0.044715f * 2.302208198f, -2.302208198f);
#pragma unroll
            for (int r = 0; r < 4; ++r) e[r] = __builtin_amdgcn_exp2f(z[r]);
#pragma unroll
            for (int r = 0; r < 4; ++r) e[r] = __builtin_amdgcn_rcpf(1.f + e[r]);
            hp[u][0] = pk2(pre[0] * e[0], pre[1] * e[1]); hp[u][1] = pk2(pre[2] * e[2], pre[3] * e[3]);
            if (u < 6 && (u & 1)) __builtin_amdgcn_raw_buffer_store_b128((u32x4){hp[u - 1][0], hp[u - 1][1], hp[u][0], hp[u][1]}, rws, hb + (s0 + (u >> 1)) * 1024 + lane * 16, 0, 16);
            SBAR();
        }
        __builtin_amdgcn_raw_buffer_store_b64((u32x2){hp[6][0], hp[6][1]}, rws, hb + ((7 * j + (odd ? 0 : 6)) >> 1) * 1024 + odd * 512 + lane * 8, 0, 16);
        WSTAMP(4);
    }
    VMWAIT(0); WSTAMP(5);
    BAR();
    if (tid == 0) stg_sc1(ws, WS_FLAG + (3 * 256 + g * 16 + j) * 256, epoch);
    STAMP(15);
    if (w == 0) poll_flags(ws, WS_FLAG + (3 * 256 + g * 16) * 256, 16, epoch, lane);
    BAR();
    STAMP(16);
    {
        constexpr int WIN = 8;
        FRESH(lane);
        const int gi = lane >> 4, q4 = (lane & 15) >> 2, p4 = lane & 3;
        const unsigned tokP = 128 * g + 64 * rh + 16 * tt + (lane & 15), colP = 64 * ct + 32 * kh + 4 * gi;
        const unsigned hb = WS_H + (g * 8 + 4 * rh + tt) * 57344 + kh * 28 * 1024;
        u32x4 hf[28];
#define HLOAD(s) { if ((s) % 7 == 3) { const u32x2 lo = __builtin_amdgcn_raw_buffer_load_b64(rws, hb + (s) * 1024 + lane * 8, 0, 16), hi = __builtin_amdgcn_raw_buffer_load_b64(rws, hb + (s) * 1024 + 512 + lane * 8, 0, 16); hf[s] = (u32x4){lo[0], lo[1], hi[0], hi[1]}; } \
                    else hf[s] = __builtin_amdgcn_raw_buffer_load_b128(rws, hb + (s) * 1024 + lane * 16, 0, 16); }
#pragma unroll
        for (int s = 0; s < WIN; ++s) HLOAD(s)
        SBAR();
        f32x4 acc[4]; bf16x8 wf[4];
        const unsigned xv = (2 * gi + (q4 >> 1)) & 3, rowa = kh * 8192 + (4 * gi + q4) * 128 + p4 * 8;
        unsigned an[4], an2[4], an3[4];
#pragma unroll
        for (int n = 0; n < 4; ++n) { an[n] = rowa + ((n ^ xv) * 32); an2[n] = an[n] + 4 * 16384; an3[n] = an[n] + 8 * 16384; acc[n] = (f32x4){0.f, 0.f, 0.f, 0.f}; }
#define W2A(s, n) ((((s) >> 1) % 9 < 4 ? an[n] : ((s) >> 1) % 9 < 8 ? an2[n] : an3[n]) + ((((s) >> 1) % 9) & 3) * 16384 + ((s) & 1) * 4096)
#pragma unroll
        for (int n = 0; n < 4; ++n) wf[n] = trfrag(W2A(0, n), W2A(0, n) + 2048);
        const unsigned cl = (32 * kh + 4 * gi) * 2;
        const u32x2 g0 = lds_ld<u32x2>(L_GBO + cl), g1 = lds_ld<u32x2>(L_GBO + cl + 32);
        u32x2 bb0, bb1;
        SBAR();
#pragma unroll
        for (int s = 0; s < 28; ++s) {
            if (s == 24) { bb0 = *(const u32x2*)(b2 + colP); bb1 = *(const u32x2*)(b2 + colP + 16); }
            if (s == 10) BAR();
            if (s == 17) BAR();
            if (s + WIN < 28) HLOAD(s + WIN)
            SBAR();
#pragma unroll
            for (int n = 0; n < 4; ++n) { acc[n] = MFMA16(wf[n], __builtin_bit_cast(bf16x8, hf[s]), acc[n]); PIN(acc[n]); SBAR(); if (s < 27) wf[n] = trfrag(W2A(s + 1, n), W2A(s + 1, n) + 2048); SBAR(); }
        }
#undef W2A
#undef HLOAD
        BAR();
        STAMP(17);
        const unsigned ex = w * 2048 + lane * 16;
        { f32x4 s0 = acc[0], s1 = acc[1], s2 = acc[2], s3 = acc[3]; PIN(s0); PIN(s1); PIN(s2); PIN(s3);
          lds_st<f32x4>(ex, kh ? s0 : s2); lds_st<f32x4>(ex + 1024, kh ? s1 : s3); acc[0] = kh ? s2 : s0; acc[1] = kh ? s3 : s1; }
        BAR();
        const unsigned ep = (w ^ 4) * 2048 + lane * 16;
        const f32x4 a0 = lds_ld<f32x4>(ep) + acc[0], a1 = lds_ld<f32x4>(ep + 1024) + acc[1];
        float y0[4], y1[4];
#pragma unroll
        for (int r = 0; r < 4; ++r) {
            const unsigned ug0 = g0[r >> 1], ug1 = g1[r >> 1], ub0 = bb0[r >> 1], ub1 = bb1[r >> 1], ux0 = x1p0[r >> 1], ux1 = x1p1[r >> 1];
            y0[r] = fmaf((r & 1) ? bf_hi(ug0) : bf_lo(ug0), a0[r] + ((r & 1) ? bf_hi(ub0) : bf_lo(ub0)), (r & 1) ? bf_hi(ux0) : bf_lo(ux0));
            y1[r] = fmaf((r & 1) ? bf_hi(ug1) : bf_lo(ug1), a1[r] + ((r & 1) ? bf_hi(ub1) : bf_lo(ub1)), (r & 1) ? bf_hi(ux1) : bf_lo(ux1));
        }
        *(u32x2*)(out + tokP * 512 + colP) = (u32x2){pk2(y0[0], y0[1]), pk2(y0[2], y0[3])};
        *(u32x2*)(out + tokP * 512 + colP + 16) = (u32x2){pk2(y1[0], y1[1]), pk2(y1[2], y1[3])};
    }
    if (b == 0 && w == 0) {
        for (unsigned sp = 0; sp < SPIN_CAP; ++sp) { asm volatile("" ::: "memory"); const u32x4 v = __builtin_amdgcn_raw_buffer_load_b128(rws, WS_HELLO + lane * 16, 0, 16); if (__all(v[0] == epoch && v[1] == epoch && v[2] == epoch && v[3] == epoch)) break; }
        if (lane == 0) stg_sc1(ws, WS_E, epoch);
    }
    STAMP(18); STAMPC(21);
    STAMP_OUT();
    asm volatile("fb_code_end:");
}

extern "C" void kernel_launch(void* const* d_in, const int* in_sizes, int n_in, void* d_out, int out_size, void* d_ws, size_t ws_size, hipStream_t stream) {
    (void)hipFuncSetAttribute((const void*)fused_block, hipFuncAttributeMaxDynamicSharedMemorySize, LDS_BYTES);
    fused_block<<<256, 576, LDS_BYTES, stream>>>((const u16*)d_in[0], (const u16*)d_in[1], (const u16*)d_in[2], (const u16*)d_in[4], (const u16*)d_in[5], (const u16*)d_in[6], (const u16*)d_in[7],
                                                 (const u16*)d_in[8], (const u16*)d_in[9], (u16*)d_out, (char*)d_ws);
}
```

```cpp
#include <hip/hip_runtime.h>
#include <hip/hip_bf16.h>
#include <stdint.h>

typedef unsigned short u16;
typedef short bf16x8 __attribute__((ext_vector_type(8)));
typedef short s16x4 __attribute__((ext_vector_type(4)));
typedef float f32x4 __attribute__((ext_vector_type(4)));
typedef float f32x16 __attribute__((ext_vector_type(16)));
typedef unsigned u32x4 __attribute__((ext_vector_type(4)));
typedef unsigned u32x2 __attribute__((ext_vector_type(2)));
typedef __bf16 bf2 __attribute__((ext_vector_type(2)));
#define LAS __attribute__((address_space(3)))
#define GAS __attribute__((address_space(1)))
#define DI __device__ __forceinline__

#define TS 0

constexpr unsigned WS_E = 0, WS_HELLO = 4096, WS_FLAG = 8192;
constexpr unsigned WS_MODU = 0x50000, WS_MODN = 0x60000;
constexpr unsigned WS_KV = 0x100000, WS_O = 0x500000, WS_XG = 0x700000, WS_ST = 0x900000, WS_H = 0xA00000, WS_TS = 0x2000000;
constexpr unsigned WS_BYTES = 0x2100000;
constexpr unsigned L_X = 0, L_WQKV = 57344, L_RED = 155648, L_SAG = 156672, L_STAT = 160768;
constexpr unsigned L_QS = 65536, L_KIMG = 98304, L_VIMG = 122880, L_WO = 0, L_W1 = 44032;
constexpr unsigned L_GB16 = 158720, L_SB16 = 159744, L_UVW = 160768, L_CONST = 162560, L_GAO = 162592, L_GBO = 162720, L_ONES = 162848, L_DUMMY = 162880;
constexpr unsigned L_SCR = 32768;
constexpr unsigned LDS_BYTES = 163840;
constexpr unsigned SPIN_CAP = 1u << 16;

DI float bf_lo(unsigned u) { return __uint_as_float(u << 16); }
DI float bf_hi(unsigned u) { return __uint_as_float(u & 0xffff0000u); }
DI unsigned pk2(float a, float b) { bf2 v = {(__bf16)a, (__bf16)b}; return __builtin_bit_cast(unsigned, v); }
DI float rbf(float a) { return bf_lo(pk2(a, a)); }
DI void dma16(unsigned voff, const void* sbase, unsigned lds) { asm volatile("s_mov_b32 m0, %1\n\ts_nop 0\n\tglobal_load_lds_dwordx4 %0, %2" ::"v"(voff), "s"(lds), "s"(sbase) : "memory"); }
DI void dma16c(unsigned voff, const void* sbase, unsigned lds) { asm volatile("s_mov_b32 m0, %1\n\ts_nop 0\n\tglobal_load_lds_dwordx4 %0, %2 sc1" ::"v"(voff), "s"(lds), "s"(sbase) : "memory"); }
DI void warm4(unsigned voff, const void* sbase) { asm volatile("s_mov_b32 m0, %1\n\ts_nop 0\n\tglobal_load_lds_dword %0, %2" ::"v"(voff), "s"(L_DUMMY), "s"(sbase) : "memory"); }
#define FRESH(v) int v = threadIdx.x & 63; asm volatile("" : "+v"(v))
#define SBAR() __builtin_amdgcn_sched_barrier(0)
#define PIN(x) asm volatile("" : "+v"(x))
#define BAR() asm volatile("s_waitcnt lgkmcnt(0)\n\ts_barrier" ::: "memory")
#define VMWAIT(n) asm volatile("s_waitcnt vmcnt(" #n ")" ::: "memory")
DI unsigned ldg_sc1(const char* ws, unsigned off) { return __hip_atomic_load((GAS unsigned*)(ws + off), __ATOMIC_RELAXED, __HIP_MEMORY_SCOPE_AGENT); }
DI unsigned long long ldg64_sc1(const char* ws, unsigned off) { return __hip_atomic_load((GAS unsigned long long*)(ws + off), __ATOMIC_RELAXED, __HIP_MEMORY_SCOPE_AGENT); }
DI void stg_sc1(char* ws, unsigned off, unsigned v) { __hip_atomic_store((GAS unsigned*)(ws + off), v, __ATOMIC_RELAXED, __HIP_MEMORY_SCOPE_AGENT); }
DI void stg64_sc1(char* ws, unsigned off, unsigned long long v) { __hip_atomic_store((GAS unsigned long long*)(ws + off), v, __ATOMIC_RELAXED, __HIP_MEMORY_SCOPE_AGENT); }
template <class T> DI T lds_ld(unsigned a) { return *(const LAS T*)(size_t)a; }
template <class T> DI void lds_st(unsigned a, T v) { *(LAS T*)(size_t)a = v; }
DI s16x4 lds_tr(unsigned a) { return __builtin_amdgcn_ds_read_tr16_b64_v4i16((LAS s16x4*)(size_t)a); }
DI bf16x8 trfrag(unsigned a0, unsigned a1) { s16x4 lo = lds_tr(a0), hi = lds_tr(a1); return __builtin_shufflevector(lo, hi, 0, 1, 2, 3, 4, 5, 6, 7); }
#define MFMA32(a, b, c) __builtin_amdgcn_mfma_f32_32x32x16_bf16((a), (b), (c), 0, 0, 0)
#define MFMA16(a, b, c) __builtin_amdgcn_mfma_f32_16x16x32_bf16((a), (b), (c), 0, 0, 0)
template <int N> DI float bcast(float v) { return __builtin_bit_cast(float, __builtin_amdgcn_mov_dpp(__builtin_bit_cast(int, v), 0x150 + N, 0xf, 0xf, true)); }
template <int N> DI float modu(float t, float gv, float sv) {
#pragma clang fp contract(off)
    const float p = bcast<N>(gv) * t; return bcast<N>(sv) + p; }
template <int N> DI unsigned hdw(unsigned x, float r, float n, float gv, float sv) {
    unsigned o; float a, b2, c, d;
    asm("v_lshlrev_b32 %1, 16, %5\n\tv_and_b32 %2, 0xffff0000, %5\n\tv_fma_f32 %1, %1, %6, %7\n\tv_fma_f32 %2, %2, %6, %7\n\t"
        "v_mov_b32_dpp %3, %9 row_newbcast:%c10 row_mask:0xf bank_mask:0xf\n\tv_mov_b32_dpp %4, %9 row_newbcast:%c11 row_mask:0xf bank_mask:0xf\n\t"
        "v_fmac_f32_dpp %3, %8, %1 row_newbcast:%c10 row_mask:0xf bank_mask:0xf\n\tv_fmac_f32_dpp %4, %8, %2 row_newbcast:%c11 row_mask:0xf bank_mask:0xf\n\t"
        "v_cvt_pk_bf16_f32 %0, %3, %4"
        : "=v"(o), "=&v"(a), "=&v"(b2), "=&v"(c), "=&v"(d) : "v"(x), "v"(r), "v"(n), "v"(gv), "v"(sv), "i"(N), "i"(N + 1));
    return o; }
template <int C> DI float dppf(float v) { return __builtin_bit_cast(float, __builtin_amdgcn_update_dpp(0, __builtin_bit_cast(int, v), C, 0xf, 0xf, true)); }
DI float rowsum16(float v) { v += dppf<0xB1>(v); v += dppf<0x4E>(v); v += dppf<0x141>(v); v += dppf<0x140>(v); return v; }
DI float xsum32(float v) { auto r = __builtin_amdgcn_permlane32_swap(__float_as_uint(v), __float_as_uint(v), false, false); return __uint_as_float(r[0]) + __uint_as_float(r[1]); }
DI float xmax32(float v) { auto r = __builtin_amdgcn_permlane32_swap(__float_as_uint(v), __float_as_uint(v), false, false); return fmaxf(__uint_as_float(r[0]), __uint_as_float(r[1])); }
DI float xsum16(float v) { auto r = __builtin_amdgcn_permlane16_swap(__float_as_uint(v), __float_as_uint(v), false, false); return __uint_as_float(r[0]) + __uint_as_float(r[1]); }
DI unsigned long long mt() { unsigned long long t; asm volatile("s_memtime %0\n\ts_waitcnt lgkmcnt(0)" : "=s"(t)::"memory"); return t; }
DI unsigned long long rt() { unsigned long long t; asm volatile("s_memrealtime %0\n\ts_waitcnt lgkmcnt(0)" : "=s"(t)::"memory"); return t; }
#define STAMP(i) do {} while (0)
#define STAMP_OUT() do {} while (0)
#define STAMPC(i) do {} while (0)
#define WSTAMP(i) do {} while (0)

DI void poll_flags(const char* ws, unsigned off, int n, unsigned epoch, int lane) {
    const unsigned o = off + (unsigned)(lane < n ? lane : 0) * 256u;
    for (unsigned sp = 0; sp < SPIN_CAP; ++sp) { unsigned f = ldg_sc1(ws, o); if (__all(f == epoch)) break; }
}

__global__ __launch_bounds__(576, 3) void fused_block(const u16* __restrict__ x, const u16* __restrict__ wqkv, const u16* __restrict__ wo, const u16* __restrict__ w1, const u16* __restrict__ b1,
                                                      const u16* __restrict__ w2, const u16* __restrict__ b2, const u16* __restrict__ cvec, const u16* __restrict__ wmod, u16* __restrict__ out, char* __restrict__ ws) {
    unsigned long long e_t0, e_t1, e_t2;
    asm volatile("s_memrealtime %0" : "=s"(e_t0));
    asm volatile("s_memrealtime %0\n\ts_waitcnt lgkmcnt(0)" : "=s"(e_t1) : "s"(ws));
    const unsigned epoch = *(const unsigned*)(ws + WS_E) + 1u;
    asm volatile("s_memrealtime %0\n\ts_waitcnt lgkmcnt(0)" : "=s"(e_t2) : "s"(epoch));
    const int tid = threadIdx.x, lane = tid & 63, w = __builtin_amdgcn_readfirstlane(tid >> 6);
    const int b = blockIdx.x, xl = b & 7, idx = b >> 3, g = 2 * xl + (idx >> 4), j = idx & 15;
    const int ct = j & 7, rh = j >> 3;
    const bool prod = idx < 12;
    unsigned long long code_pc; unsigned code_len;
    asm volatile("s_getpc_b64 %0\nfb_code_begin:\n\ts_mov_b32 %1, fb_code_end-fb_code_begin" : "=s"(code_pc), "=s"(code_len));
    STAMP(0); STAMPC(20);
    if (w == 8) {
        {
            const unsigned o = ((unsigned)idx * 32u + (lane & 31)) * 64u;
            warm4((int)o < (int)code_len ? o : 0u, (const void*)code_pc);
        }
        if (lane == 0) stg_sc1(ws, WS_HELLO + b * 4, epoch);
        const unsigned sw128 = (lane >> 3) * 1024u + ct * 128u + (((lane & 7) >> 1) ^ ((lane >> 4) & 3)) * 32u + (lane & 1) * 16u;
        unsigned w1o[7];
#pragma unroll
        for (int r = 0; r < 7; ++r) { const unsigned u = r * 64 + lane, row = u / 14, c = u % 14; w1o[r] = row * 3584u + 224u * j + c * 16u; }
#define W1GRP(k) { _Pragma("unroll") for (int r = 0; r < 7; ++r) dma16(w1o[r] + (k) * 114688u, w1, L_W1 + (7 * (k) + r) * 1024); }
#define W2CHUNK(c, slot) { _Pragma("unroll 1") for (int pp = 0; pp < 16; ++pp) dma16(sw128 + ((pp >> 3) * 112 + 8 * (c) + (pp & 7)) * 8192u, w2, (slot) * 16384 + pp * 1024); }
        if (prod) __builtin_amdgcn_s_barrier();
        __builtin_amdgcn_s_barrier();
        __builtin_amdgcn_s_barrier();
        __builtin_amdgcn_s_barrier();
#pragma unroll 1
        for (int p = 0; p < 56; ++p) dma16(sw128 + p * 8192u, wo, L_WO + p * 1024);
        __builtin_amdgcn_s_barrier();
#pragma unroll 1
        for (int p = 56; p < 64; ++p) dma16(sw128 + p * 8192u, wo, L_WO + p * 1024);
        __builtin_amdgcn_s_barrier();
        __builtin_amdgcn_s_barrier();
        if (g >= 1) {
            const unsigned fo = WS_FLAG + (((g >= 2 ? g - 2 + (lane & 1) : 0) * 16 + j) * 256);
            for (unsigned sp = 0; sp < SPIN_CAP; ++sp) { unsigned f = ldg_sc1(ws, fo); if (__all(f == epoch)) break; }
#pragma unroll 1
            for (int p = (g >= 2 ? 0 : 16); p < 32; ++p) { const int src = p >> 4, r = p & 15;
                const unsigned dst = r < 8 ? L_KIMG + (4 * src + (r & 3)) * 2048 + (r >> 2) * 1024 : L_VIMG + (4 * src + ((r - 8) >> 1)) * 2048 + (r & 1) * 1024;
                dma16c(WS_KV + ((g - 2 + src) * 16 + j) * 16384 + r * 1024 + lane * 16, ws, dst); }
        }
        VMWAIT(0);
        __builtin_amdgcn_s_barrier();
#pragma unroll 1
        for (int k = 3; k < 7; ++k) W1GRP(k)
        W1GRP(15)
        __builtin_amdgcn_s_barrier();
#pragma unroll 1
        for (int k = 7; k < 15; ++k) W1GRP(k)
        VMWAIT(56);
        __builtin_amdgcn_s_barrier();
        __builtin_amdgcn_s_barrier();
#pragma unroll 1
        for (int k = 0; k < 3; ++k) W1GRP(k)
        W2CHUNK(1, 1)
        __builtin_amdgcn_s_barrier();
        __builtin_amdgcn_s_barrier();
        __builtin_amdgcn_s_barrier();
        W2CHUNK(0, 0)
        VMWAIT(32);
        __builtin_amdgcn_s_barrier();
        __builtin_amdgcn_s_barrier();
        __builtin_amdgcn_s_barrier();
#pragma unroll 1
        for (int c = 2; c < 9; ++c) W2CHUNK(c, c)
        __builtin_amdgcn_s_barrier();
        VMWAIT(0);
        __builtin_amdgcn_s_barrier();
        __builtin_amdgcn_s_barrier();
#pragma unroll 1
        for (int c = 9; c < 14; ++c) W2CHUNK(c, c - 9)
        VMWAIT(0);
        __builtin_amdgcn_s_barrier();
        __builtin_amdgcn_s_barrier();
        __builtin_amdgcn_s_barrier();
#undef W1GRP
#undef W2CHUNK
        return;
    }
    const __amdgpu_buffer_rsrc_t rws = __builtin_amdgcn_make_buffer_rsrc(ws, 0, WS_BYTES, 0x00020000);
    const int mpair = idx >> 1, col0 = 64 * (8 * mpair + ((xl + mpair) & 7)) + 32 * (idx & 1);
    u32x2 cv; u32x4 wm0, wm1, wm2, wm3;
    if (prod) {
        const unsigned o = (unsigned)(tid >> 2) * (4u * 6144u) + (unsigned)col0 * 2u + (tid & 3) * 16u;
        asm volatile("global_load_dwordx2 %0, %1, %2" : "=v"(cv) : "v"((tid >> 2) * 8), "s"(cvec) : "memory");
        asm volatile("global_load_dwordx4 %0, %1, %2" : "=v"(wm0) : "v"(o), "s"(wmod) : "memory");
        asm volatile("global_load_dwordx4 %0, %1, %2" : "=v"(wm1) : "v"(o + 6144u), "s"(wmod) : "memory");
        asm volatile("global_load_dwordx4 %0, %1, %2" : "=v"(wm2) : "v"(o + 12288u), "s"(wmod) : "memory");
        asm volatile("global_load_dwordx4 %0, %1, %2" : "=v"(wm3) : "v"(o + 18432u), "s"(wmod) : "memory");
    }
    { const unsigned xo = (unsigned)(128 * g + 16 * w) * 1024u + ((unsigned)lane << 4);
#pragma unroll
      for (int i = 0; i < 16; ++i) dma16((xo ^ (unsigned)(i << 4)) + i * 1024u, x, L_X + (16 * w + i) * 1024); }
    unsigned wqo[3];
#pragma unroll
    for (int r = 0; r < 3; ++r) { const unsigned u = (w + 8 * r) * 64 + lane, row = u / 12, c = u % 12; wqo[r] = row * 3072u + (c >> 2) * 1024u + 64u * j + (c & 3) * 16u; dma16(wqo[r] + 3u * 393216u, wqkv, L_WQKV + (72 + w + 8 * r) * 1024); }
    lds_st<u32x4>(L_ONES, (u32x4){0x3F803F80u, 0x3F803F80u, 0x3F803F80u, 0x3F803F80u});
    if (prod) {
        asm volatile("s_waitcnt vmcnt(19)" : "+v"(cv), "+v"(wm0), "+v"(wm1), "+v"(wm2), "+v"(wm3)::"memory");
        float a[8] = {0.f, 0.f, 0.f, 0.f, 0.f, 0.f, 0.f, 0.f};
#define MODROW(cf, wm) { const float c_ = (cf), sl = rbf(c_ * __builtin_amdgcn_rcpf(1.f + __expf(-c_))); _Pragma("unroll") for (int i = 0; i < 4; ++i) { const unsigned u_ = wm[i]; a[2 * i] = fmaf(sl, bf_lo(u_), a[2 * i]); a[2 * i + 1] = fmaf(sl, bf_hi(u_), a[2 * i + 1]); } }
        MODROW(bf_lo(cv[0]), wm0) MODROW(bf_hi(cv[0]), wm1) MODROW(bf_lo(cv[1]), wm2) MODROW(bf_hi(cv[1]), wm3)
#undef MODROW
#pragma unroll
        for (int i = 0; i < 8; ++i) { float v = a[i]; v += dppf<0x124>(v); v += dppf<0x128>(v); a[i] = xsum32(xsum16(v)); }
        if (lane < 4) { lds_st<f32x4>(L_RED + w * 128 + lane * 32, (f32x4){a[0], a[1], a[2], a[3]}); lds_st<f32x4>(L_RED + w * 128 + lane * 32 + 16, (f32x4){a[4], a[5], a[6], a[7]}); }
        BAR();
        if (w < 4) {
            float s0 = 0.f, s1 = 0.f;
#pragma unroll
            for (int r = 0; r < 8; ++r) { const u32x2 v = lds_ld<u32x2>(L_RED + r * 128 + (lane & 15) * 8); s0 += __uint_as_float(v[0]); s1 += __uint_as_float(v[1]); }
            const unsigned long long gr = ((unsigned long long)epoch << 32) | pk2(s0, s1);
            const unsigned rep = 4 * w + (lane >> 4);
            if (mpair < 2) stg64_sc1(ws, WS_MODU + rep * 4096 + (col0 / 2 + (lane & 15)) * 8, gr);
            else stg64_sc1(ws, WS_MODN + rep * 8192 + ((col0 - 1024) / 2 + (lane & 15)) * 8, gr);
        }
    }
    STAMP(1);
    VMWAIT(3);
    BAR();
    const int m = w & 3, kh = w >> 2;
    bf16x8 qf;
    {
    const int h = lane >> 5, gi = lane >> 4, q4 = (lane & 15) >> 2, p4 = lane & 3;
    u32x4 xr[16];
    { const unsigned tok = 32 * m + (lane & 31), rb = L_X + tok * 1024 + kh * 512;
#pragma unroll
      for (int s = 0; s < 16; ++s) xr[s] = lds_ld<u32x4>(rb + (s >> 3) * 256 + ((((2 * (s & 7) + h) ^ (tok & 15))) << 4)); }
#pragma unroll
    for (int s = 0; s < 16; ++s) PIN(xr[s]);
    BAR();
#pragma unroll
    for (int k = 0; k < 3; ++k)
#pragma unroll
        for (int r = 0; r < 3; ++r) dma16(wqo[r] + k * 393216u, wqkv, L_WQKV + (w + 8 * r + 24 * k) * 1024);
    STAMP(2);
    {
        float s0 = 0.f, s1 = 0.f, s2 = 0.f, s3 = 0.f; const bf2 one = {(__bf16)1.0f, (__bf16)1.0f};
#pragma unroll
        for (int s = 0; s < 16; ++s)
#pragma unroll
            for (int i = 0; i < 4; i += 2) { const unsigned ua = xr[s][i], ub = xr[s][i + 1]; const bf2 va = __builtin_bit_cast(bf2, ua), vb = __builtin_bit_cast(bf2, ub);
                s0 = __builtin_amdgcn_fdot2_f32_bf16(va, one, s0, false); s1 = __builtin_amdgcn_fdot2_f32_bf16(va, va, s1, false);
                s2 = __builtin_amdgcn_fdot2_f32_bf16(vb, one, s2, false); s3 = __builtin_amdgcn_fdot2_f32_bf16(vb, vb, s3, false); }
        s0 = xsum32(s0 + s2); s1 = xsum32(s1 + s3);
        if (lane < 32) lds_st<u32x2>(L_STAT + (kh * 128 + 32 * m + lane) * 8, (u32x2){__float_as_uint(s0), __float_as_uint(s1)});
    }
    if (w >= 4) {
        const int t = tid - 256; const unsigned o = WS_MODU + g * 4096 + t * 16; u32x4 v;
        for (unsigned sp = 0; sp < SPIN_CAP; ++sp) { asm volatile("" ::: "memory"); v = __builtin_amdgcn_raw_buffer_load_b128(rws, o, 0, 16); if (__all(v[1] == epoch && v[3] == epoch)) break; }
        const unsigned u0 = v[0], u2 = v[2]; f32x4 f = {bf_lo(u0), bf_hi(u0), bf_lo(u2), bf_hi(u2)};
        if (t >= 128) { f[0] = rbf(1.f + f[0]); f[1] = rbf(1.f + f[1]); f[2] = rbf(1.f + f[2]); f[3] = rbf(1.f + f[3]); }
        lds_st<f32x4>(L_SAG + t * 16, f);
    }
    STAMP(3);
    VMWAIT(0);
    BAR();
    STAMP(4); STAMPC(22);
    float rstd, nmr, gv[2], sv[2];
    const unsigned sga = L_SAG + (256 * kh + 16 * ((lane & 15) >> 3) + 8 * h + (lane & 7)) * 4;
    { const u32x2 a = lds_ld<u32x2>(L_STAT + (32 * m + (lane & 31)) * 8), c2 = lds_ld<u32x2>(L_STAT + (128 + 32 * m + (lane & 31)) * 8);
      sv[0] = lds_ld<float>(sga); gv[0] = lds_ld<float>(sga + 2048);
      const float s0 = __uint_as_float(a[0]) + __uint_as_float(c2[0]), s1 = __uint_as_float(a[1]) + __uint_as_float(c2[1]);
      const float mean = s0 * (1.f / 512.f), var = fmaxf(s1 * (1.f / 512.f) - mean * mean, 0.f);
      rstd = __builtin_amdgcn_rsqf(var + 1e-5f); nmr = -mean * rstd;
      asm volatile("s_nop 1" : "+v"(sv[0]), "+v"(gv[0])); }
    f32x16 aQ, aK, aV;
#pragma unroll
    for (int i = 0; i < 16; ++i) { aQ[i] = 0.f; aK[i] = 0.f; aV[i] = 0.f; }
    {
        const unsigned rowa = L_WQKV + (256 * kh + 8 * (gi >> 1) + q4) * 192 + p4 * 8;
        const unsigned wqa = rowa + (((gi & 1) ^ kh) * 32), wva = rowa + 128 + (gi & 1) * 32;
        bf16x8 fk, fv, fq; unsigned h0[2], h1[2], h2[2], h3[2];
#define HDW(s, i) hdw<8 * ((s) & 1) + 2 * (i)>(xr[s][i], rstd, nmr, gv[((s) >> 1) & 1], sv[((s) >> 1) & 1])
        fk = trfrag(wqa + 64, wqa + 64 + 768); fv = trfrag(wva, wva + 768); fq = trfrag(wqa, wqa + 768);
        h0[0] = HDW(0, 0); h1[0] = HDW(0, 1); h2[0] = HDW(0, 2); h3[0] = HDW(0, 3);
        asm volatile("s_nop 1" : "+v"(h0[0]), "+v"(h1[0]), "+v"(h2[0]), "+v"(h3[0])); SBAR();
#define QKV_STEP(s, sn) { const bf16x8 hb = __builtin_bit_cast(bf16x8, ((u32x4){h0[(s) & 1], h1[(s) & 1], h2[(s) & 1], h3[(s) & 1]})); \
            aK = MFMA32(fk, hb, aK); PIN(aK); SBAR(); if ((sn) < 16) { fk = trfrag(wqa + (sn) * 3072 + 64, wqa + (sn) * 3072 + 64 + 768); h0[(sn) & 1] = HDW((sn) & 15, 0); h1[(sn) & 1] = HDW((sn) & 15, 1); PIN(h0[(sn) & 1]); PIN(h1[(sn) & 1]); } SBAR(); \
            aV = MFMA32(hb, fv, aV); PIN(aV); SBAR(); if ((sn) < 16) { fv = trfrag(wva + (sn) * 3072, wva + (sn) * 3072 + 768); h2[(sn) & 1] = HDW((sn) & 15, 2); PIN(h2[(sn) & 1]); } SBAR(); \
            aQ = MFMA32(fq, hb, aQ); PIN(aQ); SBAR(); if ((sn) < 16) { fq = trfrag(wqa + (sn) * 3072, wqa + (sn) * 3072 + 768); h3[(sn) & 1] = HDW((sn) & 15, 3); PIN(h3[(sn) & 1]); } \
            if ((s) % 2 == 0 && (s) + 2 < 16) { sv[(((s) >> 1) + 1) & 1] = lds_ld<float>(sga + (((s) >> 1) + 1) * 128); gv[(((s) >> 1) + 1) & 1] = lds_ld<float>(sga + 2048 + (((s) >> 1) + 1) * 128); } SBAR(); }
        QKV_STEP(0, 1) QKV_STEP(1, 2) QKV_STEP(2, 3) QKV_STEP(3, 4) QKV_STEP(4, 5) QKV_STEP(5, 6) QKV_STEP(6, 7) QKV_STEP(7, 8)
        QKV_STEP(8, 9) QKV_STEP(9, 10) QKV_STEP(10, 11) QKV_STEP(11, 12) QKV_STEP(12, 13) QKV_STEP(13, 14) QKV_STEP(14, 15) QKV_STEP(15, 16)
#undef QKV_STEP
#undef HDW
    }
    STAMP(5); STAMPC(23);
    {
        float vs[8], vk[8];
#pragma unroll
        for (int i = 0; i < 8; ++i) { float lo = aV[i], hi = aV[8 + i]; PIN(lo); PIN(hi); vs[i] = kh ? lo : hi; vk[i] = kh ? hi : lo; }
        BAR();
        const unsigned ex = (kh ? L_VIMG : L_KIMG) + m * 4096 + lane * 16, eq = L_QS + w * 2048 + lane * 16;
        lds_st<f32x4>(ex, (f32x4){aK[8], aK[9], aK[10], aK[11]}); lds_st<f32x4>(ex + 1024, (f32x4){aK[12], aK[13], aK[14], aK[15]});
        lds_st<f32x4>(ex + 2048, (f32x4){vs[0], vs[1], vs[2], vs[3]}); lds_st<f32x4>(ex + 3072, (f32x4){vs[4], vs[5], vs[6], vs[7]});
        lds_st<f32x4>(eq, (f32x4){aQ[8], aQ[9], aQ[10], aQ[11]}); lds_st<f32x4>(eq + 1024, (f32x4){aQ[12], aQ[13], aQ[14], aQ[15]});
        BAR();
        const unsigned ep = (kh ? L_KIMG : L_VIMG) + m * 4096 + lane * 16, eqp = L_QS + (w ^ 4) * 2048 + lane * 16;
        const f32x4 k0 = lds_ld<f32x4>(ep), k1 = lds_ld<f32x4>(ep + 1024), v0 = lds_ld<f32x4>(ep + 2048), v1 = lds_ld<f32x4>(ep + 3072), q0 = lds_ld<f32x4>(eqp), q1 = lds_ld<f32x4>(eqp + 1024);
        const float qs = 0.25f * 1.44269504089f;
        u32x4 qp, kp, vp;
        kp[0] = pk2(aK[0] + k0[0], aK[1] + k0[1]); kp[1] = pk2(aK[2] + k0[2], aK[3] + k0[3]); kp[2] = pk2(aK[4] + k1[0], aK[5] + k1[1]); kp[3] = pk2(aK[6] + k1[2], aK[7] + k1[3]);
        vp[0] = pk2(vk[0] + v0[0], vk[1] + v0[1]); vp[1] = pk2(vk[2] + v0[2], vk[3] + v0[3]); vp[2] = pk2(vk[4] + v1[0], vk[5] + v1[1]); vp[3] = pk2(vk[6] + v1[2], vk[7] + v1[3]);
        const unsigned kvo = WS_KV + (g * 16 + j) * 16384 + lane * 16;
        __builtin_amdgcn_raw_buffer_store_b128(kp, rws, kvo + (kh * 4 + m) * 1024, 0, 16);
        __builtin_amdgcn_raw_buffer_store_b128(vp, rws, kvo + 8192 + (m * 2 + kh) * 1024, 0, 16);
        lds_st<u32x4>(L_KIMG + (8 + m) * 2048 + kh * 1024 + lane * 16, kp);
        lds_st<u32x4>(L_VIMG + (8 + m) * 2048 + kh * 1024 + lane * 16, vp);
        qp[0] = pk2((aQ[0] + q0[0]) * qs, (aQ[1] + q0[1]) * qs); qp[1] = pk2((aQ[2] + q0[2]) * qs, (aQ[3] + q0[3]) * qs);
        qp[2] = pk2((aQ[4] + q1[0]) * qs, (aQ[5] + q1[1]) * qs); qp[3] = pk2((aQ[6] + q1[2]) * qs, (aQ[7] + q1[3]) * qs);
        qf = __builtin_bit_cast(bf16x8, qp); PIN(qf);
    }
    }
    VMWAIT(0);
    BAR();
    if (tid == 0) stg_sc1(ws, WS_FLAG + (0 * 256 + g * 16 + j) * 256, epoch);
    STAMP(6);
    const int tt = w & 3;
    if (w >= 4) {
        const int t = tid - 256; const unsigned o = WS_MODN + g * 8192 + t * 32; u32x4 v, v2;
        for (unsigned sp = 0; sp < SPIN_CAP; ++sp) { asm volatile("" ::: "memory"); v = __builtin_amdgcn_raw_buffer_load_b128(rws, o, 0, 16); v2 = __builtin_amdgcn_raw_buffer_load_b128(rws, o + 16, 0, 16);
            if (__all(v[1] == epoch && v[3] == epoch && v2[1] == epoch && v2[3] == epoch)) break; }
        u32x4 val = {v[0], v[2], v2[0], v2[2]};
        const int vec = t >> 6, c8 = (t & 63) * 8;
        if (vec == 1) lds_st<u32x4>(L_SB16 + c8 * 2, val);
        else if (vec == 2) {
#pragma unroll
            for (int i = 0; i < 4; ++i) { const unsigned u_ = val[i]; val[i] = pk2(1.f + bf_lo(u_), 1.f + bf_hi(u_)); }
            lds_st<u32x4>(L_GB16 + c8 * 2, val);
        } else if ((c8 >> 6) == ct) lds_st<u32x4>((vec == 0 ? L_GAO : L_GBO) + (c8 & 63) * 2, val);
    }
    {
    FRESH(lane);
    const int hh = kh, qq = lane & 31, h = lane >> 5;
    const float NEG = -__builtin_inff();
    f32x16 c0, c8;
#pragma unroll
    for (int r = 0; r < 16; ++r) { const int kk = (r & 3) + 8 * (r >> 2) + 4 * h; c0[r] = kk > qq ? 0.f : NEG; c8[r] = kk <= qq ? 0.f : NEG; }
    PIN(c0); PIN(c8);
    BAR();
    STAMP(8);
    {
        const unsigned ka = L_KIMG + m * 2048 + hh * 1024 + lane * 16;
        const bool isd = ((lane >> 4) & 1) == hh;
        const unsigned va = isd ? L_VIMG + m * 2048 + lane * 16 : L_ONES, vst = isd ? 2048u : 0u, vs1 = isd ? 1024u : 0u;
        f32x16 o;
#pragma unroll
        for (int r = 0; r < 16; ++r) o[r] = 0.f;
#define MAX16(mx, s) { _Pragma("unroll") for (int r = 0; r < 16; r += 2) mx = fmaxf(fmaxf(mx, s[r]), s[r + 1]); }
#define EXPH(P, S, b) { P[0] = pk2(__builtin_amdgcn_exp2f(S[b]), __builtin_amdgcn_exp2f(S[b + 1])); P[1] = pk2(__builtin_amdgcn_exp2f(S[b + 2]), __builtin_amdgcn_exp2f(S[b + 3])); \
                        P[2] = pk2(__builtin_amdgcn_exp2f(S[b + 4]), __builtin_amdgcn_exp2f(S[b + 5])); P[3] = pk2(__builtin_amdgcn_exp2f(S[b + 6]), __builtin_amdgcn_exp2f(S[b + 7])); }
        if (g >= 2) {
            f32x16 z;
#pragma unroll
            for (int r = 0; r < 16; ++r) z[r] = 0.f;
            PIN(z);
            float mx = NEG, mx2 = NEG; f32x16 sA, sB;
#define KF(i) lds_ld<bf16x8>(ka + (i) * 2048)
            bf16x8 k0 = KF(0), k1 = KF(8), k2 = KF(1), k3 = KF(2);
            SBAR();
            sA = MFMA32(k0, qf, c0); k0 = KF(3); SBAR();
            sB = MFMA32(k1, qf, c8); k1 = KF(4); MAX16(mx, sA); PIN(mx); SBAR();
            sA = MFMA32(k2, qf, z); k2 = KF(5); MAX16(mx2, sB); PIN(mx2); SBAR();
            sB = MFMA32(k3, qf, z); k3 = KF(6); MAX16(mx, sA); PIN(mx); SBAR();
            sA = MFMA32(k0, qf, z); k0 = KF(7); MAX16(mx2, sB); PIN(mx2); SBAR();
            sB = MFMA32(k1, qf, z); k1 = KF(0); MAX16(mx, sA); PIN(mx); SBAR();
            sA = MFMA32(k2, qf, z); k2 = KF(1); MAX16(mx2, sB); PIN(mx2); SBAR();
            sB = MFMA32(k3, qf, z); MAX16(mx, sA); PIN(mx); SBAR();
            sA = MFMA32(k0, qf, z); MAX16(mx2, sB); PIN(mx2); SBAR();
            bf16x8 v0 = lds_ld<bf16x8>(va), v1 = lds_ld<bf16x8>(va + vs1);
            MAX16(mx, sA);
            mx = xmax32(fmaxf(mx, mx2));
#pragma unroll
            for (int r = 0; r < 16; ++r) { z[r] = -mx; c0[r] -= mx; c8[r] -= mx; }
            PIN(z); PIN(c0); PIN(c8); SBAR();
            sA = MFMA32(k1, qf, c0); SBAR();
            u32x4 pa, pb; bf16x8 v0n, v1n, kn = k2; unsigned vp = va;
#define ABLK(I, SC, SN, CN) { bf16x8 kc = kn; if ((I) < 8) { vp += vst; v0n = lds_ld<bf16x8>(vp); v1n = lds_ld<bf16x8>(vp + vs1); } if ((I) < 7) kn = KF((I) + 2); SBAR(); \
                if ((I) < 8) SN = MFMA32(kc, qf, CN); EXPH(pa, SC, 0); PIN(pa); SBAR(); \
                o = MFMA32(v0, __builtin_bit_cast(bf16x8, pa), o); EXPH(pb, SC, 8); PIN(pb); SBAR(); \
                o = MFMA32(v1, __builtin_bit_cast(bf16x8, pb), o); SBAR(); if ((I) < 8) { v0 = v0n; v1 = v1n; } }
            ABLK(0, sA, sB, z) ABLK(1, sB, sA, z) ABLK(2, sA, sB, z) ABLK(3, sB, sA, z)
            ABLK(4, sA, sB, z) ABLK(5, sB, sA, z) ABLK(6, sA, sB, z) ABLK(7, sB, sA, c8) ABLK(8, sA, sB, c8)
#undef KF
#undef ABLK
        } else {
            const int i0 = 8 - 4 * g - m;
            f32x16 z;
#pragma unroll
            for (int r = 0; r < 16; ++r) z[r] = 0.f;
            float mx = NEG;
            f32x16 s = MFMA32(lds_ld<bf16x8>(ka + 8 * 2048), qf, c8); MAX16(mx, s);
#pragma unroll 1
            for (int i = i0; i < 8; ++i) { s = MFMA32(lds_ld<bf16x8>(ka + i * 2048), qf, z); MAX16(mx, s); }
            mx = xmax32(mx);
#pragma unroll
            for (int r = 0; r < 16; ++r) { z[r] = -mx; c8[r] -= mx; }
#pragma unroll 1
            for (int i = i0; i <= 8; ++i) { u32x4 pa, pb;
                if (i < 8) s = MFMA32(lds_ld<bf16x8>(ka + i * 2048), qf, z); else s = MFMA32(lds_ld<bf16x8>(ka + i * 2048), qf, c8);
                EXPH(pa, s, 0); EXPH(pb, s, 8);
                o = MFMA32(lds_ld<bf16x8>(va + i * vst), __builtin_bit_cast(bf16x8, pa), o); o = MFMA32(lds_ld<bf16x8>(va + i * vst + vs1), __builtin_bit_cast(bf16x8, pb), o); }
        }
#undef EXPH
#undef MAX16
        float ov[8], l;
        { float lo = o[0], hi = o[8]; PIN(lo); PIN(hi); l = hh ? lo : hi; }
#pragma unroll
        for (int r = 0; r < 8; ++r) { float lo = o[r], hi = o[8 + r]; PIN(lo); PIN(hi); ov[r] = hh ? hi : lo; }
        const float inv = __builtin_amdgcn_rcpf(l);
        const u32x4 op = {pk2(ov[0] * inv, ov[1] * inv), pk2(ov[2] * inv, ov[3] * inv), pk2(ov[4] * inv, ov[5] * inv), pk2(ov[6] * inv, ov[7] * inv)};
        __builtin_amdgcn_raw_buffer_store_b128(op, rws, WS_O + ((g * 8 + 2 * m + (qq >> 4)) * 16 + j) * 1024 + ((qq & 15) + 16 * (2 * hh + h)) * 16, 0, 16);
    }
    }
    VMWAIT(0);
    BAR();
    if (tid == 0) stg_sc1(ws, WS_FLAG + (1 * 256 + g * 16 + j) * 256, epoch);
    STAMP(9);
    if (w == 0) poll_flags(ws, WS_FLAG + (1 * 256 + g * 16) * 256, 16, epoch, lane);
    BAR();
    STAMP(10);
    u32x2 x1p0, x1p1;
    {
        FRESH(lane);
        const int gi = lane >> 4, q4 = (lane & 15) >> 2, p4 = lane & 3;
        const unsigned tokP = 128 * g + 64 * rh + 16 * tt + (lane & 15), colP = 64 * ct + 32 * kh + 4 * gi;
        u32x4 of[8];
#pragma unroll
        for (int s = 0; s < 8; ++s) of[s] = __builtin_amdgcn_raw_buffer_load_b128(rws, WS_O + ((g * 8 + 4 * rh + tt) * 16 + 8 * kh + s) * 1024 + lane * 16, 0, 16);
        const u32x2 xres0 = *(const u32x2*)(x + tokP * 512 + colP), xres1 = *(const u32x2*)(x + tokP * 512 + colP + 16);
        SBAR();
        f32x4 acc[4];
#pragma unroll
        for (int n = 0; n < 4; ++n) acc[n] = (f32x4){0.f, 0.f, 0.f, 0.f};
        const unsigned xv = 2 * (gi & 1) + (q4 >> 1), rowa = L_WO + (256 * kh + 16 * (gi >> 1) + 4 * (gi & 1) + q4) * 128 + p4 * 8;
        unsigned an[4]; bf16x8 wf[4];
#pragma unroll
        for (int n = 0; n < 4; ++n) { an[n] = rowa + ((n ^ xv) * 32); wf[n] = trfrag(an[n], an[n] + 1024); }
        const unsigned cl = (32 * kh + 4 * gi) * 2, cg = colP * 2;
        const u32x2 ga0 = lds_ld<u32x2>(L_GAO + cl), ga1 = lds_ld<u32x2>(L_GAO + cl + 32);
        const u32x2 gb0 = lds_ld<u32x2>(L_GB16 + cg), gb1 = lds_ld<u32x2>(L_GB16 + cg + 32), sb0 = lds_ld<u32x2>(L_SB16 + cg), sb1 = lds_ld<u32x2>(L_SB16 + cg + 32);
        SBAR();
#pragma unroll
        for (int s = 0; s < 8; ++s)
#pragma unroll
            for (int n = 0; n < 4; ++n) { acc[n] = MFMA16(wf[n], __builtin_bit_cast(bf16x8, of[s]), acc[n]); PIN(acc[n]); SBAR(); if (s < 7) wf[n] = trfrag(an[n] + (s + 1) * 4096, an[n] + (s + 1) * 4096 + 1024); SBAR(); }
        BAR();
        STAMP(11);
        const unsigned ex = w * 2048 + lane * 16;
        { f32x4 s0 = acc[0], s1 = acc[1], s2 = acc[2], s3 = acc[3]; PIN(s0); PIN(s1); PIN(s2); PIN(s3);
          lds_st<f32x4>(ex, kh ? s0 : s2); lds_st<f32x4>(ex + 1024, kh ? s1 : s3); acc[0] = kh ? s2 : s0; acc[1] = kh ? s3 : s1; }
        BAR();
        const unsigned ep = (w ^ 4) * 2048 + lane * 16;
        const f32x4 a0 = lds_ld<f32x4>(ep) + acc[0], a1 = lds_ld<f32x4>(ep + 1024) + acc[1];
        float xv1[8], gg[8], ss[8];
#pragma unroll
        for (int r = 0; r < 4; ++r) {
            const unsigned ua0 = ga0[r >> 1], ua1 = ga1[r >> 1], ux0 = xres0[r >> 1], ux1 = xres1[r >> 1], ug0 = gb0[r >> 1], ug1 = gb1[r >> 1], us0 = sb0[r >> 1], us1 = sb1[r >> 1];
            xv1[r] = rbf(fmaf((r & 1) ? bf_hi(ua0) : bf_lo(ua0), a0[r], (r & 1) ? bf_hi(ux0) : bf_lo(ux0))); xv1[4 + r] = rbf(fmaf((r & 1) ? bf_hi(ua1) : bf_lo(ua1), a1[r], (r & 1) ? bf_hi(ux1) : bf_lo(ux1)));
            gg[r] = (r & 1) ? bf_hi(ug0) : bf_lo(ug0); gg[4 + r] = (r & 1) ? bf_hi(ug1) : bf_lo(ug1);
            ss[r] = (r & 1) ? bf_hi(us0) : bf_lo(us0); ss[4 + r] = (r & 1) ? bf_hi(us1) : bf_lo(us1);
        }
        x1p0 = (u32x2){pk2(xv1[0], xv1[1]), pk2(xv1[2], xv1[3])}; x1p1 = (u32x2){pk2(xv1[4], xv1[5]), pk2(xv1[6], xv1[7])};
        float st[6] = {0.f, 0.f, 0.f, 0.f, 0.f, 0.f}, pg[8];
#pragma unroll
        for (int r = 0; r < 8; ++r) pg[r] = xv1[r] * gg[r];
        const u32x4 xg = {pk2(pg[0], pg[1]), pk2(pg[2], pg[3]), pk2(pg[4], pg[5]), pk2(pg[6], pg[7])};
        __builtin_amdgcn_raw_buffer_store_b128(xg, rws, WS_XG + ((g * 8 + 4 * rh + tt) * 16 + 2 * ct + kh) * 1024 + lane * 16, 0, 16);
#pragma unroll
        for (int r = 0; r < 8; ++r) { const float p = pg[r]; st[0] += xv1[r]; st[1] = fmaf(xv1[r], xv1[r], st[1]); st[2] += p; st[3] = fmaf(p, p, st[3]); st[4] = fmaf(p, gg[r], st[4]); st[5] = fmaf(p, ss[r], st[5]); }
#pragma unroll
        for (int i = 0; i < 6; ++i) st[i] = xsum32(xsum16(st[i]));
        if (kh == 1 && lane < 16) { lds_st<f32x4>(L_SCR + (tt * 16 + lane) * 32, (f32x4){st[0], st[1], st[2], st[3]}); lds_st<u32x2>(L_SCR + (tt * 16 + lane) * 32 + 16, (u32x2){__float_as_uint(st[4]), __float_as_uint(st[5])}); }
        BAR();
        if (kh == 0 && lane < 16) {
            const f32x4 o4 = lds_ld<f32x4>(L_SCR + (tt * 16 + lane) * 32); const u32x2 o2 = lds_ld<u32x2>(L_SCR + (tt * 16 + lane) * 32 + 16);
            const unsigned so = WS_ST + g * 24576, T = 4 * rh + tt;
            const u32x4 sa4 = {__float_as_uint(st[0] + o4[0]), __float_as_uint(st[1] + o4[1]), __float_as_uint(st[2] + o4[2]), __float_as_uint(st[3] + o4[3])};
            const u32x2 sb2 = {__float_as_uint(st[4] + __uint_as_float(o2[0])), __float_as_uint(st[5] + __uint_as_float(o2[1]))};
            __builtin_amdgcn_raw_buffer_store_b128(sa4, rws, so + ((T * 8 + ct) * 16 + lane) * 16, 0, 16);
            __builtin_amdgcn_raw_buffer_store_b64(sb2, rws, so + 16384 + ((T * 8 + ct) * 16 + lane) * 8, 0, 16);
        }
    }
    VMWAIT(0);
    BAR();
    if (tid == 0) stg_sc1(ws, WS_FLAG + (2 * 256 + g * 16 + j) * 256, epoch);
    STAMP(12);
    const int odd = j & 1;
    {
    FRESH(lane);
    const int gi = lane >> 4, q4 = (lane & 15) >> 2, p4 = lane & 3;
    const u32x2 b1v = *(const u32x2*)(b1 + 112 * j + (w > 0 ? w - 1 : 0) * 16 + 4 * gi);
    BAR();
    if (w > 0) {
        const int nt = w - 1; f32x4 acc = {0.f, 0.f, 0.f, 0.f};
        const unsigned ra = L_W1 + (4 * gi + q4) * 224 + p4 * 8 + nt * 32; const int tk = lane & 15;
        const unsigned va = (tk == 0 ? L_GB16 : L_SB16) + 8 * gi;
#pragma unroll
        for (int s = 0; s < 16; ++s) {
            const u32x2 lo = lds_ld<u32x2>(va + s * 64), hi = lds_ld<u32x2>(va + s * 64 + 32);
            u32x4 bv = {lo[0], lo[1], hi[0], hi[1]};
            if (tk == 2) bv = (u32x4){0x3F803F80u, 0x3F803F80u, 0x3F803F80u, 0x3F803F80u};
            if (tk > 2) bv = (u32x4){0u, 0u, 0u, 0u};
            acc = MFMA16(trfrag(ra + s * 7168, ra + s * 7168 + 3584), __builtin_bit_cast(bf16x8, bv), acc);
        }
        if (tk < 3) lds_st<f32x4>(L_UVW + (tk * 112 + nt * 16 + 4 * gi) * 4, acc);
        if (tk == 3) lds_st<f32x4>(L_UVW + (3 * 112 + nt * 16 + 4 * gi) * 4, (f32x4){bf_lo(b1v[0]), bf_hi(b1v[0]), bf_lo(b1v[1]), bf_hi(b1v[1])});
    } else {
        const u32x4 gq = lds_ld<u32x4>(L_GB16 + lane * 16), sq = lds_ld<u32x4>(L_SB16 + lane * 16);
        float c5[5] = {0.f, 0.f, 0.f, 0.f, 0.f};
#pragma unroll
        for (int i = 0; i < 4; ++i) { const unsigned ug = gq[i], us = sq[i]; const float g0 = bf_lo(ug), g1 = bf_hi(ug), s0 = bf_lo(us), s1 = bf_hi(us);
            c5[0] += g0 + g1; c5[1] += s0 + s1; c5[2] += g0 * g0 + g1 * g1; c5[3] += g0 * s0 + g1 * s1; c5[4] += s0 * s0 + s1 * s1; }
#pragma unroll
        for (int i = 0; i < 5; ++i) c5[i] = xsum32(xsum16(rowsum16(c5[i])));
        if (lane == 0) { lds_st<f32x4>(L_CONST, (f32x4){c5[0], c5[1], c5[2], c5[3]}); lds_st<float>(L_CONST + 16, c5[4]); }
        poll_flags(ws, WS_FLAG + (2 * 256 + g * 16) * 256, 16, epoch, lane);
    }
    }
    BAR();
    STAMP(13);
    {
        constexpr int WIN = 6;
        FRESH(lane);
        const int gi = lane >> 4, q4 = (lane & 15) >> 2, p4 = lane & 3;
        u32x4 xf[16];
        const unsigned xo = WS_XG + (g * 8 + w) * 16384 + lane * 16;
#pragma unroll
        for (int s = 0; s < WIN; ++s) xf[s] = __builtin_amdgcn_raw_buffer_load_b128(rws, xo + s * 1024, 0, 16);
        SBAR(); WSTAMP(0);
        f32x4 acc[7]; bf16x8 wf[7];
        const unsigned ra = L_W1 + (4 * gi + q4) * 224 + p4 * 8, raA = ra + 32 * odd, raB = ra + (odd ? 0 : 192);
        const unsigned raA2 = raA + 8 * 7168, raB2 = raB + 8 * 7168;
#define W1A(s, u) (((s) < 8 ? ((u) < 6 ? raA : raB) : ((u) < 6 ? raA2 : raB2)) + ((s) & 7) * 7168 + ((u) < 6 ? (u) * 32 : 0))
#pragma unroll
        for (int u = 0; u < 7; ++u) { acc[u] = (f32x4){0.f, 0.f, 0.f, 0.f}; wf[u] = trfrag(W1A(0, u), W1A(0, u) + 3584); }
        SBAR();
        u32x4 sA0, sA1; u32x2 sB0, sB1;
        const unsigned so = WS_ST + g * 24576 + w * 2048, sob = WS_ST + g * 24576 + 16384 + w * 1024;
#pragma unroll
        for (int s = 0; s < 16; ++s) {
            if (s + WIN < 16) xf[s + WIN] = __builtin_amdgcn_raw_buffer_load_b128(rws, xo + (s + WIN) * 1024, 0, 16);
            if (s == 10) { sA0 = __builtin_amdgcn_raw_buffer_load_b128(rws, so + ((2 * gi) * 16 + (lane & 15)) * 16, 0, 16); sA1 = __builtin_amdgcn_raw_buffer_load_b128(rws, so + ((2 * gi + 1) * 16 + (lane & 15)) * 16, 0, 16); }
            if (s == 11) { sB0 = __builtin_amdgcn_raw_buffer_load_b64(rws, sob + ((2 * gi) * 16 + (lane & 15)) * 8, 0, 16); sB1 = __builtin_amdgcn_raw_buffer_load_b64(rws, sob + ((2 * gi + 1) * 16 + (lane & 15)) * 8, 0, 16); }
            SBAR();
#pragma unroll
            for (int u = 0; u < 7; ++u) { acc[u] = MFMA16(wf[u], __builtin_bit_cast(bf16x8, xf[s]), acc[u]); PIN(acc[u]); SBAR(); if (s < 15) wf[u] = trfrag(W1A(s + 1, u), W1A(s + 1, u) + 3584); SBAR(); }
            if (s == 0) WSTAMP(1); if (s == 7) WSTAMP(2); if (s == 15) WSTAMP(3);
        }
#undef W1A
        const unsigned uo = L_UVW + 16 * gi + 64 * odd, uo6 = L_UVW + 16 * gi + (odd ? 0 : 384);
        f32x4 uu = lds_ld<f32x4>(uo), vv = lds_ld<f32x4>(uo + 448), ww = lds_ld<f32x4>(uo + 896), bb = lds_ld<f32x4>(uo + 1344);
        const f32x4 cc4 = lds_ld<f32x4>(L_CONST); const float SB2 = lds_ld<float>(L_CONST + 16);
        float S[6];
#pragma unroll
        for (int i = 0; i < 4; ++i) S[i] = xsum32(xsum16(__uint_as_float(sA0[i]) + __uint_as_float(sA1[i])));
#pragma unroll
        for (int i = 0; i < 2; ++i) S[4 + i] = xsum32(xsum16(__uint_as_float(sB0[i]) + __uint_as_float(sB1[i])));
        const float iD = 1.f / 512.f, m1 = S[0] * iD, r1 = __builtin_amdgcn_rsqf(fmaxf(S[1] * iD - m1 * m1, 0.f) + 1e-5f);
        const float m2 = (r1 * (S[2] - m1 * cc4[0]) + cc4[1]) * iD;
        const float ez2 = (r1 * r1 * (S[3] - 2.f * m1 * S[4] + m1 * m1 * cc4[2]) + 2.f * r1 * (S[5] - m1 * cc4[3]) + SB2) * iD;
        const float r2 = __builtin_amdgcn_rsqf(fmaxf(ez2 - m2 * m2, 0.f) + 1e-5f);
        const float ca = r1 * r2, cb = -ca * m1, cc = -r2 * m2;
        BAR();
        STAMP(14);
        unsigned hp[7][2];
        const unsigned hb = WS_H + (g * 8 + w) * 57344, s0 = (7 * j + odd) >> 1;
#pragma unroll
        for (int u = 0; u < 7; ++u) {
            float pre[4], z[4], e[4];
#pragma unroll
            for (int r = 0; r < 4; ++r) pre[r] = fmaf(ca, acc[u][r], fmaf(cb, uu[r], fmaf(r2, vv[r], fmaf(cc, ww[r], bb[r]))));
            if (u < 6) { const unsigned o = u < 5 ? uo + (u + 1) * 64 : uo6; uu = lds_ld<f32x4>(o); vv = lds_ld<f32x4>(o + 448); ww = lds_ld<f32x4>(o + 896); bb = lds_ld<f32x4>(o + 1344); }
            SBAR();
#pragma unroll
            for (int r = 0; r < 4; ++r) z[r] = pre[r] * fmaf(pre[r] * pre[r], -0.044715f * 2.302208198f, -2.302208198f);
#pragma unroll
            for (int r = 0; r < 4; ++r) e[r] = __builtin_amdgcn_exp2f(z[r]);
#pragma unroll
            for (int r = 0; r < 4; ++r) e[r] = __builtin_amdgcn_rcpf(1.f + e[r]);
            hp[u][0] = pk2(pre[0] * e[0], pre[1] * e[1]); hp[u][1] = pk2(pre[2] * e[2], pre[3] * e[3]);
            if (u < 6 && (u & 1)) __builtin_amdgcn_raw_buffer_store_b128((u32x4){hp[u - 1][0], hp[u - 1][1], hp[u][0], hp[u][1]}, rws, hb + (s0 + (u >> 1)) * 1024 + lane * 16, 0, 16);
            SBAR();
        }
        __builtin_amdgcn_raw_buffer_store_b64((u32x2){hp[6][0], hp[6][1]}, rws, hb + ((7 * j + (odd ? 0 : 6)) >> 1) * 1024 + odd * 512 + lane * 8, 0, 16);
        WSTAMP(4);
    }
    VMWAIT(0); WSTAMP(5);
    BAR();
    if (tid == 0) stg_sc1(ws, WS_FLAG + (3 * 256 + g * 16 + j) * 256, epoch);
    STAMP(15);
    if (w == 0) poll_flags(ws, WS_FLAG + (3 * 256 + g * 16) * 256, 16, epoch, lane);
    BAR();
    STAMP(16);
    {
        constexpr int WIN = 8;
        FRESH(lane);
        const int gi = lane >> 4, q4 = (lane & 15) >> 2, p4 = lane & 3;
        const unsigned tokP = 128 * g + 64 * rh + 16 * tt + (lane & 15), colP = 64 * ct + 32 * kh + 4 * gi;
        const unsigned hb = WS_H + (g * 8 + 4 * rh + tt) * 57344 + kh * 28 * 1024;
        u32x4 hf[28];
#define HLOAD(s) { if ((s) % 7 == 3) { const u32x2 lo = __builtin_amdgcn_raw_buffer_load_b64(rws, hb + (s) * 1024 + lane * 8, 0, 16), hi = __builtin_amdgcn_raw_buffer_load_b64(rws, hb + (s) * 1024 + 512 + lane * 8, 0, 16); hf[s] = (u32x4){lo[0], lo[1], hi[0], hi[1]}; } \
                    else hf[s] = __builtin_amdgcn_raw_buffer_load_b128(rws, hb + (s) * 1024 + lane * 16, 0, 16); }
#pragma unroll
        for (int s = 0; s < WIN; ++s) HLOAD(s)
        SBAR();
        f32x4 acc[4]; bf16x8 wf[4];
        const unsigned xv = (2 * gi + (q4 >> 1)) & 3, rowa = kh * 8192 + (4 * gi + q4) * 128 + p4 * 8;
        unsigned an[4], an2[4], an3[4];
#pragma unroll
        for (int n = 0; n < 4; ++n) { an[n] = rowa + ((n ^ xv) * 32); an2[n] = an[n] + 4 * 16384; an3[n] = an[n] + 8 * 16384; acc[n] = (f32x4){0.f, 0.f, 0.f, 0.f}; }
#define W2A(s, n) ((((s) >> 1) % 9 < 4 ? an[n] : ((s) >> 1) % 9 < 8 ? an2[n] : an3[n]) + ((((s) >> 1) % 9) & 3) * 16384 + ((s) & 1) * 4096)
#pragma unroll
        for (int n = 0; n < 4; ++n) wf[n] = trfrag(W2A(0, n), W2A(0, n) + 2048);
        const unsigned cl = (32 * kh + 4 * gi) * 2;
        const u32x2 g0 = lds_ld<u32x2>(L_GBO + cl), g1 = lds_ld<u32x2>(L_GBO + cl + 32);
        u32x2 bb0, bb1;
        SBAR();
#pragma unroll
        for (int s = 0; s < 28; ++s) {
            if (s == 24) { bb0 = *(const u32x2*)(b2 + colP); bb1 = *(const u32x2*)(b2 + colP + 16); }
            if (s == 10) BAR();
            if (s == 17) BAR();
            if (s + WIN < 28) HLOAD(s + WIN)
            SBAR();
#pragma unroll
            for (int n = 0; n < 4; ++n) { acc[n] = MFMA16(wf[n], __builtin_bit_cast(bf16x8, hf[s]), acc[n]); PIN(acc[n]); SBAR(); if (s < 27) wf[n] = trfrag(W2A(s + 1, n), W2A(s + 1, n) + 2048); SBAR(); }
        }
#undef W2A
#undef HLOAD
        BAR();
        STAMP(17);
        const unsigned ex = w * 2048 + lane * 16;
        { f32x4 s0 = acc[0], s1 = acc[1], s2 = acc[2], s3 = acc[3]; PIN(s0); PIN(s1); PIN(s2); PIN(s3);
          lds_st<f32x4>(ex, kh ? s0 : s2); lds_st<f32x4>(ex + 1024, kh ? s1 : s3); acc[0] = kh ? s2 : s0; acc[1] = kh ? s3 : s1; }
        BAR();
        const unsigned ep = (w ^ 4) * 2048 + lane * 16;
        const f32x4 a0 = lds_ld<f32x4>(ep) + acc[0], a1 = lds_ld<f32x4>(ep + 1024) + acc[1];
        float y0[4], y1[4];
#pragma unroll
        for (int r = 0; r < 4; ++r) {
            const unsigned ug0 = g0[r >> 1], ug1 = g1[r >> 1], ub0 = bb0[r >> 1], ub1 = bb1[r >> 1], ux0 = x1p0[r >> 1], ux1 = x1p1[r >> 1];
            y0[r] = fmaf((r & 1) ? bf_hi(ug0) : bf_lo(ug0), a0[r] + ((r & 1) ? bf_hi(ub0) : bf_lo(ub0)), (r & 1) ? bf_hi(ux0) : bf_lo(ux0));
            y1[r] = fmaf((r & 1) ? bf_hi(ug1) : bf_lo(ug1), a1[r] + ((r & 1) ? bf_hi(ub1) : bf_lo(ub1)), (r & 1) ? bf_hi(ux1) : bf_lo(ux1));
        }
        *(u32x2*)(out + tokP * 512 + colP) = (u32x2){pk2(y0[0], y0[1]), pk2(y0[2], y0[3])};
        *(u32x2*)(out + tokP * 512 + colP + 16) = (u32x2){pk2(y1[0], y1[1]), pk2(y1[2], y1[3])};
    }
    if (b == 0 && w == 0) {
        for (unsigned sp = 0; sp < SPIN_CAP; ++sp) { asm volatile("" ::: "memory"); const u32x4 v = __builtin_amdgcn_raw_buffer_load_b128(rws, WS_HELLO + lane * 16, 0, 16); if (__all(v[0] == epoch && v[1] == epoch && v[2] == epoch && v[3] == epoch)) break; }
        if (lane == 0) stg_sc1(ws, WS_E, epoch);
    }
    if (b == 0 && w == 0) { unsigned long long dl = 20 * (e_t2 - e_t1); if (dl > 20000) dl = 20000; const unsigned long long t_end = rt() + dl; while (rt() < t_end) __builtin_amdgcn_s_sleep(1); }
    STAMP(18); STAMPC(21);
    STAMP_OUT();
    asm volatile("fb_code_end:");
}

extern "C" void kernel_launch(void* const* d_in, const int* in_sizes, int n_in, void* d_out, int out_size, void* d_ws, size_t ws_size, hipStream_t stream) {
    (void)hipFuncSetAttribute((const void*)fused_block, hipFuncAttributeMaxDynamicSharedMemorySize, LDS_BYTES);
    fused_block<<<256, 576, LDS_BYTES, stream>>>((const u16*)d_in[0], (const u16*)d_in[1], (const u16*)d_in[2], (const u16*)d_in[4], (const u16*)d_in[5], (const u16*)d_in[6], (const u16*)d_in[7],
                                                 (const u16*)d_in[8], (const u16*)d_in[9], (u16*)d_out, (char*)d_ws);
}
```

```cpp
#include <hip/hip_runtime.h>
#include <hip/hip_bf16.h>
#include <stdint.h>

typedef unsigned short u16;
typedef short bf16x8 __attribute__((ext_vector_type(8)));
typedef short s16x4 __attribute__((ext_vector_type(4)));
typedef float f32x4 __attribute__((ext_vector_type(4)));
typedef float f32x16 __attribute__((ext_vector_type(16)));
typedef unsigned u32x4 __attribute__((ext_vector_type(4)));
typedef unsigned u32x2 __attribute__((ext_vector_type(2)));
typedef __bf16 bf2 __attribute__((ext_vector_type(2)));
#define LAS __attribute__((address_space(3)))
#define GAS __attribute__((address_space(1)))
#define DI __device__ __forceinline__

#define TS 0

constexpr unsigned WS_E = 0, WS_HELLO = 4096, WS_FLAG = 8192;
constexpr unsigned WS_MODU = 0x50000, WS_MODN = 0x60000;
constexpr unsigned WS_KV = 0x100000, WS_O = 0x500000, WS_XG = 0x700000, WS_ST = 0x900000, WS_H = 0xA00000, WS_TS = 0x2000000;
constexpr unsigned WS_BYTES = 0x2100000;
constexpr unsigned L_X = 0, L_WQKV = 57344, L_RED = 155648, L_SAG = 156672, L_STAT = 160768;
constexpr unsigned L_QS = 65536, L_KIMG = 98304, L_VIMG = 122880, L_WO = 0, L_W1 = 44032;
constexpr unsigned L_GB16 = 158720, L_SB16 = 159744, L_UVW = 160768, L_CONST = 162560, L_GAO = 162592, L_GBO = 162720, L_ONES = 162848, L_DUMMY = 162880;
constexpr unsigned L_SCR = 32768;
constexpr unsigned LDS_BYTES = 163840;
constexpr unsigned SPIN_CAP = 1u << 16;

DI float bf_lo(unsigned u) { return __uint_as_float(u << 16); }
DI float bf_hi(unsigned u) { return __uint_as_float(u & 0xffff0000u); }
DI unsigned pk2(float a, float b) { bf2 v = {(__bf16)a, (__bf16)b}; return __builtin_bit_cast(unsigned, v); }
DI float rbf(float a) { return bf_lo(pk2(a, a)); }
DI void dma16(unsigned voff, const void* sbase, unsigned lds) { asm volatile("s_mov_b32 m0, %1\n\ts_nop 0\n\tglobal_load_lds_dwordx4 %0, %2" ::"v"(voff), "s"(lds), "s"(sbase) : "memory"); }
DI void dma16c(unsigned voff, const void* sbase, unsigned lds) { asm volatile("s_mov_b32 m0, %1\n\ts_nop 0\n\tglobal_load_lds_dwordx4 %0, %2 sc1" ::"v"(voff), "s"(lds), "s"(sbase) : "memory"); }
DI void warm4(unsigned voff, const void* sbase) { asm volatile("s_mov_b32 m0, %1\n\ts_nop 0\n\tglobal_load_lds_dword %0, %2" ::"v"(voff), "s"(L_DUMMY), "s"(sbase) : "memory"); }
#define FRESH(v) int v = threadIdx.x & 63; asm volatile("" : "+v"(v))
#define SBAR() __builtin_amdgcn_sched_barrier(0)
#define PIN(x) asm volatile("" : "+v"(x))
#define BAR() asm volatile("s_waitcnt lgkmcnt(0)\n\ts_barrier" ::: "memory")
#define VMWAIT(n) asm volatile("s_waitcnt vmcnt(" #n ")" ::: "memory")
DI unsigned ldg_sc1(const char* ws, unsigned off) { return __hip_atomic_load((GAS unsigned*)(ws + off), __ATOMIC_RELAXED, __HIP_MEMORY_SCOPE_AGENT); }
DI unsigned long long ldg64_sc1(const char* ws, unsigned off) { return __hip_atomic_load((GAS unsigned long long*)(ws + off), __ATOMIC_RELAXED, __HIP_MEMORY_SCOPE_AGENT); }
DI void stg_sc1(char* ws, unsigned off, unsigned v) { __hip_atomic_store((GAS unsigned*)(ws + off), v, __ATOMIC_RELAXED, __HIP_MEMORY_SCOPE_AGENT); }
DI void stg64_sc1(char* ws, unsigned off, unsigned long long v) { __hip_atomic_store((GAS unsigned long long*)(ws + off), v, __ATOMIC_RELAXED, __HIP_MEMORY_SCOPE_AGENT); }
template <class T> DI T lds_ld(unsigned a) { return *(const LAS T*)(size_t)a; }
template <class T> DI void lds_st(unsigned a, T v) { *(LAS T*)(size_t)a = v; }
DI s16x4 lds_tr(unsigned a) { return __builtin_amdgcn_ds_read_tr16_b64_v4i16((LAS s16x4*)(size_t)a); }
DI bf16x8 trfrag(unsigned a0, unsigned a1) { s16x4 lo = lds_tr(a0), hi = lds_tr(a1); return __builtin_shufflevector(lo, hi, 0, 1, 2, 3, 4, 5, 6, 7); }
#define MFMA32(a, b, c) __builtin_amdgcn_mfma_f32_32x32x16_bf16((a), (b), (c), 0, 0, 0)
#define MFMA16(a, b, c) __builtin_amdgcn_mfma_f32_16x16x32_bf16((a), (b), (c), 0, 0, 0)
template <int N> DI float bcast(float v) { return __builtin_bit_cast(float, __builtin_amdgcn_mov_dpp(__builtin_bit_cast(int, v), 0x150 + N, 0xf, 0xf, true)); }
template <int N> DI float modu(float t, float gv, float sv) {
#pragma clang fp contract(off)
    const float p = bcast<N>(gv) * t; return bcast<N>(sv) + p; }
template <int N> DI unsigned hdw(unsigned x, float r, float n, float gv, float sv) {
    unsigned o; float a, b2, c, d;
    asm("v_lshlrev_b32 %1, 16, %5\n\tv_and_b32 %2, 0xffff0000, %5\n\tv_fma_f32 %1, %1, %6, %7\n\tv_fma_f32 %2, %2, %6, %7\n\t"
        "v_mov_b32_dpp %3, %9 row_newbcast:%c10 row_mask:0xf bank_mask:0xf\n\tv_mov_b32_dpp %4, %9 row_newbcast:%c11 row_mask:0xf bank_mask:0xf\n\t"
        "v_fmac_f32_dpp %3, %8, %1 row_newbcast:%c10 row_mask:0xf bank_mask:0xf\n\tv_fmac_f32_dpp %4, %8, %2 row_newbcast:%c11 row_mask:0xf bank_mask:0xf\n\t"
        "v_cvt_pk_bf16_f32 %0, %3, %4"
        : "=v"(o), "=&v"(a), "=&v"(b2), "=&v"(c), "=&v"(d) : "v"(x), "v"(r), "v"(n), "v"(gv), "v"(sv), "i"(N), "i"(N + 1));
    return o; }
template <int C> DI float dppf(float v) { return __builtin_bit_cast(float, __builtin_amdgcn_update_dpp(0, __builtin_bit_cast(int, v), C, 0xf, 0xf, true)); }
DI float rowsum16(float v) { v += dppf<0xB1>(v); v += dppf<0x4E>(v); v += dppf<0x141>(v); v += dppf<0x140>(v); return v; }
DI float xsum32(float v) { auto r = __builtin_amdgcn_permlane32_swap(__float_as_uint(v), __float_as_uint(v), false, false); return __uint_as_float(r[0]) + __uint_as_float(r[1]); }
DI float xmax32(float v) { auto r = __builtin_amdgcn_permlane32_swap(__float_as_uint(v), __float_as_uint(v), false, false); return fmaxf(__uint_as_float(r[0]), __uint_as_float(r[1])); }
DI float xsum16(float v) { auto r = __builtin_amdgcn_permlane16_swap(__float_as_uint(v), __float_as_uint(v), false, false); return __uint_as_float(r[0]) + __uint_as_float(r[1]); }
DI unsigned long long mt() { unsigned long long t; asm volatile("s_memtime %0\n\ts_waitcnt lgkmcnt(0)" : "=s"(t)::"memory"); return t; }
DI unsigned long long rt() { unsigned long long t; asm volatile("s_memrealtime %0\n\ts_waitcnt lgkmcnt(0)" : "=s"(t)::"memory"); return t; }
#define STAMP(i) do {} while (0)
#define STAMP_OUT() do {} while (0)
#define STAMPC(i) do {} while (0)
#define WSTAMP(i) do {} while (0)

DI void poll_flags(const char* ws, unsigned off, int n, unsigned epoch, int lane) {
    const unsigned o = off + (unsigned)(lane < n ? lane : 0) * 256u;
    for (unsigned sp = 0; sp < SPIN_CAP; ++sp) { unsigned f = ldg_sc1(ws, o); if (__all(f == epoch)) break; }
}

__global__ __launch_bounds__(576, 3) void fused_block(const u16* __restrict__ x, const u16* __restrict__ wqkv, const u16* __restrict__ wo, const u16* __restrict__ w1, const u16* __restrict__ b1,
                                                      const u16* __restrict__ w2, const u16* __restrict__ b2, const u16* __restrict__ cvec, const u16* __restrict__ wmod, u16* __restrict__ out, char* __restrict__ ws) {
    unsigned long long e_t0, e_t1 = 0;
    asm volatile("s_memrealtime %0\n\ts_waitcnt lgkmcnt(0)" : "=s"(e_t0));
    const unsigned epoch = *(const unsigned*)(ws + WS_E) + 1u;
    const int tid = threadIdx.x, lane = tid & 63, w = __builtin_amdgcn_readfirstlane(tid >> 6);
    const int b = blockIdx.x, xl = b & 7, idx = b >> 3, g = 2 * xl + (idx >> 4), j = idx & 15;
    const int ct = j & 7, rh = j >> 3;
    const bool prod = idx < 12;
    unsigned long long code_pc; unsigned code_len;
    asm volatile("s_getpc_b64 %0\nfb_code_begin:\n\ts_mov_b32 %1, fb_code_end-fb_code_begin" : "=s"(code_pc), "=s"(code_len));
    STAMP(0); STAMPC(20);
    if (w == 8) {
        {
            const unsigned o = ((unsigned)idx * 32u + (lane & 31)) * 64u;
            warm4((int)o < (int)code_len ? o : 0u, (const void*)code_pc);
        }
        if (lane == 0) stg_sc1(ws, WS_HELLO + b * 4, epoch);
        const unsigned sw128 = (lane >> 3) * 1024u + ct * 128u + (((lane & 7) >> 1) ^ ((lane >> 4) & 3)) * 32u + (lane & 1) * 16u;
        unsigned w1o[7];
#pragma unroll
        for (int r = 0; r < 7; ++r) { const unsigned u = r * 64 + lane, row = u / 14, c = u % 14; w1o[r] = row * 3584u + 224u * j + c * 16u; }
#define W1GRP(k) { _Pragma("unroll") for (int r = 0; r < 7; ++r) dma16(w1o[r] + (k) * 114688u, w1, L_W1 + (7 * (k) + r) * 1024); }
#define W2CHUNK(c, slot) { _Pragma("unroll 1") for (int pp = 0; pp < 16; ++pp) dma16(sw128 + ((pp >> 3) * 112 + 8 * (c) + (pp & 7)) * 8192u, w2, (slot) * 16384 + pp * 1024); }
        if (prod) __builtin_amdgcn_s_barrier();
        __builtin_amdgcn_s_barrier();
        __builtin_amdgcn_s_barrier();
        __builtin_amdgcn_s_barrier();
#pragma unroll 1
        for (int p = 0; p < 56; ++p) dma16(sw128 + p * 8192u, wo, L_WO + p * 1024);
        __builtin_amdgcn_s_barrier();
#pragma unroll 1
        for (int p = 56; p < 64; ++p) dma16(sw128 + p * 8192u, wo, L_WO + p * 1024);
        __builtin_amdgcn_s_barrier();
        __builtin_amdgcn_s_barrier();
        if (g >= 1) {
            const unsigned fo = WS_FLAG + (((g >= 2 ? g - 2 + (lane & 1) : 0) * 16 + j) * 256);
            for (unsigned sp = 0; sp < SPIN_CAP; ++sp) { unsigned f = ldg_sc1(ws, fo); if (__all(f == epoch)) break; }
#pragma unroll 1
            for (int p = (g >= 2 ? 0 : 16); p < 32; ++p) { const int src = p >> 4, r = p & 15;
                const unsigned dst = r < 8 ? L_KIMG + (4 * src + (r & 3)) * 2048 + (r >> 2) * 1024 : L_VIMG + (4 * src + ((r - 8) >> 1)) * 2048 + (r & 1) * 1024;
                dma16c(WS_KV + ((g - 2 + src) * 16 + j) * 16384 + r * 1024 + lane * 16, ws, dst); }
        }
        VMWAIT(0);
        __builtin_amdgcn_s_barrier();
#pragma unroll 1
        for (int k = 3; k < 7; ++k) W1GRP(k)
        W1GRP(15)
        __builtin_amdgcn_s_barrier();
#pragma unroll 1
        for (int k = 7; k < 15; ++k) W1GRP(k)
        VMWAIT(56);
        __builtin_amdgcn_s_barrier();
        __builtin_amdgcn_s_barrier();
#pragma unroll 1
        for (int k = 0; k < 3; ++k) W1GRP(k)
        W2CHUNK(1, 1)
        __builtin_amdgcn_s_barrier();
        __builtin_amdgcn_s_barrier();
        __builtin_amdgcn_s_barrier();
        W2CHUNK(0, 0)
        VMWAIT(32);
        __builtin_amdgcn_s_barrier();
        __builtin_amdgcn_s_barrier();
        __builtin_amdgcn_s_barrier();
#pragma unroll 1
        for (int c = 2; c < 9; ++c) W2CHUNK(c, c)
        __builtin_amdgcn_s_barrier();
        VMWAIT(0);
        __builtin_amdgcn_s_barrier();
        __builtin_amdgcn_s_barrier();
#pragma unroll 1
        for (int c = 9; c < 14; ++c) W2CHUNK(c, c - 9)
        VMWAIT(0);
        __builtin_amdgcn_s_barrier();
        __builtin_amdgcn_s_barrier();
        __builtin_amdgcn_s_barrier();
#undef W1GRP
#undef W2CHUNK
        return;
    }
    const __amdgpu_buffer_rsrc_t rws = __builtin_amdgcn_make_buffer_rsrc(ws, 0, WS_BYTES, 0x00020000);
    const int mpair = idx >> 1, col0 = 64 * (8 * mpair + ((xl + mpair) & 7)) + 32 * (idx & 1);
    u32x2 cv; u32x4 wm0, wm1, wm2, wm3;
    if (prod) {
        const unsigned o = (unsigned)(tid >> 2) * (4u * 6144u) + (unsigned)col0 * 2u + (tid & 3) * 16u;
        asm volatile("global_load_dwordx2 %0, %1, %2" : "=v"(cv) : "v"((tid >> 2) * 8), "s"(cvec) : "memory");
        asm volatile("global_load_dwordx4 %0, %1, %2" : "=v"(wm0) : "v"(o), "s"(wmod) : "memory");
        asm volatile("global_load_dwordx4 %0, %1, %2" : "=v"(wm1) : "v"(o + 6144u), "s"(wmod) : "memory");
        asm volatile("global_load_dwordx4 %0, %1, %2" : "=v"(wm2) : "v"(o + 12288u), "s"(wmod) : "memory");
        asm volatile("global_load_dwordx4 %0, %1, %2" : "=v"(wm3) : "v"(o + 18432u), "s"(wmod) : "memory");
    }
    { const unsigned xo = (unsigned)(128 * g + 16 * w) * 1024u + ((unsigned)lane << 4);
#pragma unroll
      for (int i = 0; i < 16; ++i) dma16((xo ^ (unsigned)(i << 4)) + i * 1024u, x, L_X + (16 * w + i) * 1024); }
    unsigned wqo[3];
#pragma unroll
    for (int r = 0; r < 3; ++r) { const unsigned u = (w + 8 * r) * 64 + lane, row = u / 12, c = u % 12; wqo[r] = row * 3072u + (c >> 2) * 1024u + 64u * j + (c & 3) * 16u; dma16(wqo[r] + 3u * 393216u, wqkv, L_WQKV + (72 + w + 8 * r) * 1024); }
    lds_st<u32x4>(L_ONES, (u32x4){0x3F803F80u, 0x3F803F80u, 0x3F803F80u, 0x3F803F80u});
    if (prod) {
        asm volatile("s_waitcnt vmcnt(19)" : "+v"(cv), "+v"(wm0), "+v"(wm1), "+v"(wm2), "+v"(wm3)::"memory");
        float a[8] = {0.f, 0.f, 0.f, 0.f, 0.f, 0.f, 0.f, 0.f};
#define MODROW(cf, wm) { const float c_ = (cf), sl = rbf(c_ * __builtin_amdgcn_rcpf(1.f + __expf(-c_))); _Pragma("unroll") for (int i = 0; i < 4; ++i) { const unsigned u_ = wm[i]; a[2 * i] = fmaf(sl, bf_lo(u_), a[2 * i]); a[2 * i + 1] = fmaf(sl, bf_hi(u_), a[2 * i + 1]); } }
        MODROW(bf_lo(cv[0]), wm0) MODROW(bf_hi(cv[0]), wm1) MODROW(bf_lo(cv[1]), wm2) MODROW(bf_hi(cv[1]), wm3)
#undef MODROW
#pragma unroll
        for (int i = 0; i < 8; ++i) { float v = a[i]; v += dppf<0x124>(v); v += dppf<0x128>(v); a[i] = xsum32(xsum16(v)); }
        if (lane < 4) { lds_st<f32x4>(L_RED + w * 128 + lane * 32, (f32x4){a[0], a[1], a[2], a[3]}); lds_st<f32x4>(L_RED + w * 128 + lane * 32 + 16, (f32x4){a[4], a[5], a[6], a[7]}); }
        BAR();
        if (w < 4) {
            float s0 = 0.f, s1 = 0.f;
#pragma unroll
            for (int r = 0; r < 8; ++r) { const u32x2 v = lds_ld<u32x2>(L_RED + r * 128 + (lane & 15) * 8); s0 += __uint_as_float(v[0]); s1 += __uint_as_float(v[1]); }
            const unsigned long long gr = ((unsigned long long)epoch << 32) | pk2(s0, s1);
            const unsigned rep = 4 * w + (lane >> 4);
            if (mpair < 2) stg64_sc1(ws, WS_MODU + rep * 4096 + (col0 / 2 + (lane & 15)) * 8, gr);
            else stg64_sc1(ws, WS_MODN + rep * 8192 + ((col0 - 1024) / 2 + (lane & 15)) * 8, gr);
        }
    }
    STAMP(1);
    VMWAIT(3);
    BAR();
    const int m = w & 3, kh = w >> 2;
    bf16x8 qf;
    {
    const int h = lane >> 5, gi = lane >> 4, q4 = (lane & 15) >> 2, p4 = lane & 3;
    u32x4 xr[16];
    { const unsigned tok = 32 * m + (lane & 31), rb = L_X + tok * 1024 + kh * 512;
#pragma unroll
      for (int s = 0; s < 16; ++s) xr[s] = lds_ld<u32x4>(rb + (s >> 3) * 256 + ((((2 * (s & 7) + h) ^ (tok & 15))) << 4)); }
#pragma unroll
    for (int s = 0; s < 16; ++s) PIN(xr[s]);
    BAR();
#pragma unroll
    for (int k = 0; k < 3; ++k)
#pragma unroll
        for (int r = 0; r < 3; ++r) dma16(wqo[r] + k * 393216u, wqkv, L_WQKV + (w + 8 * r + 24 * k) * 1024);
    STAMP(2);
    {
        float s0 = 0.f, s1 = 0.f, s2 = 0.f, s3 = 0.f; const bf2 one = {(__bf16)1.0f, (__bf16)1.0f};
#pragma unroll
        for (int s = 0; s < 16; ++s)
#pragma unroll
            for (int i = 0; i < 4; i += 2) { const unsigned ua = xr[s][i], ub = xr[s][i + 1]; const bf2 va = __builtin_bit_cast(bf2, ua), vb = __builtin_bit_cast(bf2, ub);
                s0 = __builtin_amdgcn_fdot2_f32_bf16(va, one, s0, false); s1 = __builtin_amdgcn_fdot2_f32_bf16(va, va, s1, false);
                s2 = __builtin_amdgcn_fdot2_f32_bf16(vb, one, s2, false); s3 = __builtin_amdgcn_fdot2_f32_bf16(vb, vb, s3, false); }
        s0 = xsum32(s0 + s2); s1 = xsum32(s1 + s3);
        if (lane < 32) lds_st<u32x2>(L_STAT + (kh * 128 + 32 * m + lane) * 8, (u32x2){__float_as_uint(s0), __float_as_uint(s1)});
    }
    if (w >= 4) {
        const int t = tid - 256; const unsigned o = WS_MODU + g * 4096 + t * 16; u32x4 v;
        for (unsigned sp = 0; sp < SPIN_CAP; ++sp) { asm volatile("" ::: "memory"); v = __builtin_amdgcn_raw_buffer_load_b128(rws, o, 0, 16); if (__all(v[1] == epoch && v[3] == epoch)) break; }
        const unsigned u0 = v[0], u2 = v[2]; f32x4 f = {bf_lo(u0), bf_hi(u0), bf_lo(u2), bf_hi(u2)};
        if (t >= 128) { f[0] = rbf(1.f + f[0]); f[1] = rbf(1.f + f[1]); f[2] = rbf(1.f + f[2]); f[3] = rbf(1.f + f[3]); }
        lds_st<f32x4>(L_SAG + t * 16, f);
    }
    STAMP(3);
    VMWAIT(0);
    BAR();
    STAMP(4); STAMPC(22);
    if (w == 0) e_t1 = rt();
    float rstd, nmr, gv[2], sv[2];
    const unsigned sga = L_SAG + (256 * kh + 16 * ((lane & 15) >> 3) + 8 * h + (lane & 7)) * 4;
    { const u32x2 a = lds_ld<u32x2>(L_STAT + (32 * m + (lane & 31)) * 8), c2 = lds_ld<u32x2>(L_STAT + (128 + 32 * m + (lane & 31)) * 8);
      sv[0] = lds_ld<float>(sga); gv[0] = lds_ld<float>(sga + 2048);
      const float s0 = __uint_as_float(a[0]) + __uint_as_float(c2[0]), s1 = __uint_as_float(a[1]) + __uint_as_float(c2[1]);
      const float mean = s0 * (1.f / 512.f), var = fmaxf(s1 * (1.f / 512.f) - mean * mean, 0.f);
      rstd = __builtin_amdgcn_rsqf(var + 1e-5f); nmr = -mean * rstd;
      asm volatile("s_nop 1" : "+v"(sv[0]), "+v"(gv[0])); }
    f32x16 aQ, aK, aV;
#pragma unroll
    for (int i = 0; i < 16; ++i) { aQ[i] = 0.f; aK[i] = 0.f; aV[i] = 0.f; }
    {
        const unsigned rowa = L_WQKV + (256 * kh + 8 * (gi >> 1) + q4) * 192 + p4 * 8;
        const unsigned wqa = rowa + (((gi & 1) ^ kh) * 32), wva = rowa + 128 + (gi & 1) * 32;
        bf16x8 fk, fv, fq; unsigned h0[2], h1[2], h2[2], h3[2];
#define HDW(s, i) hdw<8 * ((s) & 1) + 2 * (i)>(xr[s][i], rstd, nmr, gv[((s) >> 1) & 1], sv[((s) >> 1) & 1])
        fk = trfrag(wqa + 64, wqa + 64 + 768); fv = trfrag(wva, wva + 768); fq = trfrag(wqa, wqa + 768);
        h0[0] = HDW(0, 0); h1[0] = HDW(0, 1); h2[0] = HDW(0, 2); h3[0] = HDW(0, 3);
        asm volatile("s_nop 1" : "+v"(h0[0]), "+v"(h1[0]), "+v"(h2[0]), "+v"(h3[0])); SBAR();
#define QKV_STEP(s, sn) { const bf16x8 hb = __builtin_bit_cast(bf16x8, ((u32x4){h0[(s) & 1], h1[(s) & 1], h2[(s) & 1], h3[(s) & 1]})); \
            aK = MFMA32(fk, hb, aK); PIN(aK); SBAR(); if ((sn) < 16) { fk = trfrag(wqa + (sn) * 3072 + 64, wqa + (sn) * 3072 + 64 + 768); h0[(sn) & 1] = HDW((sn) & 15, 0); h1[(sn) & 1] = HDW((sn) & 15, 1); PIN(h0[(sn) & 1]); PIN(h1[(sn) & 1]); } SBAR(); \
            aV = MFMA32(hb, fv, aV); PIN(aV); SBAR(); if ((sn) < 16) { fv = trfrag(wva + (sn) * 3072, wva + (sn) * 3072 + 768); h2[(sn) & 1] = HDW((sn) & 15, 2); PIN(h2[(sn) & 1]); } SBAR(); \
            aQ = MFMA32(fq, hb, aQ); PIN(aQ); SBAR(); if ((sn) < 16) { fq = trfrag(wqa + (sn) * 3072, wqa + (sn) * 3072 + 768); h3[(sn) & 1] = HDW((sn) & 15, 3); PIN(h3[(sn) & 1]); } \
            if ((s) % 2 == 0 && (s) + 2 < 16) { sv[(((s) >> 1) + 1) & 1] = lds_ld<float>(sga + (((s) >> 1) + 1) * 128); gv[(((s) >> 1) + 1) & 1] = lds_ld<float>(sga + 2048 + (((s) >> 1) + 1) * 128); } SBAR(); }
        QKV_STEP(0, 1) QKV_STEP(1, 2) QKV_STEP(2, 3) QKV_STEP(3, 4) QKV_STEP(4, 5) QKV_STEP(5, 6) QKV_STEP(6, 7) QKV_STEP(7, 8)
        QKV_STEP(8, 9) QKV_STEP(9, 10) QKV_STEP(10, 11) QKV_STEP(11, 12) QKV_STEP(12, 13) QKV_STEP(13, 14) QKV_STEP(14, 15) QKV_STEP(15, 16)
#undef QKV_STEP
#undef HDW
    }
    STAMP(5); STAMPC(23);
    {
        float vs[8], vk[8];
#pragma unroll
        for (int i = 0; i < 8; ++i) { float lo = aV[i], hi = aV[8 + i]; PIN(lo); PIN(hi); vs[i] = kh ? lo : hi; vk[i] = kh ? hi : lo; }
        BAR();
        const unsigned ex = (kh ? L_VIMG : L_KIMG) + m * 4096 + lane * 16, eq = L_QS + w * 2048 + lane * 16;
        lds_st<f32x4>(ex, (f32x4){aK[8], aK[9], aK[10], aK[11]}); lds_st<f32x4>(ex + 1024, (f32x4){aK[12], aK[13], aK[14], aK[15]});
        lds_st<f32x4>(ex + 2048, (f32x4){vs[0], vs[1], vs[2], vs[3]}); lds_st<f32x4>(ex + 3072, (f32x4){vs[4], vs[5], vs[6], vs[7]});
        lds_st<f32x4>(eq, (f32x4){aQ[8], aQ[9], aQ[10], aQ[11]}); lds_st<f32x4>(eq + 1024, (f32x4){aQ[12], aQ[13], aQ[14], aQ[15]});
        BAR();
        const unsigned ep = (kh ? L_KIMG : L_VIMG) + m * 4096 + lane * 16, eqp = L_QS + (w ^ 4) * 2048 + lane * 16;
        const f32x4 k0 = lds_ld<f32x4>(ep), k1 = lds_ld<f32x4>(ep + 1024), v0 = lds_ld<f32x4>(ep + 2048), v1 = lds_ld<f32x4>(ep + 3072), q0 = lds_ld<f32x4>(eqp), q1 = lds_ld<f32x4>(eqp + 1024);
        const float qs = 0.25f * 1.44269504089f;
        u32x4 qp, kp, vp;
        kp[0] = pk2(aK[0] + k0[0], aK[1] + k0[1]); kp[1] = pk2(aK[2] + k0[2], aK[3] + k0[3]); kp[2] = pk2(aK[4] + k1[0], aK[5] + k1[1]); kp[3] = pk2(aK[6] + k1[2], aK[7] + k1[3]);
        vp[0] = pk2(vk[0] + v0[0], vk[1] + v0[1]); vp[1] = pk2(vk[2] + v0[2], vk[3] + v0[3]); vp[2] = pk2(vk[4] + v1[0], vk[5] + v1[1]); vp[3] = pk2(vk[6] + v1[2], vk[7] + v1[3]);
        const unsigned kvo = WS_KV + (g * 16 + j) * 16384 + lane * 16;
        __builtin_amdgcn_raw_buffer_store_b128(kp, rws, kvo + (kh * 4 + m) * 1024, 0, 16);
        __builtin_amdgcn_raw_buffer_store_b128(vp, rws, kvo + 8192 + (m * 2 + kh) * 1024, 0, 16);
        lds_st<u32x4>(L_KIMG + (8 + m) * 2048 + kh * 1024 + lane * 16, kp);
        lds_st<u32x4>(L_VIMG + (8 + m) * 2048 + kh * 1024 + lane * 16, vp);
        qp[0] = pk2((aQ[0] + q0[0]) * qs, (aQ[1] + q0[1]) * qs); qp[1] = pk2((aQ[2] + q0[2]) * qs, (aQ[3] + q0[3]) * qs);
        qp[2] = pk2((aQ[4] + q1[0]) * qs, (aQ[5] + q1[1]) * qs); qp[3] = pk2((aQ[6] + q1[2]) * qs, (aQ[7] + q1[3]) * qs);
        qf = __builtin_bit_cast(bf16x8, qp); PIN(qf);
    }
    }
    VMWAIT(0);
    BAR();
    if (tid == 0) stg_sc1(ws, WS_FLAG + (0 * 256 + g * 16 + j) * 256, epoch);
    STAMP(6);
    const int tt = w & 3;
    if (w >= 4) {
        const int t = tid - 256; const unsigned o = WS_MODN + g * 8192 + t * 32; u32x4 v, v2;
        for (unsigned sp = 0; sp < SPIN_CAP; ++sp) { asm volatile("" ::: "memory"); v = __builtin_amdgcn_raw_buffer_load_b128(rws, o, 0, 16); v2 = __builtin_amdgcn_raw_buffer_load_b128(rws, o + 16, 0, 16);
            if (__all(v[1] == epoch && v[3] == epoch && v2[1] == epoch && v2[3] == epoch)) break; }
        u32x4 val = {v[0], v[2], v2[0], v2[2]};
        const int vec = t >> 6, c8 = (t & 63) * 8;
        if (vec == 1) lds_st<u32x4>(L_SB16 + c8 * 2, val);
        else if (vec == 2) {
#pragma unroll
            for (int i = 0; i < 4; ++i) { const unsigned u_ = val[i]; val[i] = pk2(1.f + bf_lo(u_), 1.f + bf_hi(u_)); }
            lds_st<u32x4>(L_GB16 + c8 * 2, val);
        } else if ((c8 >> 6) == ct) lds_st<u32x4>((vec == 0 ? L_GAO : L_GBO) + (c8 & 63) * 2, val);
    }
    {
    FRESH(lane);
    const int hh = kh, qq = lane & 31, h = lane >> 5;
    const float NEG = -__builtin_inff();
    f32x16 c0, c8;
#pragma unroll
    for (int r = 0; r < 16; ++r) { const int kk = (r & 3) + 8 * (r >> 2) + 4 * h; c0[r] = kk > qq ? 0.f : NEG; c8[r] = kk <= qq ? 0.f : NEG; }
    PIN(c0); PIN(c8);
    BAR();
    STAMP(8);
    {
        const unsigned ka = L_KIMG + m * 2048 + hh * 1024 + lane * 16;
        const bool isd = ((lane >> 4) & 1) == hh;
        const unsigned va = isd ? L_VIMG + m * 2048 + lane * 16 : L_ONES, vst = isd ? 2048u : 0u, vs1 = isd ? 1024u : 0u;
        f32x16 o;
#pragma unroll
        for (int r = 0; r < 16; ++r) o[r] = 0.f;
#define MAX16(mx, s) { _Pragma("unroll") for (int r = 0; r < 16; r += 2) mx = fmaxf(fmaxf(mx, s[r]), s[r + 1]); }
#define EXPH(P, S, b) { P[0] = pk2(__builtin_amdgcn_exp2f(S[b]), __builtin_amdgcn_exp2f(S[b + 1])); P[1] = pk2(__builtin_amdgcn_exp2f(S[b + 2]), __builtin_amdgcn_exp2f(S[b + 3])); \
                        P[2] = pk2(__builtin_amdgcn_exp2f(S[b + 4]), __builtin_amdgcn_exp2f(S[b + 5])); P[3] = pk2(__builtin_amdgcn_exp2f(S[b + 6]), __builtin_amdgcn_exp2f(S[b + 7])); }
        if (g >= 2) {
            f32x16 z;
#pragma unroll
            for (int r = 0; r < 16; ++r) z[r] = 0.f;
            PIN(z);
            float mx = NEG, mx2 = NEG; f32x16 sA, sB;
#define KF(i) lds_ld<bf16x8>(ka + (i) * 2048)
            bf16x8 k0 = KF(0), k1 = KF(8), k2 = KF(1), k3 = KF(2);
            SBAR();
            sA = MFMA32(k0, qf, c0); k0 = KF(3); SBAR();
            sB = MFMA32(k1, qf, c8); k1 = KF(4); MAX16(mx, sA); PIN(mx); SBAR();
            sA = MFMA32(k2, qf, z); k2 = KF(5); MAX16(mx2, sB); PIN(mx2); SBAR();
            sB = MFMA32(k3, qf, z); k3 = KF(6); MAX16(mx, sA); PIN(mx); SBAR();
            sA = MFMA32(k0, qf, z); k0 = KF(7); MAX16(mx2, sB); PIN(mx2); SBAR();
            sB = MFMA32(k1, qf, z); k1 = KF(0); MAX16(mx, sA); PIN(mx); SBAR();
            sA = MFMA32(k2, qf, z); k2 = KF(1); MAX16(mx2, sB); PIN(mx2); SBAR();
            sB = MFMA32(k3, qf, z); MAX16(mx, sA); PIN(mx); SBAR();
            sA = MFMA32(k0, qf, z); MAX16(mx2, sB); PIN(mx2); SBAR();
            bf16x8 v0 = lds_ld<bf16x8>(va), v1 = lds_ld<bf16x8>(va + vs1);
            MAX16(mx, sA);
            mx = xmax32(fmaxf(mx, mx2));
#pragma unroll
            for (int r = 0; r < 16; ++r) { z[r] = -mx; c0[r] -= mx; c8[r] -= mx; }
            PIN(z); PIN(c0); PIN(c8); SBAR();
            sA = MFMA32(k1, qf, c0); SBAR();
            u32x4 pa, pb; bf16x8 v0n, v1n, kn = k2; unsigned vp = va;
#define ABLK(I, SC, SN, CN) { bf16x8 kc = kn; if ((I) < 8) { vp += vst; v0n = lds_ld<bf16x8>(vp); v1n = lds_ld<bf16x8>(vp + vs1); } if ((I) < 7) kn = KF((I) + 2); SBAR(); \
                if ((I) < 8) SN = MFMA32(kc, qf, CN); EXPH(pa, SC, 0); PIN(pa); SBAR(); \
                o = MFMA32(v0, __builtin_bit_cast(bf16x8, pa), o); EXPH(pb, SC, 8); PIN(pb); SBAR(); \
                o = MFMA32(v1, __builtin_bit_cast(bf16x8, pb), o); SBAR(); if ((I) < 8) { v0 = v0n; v1 = v1n; } }
            ABLK(0, sA, sB, z) ABLK(1, sB, sA, z) ABLK(2, sA, sB, z) ABLK(3, sB, sA, z)
            ABLK(4, sA, sB, z) ABLK(5, sB, sA, z) ABLK(6, sA, sB, z) ABLK(7, sB, sA, c8) ABLK(8, sA, sB, c8)
#undef KF
#undef ABLK
        } else {
            const int i0 = 8 - 4 * g - m;
            f32x16 z;
#pragma unroll
            for (int r = 0; r < 16; ++r) z[r] = 0.f;
            float mx = NEG;
            f32x16 s = MFMA32(lds_ld<bf16x8>(ka + 8 * 2048), qf, c8); MAX16(mx, s);
#pragma unroll 1
            for (int i = i0; i < 8; ++i) { s = MFMA32(lds_ld<bf16x8>(ka + i * 2048), qf, z); MAX16(mx, s); }
            mx = xmax32(mx);
#pragma unroll
            for (int r = 0; r < 16; ++r) { z[r] = -mx; c8[r] -= mx; }
#pragma unroll 1
            for (int i = i0; i <= 8; ++i) { u32x4 pa, pb;
                if (i < 8) s = MFMA32(lds_ld<bf16x8>(ka + i * 2048), qf, z); else s = MFMA32(lds_ld<bf16x8>(ka + i * 2048), qf, c8);
                EXPH(pa, s, 0); EXPH(pb, s, 8);
                o = MFMA32(lds_ld<bf16x8>(va + i * vst), __builtin_bit_cast(bf16x8, pa), o); o = MFMA32(lds_ld<bf16x8>(va + i * vst + vs1), __builtin_bit_cast(bf16x8, pb), o); }
        }
#undef EXPH
#undef MAX16
        float ov[8], l;
        { float lo = o[0], hi = o[8]; PIN(lo); PIN(hi); l = hh ? lo : hi; }
#pragma unroll
        for (int r = 0; r < 8; ++r) { float lo = o[r], hi = o[8 + r]; PIN(lo); PIN(hi); ov[r] = hh ? hi : lo; }
        const float inv = __builtin_amdgcn_rcpf(l);
        const u32x4 op = {pk2(ov[0] * inv, ov[1] * inv), pk2(ov[2] * inv, ov[3] * inv), pk2(ov[4] * inv, ov[5] * inv), pk2(ov[6] * inv, ov[7] * inv)};
        __builtin_amdgcn_raw_buffer_store_b128(op, rws, WS_O + ((g * 8 + 2 * m + (qq >> 4)) * 16 + j) * 1024 + ((qq & 15) + 16 * (2 * hh + h)) * 16, 0, 16);
    }
    }
    VMWAIT(0);
    BAR();
    if (tid == 0) stg_sc1(ws, WS_FLAG + (1 * 256 + g * 16 + j) * 256, epoch);
    STAMP(9);
    if (w == 0) poll_flags(ws, WS_FLAG + (1 * 256 + g * 16) * 256, 16, epoch, lane);
    BAR();
    STAMP(10);
    u32x2 x1p0, x1p1;
    {
        FRESH(lane);
        const int gi = lane >> 4, q4 = (lane & 15) >> 2, p4 = lane & 3;
        const unsigned tokP = 128 * g + 64 * rh + 16 * tt + (lane & 15), colP = 64 * ct + 32 * kh + 4 * gi;
        u32x4 of[8];
#pragma unroll
        for (int s = 0; s < 8; ++s) of[s] = __builtin_amdgcn_raw_buffer_load_b128(rws, WS_O + ((g * 8 + 4 * rh + tt) * 16 + 8 * kh + s) * 1024 + lane * 16, 0, 16);
        const u32x2 xres0 = *(const u32x2*)(x + tokP * 512 + colP), xres1 = *(const u32x2*)(x + tokP * 512 + colP + 16);
        SBAR();
        f32x4 acc[4];
#pragma unroll
        for (int n = 0; n < 4; ++n) acc[n] = (f32x4){0.f, 0.f, 0.f, 0.f};
        const unsigned xv = 2 * (gi & 1) + (q4 >> 1), rowa = L_WO + (256 * kh + 16 * (gi >> 1) + 4 * (gi & 1) + q4) * 128 + p4 * 8;
        unsigned an[4]; bf16x8 wf[4];
#pragma unroll
        for (int n = 0; n < 4; ++n) { an[n] = rowa + ((n ^ xv) * 32); wf[n] = trfrag(an[n], an[n] + 1024); }
        const unsigned cl = (32 * kh + 4 * gi) * 2, cg = colP * 2;
        const u32x2 ga0 = lds_ld<u32x2>(L_GAO + cl), ga1 = lds_ld<u32x2>(L_GAO + cl + 32);
        const u32x2 gb0 = lds_ld<u32x2>(L_GB16 + cg), gb1 = lds_ld<u32x2>(L_GB16 + cg + 32), sb0 = lds_ld<u32x2>(L_SB16 + cg), sb1 = lds_ld<u32x2>(L_SB16 + cg + 32);
        SBAR();
#pragma unroll
        for (int s = 0; s < 8; ++s)
#pragma unroll
            for (int n = 0; n < 4; ++n) { acc[n] = MFMA16(wf[n], __builtin_bit_cast(bf16x8, of[s]), acc[n]); PIN(acc[n]); SBAR(); if (s < 7) wf[n] = trfrag(an[n] + (s + 1) * 4096, an[n] + (s + 1) * 4096 + 1024); SBAR(); }
        BAR();
        STAMP(11);
        const unsigned ex = w * 2048 + lane * 16;
        { f32x4 s0 = acc[0], s1 = acc[1], s2 = acc[2], s3 = acc[3]; PIN(s0); PIN(s1); PIN(s2); PIN(s3);
          lds_st<f32x4>(ex, kh ? s0 : s2); lds_st<f32x4>(ex + 1024, kh ? s1 : s3); acc[0] = kh ? s2 : s0; acc[1] = kh ? s3 : s1; }
        BAR();
        const unsigned ep = (w ^ 4) * 2048 + lane * 16;
        const f32x4 a0 = lds_ld<f32x4>(ep) + acc[0], a1 = lds_ld<f32x4>(ep + 1024) + acc[1];
        float xv1[8], gg[8], ss[8];
#pragma unroll
        for (int r = 0; r < 4; ++r) {
            const unsigned ua0 = ga0[r >> 1], ua1 = ga1[r >> 1], ux0 = xres0[r >> 1], ux1 = xres1[r >> 1], ug0 = gb0[r >> 1], ug1 = gb1[r >> 1], us0 = sb0[r >> 1], us1 = sb1[r >> 1];
            xv1[r] = rbf(fmaf((r & 1) ? bf_hi(ua0) : bf_lo(ua0), a0[r], (r & 1) ? bf_hi(ux0) : bf_lo(ux0))); xv1[4 + r] = rbf(fmaf((r & 1) ? bf_hi(ua1) : bf_lo(ua1), a1[r], (r & 1) ? bf_hi(ux1) : bf_lo(ux1)));
            gg[r] = (r & 1) ? bf_hi(ug0) : bf_lo(ug0); gg[4 + r] = (r & 1) ? bf_hi(ug1) : bf_lo(ug1);
            ss[r] = (r & 1) ? bf_hi(us0) : bf_lo(us0); ss[4 + r] = (r & 1) ? bf_hi(us1) : bf_lo(us1);
        }
        x1p0 = (u32x2){pk2(xv1[0], xv1[1]), pk2(xv1[2], xv1[3])}; x1p1 = (u32x2){pk2(xv1[4], xv1[5]), pk2(xv1[6], xv1[7])};
        float st[6] = {0.f, 0.f, 0.f, 0.f, 0.f, 0.f}, pg[8];
#pragma unroll
        for (int r = 0; r < 8; ++r) pg[r] = xv1[r] * gg[r];
        const u32x4 xg = {pk2(pg[0], pg[1]), pk2(pg[2], pg[3]), pk2(pg[4], pg[5]), pk2(pg[6], pg[7])};
        __builtin_amdgcn_raw_buffer_store_b128(xg, rws, WS_XG + ((g * 8 + 4 * rh + tt) * 16 + 2 * ct + kh) * 1024 + lane * 16, 0, 16);
#pragma unroll
        for (int r = 0; r < 8; ++r) { const float p = pg[r]; st[0] += xv1[r]; st[1] = fmaf(xv1[r], xv1[r], st[1]); st[2] += p; st[3] = fmaf(p, p, st[3]); st[4] = fmaf(p, gg[r], st[4]); st[5] = fmaf(p, ss[r], st[5]); }
#pragma unroll
        for (int i = 0; i < 6; ++i) st[i] = xsum32(xsum16(st[i]));
        if (kh == 1 && lane < 16) { lds_st<f32x4>(L_SCR + (tt * 16 + lane) * 32, (f32x4){st[0], st[1], st[2], st[3]}); lds_st<u32x2>(L_SCR + (tt * 16 + lane) * 32 + 16, (u32x2){__float_as_uint(st[4]), __float_as_uint(st[5])}); }
        BAR();
        if (kh == 0 && lane < 16) {
            const f32x4 o4 = lds_ld<f32x4>(L_SCR + (tt * 16 + lane) * 32); const u32x2 o2 = lds_ld<u32x2>(L_SCR + (tt * 16 + lane) * 32 + 16);
            const unsigned so = WS_ST + g * 24576, T = 4 * rh + tt;
            const u32x4 sa4 = {__float_as_uint(st[0] + o4[0]), __float_as_uint(st[1] + o4[1]), __float_as_uint(st[2] + o4[2]), __float_as_uint(st[3] + o4[3])};
            const u32x2 sb2 = {__float_as_uint(st[4] + __uint_as_float(o2[0])), __float_as_uint(st[5] + __uint_as_float(o2[1]))};
            __builtin_amdgcn_raw_buffer_store_b128(sa4, rws, so + ((T * 8 + ct) * 16 + lane) * 16, 0, 16);
            __builtin_amdgcn_raw_buffer_store_b64(sb2, rws, so + 16384 + ((T * 8 + ct) * 16 + lane) * 8, 0, 16);
        }
    }
    VMWAIT(0);
    BAR();
    if (tid == 0) stg_sc1(ws, WS_FLAG + (2 * 256 + g * 16 + j) * 256, epoch);
    STAMP(12);
    const int odd = j & 1;
    {
    FRESH(lane);
    const int gi = lane >> 4, q4 = (lane & 15) >> 2, p4 = lane & 3;
    const u32x2 b1v = *(const u32x2*)(b1 + 112 * j + (w > 0 ? w - 1 : 0) * 16 + 4 * gi);
    BAR();
    if (w > 0) {
        const int nt = w - 1; f32x4 acc = {0.f, 0.f, 0.f, 0.f};
        const unsigned ra = L_W1 + (4 * gi + q4) * 224 + p4 * 8 + nt * 32; const int tk = lane & 15;
        const unsigned va = (tk == 0 ? L_GB16 : L_SB16) + 8 * gi;
#pragma unroll
        for (int s = 0; s < 16; ++s) {
            const u32x2 lo = lds_ld<u32x2>(va + s * 64), hi = lds_ld<u32x2>(va + s * 64 + 32);
            u32x4 bv = {lo[0], lo[1], hi[0], hi[1]};
            if (tk == 2) bv = (u32x4){0x3F803F80u, 0x3F803F80u, 0x3F803F80u, 0x3F803F80u};
            if (tk > 2) bv = (u32x4){0u, 0u, 0u, 0u};
            acc = MFMA16(trfrag(ra + s * 7168, ra + s * 7168 + 3584), __builtin_bit_cast(bf16x8, bv), acc);
        }
        if (tk < 3) lds_st<f32x4>(L_UVW + (tk * 112 + nt * 16 + 4 * gi) * 4, acc);
        if (tk == 3) lds_st<f32x4>(L_UVW + (3 * 112 + nt * 16 + 4 * gi) * 4, (f32x4){bf_lo(b1v[0]), bf_hi(b1v[0]), bf_lo(b1v[1]), bf_hi(b1v[1])});
    } else {
        const u32x4 gq = lds_ld<u32x4>(L_GB16 + lane * 16), sq = lds_ld<u32x4>(L_SB16 + lane * 16);
        float c5[5] = {0.f, 0.f, 0.f, 0.f, 0.f};
#pragma unroll
        for (int i = 0; i < 4; ++i) { const unsigned ug = gq[i], us = sq[i]; const float g0 = bf_lo(ug), g1 = bf_hi(ug), s0 = bf_lo(us), s1 = bf_hi(us);
            c5[0] += g0 + g1; c5[1] += s0 + s1; c5[2] += g0 * g0 + g1 * g1; c5[3] += g0 * s0 + g1 * s1; c5[4] += s0 * s0 + s1 * s1; }
#pragma unroll
        for (int i = 0; i < 5; ++i) c5[i] = xsum32(xsum16(rowsum16(c5[i])));
        if (lane == 0) { lds_st<f32x4>(L_CONST, (f32x4){c5[0], c5[1], c5[2], c5[3]}); lds_st<float>(L_CONST + 16, c5[4]); }
        poll_flags(ws, WS_FLAG + (2 * 256 + g * 16) * 256, 16, epoch, lane);
    }
    }
    BAR();
    STAMP(13);
    {
        constexpr int WIN = 6;
        FRESH(lane);
        const int gi = lane >> 4, q4 = (lane & 15) >> 2, p4 = lane & 3;
        u32x4 xf[16];
        const unsigned xo = WS_XG + (g * 8 + w) * 16384 + lane * 16;
#pragma unroll
        for (int s = 0; s < WIN; ++s) xf[s] = __builtin_amdgcn_raw_buffer_load_b128(rws, xo + s * 1024, 0, 16);
        SBAR(); WSTAMP(0);
        f32x4 acc[7]; bf16x8 wf[7];
        const unsigned ra = L_W1 + (4 * gi + q4) * 224 + p4 * 8, raA = ra + 32 * odd, raB = ra + (odd ? 0 : 192);
        const unsigned raA2 = raA + 8 * 7168, raB2 = raB + 8 * 7168;
#define W1A(s, u) (((s) < 8 ? ((u) < 6 ? raA : raB) : ((u) < 6 ? raA2 : raB2)) + ((s) & 7) * 7168 + ((u) < 6 ? (u) * 32 : 0))
#pragma unroll
        for (int u = 0; u < 7; ++u) { acc[u] = (f32x4){0.f, 0.f, 0.f, 0.f}; wf[u] = trfrag(W1A(0, u), W1A(0, u) + 3584); }
        SBAR();
        u32x4 sA0, sA1; u32x2 sB0, sB1;
        const unsigned so = WS_ST + g * 24576 + w * 2048, sob = WS_ST + g * 24576 + 16384 + w * 1024;
#pragma unroll
        for (int s = 0; s < 16; ++s) {
            if (s + WIN < 16) xf[s + WIN] = __builtin_amdgcn_raw_buffer_load_b128(rws, xo + (s + WIN) * 1024, 0, 16);
            if (s == 10) { sA0 = __builtin_amdgcn_raw_buffer_load_b128(rws, so + ((2 * gi) * 16 + (lane & 15)) * 16, 0, 16); sA1 = __builtin_amdgcn_raw_buffer_load_b128(rws, so + ((2 * gi + 1) * 16 + (lane & 15)) * 16, 0, 16); }
            if (s == 11) { sB0 = __builtin_amdgcn_raw_buffer_load_b64(rws, sob + ((2 * gi) * 16 + (lane & 15)) * 8, 0, 16); sB1 = __builtin_amdgcn_raw_buffer_load_b64(rws, sob + ((2 * gi + 1) * 16 + (lane & 15)) * 8, 0, 16); }
            SBAR();
#pragma unroll
            for (int u = 0; u < 7; ++u) { acc[u] = MFMA16(wf[u], __builtin_bit_cast(bf16x8, xf[s]), acc[u]); PIN(acc[u]); SBAR(); if (s < 15) wf[u] = trfrag(W1A(s + 1, u), W1A(s + 1, u) + 3584); SBAR(); }
            if (s == 0) WSTAMP(1); if (s == 7) WSTAMP(2); if (s == 15) WSTAMP(3);
        }
#undef W1A
        const unsigned uo = L_UVW + 16 * gi + 64 * odd, uo6 = L_UVW + 16 * gi + (odd ? 0 : 384);
        f32x4 uu = lds_ld<f32x4>(uo), vv = lds_ld<f32x4>(uo + 448), ww = lds_ld<f32x4>(uo + 896), bb = lds_ld<f32x4>(uo + 1344);
        const f32x4 cc4 = lds_ld<f32x4>(L_CONST); const float SB2 = lds_ld<float>(L_CONST + 16);
        float S[6];
#pragma unroll
        for (int i = 0; i < 4; ++i) S[i] = xsum32(xsum16(__uint_as_float(sA0[i]) + __uint_as_float(sA1[i])));
#pragma unroll
        for (int i = 0; i < 2; ++i) S[4 + i] = xsum32(xsum16(__uint_as_float(sB0[i]) + __uint_as_float(sB1[i])));
        const float iD = 1.f / 512.f, m1 = S[0] * iD, r1 = __builtin_amdgcn_rsqf(fmaxf(S[1] * iD - m1 * m1, 0.f) + 1e-5f);
        const float m2 = (r1 * (S[2] - m1 * cc4[0]) + cc4[1]) * iD;
        const float ez2 = (r1 * r1 * (S[3] - 2.f * m1 * S[4] + m1 * m1 * cc4[2]) + 2.f * r1 * (S[5] - m1 * cc4[3]) + SB2) * iD;
        const float r2 = __builtin_amdgcn_rsqf(fmaxf(ez2 - m2 * m2, 0.f) + 1e-5f);
        const float ca = r1 * r2, cb = -ca * m1, cc = -r2 * m2;
        BAR();
        STAMP(14);
        unsigned hp[7][2];
        const unsigned hb = WS_H + (g * 8 + w) * 57344, s0 = (7 * j + odd) >> 1;
#pragma unroll
        for (int u = 0; u < 7; ++u) {
            float pre[4], z[4], e[4];
#pragma unroll
            for (int r = 0; r < 4; ++r) pre[r] = fmaf(ca, acc[u][r], fmaf(cb, uu[r], fmaf(r2, vv[r], fmaf(cc, ww[r], bb[r]))));
            if (u < 6) { const unsigned o = u < 5 ? uo + (u + 1) * 64 : uo6; uu = lds_ld<f32x4>(o); vv = lds_ld<f32x4>(o + 448); ww = lds_ld<f32x4>(o + 896); bb = lds_ld<f32x4>(o + 1344); }
            SBAR();
#pragma unroll
            for (int r = 0; r < 4; ++r) z[r] = pre[r] * fmaf(pre[r] * pre[r], -0.044715f * 2.302208198f, -2.302208198f);
#pragma unroll
            for (int r = 0; r < 4; ++r) e[r] = __builtin_amdgcn_exp2f(z[r]);
#pragma unroll
            for (int r = 0; r < 4; ++r) e[r] = __builtin_amdgcn_rcpf(1.f + e[r]);
            hp[u][0] = pk2(pre[0] * e[0], pre[1] * e[1]); hp[u][1] = pk2(pre[2] * e[2], pre[3] * e[3]);
            if (u < 6 && (u & 1)) __builtin_amdgcn_raw_buffer_store_b128((u32x4){hp[u - 1][0], hp[u - 1][1], hp[u][0], hp[u][1]}, rws, hb + (s0 + (u >> 1)) * 1024 + lane * 16, 0, 16);
            SBAR();
        }
        __builtin_amdgcn_raw_buffer_store_b64((u32x2){hp[6][0], hp[6][1]}, rws, hb + ((7 * j + (odd ? 0 : 6)) >> 1) * 1024 + odd * 512 + lane * 8, 0, 16);
        WSTAMP(4);
    }
    VMWAIT(0); WSTAMP(5);
    BAR();
    if (tid == 0) stg_sc1(ws, WS_FLAG + (3 * 256 + g * 16 + j) * 256, epoch);
    STAMP(15);
    if (w == 0) poll_flags(ws, WS_FLAG + (3 * 256 + g * 16) * 256, 16, epoch, lane);
    BAR();
    STAMP(16);
    {
        constexpr int WIN = 8;
        FRESH(lane);
        const int gi = lane >> 4, q4 = (lane & 15) >> 2, p4 = lane & 3;
        const unsigned tokP = 128 * g + 64 * rh + 16 * tt + (lane & 15), colP = 64 * ct + 32 * kh + 4 * gi;
        const unsigned hb = WS_H + (g * 8 + 4 * rh + tt) * 57344 + kh * 28 * 1024;
        u32x4 hf[28];
#define HLOAD(s) { if ((s) % 7 == 3) { const u32x2 lo = __builtin_amdgcn_raw_buffer_load_b64(rws, hb + (s) * 1024 + lane * 8, 0, 16), hi = __builtin_amdgcn_raw_buffer_load_b64(rws, hb + (s) * 1024 + 512 + lane * 8, 0, 16); hf[s] = (u32x4){lo[0], lo[1], hi[0], hi[1]}; } \
                    else hf[s] = __builtin_amdgcn_raw_buffer_load_b128(rws, hb + (s) * 1024 + lane * 16, 0, 16); }
#pragma unroll
        for (int s = 0; s < WIN; ++s) HLOAD(s)
        SBAR();
        f32x4 acc[4]; bf16x8 wf[4];
        const unsigned xv = (2 * gi + (q4 >> 1)) & 3, rowa = kh * 8192 + (4 * gi + q4) * 128 + p4 * 8;
        unsigned an[4], an2[4], an3[4];
#pragma unroll
        for (int n = 0; n < 4; ++n) { an[n] = rowa + ((n ^ xv) * 32); an2[n] = an[n] + 4 * 16384; an3[n] = an[n] + 8 * 16384; acc[n] = (f32x4){0.f, 0.f, 0.f, 0.f}; }
#define W2A(s, n) ((((s) >> 1) % 9 < 4 ? an[n] : ((s) >> 1) % 9 < 8 ? an2[n] : an3[n]) + ((((s) >> 1) % 9) & 3) * 16384 + ((s) & 1) * 4096)
#pragma unroll
        for (int n = 0; n < 4; ++n) wf[n] = trfrag(W2A(0, n), W2A(0, n) + 2048);
        const unsigned cl = (32 * kh + 4 * gi) * 2;
        const u32x2 g0 = lds_ld<u32x2>(L_GBO + cl), g1 = lds_ld<u32x2>(L_GBO + cl + 32);
        u32x2 bb0, bb1;
        SBAR();
#pragma unroll
        for (int s = 0; s < 28; ++s) {
            if (s == 24) { bb0 = *(const u32x2*)(b2 + colP); bb1 = *(const u32x2*)(b2 + colP + 16); }
            if (s == 10) BAR();
            if (s == 17) BAR();
            if (s + WIN < 28) HLOAD(s + WIN)
            SBAR();
#pragma unroll
            for (int n = 0; n < 4; ++n) { acc[n] = MFMA16(wf[n], __builtin_bit_cast(bf16x8, hf[s]), acc[n]); PIN(acc[n]); SBAR(); if (s < 27) wf[n] = trfrag(W2A(s + 1, n), W2A(s + 1, n) + 2048); SBAR(); }
        }
#undef W2A
#undef HLOAD
        BAR();
        STAMP(17);
        const unsigned ex = w * 2048 + lane * 16;
        { f32x4 s0 = acc[0], s1 = acc[1], s2 = acc[2], s3 = acc[3]; PIN(s0); PIN(s1); PIN(s2); PIN(s3);
          lds_st<f32x4>(ex, kh ? s0 : s2); lds_st<f32x4>(ex + 1024, kh ? s1 : s3); acc[0] = kh ? s2 : s0; acc[1] = kh ? s3 : s1; }
        BAR();
        const unsigned ep = (w ^ 4) * 2048 + lane * 16;
        const f32x4 a0 = lds_ld<f32x4>(ep) + acc[0], a1 = lds_ld<f32x4>(ep + 1024) + acc[1];
        float y0[4], y1[4];
#pragma unroll
        for (int r = 0; r < 4; ++r) {
            const unsigned ug0 = g0[r >> 1], ug1 = g1[r >> 1], ub0 = bb0[r >> 1], ub1 = bb1[r >> 1], ux0 = x1p0[r >> 1], ux1 = x1p1[r >> 1];
            y0[r] = fmaf((r & 1) ? bf_hi(ug0) : bf_lo(ug0), a0[r] + ((r & 1) ? bf_hi(ub0) : bf_lo(ub0)), (r & 1) ? bf_hi(ux0) : bf_lo(ux0));
            y1[r] = fmaf((r & 1) ? bf_hi(ug1) : bf_lo(ug1), a1[r] + ((r & 1) ? bf_hi(ub1) : bf_lo(ub1)), (r & 1) ? bf_hi(ux1) : bf_lo(ux1));
        }
        *(u32x2*)(out + tokP * 512 + colP) = (u32x2){pk2(y0[0], y0[1]), pk2(y0[2], y0[3])};
        *(u32x2*)(out + tokP * 512 + colP + 16) = (u32x2){pk2(y1[0], y1[1]), pk2(y1[2], y1[3])};
    }
    if (b == 0 && w == 0) {
        for (unsigned sp = 0; sp < SPIN_CAP; ++sp) { asm volatile("" ::: "memory"); const u32x4 v = __builtin_amdgcn_raw_buffer_load_b128(rws, WS_HELLO + lane * 16, 0, 16); if (__all(v[0] == epoch && v[1] == epoch && v[2] == epoch && v[3] == epoch)) break; }
        if (lane == 0) stg_sc1(ws, WS_E, epoch);
    }
    STAMP(18); STAMPC(21);
    if (b == 135 && w == 0) { unsigned long long dl = 2 * (e_t1 - e_t0); if (dl > 20000) dl = 20000; const unsigned long long t_end = rt() + dl; while (rt() < t_end) __builtin_amdgcn_s_sleep(1); }
    STAMP_OUT();
    asm volatile("fb_code_end:");
}

extern "C" void kernel_launch(void* const* d_in, const int* in_sizes, int n_in, void* d_out, int out_size, void* d_ws, size_t ws_size, hipStream_t stream) {
    (void)hipFuncSetAttribute((const void*)fused_block, hipFuncAttributeMaxDynamicSharedMemorySize, LDS_BYTES);
    fused_block<<<256, 576, LDS_BYTES, stream>>>((const u16*)d_in[0], (const u16*)d_in[1], (const u16*)d_in[2], (const u16*)d_in[4], (const u16*)d_in[5], (const u16*)d_in[6], (const u16*)d_in[7],
                                                 (const u16*)d_in[8], (const u16*)d_in[9], (u16*)d_out, (char*)d_ws);
}
```

```cpp
#include <hip/hip_runtime.h>
#include <hip/hip_bf16.h>
#include <stdint.h>

typedef unsigned short u16;
typedef short bf16x8 __attribute__((ext_vector_type(8)));
typedef short s16x4 __attribute__((ext_vector_type(4)));
typedef float f32x4 __attribute__((ext_vector_type(4)));
typedef float f32x2 __attribute__((ext_vector_type(2)));
typedef float f32x16 __attribute__((ext_vector_type(16)));
typedef unsigned u32x4 __attribute__((ext_vector_type(4)));
typedef unsigned u32x2 __attribute__((ext_vector_type(2)));
typedef __bf16 bf2 __attribute__((ext_vector_type(2)));
#define LAS __attribute__((address_space(3)))
#define GAS __attribute__((address_space(1)))
#define DI __device__ __forceinline__

#define TS 0

constexpr unsigned WS_E = 0, WS_HELLO = 4096, WS_FLAG = 8192;
constexpr unsigned WS_MODU = 0x50000, WS_MODN = 0x60000;
constexpr unsigned WS_PART = 0x80000;
constexpr unsigned WS_KV = 0x100000, WS_O = 0x500000, WS_XG = 0x700000, WS_ST = 0x900000, WS_H = 0xA00000, WS_TS = 0x2000000;
constexpr unsigned WS_BYTES = 0x2100000;
constexpr unsigned L_X = 0, L_WQKV = 57344, L_RED = 155648, L_SAG = 156672, L_STAT = 160768;
constexpr unsigned L_QS = 65536, L_KIMG = 98304, L_VIMG = 122880, L_WO = 0, L_W1 = 44032;
constexpr unsigned L_GB16 = 158720, L_SB16 = 159744, L_UVW = 160768, L_CONST = 162560, L_GAO = 162592, L_GBO = 162720, L_ONES = 162848, L_DUMMY = 162880;
constexpr unsigned L_TAGM = L_RED + 512, L_TAGL = L_RED + 544, L_TAGC = L_RED + 560;
constexpr unsigned L_RED2 = 81920;
constexpr unsigned L_SCR = 32768;
constexpr unsigned LDS_BYTES = 163840;
constexpr unsigned SPIN_CAP = 1u << 16;

DI float bf_lo(unsigned u) { return __uint_as_float(u << 16); }
DI float bf_hi(unsigned u) { return __uint_as_float(u & 0xffff0000u); }
DI unsigned pk2(float a, float b) { bf2 v = {(__bf16)a, (__bf16)b}; return __builtin_bit_cast(unsigned, v); }
DI float rbf(float a) { return bf_lo(pk2(a, a)); }
DI void dma16(unsigned voff, const void* sbase, unsigned lds) { asm volatile("s_mov_b32 m0, %1\n\ts_nop 0\n\tglobal_load_lds_dwordx4 %0, %2" ::"v"(voff), "s"(lds), "s"(sbase) : "memory"); }
DI void dma16c(unsigned voff, const void* sbase, unsigned lds) { asm volatile("s_mov_b32 m0, %1\n\ts_nop 0\n\tglobal_load_lds_dwordx4 %0, %2 sc1" ::"v"(voff), "s"(lds), "s"(sbase) : "memory"); }
DI void dma4(unsigned v0, unsigned v1, unsigned v2, unsigned v3, const void* sbase, unsigned lds) {
    asm volatile("s_mov_b32 m0, %5\n\ts_nop 0\n\tglobal_load_lds_dwordx4 %0, %4\n\tglobal_load_lds_dwordx4 %1, %4 offset:1024\n\tglobal_load_lds_dwordx4 %2, %4 offset:2048\n\tglobal_load_lds_dwordx4 %3, %4 offset:3072"
                 ::"v"(v0), "v"(v1), "v"(v2), "v"(v3), "s"(sbase), "s"(lds) : "memory"); }
DI void dma3(unsigned v0, unsigned v1, unsigned v2, const void* sbase, unsigned lds) {
    asm volatile("s_mov_b32 m0, %4\n\ts_nop 0\n\tglobal_load_lds_dwordx4 %0, %3\n\tglobal_load_lds_dwordx4 %1, %3 offset:1024\n\tglobal_load_lds_dwordx4 %2, %3 offset:2048"
                 ::"v"(v0), "v"(v1), "v"(v2), "s"(sbase), "s"(lds) : "memory"); }
DI void dma4lin_c(unsigned v, const void* sbase, unsigned lds) {
    asm volatile("s_mov_b32 m0, %2\n\ts_nop 0\n\tglobal_load_lds_dwordx4 %0, %1 sc1\n\tglobal_load_lds_dwordx4 %0, %1 offset:1024 sc1\n\tglobal_load_lds_dwordx4 %0, %1 offset:2048 sc1\n\tglobal_load_lds_dwordx4 %0, %1 offset:3072 sc1"
                 ::"v"(v), "s"(sbase), "s"(lds) : "memory"); }
DI void lds_wait_tags(unsigned a, int n, unsigned tag) {
    for (unsigned sp = 0; sp < SPIN_CAP; ++sp) { asm volatile("" ::: "memory"); const unsigned t0 = *(const volatile LAS unsigned*)(size_t)a, t1 = *(const volatile LAS unsigned*)(size_t)(a + 4 * (n - 1));
        if (__builtin_amdgcn_readfirstlane(t0) == tag && __builtin_amdgcn_readfirstlane(t1) == tag) break; __builtin_amdgcn_s_sleep(1); }
    asm volatile("" ::: "memory"); }
DI void warm4(unsigned voff, const void* sbase) { asm volatile("s_mov_b32 m0, %1\n\ts_nop 0\n\tglobal_load_lds_dword %0, %2" ::"v"(voff), "s"(L_DUMMY), "s"(sbase) : "memory"); }
#define WSP 4
#define WS1(i) do { if (WSP == 1) WSTAMP(i); } while (0)
#define WS2(i) do { if (WSP == 2) WSTAMP(i); } while (0)
#define WS3(i) do { if (WSP == 3) WSTAMP(i); } while (0)
#define WS4(i) do { if (WSP == 4) WSTAMP(i); } while (0)
#define WS5(i) do { if (WSP == 5) WSTAMP(i); } while (0)
#define WS6(i) do { if (WSP == 6) WSTAMP(i); } while (0)
#define FRESH(v) int v = threadIdx.x & 63; asm volatile("" : "+v"(v))
#define SBAR() __builtin_amdgcn_sched_barrier(0)
#define PIN(x) asm volatile("" : "+v"(x))
#define BAR() asm volatile("s_waitcnt lgkmcnt(0)\n\ts_barrier" ::: "memory")
#define VMWAIT(n) asm volatile("s_waitcnt vmcnt(" #n ")" ::: "memory")
DI unsigned ldg_sc1(const char* ws, unsigned off) { return __hip_atomic_load((GAS unsigned*)(ws + off), __ATOMIC_RELAXED, __HIP_MEMORY_SCOPE_AGENT); }
DI unsigned long long ldg64_sc1(const char* ws, unsigned off) { return __hip_atomic_load((GAS unsigned long long*)(ws + off), __ATOMIC_RELAXED, __HIP_MEMORY_SCOPE_AGENT); }
DI void stg_sc1(char* ws, unsigned off, unsigned v) { __hip_atomic_store((GAS unsigned*)(ws + off), v, __ATOMIC_RELAXED, __HIP_MEMORY_SCOPE_AGENT); }
DI void stg64_sc1(char* ws, unsigned off, unsigned long long v) { __hip_atomic_store((GAS unsigned long long*)(ws + off), v, __ATOMIC_RELAXED, __HIP_MEMORY_SCOPE_AGENT); }
template <class T> DI T lds_ld(unsigned a) { return *(const LAS T*)(size_t)a; }
template <class T> DI void lds_st(unsigned a, T v) { *(LAS T*)(size_t)a = v; }
DI s16x4 lds_tr(unsigned a) { return __builtin_amdgcn_ds_read_tr16_b64_v4i16((LAS s16x4*)(size_t)a); }
DI bf16x8 trfrag(unsigned a0, unsigned a1) { s16x4 lo = lds_tr(a0), hi = lds_tr(a1); return __builtin_shufflevector(lo, hi, 0, 1, 2, 3, 4, 5, 6, 7); }
#define MFMA32(a, b, c) __builtin_amdgcn_mfma_f32_32x32x16_bf16((a), (b), (c), 0, 0, 0)
#define MFMA16(a, b, c) __builtin_amdgcn_mfma_f32_16x16x32_bf16((a), (b), (c), 0, 0, 0)
template <int N> DI float bcast(float v) { return __builtin_bit_cast(float, __builtin_amdgcn_mov_dpp(__builtin_bit_cast(int, v), 0x150 + N, 0xf, 0xf, true)); }
template <int N> DI float modu(float t, float gv, float sv) {
#pragma clang fp contract(off)
    const float p = bcast<N>(gv) * t; return bcast<N>(sv) + p; }
template <int N> DI unsigned hdw(unsigned x, float r, float n, float gv, float c, float d) {
    unsigned o; float a, b2;
    asm("v_lshlrev_b32 %1, 16, %5\n\tv_and_b32 %2, 0xffff0000, %5\n\tv_fma_f32 %1, %1, %6, %7\n\tv_fma_f32 %2, %2, %6, %7\n\t"
        "v_fmac_f32_dpp %3, %8, %1 row_newbcast:%c9 row_mask:0xf bank_mask:0xf\n\tv_fmac_f32_dpp %4, %8, %2 row_newbcast:%c10 row_mask:0xf bank_mask:0xf\n\t"
        "v_cvt_pk_bf16_f32 %0, %3, %4"
        : "=v"(o), "=&v"(a), "=&v"(b2), "+v"(c), "+v"(d) : "v"(x), "v"(r), "v"(n), "v"(gv), "i"(N), "i"(N + 1));
    return o; }
template <int C> DI float dppf(float v) { return __builtin_bit_cast(float, __builtin_amdgcn_update_dpp(0, __builtin_bit_cast(int, v), C, 0xf, 0xf, true)); }
DI float rowsum16(float v) { v += dppf<0xB1>(v); v += dppf<0x4E>(v); v += dppf<0x141>(v); v += dppf<0x140>(v); return v; }
DI float xsum32(float v) { auto r = __builtin_amdgcn_permlane32_swap(__float_as_uint(v), __float_as_uint(v), false, false); return __uint_as_float(r[0]) + __uint_as_float(r[1]); }
DI float xmax32(float v) { auto r = __builtin_amdgcn_permlane32_swap(__float_as_uint(v), __float_as_uint(v), false, false); return fmaxf(__uint_as_float(r[0]), __uint_as_float(r[1])); }
DI float xsum16(float v) { auto r = __builtin_amdgcn_permlane16_swap(__float_as_uint(v), __float_as_uint(v), false, false); return __uint_as_float(r[0]) + __uint_as_float(r[1]); }
DI unsigned long long mt() { unsigned long long t; asm volatile("s_memtime %0\n\ts_waitcnt lgkmcnt(0)" : "=s"(t)::"memory"); return t; }
DI unsigned long long rt() { unsigned long long t; asm volatile("s_memrealtime %0\n\ts_waitcnt lgkmcnt(0)" : "=s"(t)::"memory"); return t; }
#define STAMP(i) do {} while (0)
#define STAMP_OUT() do {} while (0)
#define STAMPC(i) do {} while (0)
#define WSTAMP(i) do {} while (0)

DI void poll_flags(const char* ws, unsigned off, int n, unsigned epoch, int lane) {
    const unsigned o = off + (unsigned)(lane < n ? lane : 0) * 256u;
    for (unsigned sp = 0; sp < SPIN_CAP; ++sp) { unsigned f = ldg_sc1(ws, o); if (__all(f == epoch)) break; }
}

DI void mod_part(const u16* __restrict__ cvec, const u16* __restrict__ wmod, char* ws, unsigned epoch, int lane, int slice, int kp) {
    const unsigned row0 = 64 * kp + 4 * (lane >> 2), o = row0 * 6144u + (unsigned)slice * 64u + (lane & 3) * 16u;
    const u32x2 cv = *(const u32x2*)(cvec + row0);
    u32x4 wm[4];
#pragma unroll
    for (int i = 0; i < 4; ++i) wm[i] = *(const u32x4*)((const char*)wmod + o + i * 6144u);
    SBAR();
    unsigned sp[2];
#pragma unroll
    for (int r = 0; r < 2; ++r) { const unsigned cu = cv[r]; const float c0 = bf_lo(cu), c1 = bf_hi(cu);
        sp[r] = pk2(c0 * __builtin_amdgcn_rcpf(1.f + __builtin_amdgcn_exp2f(-1.44269504089f * c0)), c1 * __builtin_amdgcn_rcpf(1.f + __builtin_amdgcn_exp2f(-1.44269504089f * c1))); }
    float a[8] = {0.f, 0.f, 0.f, 0.f, 0.f, 0.f, 0.f, 0.f};
#pragma unroll
    for (int r = 0; r < 2; ++r)
#pragma unroll
        for (int i = 0; i < 4; ++i) { const unsigned u0 = wm[2 * r][i], u1 = wm[2 * r + 1][i], s_ = sp[r];
            const unsigned lo = __builtin_amdgcn_perm(u1, u0, 0x05040100u), hi = __builtin_amdgcn_perm(u1, u0, 0x07060302u);
            a[2 * i] = __builtin_amdgcn_fdot2_f32_bf16(__builtin_bit_cast(bf2, lo), __builtin_bit_cast(bf2, s_), a[2 * i], false);
            a[2 * i + 1] = __builtin_amdgcn_fdot2_f32_bf16(__builtin_bit_cast(bf2, hi), __builtin_bit_cast(bf2, s_), a[2 * i + 1], false); }
    float b4[4], b2[2];
#pragma unroll
    for (int i = 0; i < 4; ++i) { auto r = __builtin_amdgcn_permlane32_swap(__float_as_uint(a[i]), __float_as_uint(a[i + 4]), false, false); b4[i] = __uint_as_float(r[0]) + __uint_as_float(r[1]); }
#pragma unroll
    for (int i = 0; i < 2; ++i) { auto r = __builtin_amdgcn_permlane16_swap(__float_as_uint(b4[i]), __float_as_uint(b4[i + 2]), false, false); float v = __uint_as_float(r[0]) + __uint_as_float(r[1]);
        v += dppf<0x124>(v); v += dppf<0x128>(v); b2[i] = v; }
    const __amdgpu_buffer_rsrc_t rws = __builtin_amdgcn_make_buffer_rsrc(ws, 0, WS_BYTES, 0x00020000);
    if ((lane & 12) == 0) __builtin_amdgcn_raw_buffer_store_b128((u32x4){__float_as_uint(b2[0]), epoch, __float_as_uint(b2[1]), epoch}, rws,
                                                                 WS_PART + (slice * 8 + kp) * 256 + ((lane & 3) * 4 + (lane >> 5) * 2 + ((lane >> 4) & 1)) * 16, 0, 16);
}
struct PartSum { u32x4 v0, v1; };
DI void mod_sum_load(PartSum& p, char* ws, int lane, int slice) {
    const __amdgpu_buffer_rsrc_t rws = __builtin_amdgcn_make_buffer_rsrc(ws, 0, WS_BYTES, 0x00020000);
    const unsigned o = WS_PART + (slice * 8 + (lane >> 3)) * 256 + (lane & 7) * 16;
    p.v0 = __builtin_amdgcn_raw_buffer_load_b128(rws, o, 0, 16); p.v1 = __builtin_amdgcn_raw_buffer_load_b128(rws, o + 128, 0, 16); }
DI void mod_sum_finish(PartSum& p, char* ws, unsigned epoch, int lane, int slice) {
    for (unsigned sp = 0; sp < SPIN_CAP; ++sp) { if (__all(p.v0[1] == epoch && p.v0[3] == epoch && p.v1[1] == epoch && p.v1[3] == epoch)) break; asm volatile("" ::: "memory"); mod_sum_load(p, ws, lane, slice); }
    float s[4] = {__uint_as_float(p.v0[0]), __uint_as_float(p.v0[2]), __uint_as_float(p.v1[0]), __uint_as_float(p.v1[2])};
#pragma unroll
    for (int i = 0; i < 4; ++i) { float v = s[i]; v += dppf<0x128>(v); s[i] = xsum32(xsum16(v)); }
    const unsigned long long g0 = ((unsigned long long)epoch << 32) | pk2(s[0], s[1]), g1 = ((unsigned long long)epoch << 32) | pk2(s[2], s[3]);
    const unsigned base = slice < 32 ? WS_MODU + slice * 128 : WS_MODN + (slice - 32) * 128, rs = slice < 32 ? 4096 : 8192, rep = lane >> 3, o = base + (lane & 7) * 8;
    stg64_sc1(ws, o + rep * rs, g0); stg64_sc1(ws, o + 64 + rep * rs, g1); stg64_sc1(ws, o + (rep + 8) * rs, g0); stg64_sc1(ws, o + 64 + (rep + 8) * rs, g1);
}

__global__ __launch_bounds__(576, 3) void fused_block(const u16* __restrict__ x, const u16* __restrict__ wqkv, const u16* __restrict__ wo, const u16* __restrict__ w1, const u16* __restrict__ b1,
                                                      const u16* __restrict__ w2, const u16* __restrict__ b2, const u16* __restrict__ cvec, const u16* __restrict__ wmod, u16* __restrict__ out, char* __restrict__ ws) {
    const unsigned epoch = *(const unsigned*)(ws + WS_E) + 1u;
    const int tid = threadIdx.x, lane = tid & 63, w = __builtin_amdgcn_readfirstlane(tid >> 6);
    const int b = blockIdx.x, xl = b & 7, idx = b >> 3, g = 2 * xl + (idx >> 4), j = idx & 15;
    const int ct = j & 7, rh = j >> 3;
    unsigned long long code_pc; unsigned code_len;
    asm volatile("s_getpc_b64 %0\nfb_code_begin:\n\ts_mov_b32 %1, fb_code_end-fb_code_begin" : "=s"(code_pc), "=s"(code_len));
    STAMP(0); STAMPC(20);
    if (w == 8) {
        {
            const unsigned o = ((unsigned)idx * 32u + (lane & 31)) * 64u;
            warm4((int)o < (int)code_len ? o : 0u, (const void*)code_pc);
        }
        if (lane == 0) stg_sc1(ws, WS_HELLO + b * 4, epoch);
        __builtin_amdgcn_s_setprio(3);
        const unsigned sw0 = (lane >> 3) * 1024u + ct * 128u + (((lane & 7) >> 1) ^ ((lane >> 4) & 3)) * 32u + (lane & 1) * 16u, sw1 = sw0 + 7168, sw2 = sw0 + 14336, sw3 = sw0 + 21504;
        unsigned w1o[7], wqo[3];
#pragma unroll
        for (int r = 0; r < 7; ++r) { const unsigned u = r * 64 + lane, row = u / 14, c = u % 14; w1o[r] = row * 3584u + 224u * j + c * 16u - (r & 3) * 1024u; }
#pragma unroll
        for (int r = 0; r < 3; ++r) { const unsigned u = r * 64 + lane, row = u / 12, c = u % 12; wqo[r] = row * 3072u + (c >> 2) * 1024u + 64u * j + (c & 3) * 16u - r * 1024u; }
#define RUN128(base, first, n4, lds0) { const char* p_ = (const char*)(base) + (size_t)(first) * 8192; unsigned l_ = (lds0); _Pragma("unroll 2") for (int i_ = 0; i_ < (n4); ++i_) { dma4(sw0, sw1, sw2, sw3, p_, l_); p_ += 32768; l_ += 4096; } }
#define W1GRPS(k0, k1) { const char* p_ = (const char*)w1 + (size_t)(k0) * 114688; unsigned l_ = L_W1 + 7 * (k0) * 1024; _Pragma("unroll 1") for (int k_ = (k0); k_ < (k1); ++k_) { \
            dma4(w1o[0], w1o[1], w1o[2], w1o[3], p_, l_); dma3(w1o[4], w1o[5], w1o[6], p_, l_ + 4096); p_ += 114688; l_ += 7168; } }
#define W2CHUNKS(c0, c1, slot0) { _Pragma("unroll 1") for (int c_ = (c0); c_ < (c1); ++c_) { const char* p_ = (const char*)w2 + (size_t)c_ * 32768; const unsigned l_ = ((slot0) + c_ - (c0)) * 16384; \
            dma4(sw0, sw1, sw2, sw3, p_, l_); dma4(sw0, sw1, sw2, sw3, p_ + 56 * 8192, l_ + 4096); dma4(sw0, sw1, sw2, sw3, p_ + 112 * 8192, l_ + 8192); dma4(sw0, sw1, sw2, sw3, p_ + 168 * 8192, l_ + 12288); } }
#define WQGRPS(k0, k1) { const char* p_ = (const char*)wqkv + (size_t)(k0) * 49152; unsigned l_ = L_WQKV + 3 * (k0) * 1024; _Pragma("unroll 2") for (int k_ = (k0); k_ < (k1); ++k_) { dma3(wqo[0], wqo[1], wqo[2], p_, l_); p_ += 49152; l_ += 3072; } }
        __builtin_amdgcn_s_barrier();
        __builtin_amdgcn_s_barrier();
        WQGRPS(14, 24)
        __builtin_amdgcn_s_barrier();
        WQGRPS(0, 14)
        VMWAIT(0);
        __builtin_amdgcn_s_barrier();
        mod_part(cvec, wmod, ws, epoch, lane, 32 + 8 * xl + (idx & 3), idx >> 2); mod_part(cvec, wmod, ws, epoch, lane, 36 + 8 * xl + (idx & 3), idx >> 2);
        if (idx < 4) { PartSum p; mod_sum_load(p, ws, lane, 32 + 8 * xl + idx); mod_sum_finish(p, ws, epoch, lane, 32 + 8 * xl + idx); mod_sum_load(p, ws, lane, 36 + 8 * xl + idx); mod_sum_finish(p, ws, epoch, lane, 36 + 8 * xl + idx); }
        __builtin_amdgcn_s_barrier();
        __builtin_amdgcn_s_barrier();
        if (g >= 1) {
            const unsigned fo = WS_FLAG + (((g >= 2 ? g - 2 + (lane & 1) : 0) * 16 + j) * 256);
            for (unsigned sp = 0; sp < SPIN_CAP; ++sp) { unsigned f = ldg_sc1(ws, fo); if (__all(f == epoch)) break; }
            const unsigned lo16 = lane * 16;
#pragma unroll 1
            for (int src = (g >= 2 ? 0 : 1); src < 2; ++src) {
                const char* p_ = ws + WS_KV + (size_t)((g - 2 + src) * 16 + j) * 16384;
                dma4lin_c(lo16, p_, L_KIMG + src * 8192); dma4lin_c(lo16, p_ + 4096, L_KIMG + src * 8192 + 4096);
                dma4lin_c(lo16, p_ + 8192, L_VIMG + src * 8192); dma4lin_c(lo16, p_ + 12288, L_VIMG + src * 8192 + 4096);
            }
        }
        VMWAIT(0);
        __builtin_amdgcn_s_barrier();
        RUN128(wo, 0, 16, L_WO)
        W1GRPS(3, 7)
        W1GRPS(15, 16)
        __builtin_amdgcn_s_barrier();
        W1GRPS(7, 15)
        VMWAIT(56);
        __builtin_amdgcn_s_barrier();
        __builtin_amdgcn_s_barrier();
        __builtin_amdgcn_s_barrier();
        __builtin_amdgcn_s_barrier();
        W1GRPS(0, 3)
        __builtin_amdgcn_s_barrier();
        VMWAIT(0);
        __builtin_amdgcn_s_barrier();
        __builtin_amdgcn_s_barrier();
        W2CHUNKS(0, 2, 0)
        __builtin_amdgcn_s_barrier();
        __builtin_amdgcn_s_barrier();
        W2CHUNKS(2, 6, 2)
        __builtin_amdgcn_s_barrier();
        W2CHUNKS(6, 9, 6)
        VMWAIT(48);
        __builtin_amdgcn_s_barrier();
        VMWAIT(0);
        __builtin_amdgcn_s_barrier();
        __builtin_amdgcn_s_barrier();
        __builtin_amdgcn_s_barrier();
        __builtin_amdgcn_s_barrier();
        return;
    }
    const __amdgpu_buffer_rsrc_t rws = __builtin_amdgcn_make_buffer_rsrc(ws, 0, WS_BYTES, 0x00020000);
    const int m = w & 3, kh = w >> 2;
#define XTILE(t) { const unsigned xo = (unsigned)(128 * g + 32 * (t)) * 1024u + ((unsigned)lane << 4); \
        _Pragma("unroll 2") for (int i = 0; i < 32; i += 4) dma4(xo ^ (unsigned)((i & 15) << 4), xo ^ (unsigned)(((i & 15) + 1) << 4), xo ^ (unsigned)(((i & 15) + 2) << 4), xo ^ (unsigned)(((i & 15) + 3) << 4), \
                                                                 (const char*)x + i * 1024, L_X + (3 - (t)) * 32768 + i * 1024); }
    PartSum psum;
    if (w >= 4 && w < 7) XTILE(w - 4)
    if (w == 0) lds_st<u32x4>(L_ONES, (u32x4){0x3F803F80u, 0x3F803F80u, 0x3F803F80u, 0x3F803F80u});
    if (w == 3) { __builtin_amdgcn_s_setprio(2); mod_part(cvec, wmod, ws, epoch, lane, 4 * xl + (idx & 3), idx >> 2); __builtin_amdgcn_s_setprio(0); }
    STAMP(1); WS6(0);
    bf16x8 qf; u32x4 kpk, vpk; float qown[8];
    {
    const int h = lane >> 5, gi = lane >> 4, q4 = (lane & 15) >> 2, p4 = lane & 3;
    u32x4 xr[16];
#define XFRAGS() { const unsigned tok = lane & 31, rb = L_X + (3 - m) * 32768 + tok * 1024 + kh * 512; \
        _Pragma("unroll") for (int s = 0; s < 16; ++s) xr[s] = lds_ld<u32x4>(rb + (s >> 3) * 256 + ((((2 * (s & 7) + h) ^ (tok & 15))) << 4)); \
        _Pragma("unroll") for (int s = 0; s < 16; ++s) PIN(xr[s]); }
    if (w == 4) VMWAIT(0);
    BAR();
    WS6(1);
    if (w == 4) XTILE(3)
    if (w == 7) {
        unsigned wq[3];
#pragma unroll
        for (int r = 0; r < 3; ++r) { const unsigned u = r * 64 + lane, row = u / 12, c = u % 12; wq[r] = row * 3072u + (c >> 2) * 1024u + 64u * j + (c & 3) * 16u - r * 1024u; }
#pragma unroll 2
        for (int k = 24; k < 32; ++k) dma3(wq[0], wq[1], wq[2], (const char*)wqkv + (size_t)k * 49152, L_WQKV + 3 * k * 1024); }
    if (w == 3 && idx < 4) mod_sum_load(psum, ws, lane, 4 * xl + idx);
    if (m == 0) XFRAGS()
    if (w == 5 || w == 6) VMWAIT(0);
    BAR();
    WS6(2);
    if (w == 3 && idx < 4) mod_sum_finish(psum, ws, epoch, lane, 4 * xl + idx);
    if (m == 1 || m == 2) XFRAGS()
    if (w == 4) VMWAIT(0);
    BAR();
    if (m == 3) XFRAGS()
#undef XFRAGS
#undef XTILE
    STAMP(2);
    const unsigned gmo = WS_MODU + g * 4096 + (tid & 255) * 16; u32x4 gmv;
    if (w < 4) gmv = __builtin_amdgcn_raw_buffer_load_b128(rws, gmo, 0, 16);
    SBAR();
    {
        float s0 = 0.f, s1 = 0.f, s2 = 0.f, s3 = 0.f; const bf2 one = {(__bf16)1.0f, (__bf16)1.0f};
#pragma unroll
        for (int s = 0; s < 16; ++s)
#pragma unroll
            for (int i = 0; i < 4; i += 2) { const unsigned ua = xr[s][i], ub = xr[s][i + 1]; const bf2 va = __builtin_bit_cast(bf2, ua), vb = __builtin_bit_cast(bf2, ub);
                s0 = __builtin_amdgcn_fdot2_f32_bf16(va, one, s0, false); s1 = __builtin_amdgcn_fdot2_f32_bf16(va, va, s1, false);
                s2 = __builtin_amdgcn_fdot2_f32_bf16(vb, one, s2, false); s3 = __builtin_amdgcn_fdot2_f32_bf16(vb, vb, s3, false); }
        s0 = xsum32(s0 + s2); s1 = xsum32(s1 + s3);
        if (lane < 32) lds_st<u32x2>(L_STAT + (kh * 128 + 32 * m + lane) * 8, (u32x2){__float_as_uint(s0), __float_as_uint(s1)});
    }
    WS6(3);
    if (w < 4) {
        for (unsigned sp = 0; sp < SPIN_CAP; ++sp) { if (__all(gmv[1] == epoch && gmv[3] == epoch)) break; asm volatile("" ::: "memory"); gmv = __builtin_amdgcn_raw_buffer_load_b128(rws, gmo, 0, 16); }
        const unsigned u0 = gmv[0], u2 = gmv[2]; f32x4 f = {bf_lo(u0), bf_hi(u0), bf_lo(u2), bf_hi(u2)};
        if (tid >= 128) { f[0] = rbf(1.f + f[0]); f[1] = rbf(1.f + f[1]); f[2] = rbf(1.f + f[2]); f[3] = rbf(1.f + f[3]); }
        lds_st<f32x4>(L_SAG + tid * 16, f);
    }
    STAMP(3);
    VMWAIT(0);
    BAR();
    STAMP(4); STAMPC(22); WS1(0);
    float rstd, nmr, gv[2]; f32x4 sa0[2], sa1[2];
    const unsigned sga = L_SAG + (256 * kh + 16 * ((lane & 15) >> 3) + 8 * h + (lane & 7)) * 4;
    const unsigned saa = L_SAG + (256 * kh + 8 * h) * 4;
    { const u32x2 a = lds_ld<u32x2>(L_STAT + (32 * m + (lane & 31)) * 8), c2 = lds_ld<u32x2>(L_STAT + (128 + 32 * m + (lane & 31)) * 8);
      gv[0] = lds_ld<float>(sga + 2048);
      sa0[0] = lds_ld<f32x4>(saa); sa1[0] = lds_ld<f32x4>(saa + 16);
      const float s0 = __uint_as_float(a[0]) + __uint_as_float(c2[0]), s1 = __uint_as_float(a[1]) + __uint_as_float(c2[1]);
      const float mean = s0 * (1.f / 512.f), var = fmaxf(s1 * (1.f / 512.f) - mean * mean, 0.f);
      rstd = __builtin_amdgcn_rsqf(var + 1e-5f); nmr = -mean * rstd;
      asm volatile("s_nop 1" : "+v"(gv[0])); }
    f32x16 aQ, aK, aV;
#pragma unroll
    for (int i = 0; i < 16; ++i) { aQ[i] = 0.f; aK[i] = 0.f; aV[i] = 0.f; }
    {
        const unsigned rowa = L_WQKV + (256 * kh + 8 * (gi >> 1) + q4) * 192 + p4 * 8;
        const unsigned wqa = rowa + (((gi & 1) ^ kh) * 32), wva = rowa + 128 + (gi & 1) * 32;
        bf16x8 fk, fv;
#define HDW(s, i) hdw<8 * ((s) & 1) + 2 * (i)>(xr[s][i], rstd, nmr, gv[((s) >> 1) & 1], (i) < 2 ? sa0[(s) & 1][2 * (i) & 3] : sa1[(s) & 1][2 * (i) & 3], (i) < 2 ? sa0[(s) & 1][(2 * (i) + 1) & 3] : sa1[(s) & 1][(2 * (i) + 1) & 3])
        fk = trfrag(wqa + 64, wqa + 64 + 768); fv = trfrag(wva, wva + 768);
        sa0[1] = lds_ld<f32x4>(saa + 64); sa1[1] = lds_ld<f32x4>(saa + 64 + 16);
        { const unsigned a0 = HDW(0, 0), a1 = HDW(0, 1), a2 = HDW(0, 2), a3 = HDW(0, 3); xr[0] = (u32x4){a0, a1, a2, a3}; }
        asm volatile("s_nop 1" : "+v"(xr[0])); SBAR();
#define QKV_STEP(s, sn) { const bf16x8 hb = __builtin_bit_cast(bf16x8, xr[s]); unsigned n0, n1, n2, n3; \
            aK = MFMA32(fk, hb, aK); PIN(aK); SBAR(); if ((sn) < 16) { fk = trfrag(wqa + (sn) * 3072 + 64, wqa + (sn) * 3072 + 64 + 768); if ((s) + 2 < 16) sa0[(s) & 1] = lds_ld<f32x4>(saa + ((s) + 2) * 64); \
                n0 = HDW((sn) & 15, 0); n1 = HDW((sn) & 15, 1); PIN(n0); PIN(n1); } SBAR(); \
            aV = MFMA32(hb, fv, aV); PIN(aV); SBAR(); if ((sn) < 16) { fv = trfrag(wva + (sn) * 3072, wva + (sn) * 3072 + 768); if ((s) + 2 < 16) sa1[(s) & 1] = lds_ld<f32x4>(saa + ((s) + 2) * 64 + 16); \
                n2 = HDW((sn) & 15, 2); n3 = HDW((sn) & 15, 3); PIN(n2); PIN(n3); xr[(sn) & 15] = (u32x4){n0, n1, n2, n3}; } \
            if ((s) % 2 == 0 && (s) + 2 < 16) { gv[(((s) >> 1) + 1) & 1] = lds_ld<float>(sga + 2048 + (((s) >> 1) + 1) * 128); } SBAR(); }
        WS1(1);
        QKV_STEP(0, 1) QKV_STEP(1, 2) QKV_STEP(2, 3) QKV_STEP(3, 4) QKV_STEP(4, 5) QKV_STEP(5, 6) QKV_STEP(6, 7) QKV_STEP(7, 8)
        QKV_STEP(8, 9) QKV_STEP(9, 10) QKV_STEP(10, 11) QKV_STEP(11, 12) QKV_STEP(12, 13) QKV_STEP(13, 14) QKV_STEP(14, 15) QKV_STEP(15, 16)
#undef QKV_STEP
#undef HDW
        STAMP(5); STAMPC(23); WS1(2);
        float vs[8], vk[8];
#pragma unroll
        for (int i = 0; i < 8; ++i) { float lo = aV[i], hi = aV[8 + i]; PIN(lo); PIN(hi); vs[i] = kh ? lo : hi; vk[i] = kh ? hi : lo; }
        const unsigned ex = w * 6144 + lane * 16;
        lds_st<f32x4>(ex, (f32x4){aK[8], aK[9], aK[10], aK[11]}); lds_st<f32x4>(ex + 1024, (f32x4){aK[12], aK[13], aK[14], aK[15]});
        lds_st<f32x4>(ex + 2048, (f32x4){vs[0], vs[1], vs[2], vs[3]}); lds_st<f32x4>(ex + 3072, (f32x4){vs[4], vs[5], vs[6], vs[7]});
        bf16x8 fq = trfrag(wqa, wqa + 768), fq1 = trfrag(wqa + 3072, wqa + 3072 + 768);
        BAR();
        WS1(3);
        const unsigned ep = (w ^ 4) * 6144 + lane * 16;
        { const f32x4 k0 = lds_ld<f32x4>(ep), k1 = lds_ld<f32x4>(ep + 1024), v0 = lds_ld<f32x4>(ep + 2048), v1 = lds_ld<f32x4>(ep + 3072);
          u32x4 kp, vp;
          kp[0] = pk2(aK[0] + k0[0], aK[1] + k0[1]); kp[1] = pk2(aK[2] + k0[2], aK[3] + k0[3]); kp[2] = pk2(aK[4] + k1[0], aK[5] + k1[1]); kp[3] = pk2(aK[6] + k1[2], aK[7] + k1[3]);
          vp[0] = pk2(vk[0] + v0[0], vk[1] + v0[1]); vp[1] = pk2(vk[2] + v0[2], vk[3] + v0[3]); vp[2] = pk2(vk[4] + v1[0], vk[5] + v1[1]); vp[3] = pk2(vk[6] + v1[2], vk[7] + v1[3]);
          const unsigned kvo = WS_KV + (g * 16 + j) * 16384 + (m * 2 + kh) * 1024 + lane * 16;
          __builtin_amdgcn_raw_buffer_store_b128(kp, rws, kvo, 0, 16);
          __builtin_amdgcn_raw_buffer_store_b128(vp, rws, kvo + 8192, 0, 16);
          PIN(kp); PIN(vp); kpk = kp; vpk = vp; }
        SBAR();
#pragma unroll
        for (int s = 0; s < 16; s += 2) {
            aQ = MFMA32(fq, __builtin_bit_cast(bf16x8, xr[s]), aQ); PIN(aQ); SBAR(); if (s + 2 < 16) fq = trfrag(wqa + (s + 2) * 3072, wqa + (s + 2) * 3072 + 768); SBAR();
            aQ = MFMA32(fq1, __builtin_bit_cast(bf16x8, xr[s + 1]), aQ); PIN(aQ); SBAR(); if (s + 3 < 16) fq1 = trfrag(wqa + (s + 3) * 3072, wqa + (s + 3) * 3072 + 768); SBAR(); }
        lds_st<f32x4>(ex + 4096, (f32x4){aQ[8], aQ[9], aQ[10], aQ[11]}); lds_st<f32x4>(ex + 5120, (f32x4){aQ[12], aQ[13], aQ[14], aQ[15]});
#pragma unroll
        for (int i = 0; i < 8; ++i) qown[i] = aQ[i];
    }
    }
    WS1(4);
    VMWAIT(0); WS1(5);
    BAR();
    if (tid == 0) stg_sc1(ws, WS_FLAG + (0 * 256 + g * 16 + j) * 256, epoch);
    STAMP(6);
    { FRESH(lane);
      const unsigned ep = (w ^ 4) * 6144 + 4096 + lane * 16; const f32x4 q0 = lds_ld<f32x4>(ep), q1 = lds_ld<f32x4>(ep + 1024);
      lds_st<u32x4>(L_KIMG + (8 + m) * 2048 + kh * 1024 + lane * 16, kpk);
      lds_st<u32x4>(L_VIMG + (8 + m) * 2048 + kh * 1024 + lane * 16, vpk);
      const float qs = 0.25f * 1.44269504089f; u32x4 qp;
      qp[0] = pk2((qown[0] + q0[0]) * qs, (qown[1] + q0[1]) * qs); qp[1] = pk2((qown[2] + q0[2]) * qs, (qown[3] + q0[3]) * qs);
      qp[2] = pk2((qown[4] + q1[0]) * qs, (qown[5] + q1[1]) * qs); qp[3] = pk2((qown[6] + q1[2]) * qs, (qown[7] + q1[3]) * qs);
      qf = __builtin_bit_cast(bf16x8, qp); PIN(qf); }
    const int tt = w & 3;
    {
    FRESH(lane);
    const int hh = kh, qq = lane & 31, h = lane >> 5;
    const float NEG = -__builtin_inff(), fq0 = (float)(4 * h - qq) - 0.5f;
    f32x16 c0, c8;
#pragma unroll
    for (int r = 0; r < 16; ++r) {
        const float t = fmaf(fq0, 1e30f, (float)((r & 3) + 8 * (r >> 2)) * 1e30f); c0[r] = fminf(t, 0.f); c8[r] = fminf(-t, 0.f); }
    PIN(c0); PIN(c8);
    BAR();
    STAMP(8); WS2(0);
    {
        const unsigned ka = L_KIMG + m * 2048 + hh * 1024 + lane * 16;
        const bool isd = ((lane >> 4) & 1) == hh;
        const unsigned va = isd ? L_VIMG + m * 2048 + lane * 16 : L_ONES, vst = isd ? 2048u : 0u, vs1 = isd ? 1024u : 0u;
        f32x16 o;
#pragma unroll
        for (int r = 0; r < 16; ++r) o[r] = 0.f;
#define MAX16(mx, s) { _Pragma("unroll") for (int r = 0; r < 16; r += 2) mx = fmaxf(fmaxf(mx, s[r]), s[r + 1]); }
#define EXPH(P, S, b) { P[0] = pk2(__builtin_amdgcn_exp2f(S[b]), __builtin_amdgcn_exp2f(S[b + 1])); P[1] = pk2(__builtin_amdgcn_exp2f(S[b + 2]), __builtin_amdgcn_exp2f(S[b + 3])); \
                        P[2] = pk2(__builtin_amdgcn_exp2f(S[b + 4]), __builtin_amdgcn_exp2f(S[b + 5])); P[3] = pk2(__builtin_amdgcn_exp2f(S[b + 6]), __builtin_amdgcn_exp2f(S[b + 7])); }
        if (g >= 2) {
            f32x16 z;
#pragma unroll
            for (int r = 0; r < 16; ++r) z[r] = 0.f;
            PIN(z);
            float mx = NEG, mx2 = NEG; f32x16 sA, sB;
#define KF(i) lds_ld<bf16x8>(ka + (i) * 2048)
            bf16x8 k0 = KF(0), k1 = KF(8), k2 = KF(1), k3 = KF(2);
            SBAR();
            sA = MFMA32(k0, qf, c0); PIN(sA); k0 = KF(3); SBAR();
            sB = MFMA32(k1, qf, c8); PIN(sB); k1 = KF(4); SBAR(); MAX16(mx, sA); PIN(mx); SBAR();
            sA = MFMA32(k2, qf, z); PIN(sA); k2 = KF(5); SBAR(); MAX16(mx2, sB); PIN(mx2); SBAR();
            sB = MFMA32(k3, qf, z); PIN(sB); k3 = KF(6); SBAR(); MAX16(mx, sA); PIN(mx); SBAR();
            sA = MFMA32(k0, qf, z); PIN(sA); k0 = KF(7); SBAR(); MAX16(mx2, sB); PIN(mx2); SBAR();
            sB = MFMA32(k1, qf, z); PIN(sB); k1 = KF(0); SBAR(); MAX16(mx, sA); PIN(mx); SBAR();
            sA = MFMA32(k2, qf, z); PIN(sA); k2 = KF(1); SBAR(); MAX16(mx2, sB); PIN(mx2); SBAR();
            sB = MFMA32(k3, qf, z); PIN(sB); SBAR(); MAX16(mx, sA); PIN(mx); SBAR();
            sA = MFMA32(k0, qf, z); PIN(sA); SBAR(); MAX16(mx2, sB); PIN(mx2); SBAR();
            WS2(1);
            bf16x8 v0 = lds_ld<bf16x8>(va), v1 = lds_ld<bf16x8>(va + vs1);
            MAX16(mx, sA);
            mx = xmax32(fmaxf(mx, mx2));
#pragma unroll
            for (int r = 0; r < 16; ++r) { z[r] = -mx; c0[r] -= mx; c8[r] -= mx; }
            PIN(z); PIN(c0); PIN(c8); SBAR();
            WS2(2);
            sA = MFMA32(k1, qf, c0); SBAR();
            u32x4 pa, pb; bf16x8 v0n, v1n, kn = k2; unsigned vp = va;
#define ABLK(I, SC, SN, CN) { bf16x8 kc = kn; if ((I) < 8) { vp += vst; v0n = lds_ld<bf16x8>(vp); v1n = lds_ld<bf16x8>(vp + vs1); } if ((I) < 7) kn = KF((I) + 2); SBAR(); \
                if ((I) < 8) { SN = MFMA32(kc, qf, CN); PIN(SN); } SBAR(); EXPH(pa, SC, 0); PIN(pa); SBAR(); \
                o = MFMA32(v0, __builtin_bit_cast(bf16x8, pa), o); PIN(o); SBAR(); EXPH(pb, SC, 8); PIN(pb); SBAR(); \
                o = MFMA32(v1, __builtin_bit_cast(bf16x8, pb), o); PIN(o); SBAR(); if ((I) < 8) { v0 = v0n; v1 = v1n; } }
            ABLK(0, sA, sB, z) ABLK(1, sB, sA, z) ABLK(2, sA, sB, z) ABLK(3, sB, sA, z)
            ABLK(4, sA, sB, z) ABLK(5, sB, sA, z) ABLK(6, sA, sB, z) ABLK(7, sB, sA, c8) ABLK(8, sA, sB, c8)
#undef KF
#undef ABLK
        } else {
            const int i0 = 8 - 4 * g - m;
            f32x16 z;
#pragma unroll
            for (int r = 0; r < 16; ++r) z[r] = 0.f;
            float mx = NEG;
            f32x16 s = MFMA32(lds_ld<bf16x8>(ka + 8 * 2048), qf, c8); MAX16(mx, s);
#pragma unroll 1
            for (int i = i0; i < 8; ++i) { s = MFMA32(lds_ld<bf16x8>(ka + i * 2048), qf, z); MAX16(mx, s); }
            mx = xmax32(mx);
#pragma unroll
            for (int r = 0; r < 16; ++r) { z[r] = -mx; c8[r] -= mx; }
#pragma unroll 1
            for (int i = i0; i <= 8; ++i) { u32x4 pa, pb;
                if (i < 8) s = MFMA32(lds_ld<bf16x8>(ka + i * 2048), qf, z); else s = MFMA32(lds_ld<bf16x8>(ka + i * 2048), qf, c8);
                EXPH(pa, s, 0); EXPH(pb, s, 8);
                o = MFMA32(lds_ld<bf16x8>(va + i * vst), __builtin_bit_cast(bf16x8, pa), o); o = MFMA32(lds_ld<bf16x8>(va + i * vst + vs1), __builtin_bit_cast(bf16x8, pb), o); }
        }
#undef EXPH
#undef MAX16
        WS2(3);
        float ov[8], l;
        { float lo = o[0], hi = o[8]; PIN(lo); PIN(hi); l = hh ? lo : hi; }
#pragma unroll
        for (int r = 0; r < 8; ++r) { float lo = o[r], hi = o[8 + r]; PIN(lo); PIN(hi); ov[r] = hh ? hi : lo; }
        const float inv = __builtin_amdgcn_rcpf(l);
        const u32x4 op = {pk2(ov[0] * inv, ov[1] * inv), pk2(ov[2] * inv, ov[3] * inv), pk2(ov[4] * inv, ov[5] * inv), pk2(ov[6] * inv, ov[7] * inv)};
        __builtin_amdgcn_raw_buffer_store_b128(op, rws, WS_O + ((g * 8 + 2 * m + (qq >> 4)) * 16 + j) * 1024 + ((qq & 15) + 16 * (2 * hh + h)) * 16, 0, 16);
    }
    }
    WS2(4);
    VMWAIT(0); WS2(5);
    BAR();
    if (tid == 0) stg_sc1(ws, WS_FLAG + (1 * 256 + g * 16 + j) * 256, epoch);
    STAMP(9);
    if (w >= 4) {
        const int t = tid - 256; const unsigned o = WS_MODN + g * 8192 + t * 32; u32x4 v, v2;
        for (unsigned sp = 0; sp < SPIN_CAP; ++sp) { asm volatile("" ::: "memory"); v = __builtin_amdgcn_raw_buffer_load_b128(rws, o, 0, 16); v2 = __builtin_amdgcn_raw_buffer_load_b128(rws, o + 16, 0, 16);
            if (__all(v[1] == epoch && v[3] == epoch && v2[1] == epoch && v2[3] == epoch)) break; }
        u32x4 val = {v[0], v[2], v2[0], v2[2]};
        const int vec = t >> 6, c8 = (t & 63) * 8;
        if (vec == 1) lds_st<u32x4>(L_SB16 + c8 * 2, val);
        else if (vec == 2) {
#pragma unroll
            for (int i = 0; i < 4; ++i) { const unsigned u_ = val[i]; val[i] = pk2(1.f + bf_lo(u_), 1.f + bf_hi(u_)); }
            lds_st<u32x4>(L_GB16 + c8 * 2, val);
        } else if ((c8 >> 6) == ct) lds_st<u32x4>((vec == 0 ? L_GAO : L_GBO) + (c8 & 63) * 2, val);
    }
    if (w == 0) poll_flags(ws, WS_FLAG + (1 * 256 + g * 16) * 256, 16, epoch, lane);
    BAR();
    STAMP(10); WS3(0);
    u32x2 x1p0, x1p1;
    {
        FRESH(lane);
        const int gi = lane >> 4, q4 = (lane & 15) >> 2, p4 = lane & 3;
        const unsigned tokP = 128 * g + 64 * rh + 16 * tt + (lane & 15), colP = 64 * ct + 32 * kh + 4 * gi;
        u32x4 of[8];
#pragma unroll
        for (int s = 0; s < 8; ++s) of[s] = __builtin_amdgcn_raw_buffer_load_b128(rws, WS_O + ((g * 8 + 4 * rh + tt) * 16 + 8 * kh + s) * 1024 + lane * 16, 0, 16);
        const u32x2 xres0 = *(const u32x2*)(x + tokP * 512 + colP), xres1 = *(const u32x2*)(x + tokP * 512 + colP + 16);
        SBAR();
        f32x4 acc[4];
#pragma unroll
        for (int n = 0; n < 4; ++n) acc[n] = (f32x4){0.f, 0.f, 0.f, 0.f};
        const unsigned xv = 2 * (gi & 1) + (q4 >> 1), rowa = L_WO + (256 * kh + 16 * (gi >> 1) + 4 * (gi & 1) + q4) * 128 + p4 * 8;
        unsigned an[4]; bf16x8 wf[4];
#pragma unroll
        for (int n = 0; n < 4; ++n) { an[n] = rowa + ((n ^ xv) * 32); wf[n] = trfrag(an[n], an[n] + 1024); }
        const unsigned cl = (32 * kh + 4 * gi) * 2, cg = colP * 2;
        const u32x2 ga0 = lds_ld<u32x2>(L_GAO + cl), ga1 = lds_ld<u32x2>(L_GAO + cl + 32);
        const u32x2 gb0 = lds_ld<u32x2>(L_GB16 + cg), gb1 = lds_ld<u32x2>(L_GB16 + cg + 32), sb0 = lds_ld<u32x2>(L_SB16 + cg), sb1 = lds_ld<u32x2>(L_SB16 + cg + 32);
        SBAR();
#pragma unroll
        for (int s = 0; s < 8; ++s)
#pragma unroll
            for (int n = 0; n < 4; ++n) { acc[n] = MFMA16(wf[n], __builtin_bit_cast(bf16x8, of[s]), acc[n]); PIN(acc[n]); SBAR(); if (s < 7) wf[n] = trfrag(an[n] + (s + 1) * 4096, an[n] + (s + 1) * 4096 + 1024); SBAR(); }
        WS3(1);
        BAR();
        STAMP(11);
        const unsigned ex = w * 2048 + lane * 16;
        { f32x4 s0 = acc[0], s1 = acc[1], s2 = acc[2], s3 = acc[3]; PIN(s0); PIN(s1); PIN(s2); PIN(s3);
          lds_st<f32x4>(ex, kh ? s0 : s2); lds_st<f32x4>(ex + 1024, kh ? s1 : s3); acc[0] = kh ? s2 : s0; acc[1] = kh ? s3 : s1; }
        BAR();
        WS3(2);
        const unsigned ep = (w ^ 4) * 2048 + lane * 16;
        const f32x4 a0 = lds_ld<f32x4>(ep) + acc[0], a1 = lds_ld<f32x4>(ep + 1024) + acc[1];
        float xv1[8], gg[8], ss[8];
#pragma unroll
        for (int r = 0; r < 4; ++r) {
            const unsigned ua0 = ga0[r >> 1], ua1 = ga1[r >> 1], ux0 = xres0[r >> 1], ux1 = xres1[r >> 1], ug0 = gb0[r >> 1], ug1 = gb1[r >> 1], us0 = sb0[r >> 1], us1 = sb1[r >> 1];
            xv1[r] = rbf(fmaf((r & 1) ? bf_hi(ua0) : bf_lo(ua0), a0[r], (r & 1) ? bf_hi(ux0) : bf_lo(ux0))); xv1[4 + r] = rbf(fmaf((r & 1) ? bf_hi(ua1) : bf_lo(ua1), a1[r], (r & 1) ? bf_hi(ux1) : bf_lo(ux1)));
            gg[r] = (r & 1) ? bf_hi(ug0) : bf_lo(ug0); gg[4 + r] = (r & 1) ? bf_hi(ug1) : bf_lo(ug1);
            ss[r] = (r & 1) ? bf_hi(us0) : bf_lo(us0); ss[4 + r] = (r & 1) ? bf_hi(us1) : bf_lo(us1);
        }
        x1p0 = (u32x2){pk2(xv1[0], xv1[1]), pk2(xv1[2], xv1[3])}; x1p1 = (u32x2){pk2(xv1[4], xv1[5]), pk2(xv1[6], xv1[7])};
        float st[6] = {0.f, 0.f, 0.f, 0.f, 0.f, 0.f}, pg[8];
#pragma unroll
        for (int r = 0; r < 8; ++r) pg[r] = xv1[r] * gg[r];
        const u32x4 xg = {pk2(pg[0], pg[1]), pk2(pg[2], pg[3]), pk2(pg[4], pg[5]), pk2(pg[6], pg[7])};
        __builtin_amdgcn_raw_buffer_store_b128(xg, rws, WS_XG + ((g * 8 + 4 * rh + tt) * 16 + 2 * ct + kh) * 1024 + lane * 16, 0, 16);
#pragma unroll
        for (int r = 0; r < 8; ++r) { const float p = pg[r]; st[0] += xv1[r]; st[1] = fmaf(xv1[r], xv1[r], st[1]); st[2] += p; st[3] = fmaf(p, p, st[3]); st[4] = fmaf(p, gg[r], st[4]); st[5] = fmaf(p, ss[r], st[5]); }
#pragma unroll
        for (int i = 0; i < 6; ++i) st[i] = xsum32(xsum16(st[i]));
        if (kh == 1 && lane < 16) { lds_st<f32x4>(L_SCR + (tt * 16 + lane) * 32, (f32x4){st[0], st[1], st[2], st[3]}); lds_st<u32x2>(L_SCR + (tt * 16 + lane) * 32 + 16, (u32x2){__float_as_uint(st[4]), __float_as_uint(st[5])}); }
        WS3(3);
        BAR();
        if (kh == 0 && lane < 16) {
            const f32x4 o4 = lds_ld<f32x4>(L_SCR + (tt * 16 + lane) * 32); const u32x2 o2 = lds_ld<u32x2>(L_SCR + (tt * 16 + lane) * 32 + 16);
            const unsigned so = WS_ST + g * 24576, T = 4 * rh + tt;
            const u32x4 sa4 = {__float_as_uint(st[0] + o4[0]), __float_as_uint(st[1] + o4[1]), __float_as_uint(st[2] + o4[2]), __float_as_uint(st[3] + o4[3])};
            const u32x2 sb2 = {__float_as_uint(st[4] + __uint_as_float(o2[0])), __float_as_uint(st[5] + __uint_as_float(o2[1]))};
            __builtin_amdgcn_raw_buffer_store_b128(sa4, rws, so + ((T * 8 + ct) * 16 + lane) * 16, 0, 16);
            __builtin_amdgcn_raw_buffer_store_b64(sb2, rws, so + 16384 + ((T * 8 + ct) * 16 + lane) * 8, 0, 16);
        }
    }
    WS3(4);
    VMWAIT(0); WS3(5);
    BAR();
    if (tid == 0) stg_sc1(ws, WS_FLAG + (2 * 256 + g * 16 + j) * 256, epoch);
    STAMP(12);
    const int odd = j & 1;
    {
    FRESH(lane);
    const int gi = lane >> 4, q4 = (lane & 15) >> 2, p4 = lane & 3;
    const u32x2 b1v = *(const u32x2*)(b1 + 112 * j + (w > 0 ? w - 1 : 0) * 16 + 4 * gi);
    BAR();
    if (w > 0) {
        const int nt = w - 1; f32x4 acc = {0.f, 0.f, 0.f, 0.f};
        const unsigned ra = L_W1 + (4 * gi + q4) * 224 + p4 * 8 + nt * 32; const int tk = lane & 15;
        const unsigned va = (tk == 0 ? L_GB16 : L_SB16) + 8 * gi;
#pragma unroll
        for (int s = 0; s < 16; ++s) {
            const u32x2 lo = lds_ld<u32x2>(va + s * 64), hi = lds_ld<u32x2>(va + s * 64 + 32);
            u32x4 bv = {lo[0], lo[1], hi[0], hi[1]};
            if (tk == 2) bv = (u32x4){0x3F803F80u, 0x3F803F80u, 0x3F803F80u, 0x3F803F80u};
            if (tk > 2) bv = (u32x4){0u, 0u, 0u, 0u};
            acc = MFMA16(trfrag(ra + s * 7168, ra + s * 7168 + 3584), __builtin_bit_cast(bf16x8, bv), acc);
        }
        if (tk < 3) lds_st<f32x4>(L_UVW + (tk * 112 + nt * 16 + 4 * gi) * 4, acc);
        if (tk == 3) lds_st<f32x4>(L_UVW + (3 * 112 + nt * 16 + 4 * gi) * 4, (f32x4){bf_lo(b1v[0]), bf_hi(b1v[0]), bf_lo(b1v[1]), bf_hi(b1v[1])});
    } else {
        const u32x4 gq = lds_ld<u32x4>(L_GB16 + lane * 16), sq = lds_ld<u32x4>(L_SB16 + lane * 16);
        float c5[5] = {0.f, 0.f, 0.f, 0.f, 0.f};
#pragma unroll
        for (int i = 0; i < 4; ++i) { const unsigned ug = gq[i], us = sq[i]; const float g0 = bf_lo(ug), g1 = bf_hi(ug), s0 = bf_lo(us), s1 = bf_hi(us);
            c5[0] += g0 + g1; c5[1] += s0 + s1; c5[2] += g0 * g0 + g1 * g1; c5[3] += g0 * s0 + g1 * s1; c5[4] += s0 * s0 + s1 * s1; }
#pragma unroll
        for (int i = 0; i < 5; ++i) c5[i] = xsum32(xsum16(rowsum16(c5[i])));
        if (lane == 0) { lds_st<f32x4>(L_CONST, (f32x4){c5[0], c5[1], c5[2], c5[3]}); lds_st<float>(L_CONST + 16, c5[4]); }
        poll_flags(ws, WS_FLAG + (2 * 256 + g * 16) * 256, 16, epoch, lane);
    }
    }
    BAR();
    STAMP(13);
    {
        FRESH(lane);
        const int gi = lane >> 4, q4 = (lane & 15) >> 2, p4 = lane & 3;
        const int Tw = 2 * m + kh;
        u32x4 xf[2][8];
        const unsigned xo0 = WS_XG + (g * 8 + Tw) * 16384 + kh * 8192 + lane * 16, xo1 = WS_XG + (g * 8 + (Tw ^ 1)) * 16384 + kh * 8192 + lane * 16;
        constexpr int WIN = 5;
#pragma unroll
        for (int s = 0; s < WIN; ++s) { xf[0][s] = __builtin_amdgcn_raw_buffer_load_b128(rws, xo0 + s * 1024, 0, 16); xf[1][s] = __builtin_amdgcn_raw_buffer_load_b128(rws, xo1 + s * 1024, 0, 16); }
        const unsigned so = WS_ST + g * 24576 + Tw * 2048, sob = WS_ST + g * 24576 + 16384 + Tw * 1024;
        u32x4 sA0, sA1; u32x2 sB0, sB1;
        SBAR(); WS4(0);
        f32x4 acc[7], acb[7]; bf16x8 wf[7];
        const unsigned ra = L_W1 + (256 * kh + 4 * gi + q4) * 224 + p4 * 8, raA = ra + 32 * odd, raB = ra + (odd ? 0 : 192);
#define W1A(s, u) (((u) < 6 ? raA : raB) + (s) * 7168 + ((u) < 6 ? (u) * 32 : 0))
#pragma unroll
        for (int u = 0; u < 7; ++u) { acc[u] = (f32x4){0.f, 0.f, 0.f, 0.f}; acb[u] = acc[u]; wf[u] = trfrag(W1A(0, u), W1A(0, u) + 3584); }
        SBAR();
#pragma unroll
        for (int s = 0; s < 8; ++s) {
            if (s + WIN < 8) { xf[0][s + WIN] = __builtin_amdgcn_raw_buffer_load_b128(rws, xo0 + (s + WIN) * 1024, 0, 16); xf[1][s + WIN] = __builtin_amdgcn_raw_buffer_load_b128(rws, xo1 + (s + WIN) * 1024, 0, 16); }
            if (s == 4) { sA0 = __builtin_amdgcn_raw_buffer_load_b128(rws, so + ((2 * gi) * 16 + (lane & 15)) * 16, 0, 16); sA1 = __builtin_amdgcn_raw_buffer_load_b128(rws, so + ((2 * gi + 1) * 16 + (lane & 15)) * 16, 0, 16); }
            if (s == 5) { sB0 = __builtin_amdgcn_raw_buffer_load_b64(rws, sob + ((2 * gi) * 16 + (lane & 15)) * 8, 0, 16); sB1 = __builtin_amdgcn_raw_buffer_load_b64(rws, sob + ((2 * gi + 1) * 16 + (lane & 15)) * 8, 0, 16); }
            SBAR();
#pragma unroll
            for (int u = 0; u < 7; ++u) { acc[u] = MFMA16(wf[u], __builtin_bit_cast(bf16x8, xf[0][s]), acc[u]); PIN(acc[u]); SBAR();
                acb[u] = MFMA16(wf[u], __builtin_bit_cast(bf16x8, xf[1][s]), acb[u]); PIN(acb[u]); SBAR();
                if (s < 7) wf[u] = trfrag(W1A(s + 1, u), W1A(s + 1, u) + 3584); SBAR(); }
            if (s == 7) WS4(1);
        }
#undef W1A
        float ca, cb, cc, r2;
        { float S[6];
#pragma unroll
          for (int i = 0; i < 4; ++i) S[i] = xsum32(xsum16(__uint_as_float(sA0[i]) + __uint_as_float(sA1[i])));
#pragma unroll
          for (int i = 0; i < 2; ++i) S[4 + i] = xsum32(xsum16(__uint_as_float(sB0[i]) + __uint_as_float(sB1[i])));
          const f32x4 cc4 = lds_ld<f32x4>(L_CONST); const float SB2 = lds_ld<float>(L_CONST + 16);
          const float iD = 1.f / 512.f, m1 = S[0] * iD, r1 = __builtin_amdgcn_rsqf(fmaxf(S[1] * iD - m1 * m1, 0.f) + 1e-5f);
          const float m2 = (r1 * (S[2] - m1 * cc4[0]) + cc4[1]) * iD;
          const float ez2 = (r1 * r1 * (S[3] - 2.f * m1 * S[4] + m1 * m1 * cc4[2]) + 2.f * r1 * (S[5] - m1 * cc4[3]) + SB2) * iD;
          r2 = __builtin_amdgcn_rsqf(fmaxf(ez2 - m2 * m2, 0.f) + 1e-5f);
          ca = r1 * r2; cb = -ca * m1; cc = -r2 * m2; PIN(ca); PIN(cb); PIN(cc); PIN(r2); }
        const unsigned uo = L_UVW + 16 * gi + 64 * odd, uo6 = L_UVW + 16 * gi + (odd ? 0 : 384);
        f32x4 uu = lds_ld<f32x4>(uo), vv = lds_ld<f32x4>(uo + 448), ww = lds_ld<f32x4>(uo + 896), bb = lds_ld<f32x4>(uo + 1344);
        f32x4 u2 = lds_ld<f32x4>(uo + 64), v2 = lds_ld<f32x4>(uo + 64 + 448), w2v = lds_ld<f32x4>(uo + 64 + 896), b2v = lds_ld<f32x4>(uo + 64 + 1344);
        BAR();
        WS4(2);
        { const unsigned ex = 101376 + w * 7168 + lane * 16;
#pragma unroll
          for (int u = 0; u < 7; ++u) lds_st<f32x4>(ex + u * 1024, acb[u]); }
        WS4(3);
        BAR();
        WS4(4);
        { const unsigned ep = 101376 + (w ^ 4) * 7168 + lane * 16;
#pragma unroll
          for (int u = 0; u < 7; ++u) acc[u] += lds_ld<f32x4>(ep + u * 1024); }
        STAMP(14);
        const unsigned hb = WS_H + (g * 8 + Tw) * 57344, s0 = (7 * j + odd) >> 1;
        const f32x2 kA = {-0.044715f * 2.302208198f, -0.044715f * 2.302208198f}, kB = {-2.302208198f, -2.302208198f}, one2 = {1.f, 1.f};
#define LO2(v) ((f32x2){(v)[0], (v)[1]})
#define HI2(v) ((f32x2){(v)[2], (v)[3]})
#define PRE2(a_, u_, v_, w_, b_) (ca * (a_) + (cb * (u_) + (r2 * (v_) + (cc * (w_) + (b_)))))
#define GELU(N, p, y) { f32x2 z_[N], e_[N]; \
            _Pragma("unroll") for (int r = 0; r < N; ++r) { z_[r] = p[r] * p[r]; PIN(z_[r]); } \
            _Pragma("unroll") for (int r = 0; r < N; ++r) { z_[r] = z_[r] * kA + kB; PIN(z_[r]); } \
            _Pragma("unroll") for (int r = 0; r < N; ++r) { z_[r] = z_[r] * p[r]; PIN(z_[r]); } \
            SBAR(); _Pragma("unroll") for (int r = 0; r < N; ++r) { e_[r] = (f32x2){__builtin_amdgcn_exp2f(z_[r][0]), __builtin_amdgcn_exp2f(z_[r][1])}; PIN(e_[r]); } \
            SBAR(); _Pragma("unroll") for (int r = 0; r < N; ++r) { e_[r] = e_[r] + one2; PIN(e_[r]); } \
            SBAR(); _Pragma("unroll") for (int r = 0; r < N; ++r) { e_[r] = (f32x2){__builtin_amdgcn_rcpf(e_[r][0]), __builtin_amdgcn_rcpf(e_[r][1])}; PIN(e_[r]); } \
            SBAR(); _Pragma("unroll") for (int r = 0; r < N; ++r) y[r] = p[r] * e_[r]; }
#pragma unroll
        for (int u = 0; u < 6; u += 2) {
            f32x2 pre[4], y[4];
            pre[0] = PRE2(LO2(acc[u]), LO2(uu), LO2(vv), LO2(ww), LO2(bb)); pre[1] = PRE2(HI2(acc[u]), HI2(uu), HI2(vv), HI2(ww), HI2(bb));
            pre[2] = PRE2(LO2(acc[u + 1]), LO2(u2), LO2(v2), LO2(w2v), LO2(b2v)); pre[3] = PRE2(HI2(acc[u + 1]), HI2(u2), HI2(v2), HI2(w2v), HI2(b2v));
            PIN(pre[0]); PIN(pre[1]); PIN(pre[2]); PIN(pre[3]);
            SBAR();
            { const unsigned o = u < 4 ? uo + (u + 2) * 64 : uo6; uu = lds_ld<f32x4>(o); vv = lds_ld<f32x4>(o + 448); ww = lds_ld<f32x4>(o + 896); bb = lds_ld<f32x4>(o + 1344); }
            if (u < 4) { const unsigned o = uo + (u + 3) * 64; u2 = lds_ld<f32x4>(o); v2 = lds_ld<f32x4>(o + 448); w2v = lds_ld<f32x4>(o + 896); b2v = lds_ld<f32x4>(o + 1344); }
            SBAR();
            GELU(4, pre, y)
            __builtin_amdgcn_raw_buffer_store_b128((u32x4){pk2(y[0][0], y[0][1]), pk2(y[1][0], y[1][1]), pk2(y[2][0], y[2][1]), pk2(y[3][0], y[3][1])}, rws, hb + (s0 + (u >> 1)) * 1024 + lane * 16, 0, 16);
            SBAR();
        }
        {   f32x2 pre[2], y[2];
            pre[0] = PRE2(LO2(acc[6]), LO2(uu), LO2(vv), LO2(ww), LO2(bb)); pre[1] = PRE2(HI2(acc[6]), HI2(uu), HI2(vv), HI2(ww), HI2(bb));
            GELU(2, pre, y)
            __builtin_amdgcn_raw_buffer_store_b64((u32x2){pk2(y[0][0], y[0][1]), pk2(y[1][0], y[1][1])}, rws, hb + ((7 * j + (odd ? 0 : 6)) >> 1) * 1024 + odd * 512 + lane * 8, 0, 16);
        }
#undef GELU
#undef PRE2
#undef LO2
#undef HI2
        WS4(5);
    }
    VMWAIT(0);
    BAR();
    if (tid == 0) stg_sc1(ws, WS_FLAG + (3 * 256 + g * 16 + j) * 256, epoch);
    STAMP(15);
    u32x4 park[10];
    { FRESH(lane);
      const unsigned sw = (lane >> 3) * 1024u + ct * 128u + (((lane & 7) >> 1) ^ ((lane >> 4) & 3)) * 32u + (lane & 1) * 16u;
      const char* p0 = (const char*)w2 + (size_t)(56 * (w >> 2) + 36 + (w & 3)) * 8192, * p1 = p0 + (size_t)112 * 8192;
#pragma unroll
      for (int i = 0; i < 5; ++i) { park[2 * i] = *(const u32x4*)(p0 + i * 32768 + sw); park[2 * i + 1] = *(const u32x4*)(p1 + i * 32768 + sw); }
      SBAR();
      if (w == 0) poll_flags(ws, WS_FLAG + (3 * 256 + g * 16) * 256, 16, epoch, lane); }
    SBAR();
    BAR();
    STAMP(16); WS5(0);
    {
        constexpr int WIN = 3;
        FRESH(lane);
        const int gi = lane >> 4, q4 = (lane & 15) >> 2, p4 = lane & 3;
        const unsigned tokP = 128 * g + 64 * rh + 16 * tt + (lane & 15), colP = 64 * ct + 32 * kh + 4 * gi;
        const int kq = (w & 1) | (kh << 1);
        const unsigned hb0 = WS_H + (g * 8 + 4 * rh + tt) * 57344 + kq * 14 * 1024, hb1 = WS_H + (g * 8 + 4 * rh + (tt ^ 1)) * 57344 + kq * 14 * 1024;
        u32x4 hf[2][14];
#define HLD1(hb_, dst, s) { if ((s) % 7 == 3) { const u32x2 lo = __builtin_amdgcn_raw_buffer_load_b64(rws, hb_ + (s) * 1024 + lane * 8, 0, 16), hi = __builtin_amdgcn_raw_buffer_load_b64(rws, hb_ + (s) * 1024 + 512 + lane * 8, 0, 16); dst = (u32x4){lo[0], lo[1], hi[0], hi[1]}; } \
                    else dst = __builtin_amdgcn_raw_buffer_load_b128(rws, hb_ + (s) * 1024 + lane * 16, 0, 16); }
#define HLOAD(s) { HLD1(hb0, hf[0][s], s) HLD1(hb1, hf[1][s], s) }
#pragma unroll
        for (int s = 0; s < WIN; ++s) HLOAD(s)
        SBAR();
        f32x4 acc[2][4]; bf16x8 wf[4];
        const unsigned xv = (2 * gi + (q4 >> 1)) & 3, rowa = kq * 4096 + (4 * gi + q4) * 128 + p4 * 8;
        unsigned an[4];
#pragma unroll
        for (int n = 0; n < 4; ++n) { an[n] = rowa + (((n ^ (2 * kh)) ^ xv) * 32); acc[0][n] = (f32x4){0.f, 0.f, 0.f, 0.f}; acc[1][n] = acc[0][n]; }
#define W2A(s, n) (an[n] + (((s) % 9) & 3) * 16384)
#pragma unroll
        for (int n = 0; n < 4; ++n) wf[n] = trfrag(W2A(0, n), W2A(0, n) + 2048);
        const unsigned cl = (32 * kh + 4 * gi) * 2;
        const u32x2 g0 = lds_ld<u32x2>(L_GBO + cl), g1 = lds_ld<u32x2>(L_GBO + cl + 32);
        u32x2 bb0, bb1;
        SBAR();
#pragma unroll
        for (int s = 0; s < 14; ++s) {
            if (s == 11) { bb0 = *(const u32x2*)(b2 + colP); bb1 = *(const u32x2*)(b2 + colP + 16); }
            if (s == 1) WS5(1); if (s == 5) WS5(2); if (s == 8) WS5(3);
            if (s == 5) BAR();
            if (s == 8) BAR();
            if (s == 8) WS5(4);
            if (s + WIN < 14) HLOAD(s + WIN)
            SBAR();
#pragma unroll
            for (int n = 0; n < 4; ++n) { acc[0][n] = MFMA16(wf[n], __builtin_bit_cast(bf16x8, hf[0][s]), acc[0][n]); PIN(acc[0][n]); SBAR();
                acc[1][n] = MFMA16(wf[n], __builtin_bit_cast(bf16x8, hf[1][s]), acc[1][n]); PIN(acc[1][n]); SBAR();
                if (s >= 5 && s <= 7 && 4 * (s - 5) + n < 10) { const int i = 4 * (s - 5) + n; lds_st<u32x4>((i >> 1) * 16384 + (w + 8 * (i & 1)) * 1024 + lane * 16, park[i]); }
                if (s == 3 || s == 7 || s == 12) { an[n] += 65536; PIN(an[n]); } if (s == 8) { an[n] -= 131072; PIN(an[n]); }
                if (s < 13) wf[n] = trfrag(W2A(s + 1, n), W2A(s + 1, n) + 2048); SBAR(); }
        }
#undef W2A
#undef HLOAD
#undef HLD1
        WS5(5);
        BAR();
        STAMP(17);
        { const unsigned ex = w * 6144 + lane * 16;
          lds_st<f32x4>(ex, acc[0][2]); lds_st<f32x4>(ex + 1024, acc[0][3]); lds_st<f32x4>(ex + 2048, acc[1][0]); lds_st<f32x4>(ex + 3072, acc[1][1]); lds_st<f32x4>(ex + 4096, acc[1][2]); lds_st<f32x4>(ex + 5120, acc[1][3]); }
        BAR();
        const unsigned e4 = (w ^ 4) * 6144 + lane * 16, e1 = (w ^ 1) * 6144 + 2048 + lane * 16, e5 = (w ^ 5) * 6144 + 4096 + lane * 16;
        const f32x4 a0 = (acc[0][0] + lds_ld<f32x4>(e4)) + (lds_ld<f32x4>(e1) + lds_ld<f32x4>(e5)), a1 = (acc[0][1] + lds_ld<f32x4>(e4 + 1024)) + (lds_ld<f32x4>(e1 + 1024) + lds_ld<f32x4>(e5 + 1024));
        float y0[4], y1[4];
#pragma unroll
        for (int r = 0; r < 4; ++r) {
            const unsigned ug0 = g0[r >> 1], ug1 = g1[r >> 1], ub0 = bb0[r >> 1], ub1 = bb1[r >> 1], ux0 = x1p0[r >> 1], ux1 = x1p1[r >> 1];
            y0[r] = fmaf((r & 1) ? bf_hi(ug0) : bf_lo(ug0), a0[r] + ((r & 1) ? bf_hi(ub0) : bf_lo(ub0)), (r & 1) ? bf_hi(ux0) : bf_lo(ux0));
            y1[r] = fmaf((r & 1) ? bf_hi(ug1) : bf_lo(ug1), a1[r] + ((r & 1) ? bf_hi(ub1) : bf_lo(ub1)), (r & 1) ? bf_hi(ux1) : bf_lo(ux1));
        }
        *(u32x2*)(out + tokP * 512 + colP) = (u32x2){pk2(y0[0], y0[1]), pk2(y0[2], y0[3])};
        *(u32x2*)(out + tokP * 512 + colP + 16) = (u32x2){pk2(y1[0], y1[1]), pk2(y1[2], y1[3])};
    }
    if (b == 0 && w == 0) {
        for (unsigned sp = 0; sp < SPIN_CAP; ++sp) { asm volatile("" ::: "memory"); const u32x4 v = __builtin_amdgcn_raw_buffer_load_b128(rws, WS_HELLO + lane * 16, 0, 16); if (__all(v[0] == epoch && v[1] == epoch && v[2] == epoch && v[3] == epoch)) break; }
        if (lane == 0) stg_sc1(ws, WS_E, epoch);
    }
    STAMP(18); STAMPC(21);
    STAMP_OUT();
    asm volatile("fb_code_end:");
}

extern "C" void kernel_launch(void* const* d_in, const int* in_sizes, int n_in, void* d_out, int out_size, void* d_ws, size_t ws_size, hipStream_t stream) {
    (void)hipFuncSetAttribute((const void*)fused_block, hipFuncAttributeMaxDynamicSharedMemorySize, LDS_BYTES);
    fused_block<<<256, 576, LDS_BYTES, stream>>>((const u16*)d_in[0], (const u16*)d_in[1], (const u16*)d_in[2], (const u16*)d_in[4], (const u16*)d_in[5], (const u16*)d_in[6], (const u16*)d_in[7],
                                                 (const u16*)d_in[8], (const u16*)d_in[9], (u16*)d_out, (char*)d_ws);
}
```

```cpp
#include <hip/hip_runtime.h>
#include <hip/hip_bf16.h>
#include <stdint.h>

typedef unsigned short u16;
typedef short bf16x8 __attribute__((ext_vector_type(8)));
typedef short s16x4 __attribute__((ext_vector_type(4)));
typedef float f32x4 __attribute__((ext_vector_type(4)));
typedef float f32x2 __attribute__((ext_vector_type(2)));
typedef float f32x16 __attribute__((ext_vector_type(16)));
typedef unsigned u32x4 __attribute__((ext_vector_type(4)));
typedef unsigned u32x2 __attribute__((ext_vector_type(2)));
typedef __bf16 bf2 __attribute__((ext_vector_type(2)));
#define LAS __attribute__((address_space(3)))
#define GAS __attribute__((address_space(1)))
#define DI __device__ __forceinline__

#define TS 0

constexpr unsigned WS_E = 0, WS_HELLO = 4096, WS_FLAG = 8192;
constexpr unsigned WS_MODU = 0x50000, WS_MODN = 0x60000;
constexpr unsigned WS_PART = 0x80000;
constexpr unsigned WS_KV = 0x100000, WS_O = 0x500000, WS_XG = 0x700000, WS_ST = 0x900000, WS_H = 0xA00000, WS_TS = 0x2000000;
constexpr unsigned WS_BYTES = 0x2100000;
constexpr unsigned L_X = 0, L_WQKV = 57344, L_RED = 155648, L_SAG = 156672, L_STAT = 160768;
constexpr unsigned L_QS = 65536, L_KIMG = 98304, L_VIMG = 122880, L_WO = 0, L_W1 = 44032;
constexpr unsigned L_GB16 = 158720, L_SB16 = 159744, L_UVW = 160768, L_CONST = 162560, L_GAO = 162592, L_GBO = 162720, L_ONES = 162848, L_DUMMY = 162880;
constexpr unsigned L_TAGM = L_RED + 512, L_TAGL = L_RED + 544, L_TAGC = L_RED + 560;
constexpr unsigned L_RED2 = 81920;
constexpr unsigned L_SCR = 32768;
constexpr unsigned LDS_BYTES = 163840;
constexpr unsigned SPIN_CAP = 1u << 16;

DI float bf_lo(unsigned u) { return __uint_as_float(u << 16); }
DI float bf_hi(unsigned u) { return __uint_as_float(u & 0xffff0000u); }
DI unsigned pk2(float a, float b) { bf2 v = {(__bf16)a, (__bf16)b}; return __builtin_bit_cast(unsigned, v); }
DI float rbf(float a) { return bf_lo(pk2(a, a)); }
DI void dma16(unsigned voff, const void* sbase, unsigned lds) { asm volatile("s_mov_b32 m0, %1\n\ts_nop 0\n\tglobal_load_lds_dwordx4 %0, %2" ::"v"(voff), "s"(lds), "s"(sbase) : "memory"); }
DI void dma16c(unsigned voff, const void* sbase, unsigned lds) { asm volatile("s_mov_b32 m0, %1\n\ts_nop 0\n\tglobal_load_lds_dwordx4 %0, %2 sc1" ::"v"(voff), "s"(lds), "s"(sbase) : "memory"); }
DI void dma4(unsigned v0, unsigned v1, unsigned v2, unsigned v3, const void* sbase, unsigned lds) {
    asm volatile("s_mov_b32 m0, %5\n\ts_nop 0\n\tglobal_load_lds_dwordx4 %0, %4\n\tglobal_load_lds_dwordx4 %1, %4 offset:1024\n\tglobal_load_lds_dwordx4 %2, %4 offset:2048\n\tglobal_load_lds_dwordx4 %3, %4 offset:3072"
                 ::"v"(v0), "v"(v1), "v"(v2), "v"(v3), "s"(sbase), "s"(lds) : "memory"); }
DI void dma3(unsigned v0, unsigned v1, unsigned v2, const void* sbase, unsigned lds) {
    asm volatile("s_mov_b32 m0, %4\n\ts_nop 0\n\tglobal_load_lds_dwordx4 %0, %3\n\tglobal_load_lds_dwordx4 %1, %3 offset:1024\n\tglobal_load_lds_dwordx4 %2, %3 offset:2048"
                 ::"v"(v0), "v"(v1), "v"(v2), "s"(sbase), "s"(lds) : "memory"); }
DI void dma4lin_c(unsigned v, const void* sbase, unsigned lds) {
    asm volatile("s_mov_b32 m0, %2\n\ts_nop 0\n\tglobal_load_lds_dwordx4 %0, %1 sc1\n\tglobal_load_lds_dwordx4 %0, %1 offset:1024 sc1\n\tglobal_load_lds_dwordx4 %0, %1 offset:2048 sc1\n\tglobal_load_lds_dwordx4 %0, %1 offset:3072 sc1"
                 ::"v"(v), "s"(sbase), "s"(lds) : "memory"); }
DI void lds_wait_tags(unsigned a, int n, unsigned tag) {
    for (unsigned sp = 0; sp < SPIN_CAP; ++sp) { asm volatile("" ::: "memory"); const unsigned t0 = *(const volatile LAS unsigned*)(size_t)a, t1 = *(const volatile LAS unsigned*)(size_t)(a + 4 * (n - 1));
        if (__builtin_amdgcn_readfirstlane(t0) == tag && __builtin_amdgcn_readfirstlane(t1) == tag) break; __builtin_amdgcn_s_sleep(1); }
    asm volatile("" ::: "memory"); }
DI void warm4(unsigned voff, const void* sbase) { asm volatile("s_mov_b32 m0, %1\n\ts_nop 0\n\tglobal_load_lds_dword %0, %2" ::"v"(voff), "s"(L_DUMMY), "s"(sbase) : "memory"); }
#define WSP 4
#define WS1(i) do { if (WSP == 1) WSTAMP(i); } while (0)
#define WS2(i) do { if (WSP == 2) WSTAMP(i); } while (0)
#define WS3(i) do { if (WSP == 3) WSTAMP(i); } while (0)
#define WS4(i) do { if (WSP == 4) WSTAMP(i); } while (0)
#define WS5(i) do { if (WSP == 5) WSTAMP(i); } while (0)
#define WS6(i) do { if (WSP == 6) WSTAMP(i); } while (0)
#define FRESH(v) int v = threadIdx.x & 63; asm volatile("" : "+v"(v))
#define SBAR() __builtin_amdgcn_sched_barrier(0)
#define PIN(x) asm volatile("" : "+v"(x))
#define BAR() asm volatile("s_waitcnt lgkmcnt(0)\n\ts_barrier" ::: "memory")
#define VMWAIT(n) asm volatile("s_waitcnt vmcnt(" #n ")" ::: "memory")
DI unsigned ldg_sc1(const char* ws, unsigned off) { return __hip_atomic_load((GAS unsigned*)(ws + off), __ATOMIC_RELAXED, __HIP_MEMORY_SCOPE_AGENT); }
DI unsigned long long ldg64_sc1(const char* ws, unsigned off) { return __hip_atomic_load((GAS unsigned long long*)(ws + off), __ATOMIC_RELAXED, __HIP_MEMORY_SCOPE_AGENT); }
DI void stg_sc1(char* ws, unsigned off, unsigned v) { __hip_atomic_store((GAS unsigned*)(ws + off), v, __ATOMIC_RELAXED, __HIP_MEMORY_SCOPE_AGENT); }
DI void stg64_sc1(char* ws, unsigned off, unsigned long long v) { __hip_atomic_store((GAS unsigned long long*)(ws + off), v, __ATOMIC_RELAXED, __HIP_MEMORY_SCOPE_AGENT); }
template <class T> DI T lds_ld(unsigned a) { return *(const LAS T*)(size_t)a; }
template <class T> DI void lds_st(unsigned a, T v) { *(LAS T*)(size_t)a = v; }
DI s16x4 lds_tr(unsigned a) { return __builtin_amdgcn_ds_read_tr16_b64_v4i16((LAS s16x4*)(size_t)a); }
DI bf16x8 trfrag(unsigned a0, unsigned a1) { s16x4 lo = lds_tr(a0), hi = lds_tr(a1); return __builtin_shufflevector(lo, hi, 0, 1, 2, 3, 4, 5, 6, 7); }
#define MFMA32(a, b, c) __builtin_amdgcn_mfma_f32_32x32x16_bf16((a), (b), (c), 0, 0, 0)
#define MFMA16(a, b, c) __builtin_amdgcn_mfma_f32_16x16x32_bf16((a), (b), (c), 0, 0, 0)
template <int N> DI float bcast(float v) { return __builtin_bit_cast(float, __builtin_amdgcn_mov_dpp(__builtin_bit_cast(int, v), 0x150 + N, 0xf, 0xf, true)); }
template <int N> DI float modu(float t, float gv, float sv) {
#pragma clang fp contract(off)
    const float p = bcast<N>(gv) * t; return bcast<N>(sv) + p; }
template <int N> DI unsigned hdw(unsigned x, float r, float n, float gv, float c, float d) {
    unsigned o; float a, b2;
    asm("v_lshlrev_b32 %1, 16, %5\n\tv_and_b32 %2, 0xffff0000, %5\n\tv_fma_f32 %1, %1, %6, %7\n\tv_fma_f32 %2, %2, %6, %7\n\t"
        "v_fmac_f32_dpp %3, %8, %1 row_newbcast:%c9 row_mask:0xf bank_mask:0xf\n\tv_fmac_f32_dpp %4, %8, %2 row_newbcast:%c10 row_mask:0xf bank_mask:0xf\n\t"
        "v_cvt_pk_bf16_f32 %0, %3, %4"
        : "=v"(o), "=&v"(a), "=&v"(b2), "+v"(c), "+v"(d) : "v"(x), "v"(r), "v"(n), "v"(gv), "i"(N), "i"(N + 1));
    return o; }
template <int C> DI float dppf(float v) { return __builtin_bit_cast(float, __builtin_amdgcn_update_dpp(0, __builtin_bit_cast(int, v), C, 0xf, 0xf, true)); }
DI float rowsum16(float v) { v += dppf<0xB1>(v); v += dppf<0x4E>(v); v += dppf<0x141>(v); v += dppf<0x140>(v); return v; }
DI float xsum32(float v) { auto r = __builtin_amdgcn_permlane32_swap(__float_as_uint(v), __float_as_uint(v), false, false); return __uint_as_float(r[0]) + __uint_as_float(r[1]); }
DI float xmax32(float v) { auto r = __builtin_amdgcn_permlane32_swap(__float_as_uint(v), __float_as_uint(v), false, false); return fmaxf(__uint_as_float(r[0]), __uint_as_float(r[1])); }
DI float xsum16(float v) { auto r = __builtin_amdgcn_permlane16_swap(__float_as_uint(v), __float_as_uint(v), false, false); return __uint_as_float(r[0]) + __uint_as_float(r[1]); }
DI unsigned long long mt() { unsigned long long t; asm volatile("s_memtime %0\n\ts_waitcnt lgkmcnt(0)" : "=s"(t)::"memory"); return t; }
DI unsigned long long rt() { unsigned long long t; asm volatile("s_memrealtime %0\n\ts_waitcnt lgkmcnt(0)" : "=s"(t)::"memory"); return t; }
#define STAMP(i) do {} while (0)
#define STAMP_OUT() do {} while (0)
#define STAMPC(i) do {} while (0)
#define WSTAMP(i) do {} while (0)

DI void poll_flags(const char* ws, unsigned off, int n, unsigned epoch, int lane) {
    const unsigned o = off + (unsigned)(lane < n ? lane : 0) * 256u;
    for (unsigned sp = 0; sp < SPIN_CAP; ++sp) { unsigned f = ldg_sc1(ws, o); if (__all(f == epoch)) break; }
}

DI void mod_part(const u16* __restrict__ cvec, const u16* __restrict__ wmod, char* ws, unsigned epoch, int lane, int slice, int kp) {
    const unsigned row0 = 64 * kp + 4 * (lane >> 2), o = row0 * 6144u + (unsigned)slice * 64u + (lane & 3) * 16u;
    const u32x2 cv = *(const u32x2*)(cvec + row0);
    u32x4 wm[4];
#pragma unroll
    for (int i = 0; i < 4; ++i) wm[i] = *(const u32x4*)((const char*)wmod + o + i * 6144u);
    SBAR();
    unsigned sp[2];
#pragma unroll
    for (int r = 0; r < 2; ++r) { const unsigned cu = cv[r]; const float c0 = bf_lo(cu), c1 = bf_hi(cu);
        sp[r] = pk2(c0 * __builtin_amdgcn_rcpf(1.f + __builtin_amdgcn_exp2f(-1.44269504089f * c0)), c1 * __builtin_amdgcn_rcpf(1.f + __builtin_amdgcn_exp2f(-1.44269504089f * c1))); }
    float a[8] = {0.f, 0.f, 0.f, 0.f, 0.f, 0.f, 0.f, 0.f};
#pragma unroll
    for (int r = 0; r < 2; ++r)
#pragma unroll
        for (int i = 0; i < 4; ++i) { const unsigned u0 = wm[2 * r][i], u1 = wm[2 * r + 1][i], s_ = sp[r];
            const unsigned lo = __builtin_amdgcn_perm(u1, u0, 0x05040100u), hi = __builtin_amdgcn_perm(u1, u0, 0x07060302u);
            a[2 * i] = __builtin_amdgcn_fdot2_f32_bf16(__builtin_bit_cast(bf2, lo), __builtin_bit_cast(bf2, s_), a[2 * i], false);
            a[2 * i + 1] = __builtin_amdgcn_fdot2_f32_bf16(__builtin_bit_cast(bf2, hi), __builtin_bit_cast(bf2, s_), a[2 * i + 1], false); }
    float b4[4], b2[2];
#pragma unroll
    for (int i = 0; i < 4; ++i) { auto r = __builtin_amdgcn_permlane32_swap(__float_as_uint(a[i]), __float_as_uint(a[i + 4]), false, false); b4[i] = __uint_as_float(r[0]) + __uint_as_float(r[1]); }
#pragma unroll
    for (int i = 0; i < 2; ++i) { auto r = __builtin_amdgcn_permlane16_swap(__float_as_uint(b4[i]), __float_as_uint(b4[i + 2]), false, false); float v = __uint_as_float(r[0]) + __uint_as_float(r[1]);
        v += dppf<0x124>(v); v += dppf<0x128>(v); b2[i] = v; }
    const __amdgpu_buffer_rsrc_t rws = __builtin_amdgcn_make_buffer_rsrc(ws, 0, WS_BYTES, 0x00020000);
    if ((lane & 12) == 0) __builtin_amdgcn_raw_buffer_store_b128((u32x4){__float_as_uint(b2[0]), epoch, __float_as_uint(b2[1]), epoch}, rws,
                                                                 WS_PART + (slice * 8 + kp) * 256 + ((lane & 3) * 4 + (lane >> 5) * 2 + ((lane >> 4) & 1)) * 16, 0, 16);
}
struct PartSum { u32x4 v0, v1; };
DI void mod_sum_load(PartSum& p, char* ws, int lane, int slice) {
    const __amdgpu_buffer_rsrc_t rws = __builtin_amdgcn_make_buffer_rsrc(ws, 0, WS_BYTES, 0x00020000);
    const unsigned o = WS_PART + (slice * 8 + (lane >> 3)) * 256 + (lane & 7) * 16;
    p.v0 = __builtin_amdgcn_raw_buffer_load_b128(rws, o, 0, 16); p.v1 = __builtin_amdgcn_raw_buffer_load_b128(rws, o + 128, 0, 16); }
DI void mod_sum_finish(PartSum& p, char* ws, unsigned epoch, int lane, int slice) {
    for (unsigned sp = 0; sp < SPIN_CAP; ++sp) { if (__all(p.v0[1] == epoch && p.v0[3] == epoch && p.v1[1] == epoch && p.v1[3] == epoch)) break; asm volatile("" ::: "memory"); mod_sum_load(p, ws, lane, slice); }
    float s[4] = {__uint_as_float(p.v0[0]), __uint_as_float(p.v0[2]), __uint_as_float(p.v1[0]), __uint_as_float(p.v1[2])};
#pragma unroll
    for (int i = 0; i < 4; ++i) { float v = s[i]; v += dppf<0x128>(v); s[i] = xsum32(xsum16(v)); }
    const unsigned long long g0 = ((unsigned long long)epoch << 32) | pk2(s[0], s[1]), g1 = ((unsigned long long)epoch << 32) | pk2(s[2], s[3]);
    const unsigned base = slice < 32 ? WS_MODU + slice * 128 : WS_MODN + (slice - 32) * 128, rs = slice < 32 ? 4096 : 8192, rep = lane >> 3, o = base + (lane & 7) * 8;
    stg64_sc1(ws, o + rep * rs, g0); stg64_sc1(ws, o + 64 + rep * rs, g1); stg64_sc1(ws, o + (rep + 8) * rs, g0); stg64_sc1(ws, o + 64 + (rep + 8) * rs, g1);
}

__global__ __launch_bounds__(576, 3) void fused_block(const u16* __restrict__ x, const u16* __restrict__ wqkv, const u16* __restrict__ wo, const u16* __restrict__ w1, const u16* __restrict__ b1,
                                                      const u16* __restrict__ w2, const u16* __restrict__ b2, const u16* __restrict__ cvec, const u16* __restrict__ wmod, u16* __restrict__ out, char* __restrict__ ws) {
    const unsigned epoch = *(const unsigned*)(ws + WS_E) + 1u;
    const int tid = threadIdx.x, lane = tid & 63, w = __builtin_amdgcn_readfirstlane(tid >> 6);
    const int b = blockIdx.x, xl = b & 7, idx = b >> 3, g = 2 * xl + (idx >> 4), j = idx & 15;
    const int ct = j & 7, rh = j >> 3;
    unsigned long long code_pc; unsigned code_len;
    asm volatile("s_getpc_b64 %0\nfb_code_begin:\n\ts_mov_b32 %1, fb_code_end-fb_code_begin" : "=s"(code_pc), "=s"(code_len));
    unsigned long long dbg_t0 = rt();
    if (tid == 0) stg64_sc1(ws, 0x1F00000 + b * 8, ((unsigned long long)epoch << 32) | (unsigned)dbg_t0);
    STAMP(0); STAMPC(20);
    if (w == 8) {
        {
            const unsigned o = ((unsigned)idx * 32u + (lane & 31)) * 64u;
            warm4((int)o < (int)code_len ? o : 0u, (const void*)code_pc);
        }
        if (lane == 0) stg_sc1(ws, WS_HELLO + b * 4, epoch);
        __builtin_amdgcn_s_setprio(3);
        const unsigned sw0 = (lane >> 3) * 1024u + ct * 128u + (((lane & 7) >> 1) ^ ((lane >> 4) & 3)) * 32u + (lane & 1) * 16u, sw1 = sw0 + 7168, sw2 = sw0 + 14336, sw3 = sw0 + 21504;
        unsigned w1o[7], wqo[3];
#pragma unroll
        for (int r = 0; r < 7; ++r) { const unsigned u = r * 64 + lane, row = u / 14, c = u % 14; w1o[r] = row * 3584u + 224u * j + c * 16u - (r & 3) * 1024u; }
#pragma unroll
        for (int r = 0; r < 3; ++r) { const unsigned u = r * 64 + lane, row = u / 12, c = u % 12; wqo[r] = row * 3072u + (c >> 2) * 1024u + 64u * j + (c & 3) * 16u - r * 1024u; }
#define RUN128(base, first, n4, lds0) { const char* p_ = (const char*)(base) + (size_t)(first) * 8192; unsigned l_ = (lds0); _Pragma("unroll 2") for (int i_ = 0; i_ < (n4); ++i_) { dma4(sw0, sw1, sw2, sw3, p_, l_); p_ += 32768; l_ += 4096; } }
#define W1GRPS(k0, k1) { const char* p_ = (const char*)w1 + (size_t)(k0) * 114688; unsigned l_ = L_W1 + 7 * (k0) * 1024; _Pragma("unroll 1") for (int k_ = (k0); k_ < (k1); ++k_) { \
            dma4(w1o[0], w1o[1], w1o[2], w1o[3], p_, l_); dma3(w1o[4], w1o[5], w1o[6], p_, l_ + 4096); p_ += 114688; l_ += 7168; } }
#define W2CHUNKS(c0, c1, slot0) { _Pragma("unroll 1") for (int c_ = (c0); c_ < (c1); ++c_) { const char* p_ = (const char*)w2 + (size_t)c_ * 32768; const unsigned l_ = ((slot0) + c_ - (c0)) * 16384; \
            dma4(sw0, sw1, sw2, sw3, p_, l_); dma4(sw0, sw1, sw2, sw3, p_ + 56 * 8192, l_ + 4096); dma4(sw0, sw1, sw2, sw3, p_ + 112 * 8192, l_ + 8192); dma4(sw0, sw1, sw2, sw3, p_ + 168 * 8192, l_ + 12288); } }
#define WQGRPS(k0, k1) { const char* p_ = (const char*)wqkv + (size_t)(k0) * 49152; unsigned l_ = L_WQKV + 3 * (k0) * 1024; _Pragma("unroll 2") for (int k_ = (k0); k_ < (k1); ++k_) { dma3(wqo[0], wqo[1], wqo[2], p_, l_); p_ += 49152; l_ += 3072; } }
        __builtin_amdgcn_s_barrier();
        __builtin_amdgcn_s_barrier();
        WQGRPS(14, 24)
        __builtin_amdgcn_s_barrier();
        WQGRPS(0, 14)
        VMWAIT(0);
        __builtin_amdgcn_s_barrier();
        mod_part(cvec, wmod, ws, epoch, lane, 32 + 8 * xl + (idx & 3), idx >> 2); mod_part(cvec, wmod, ws, epoch, lane, 36 + 8 * xl + (idx & 3), idx >> 2);
        if (idx < 4) { PartSum p; mod_sum_load(p, ws, lane, 32 + 8 * xl + idx); mod_sum_finish(p, ws, epoch, lane, 32 + 8 * xl + idx); mod_sum_load(p, ws, lane, 36 + 8 * xl + idx); mod_sum_finish(p, ws, epoch, lane, 36 + 8 * xl + idx); }
        __builtin_amdgcn_s_barrier();
        __builtin_amdgcn_s_barrier();
        if (g >= 1) {
            const unsigned fo = WS_FLAG + (((g >= 2 ? g - 2 + (lane & 1) : 0) * 16 + j) * 256);
            for (unsigned sp = 0; sp < SPIN_CAP; ++sp) { unsigned f = ldg_sc1(ws, fo); if (__all(f == epoch)) break; }
            const unsigned lo16 = lane * 16;
#pragma unroll 1
            for (int src = (g >= 2 ? 0 : 1); src < 2; ++src) {
                const char* p_ = ws + WS_KV + (size_t)((g - 2 + src) * 16 + j) * 16384;
                dma4lin_c(lo16, p_, L_KIMG + src * 8192); dma4lin_c(lo16, p_ + 4096, L_KIMG + src * 8192 + 4096);
                dma4lin_c(lo16, p_ + 8192, L_VIMG + src * 8192); dma4lin_c(lo16, p_ + 12288, L_VIMG + src * 8192 + 4096);
            }
        }
        VMWAIT(0);
        __builtin_amdgcn_s_barrier();
        RUN128(wo, 0, 16, L_WO)
        W1GRPS(3, 7)
        W1GRPS(15, 16)
        __builtin_amdgcn_s_barrier();
        W1GRPS(7, 15)
        VMWAIT(56);
        __builtin_amdgcn_s_barrier();
        __builtin_amdgcn_s_barrier();
        __builtin_amdgcn_s_barrier();
        __builtin_amdgcn_s_barrier();
        W1GRPS(0, 3)
        __builtin_amdgcn_s_barrier();
        VMWAIT(0);
        __builtin_amdgcn_s_barrier();
        __builtin_amdgcn_s_barrier();
        W2CHUNKS(0, 2, 0)
        __builtin_amdgcn_s_barrier();
        __builtin_amdgcn_s_barrier();
        W2CHUNKS(2, 6, 2)
        __builtin_amdgcn_s_barrier();
        W2CHUNKS(6, 9, 6)
        VMWAIT(48);
        __builtin_amdgcn_s_barrier();
        VMWAIT(0);
        __builtin_amdgcn_s_barrier();
        __builtin_amdgcn_s_barrier();
        __builtin_amdgcn_s_barrier();
        __builtin_amdgcn_s_barrier();
        return;
    }
    const __amdgpu_buffer_rsrc_t rws = __builtin_amdgcn_make_buffer_rsrc(ws, 0, WS_BYTES, 0x00020000);
    const int m = w & 3, kh = w >> 2;
#define XTILE(t) { const unsigned xo = (unsigned)(128 * g + 32 * (t)) * 1024u + ((unsigned)lane << 4); \
        _Pragma("unroll 2") for (int i = 0; i < 32; i += 4) dma4(xo ^ (unsigned)((i & 15) << 4), xo ^ (unsigned)(((i & 15) + 1) << 4), xo ^ (unsigned)(((i & 15) + 2) << 4), xo ^ (unsigned)(((i & 15) + 3) << 4), \
                                                                 (const char*)x + i * 1024, L_X + (3 - (t)) * 32768 + i * 1024); }
    PartSum psum;
    if (w >= 4 && w < 7) XTILE(w - 4)
    if (w == 0) lds_st<u32x4>(L_ONES, (u32x4){0x3F803F80u, 0x3F803F80u, 0x3F803F80u, 0x3F803F80u});
    if (w == 3) { __builtin_amdgcn_s_setprio(2); mod_part(cvec, wmod, ws, epoch, lane, 4 * xl + (idx & 3), idx >> 2); __builtin_amdgcn_s_setprio(0); }
    STAMP(1); WS6(0);
    bf16x8 qf; u32x4 kpk, vpk; float qown[8];
    {
    const int h = lane >> 5, gi = lane >> 4, q4 = (lane & 15) >> 2, p4 = lane & 3;
    u32x4 xr[16];
#define XFRAGS() { const unsigned tok = lane & 31, rb = L_X + (3 - m) * 32768 + tok * 1024 + kh * 512; \
        _Pragma("unroll") for (int s = 0; s < 16; ++s) xr[s] = lds_ld<u32x4>(rb + (s >> 3) * 256 + ((((2 * (s & 7) + h) ^ (tok & 15))) << 4)); \
        _Pragma("unroll") for (int s = 0; s < 16; ++s) PIN(xr[s]); }
    if (w == 4) VMWAIT(0);
    BAR();
    WS6(1);
    if (w == 4) XTILE(3)
    if (w == 7) {
        unsigned wq[3];
#pragma unroll
        for (int r = 0; r < 3; ++r) { const unsigned u = r * 64 + lane, row = u / 12, c = u % 12; wq[r] = row * 3072u + (c >> 2) * 1024u + 64u * j + (c & 3) * 16u - r * 1024u; }
#pragma unroll 2
        for (int k = 24; k < 32; ++k) dma3(wq[0], wq[1], wq[2], (const char*)wqkv + (size_t)k * 49152, L_WQKV + 3 * k * 1024); }
    if (w == 3 && idx < 4) mod_sum_load(psum, ws, lane, 4 * xl + idx);
    if (m == 0) XFRAGS()
    if (w == 5 || w == 6) VMWAIT(0);
    BAR();
    WS6(2);
    if (w == 3 && idx < 4) mod_sum_finish(psum, ws, epoch, lane, 4 * xl + idx);
    if (m == 1 || m == 2) XFRAGS()
    if (w == 4) VMWAIT(0);
    BAR();
    if (m == 3) XFRAGS()
#undef XFRAGS
#undef XTILE
    STAMP(2);
    const unsigned gmo = WS_MODU + g * 4096 + (tid & 255) * 16; u32x4 gmv;
    if (w < 4) gmv = __builtin_amdgcn_raw_buffer_load_b128(rws, gmo, 0, 16);
    SBAR();
    {
        float s0 = 0.f, s1 = 0.f, s2 = 0.f, s3 = 0.f; const bf2 one = {(__bf16)1.0f, (__bf16)1.0f};
#pragma unroll
        for (int s = 0; s < 16; ++s)
#pragma unroll
            for (int i = 0; i < 4; i += 2) { const unsigned ua = xr[s][i], ub = xr[s][i + 1]; const bf2 va = __builtin_bit_cast(bf2, ua), vb = __builtin_bit_cast(bf2, ub);
                s0 = __builtin_amdgcn_fdot2_f32_bf16(va, one, s0, false); s1 = __builtin_amdgcn_fdot2_f32_bf16(va, va, s1, false);
                s2 = __builtin_amdgcn_fdot2_f32_bf16(vb, one, s2, false); s3 = __builtin_amdgcn_fdot2_f32_bf16(vb, vb, s3, false); }
        s0 = xsum32(s0 + s2); s1 = xsum32(s1 + s3);
        if (lane < 32) lds_st<u32x2>(L_STAT + (kh * 128 + 32 * m + lane) * 8, (u32x2){__float_as_uint(s0), __float_as_uint(s1)});
    }
    WS6(3);
    if (w < 4) {
        for (unsigned sp = 0; sp < SPIN_CAP; ++sp) { if (__all(gmv[1] == epoch && gmv[3] == epoch)) break; asm volatile("" ::: "memory"); gmv = __builtin_amdgcn_raw_buffer_load_b128(rws, gmo, 0, 16); }
        const unsigned u0 = gmv[0], u2 = gmv[2]; f32x4 f = {bf_lo(u0), bf_hi(u0), bf_lo(u2), bf_hi(u2)};
        if (tid >= 128) { f[0] = rbf(1.f + f[0]); f[1] = rbf(1.f + f[1]); f[2] = rbf(1.f + f[2]); f[3] = rbf(1.f + f[3]); }
        lds_st<f32x4>(L_SAG + tid * 16, f);
    }
    STAMP(3);
    VMWAIT(0);
    BAR();
    STAMP(4); STAMPC(22); WS1(0);
    float rstd, nmr, gv[2]; f32x4 sa0[2], sa1[2];
    const unsigned sga = L_SAG + (256 * kh + 16 * ((lane & 15) >> 3) + 8 * h + (lane & 7)) * 4;
    const unsigned saa = L_SAG + (256 * kh + 8 * h) * 4;
    { const u32x2 a = lds_ld<u32x2>(L_STAT + (32 * m + (lane & 31)) * 8), c2 = lds_ld<u32x2>(L_STAT + (128 + 32 * m + (lane & 31)) * 8);
      gv[0] = lds_ld<float>(sga + 2048);
      sa0[0] = lds_ld<f32x4>(saa); sa1[0] = lds_ld<f32x4>(saa + 16);
      const float s0 = __uint_as_float(a[0]) + __uint_as_float(c2[0]), s1 = __uint_as_float(a[1]) + __uint_as_float(c2[1]);
      const float mean = s0 * (1.f / 512.f), var = fmaxf(s1 * (1.f / 512.f) - mean * mean, 0.f);
      rstd = __builtin_amdgcn_rsqf(var + 1e-5f); nmr = -mean * rstd;
      asm volatile("s_nop 1" : "+v"(gv[0])); }
    f32x16 aQ, aK, aV;
#pragma unroll
    for (int i = 0; i < 16; ++i) { aQ[i] = 0.f; aK[i] = 0.f; aV[i] = 0.f; }
    {
        const unsigned rowa = L_WQKV + (256 * kh + 8 * (gi >> 1) + q4) * 192 + p4 * 8;
        const unsigned wqa = rowa + (((gi & 1) ^ kh) * 32), wva = rowa + 128 + (gi & 1) * 32;
        bf16x8 fk, fv;
#define HDW(s, i) hdw<8 * ((s) & 1) + 2 * (i)>(xr[s][i], rstd, nmr, gv[((s) >> 1) & 1], (i) < 2 ? sa0[(s) & 1][2 * (i) & 3] : sa1[(s) & 1][2 * (i) & 3], (i) < 2 ? sa0[(s) & 1][(2 * (i) + 1) & 3] : sa1[(s) & 1][(2 * (i) + 1) & 3])
        fk = trfrag(wqa + 64, wqa + 64 + 768); fv = trfrag(wva, wva + 768);
        sa0[1] = lds_ld<f32x4>(saa + 64); sa1[1] = lds_ld<f32x4>(saa + 64 + 16);
        { const unsigned a0 = HDW(0, 0), a1 = HDW(0, 1), a2 = HDW(0, 2), a3 = HDW(0, 3); xr[0] = (u32x4){a0, a1, a2, a3}; }
        asm volatile("s_nop 1" : "+v"(xr[0])); SBAR();
#define QKV_STEP(s, sn) { const bf16x8 hb = __builtin_bit_cast(bf16x8, xr[s]); unsigned n0, n1, n2, n3; \
            aK = MFMA32(fk, hb, aK); PIN(aK); SBAR(); if ((sn) < 16) { fk = trfrag(wqa + (sn) * 3072 + 64, wqa + (sn) * 3072 + 64 + 768); if ((s) + 2 < 16) sa0[(s) & 1] = lds_ld<f32x4>(saa + ((s) + 2) * 64); \
                n0 = HDW((sn) & 15, 0); n1 = HDW((sn) & 15, 1); PIN(n0); PIN(n1); } SBAR(); \
            aV = MFMA32(hb, fv, aV); PIN(aV); SBAR(); if ((sn) < 16) { fv = trfrag(wva + (sn) * 3072, wva + (sn) * 3072 + 768); if ((s) + 2 < 16) sa1[(s) & 1] = lds_ld<f32x4>(saa + ((s) + 2) * 64 + 16); \
                n2 = HDW((sn) & 15, 2); n3 = HDW((sn) & 15, 3); PIN(n2); PIN(n3); xr[(sn) & 15] = (u32x4){n0, n1, n2, n3}; } \
            if ((s) % 2 == 0 && (s) + 2 < 16) { gv[(((s) >> 1) + 1) & 1] = lds_ld<float>(sga + 2048 + (((s) >> 1) + 1) * 128); } SBAR(); }
        WS1(1);
        QKV_STEP(0, 1) QKV_STEP(1, 2) QKV_STEP(2, 3) QKV_STEP(3, 4) QKV_STEP(4, 5) QKV_STEP(5, 6) QKV_STEP(6, 7) QKV_STEP(7, 8)
        QKV_STEP(8, 9) QKV_STEP(9, 10) QKV_STEP(10, 11) QKV_STEP(11, 12) QKV_STEP(12, 13) QKV_STEP(13, 14) QKV_STEP(14, 15) QKV_STEP(15, 16)
#undef QKV_STEP
#undef HDW
        STAMP(5); STAMPC(23); WS1(2);
        float vs[8], vk[8];
#pragma unroll
        for (int i = 0; i < 8; ++i) { float lo = aV[i], hi = aV[8 + i]; PIN(lo); PIN(hi); vs[i] = kh ? lo : hi; vk[i] = kh ? hi : lo; }
        const unsigned ex = w * 6144 + lane * 16;
        lds_st<f32x4>(ex, (f32x4){aK[8], aK[9], aK[10], aK[11]}); lds_st<f32x4>(ex + 1024, (f32x4){aK[12], aK[13], aK[14], aK[15]});
        lds_st<f32x4>(ex + 2048, (f32x4){vs[0], vs[1], vs[2], vs[3]}); lds_st<f32x4>(ex + 3072, (f32x4){vs[4], vs[5], vs[6], vs[7]});
        bf16x8 fq = trfrag(wqa, wqa + 768), fq1 = trfrag(wqa + 3072, wqa + 3072 + 768);
        BAR();
        WS1(3);
        const unsigned ep = (w ^ 4) * 6144 + lane * 16;
        { const f32x4 k0 = lds_ld<f32x4>(ep), k1 = lds_ld<f32x4>(ep + 1024), v0 = lds_ld<f32x4>(ep + 2048), v1 = lds_ld<f32x4>(ep + 3072);
          u32x4 kp, vp;
          kp[0] = pk2(aK[0] + k0[0], aK[1] + k0[1]); kp[1] = pk2(aK[2] + k0[2], aK[3] + k0[3]); kp[2] = pk2(aK[4] + k1[0], aK[5] + k1[1]); kp[3] = pk2(aK[6] + k1[2], aK[7] + k1[3]);
          vp[0] = pk2(vk[0] + v0[0], vk[1] + v0[1]); vp[1] = pk2(vk[2] + v0[2], vk[3] + v0[3]); vp[2] = pk2(vk[4] + v1[0], vk[5] + v1[1]); vp[3] = pk2(vk[6] + v1[2], vk[7] + v1[3]);
          const unsigned kvo = WS_KV + (g * 16 + j) * 16384 + (m * 2 + kh) * 1024 + lane * 16;
          __builtin_amdgcn_raw_buffer_store_b128(kp, rws, kvo, 0, 16);
          __builtin_amdgcn_raw_buffer_store_b128(vp, rws, kvo + 8192, 0, 16);
          PIN(kp); PIN(vp); kpk = kp; vpk = vp; }
        SBAR();
#pragma unroll
        for (int s = 0; s < 16; s += 2) {
            aQ = MFMA32(fq, __builtin_bit_cast(bf16x8, xr[s]), aQ); PIN(aQ); SBAR(); if (s + 2 < 16) fq = trfrag(wqa + (s + 2) * 3072, wqa + (s + 2) * 3072 + 768); SBAR();
            aQ = MFMA32(fq1, __builtin_bit_cast(bf16x8, xr[s + 1]), aQ); PIN(aQ); SBAR(); if (s + 3 < 16) fq1 = trfrag(wqa + (s + 3) * 3072, wqa + (s + 3) * 3072 + 768); SBAR(); }
        lds_st<f32x4>(ex + 4096, (f32x4){aQ[8], aQ[9], aQ[10], aQ[11]}); lds_st<f32x4>(ex + 5120, (f32x4){aQ[12], aQ[13], aQ[14], aQ[15]});
#pragma unroll
        for (int i = 0; i < 8; ++i) qown[i] = aQ[i];
    }
    }
    WS1(4);
    VMWAIT(0); WS1(5);
    BAR();
    if (tid == 0) stg_sc1(ws, WS_FLAG + (0 * 256 + g * 16 + j) * 256, epoch);
    STAMP(6);
    { FRESH(lane);
      const unsigned ep = (w ^ 4) * 6144 + 4096 + lane * 16; const f32x4 q0 = lds_ld<f32x4>(ep), q1 = lds_ld<f32x4>(ep + 1024);
      lds_st<u32x4>(L_KIMG + (8 + m) * 2048 + kh * 1024 + lane * 16, kpk);
      lds_st<u32x4>(L_VIMG + (8 + m) * 2048 + kh * 1024 + lane * 16, vpk);
      const float qs = 0.25f * 1.44269504089f; u32x4 qp;
      qp[0] = pk2((qown[0] + q0[0]) * qs, (qown[1] + q0[1]) * qs); qp[1] = pk2((qown[2] + q0[2]) * qs, (qown[3] + q0[3]) * qs);
      qp[2] = pk2((qown[4] + q1[0]) * qs, (qown[5] + q1[1]) * qs); qp[3] = pk2((qown[6] + q1[2]) * qs, (qown[7] + q1[3]) * qs);
      qf = __builtin_bit_cast(bf16x8, qp); PIN(qf); }
    const int tt = w & 3;
    {
    FRESH(lane);
    const int hh = kh, qq = lane & 31, h = lane >> 5;
    const float NEG = -__builtin_inff(), fq0 = (float)(4 * h - qq) - 0.5f;
    f32x16 c0, c8;
#pragma unroll
    for (int r = 0; r < 16; ++r) {
        const float t = fmaf(fq0, 1e30f, (float)((r & 3) + 8 * (r >> 2)) * 1e30f); c0[r] = fminf(t, 0.f); c8[r] = fminf(-t, 0.f); }
    PIN(c0); PIN(c8);
    BAR();
    STAMP(8); WS2(0);
    {
        const unsigned ka = L_KIMG + m * 2048 + hh * 1024 + lane * 16;
        const bool isd = ((lane >> 4) & 1) == hh;
        const unsigned va = isd ? L_VIMG + m * 2048 + lane * 16 : L_ONES, vst = isd ? 2048u : 0u, vs1 = isd ? 1024u : 0u;
        f32x16 o;
#pragma unroll
        for (int r = 0; r < 16; ++r) o[r] = 0.f;
#define MAX16(mx, s) { _Pragma("unroll") for (int r = 0; r < 16; r += 2) mx = fmaxf(fmaxf(mx, s[r]), s[r + 1]); }
#define EXPH(P, S, b) { P[0] = pk2(__builtin_amdgcn_exp2f(S[b]), __builtin_amdgcn_exp2f(S[b + 1])); P[1] = pk2(__builtin_amdgcn_exp2f(S[b + 2]), __builtin_amdgcn_exp2f(S[b + 3])); \
                        P[2] = pk2(__builtin_amdgcn_exp2f(S[b + 4]), __builtin_amdgcn_exp2f(S[b + 5])); P[3] = pk2(__builtin_amdgcn_exp2f(S[b + 6]), __builtin_amdgcn_exp2f(S[b + 7])); }
        if (g >= 2) {
            f32x16 z;
#pragma unroll
            for (int r = 0; r < 16; ++r) z[r] = 0.f;
            PIN(z);
            float mx = NEG, mx2 = NEG; f32x16 sA, sB;
#define KF(i) lds_ld<bf16x8>(ka + (i) * 2048)
            bf16x8 k0 = KF(0), k1 = KF(8), k2 = KF(1), k3 = KF(2);
            SBAR();
            sA = MFMA32(k0, qf, c0); PIN(sA); k0 = KF(3); SBAR();
            sB = MFMA32(k1, qf, c8); PIN(sB); k1 = KF(4); SBAR(); MAX16(mx, sA); PIN(mx); SBAR();
            sA = MFMA32(k2, qf, z); PIN(sA); k2 = KF(5); SBAR(); MAX16(mx2, sB); PIN(mx2); SBAR();
            sB = MFMA32(k3, qf, z); PIN(sB); k3 = KF(6); SBAR(); MAX16(mx, sA); PIN(mx); SBAR();
            sA = MFMA32(k0, qf, z); PIN(sA); k0 = KF(7); SBAR(); MAX16(mx2, sB); PIN(mx2); SBAR();
            sB = MFMA32(k1, qf, z); PIN(sB); k1 = KF(0); SBAR(); MAX16(mx, sA); PIN(mx); SBAR();
            sA = MFMA32(k2, qf, z); PIN(sA); k2 = KF(1); SBAR(); MAX16(mx2, sB); PIN(mx2); SBAR();
            sB = MFMA32(k3, qf, z); PIN(sB); SBAR(); MAX16(mx, sA); PIN(mx); SBAR();
            sA = MFMA32(k0, qf, z); PIN(sA); SBAR(); MAX16(mx2, sB); PIN(mx2); SBAR();
            WS2(1);
            bf16x8 v0 = lds_ld<bf16x8>(va), v1 = lds_ld<bf16x8>(va + vs1);
            MAX16(mx, sA);
            mx = xmax32(fmaxf(mx, mx2));
#pragma unroll
            for (int r = 0; r < 16; ++r) { z[r] = -mx; c0[r] -= mx; c8[r] -= mx; }
            PIN(z); PIN(c0); PIN(c8); SBAR();
            WS2(2);
            sA = MFMA32(k1, qf, c0); SBAR();
            u32x4 pa, pb; bf16x8 v0n, v1n, kn = k2; unsigned vp = va;
#define ABLK(I, SC, SN, CN) { bf16x8 kc = kn; if ((I) < 8) { vp += vst; v0n = lds_ld<bf16x8>(vp); v1n = lds_ld<bf16x8>(vp + vs1); } if ((I) < 7) kn = KF((I) + 2); SBAR(); \
                if ((I) < 8) { SN = MFMA32(kc, qf, CN); PIN(SN); } SBAR(); EXPH(pa, SC, 0); PIN(pa); SBAR(); \
                o = MFMA32(v0, __builtin_bit_cast(bf16x8, pa), o); PIN(o); SBAR(); EXPH(pb, SC, 8); PIN(pb); SBAR(); \
                o = MFMA32(v1, __builtin_bit_cast(bf16x8, pb), o); PIN(o); SBAR(); if ((I) < 8) { v0 = v0n; v1 = v1n; } }
            ABLK(0, sA, sB, z) ABLK(1, sB, sA, z) ABLK(2, sA, sB, z) ABLK(3, sB, sA, z)
            ABLK(4, sA, sB, z) ABLK(5, sB, sA, z) ABLK(6, sA, sB, z) ABLK(7, sB, sA, c8) ABLK(8, sA, sB, c8)
#undef KF
#undef ABLK
        } else {
            const int i0 = 8 - 4 * g - m;
            f32x16 z;
#pragma unroll
            for (int r = 0; r < 16; ++r) z[r] = 0.f;
            float mx = NEG;
            f32x16 s = MFMA32(lds_ld<bf16x8>(ka + 8 * 2048), qf, c8); MAX16(mx, s);
#pragma unroll 1
            for (int i = i0; i < 8; ++i) { s = MFMA32(lds_ld<bf16x8>(ka + i * 2048), qf, z); MAX16(mx, s); }
            mx = xmax32(mx);
#pragma unroll
            for (int r = 0; r < 16; ++r) { z[r] = -mx; c8[r] -= mx; }
#pragma unroll 1
            for (int i = i0; i <= 8; ++i) { u32x4 pa, pb;
                if (i < 8) s = MFMA32(lds_ld<bf16x8>(ka + i * 2048), qf, z); else s = MFMA32(lds_ld<bf16x8>(ka + i * 2048), qf, c8);
                EXPH(pa, s, 0); EXPH(pb, s, 8);
                o = MFMA32(lds_ld<bf16x8>(va + i * vst), __builtin_bit_cast(bf16x8, pa), o); o = MFMA32(lds_ld<bf16x8>(va + i * vst + vs1), __builtin_bit_cast(bf16x8, pb), o); }
        }
#undef EXPH
#undef MAX16
        WS2(3);
        float ov[8], l;
        { float lo = o[0], hi = o[8]; PIN(lo); PIN(hi); l = hh ? lo : hi; }
#pragma unroll
        for (int r = 0; r < 8; ++r) { float lo = o[r], hi = o[8 + r]; PIN(lo); PIN(hi); ov[r] = hh ? hi : lo; }
        const float inv = __builtin_amdgcn_rcpf(l);
        const u32x4 op = {pk2(ov[0] * inv, ov[1] * inv), pk2(ov[2] * inv, ov[3] * inv), pk2(ov[4] * inv, ov[5] * inv), pk2(ov[6] * inv, ov[7] * inv)};
        __builtin_amdgcn_raw_buffer_store_b128(op, rws, WS_O + ((g * 8 + 2 * m + (qq >> 4)) * 16 + j) * 1024 + ((qq & 15) + 16 * (2 * hh + h)) * 16, 0, 16);
    }
    }
    WS2(4);
    VMWAIT(0); WS2(5);
    BAR();
    if (tid == 0) stg_sc1(ws, WS_FLAG + (1 * 256 + g * 16 + j) * 256, epoch);
    STAMP(9);
    if (w >= 4) {
        const int t = tid - 256; const unsigned o = WS_MODN + g * 8192 + t * 32; u32x4 v, v2;
        for (unsigned sp = 0; sp < SPIN_CAP; ++sp) { asm volatile("" ::: "memory"); v = __builtin_amdgcn_raw_buffer_load_b128(rws, o, 0, 16); v2 = __builtin_amdgcn_raw_buffer_load_b128(rws, o + 16, 0, 16);
            if (__all(v[1] == epoch && v[3] == epoch && v2[1] == epoch && v2[3] == epoch)) break; }
        u32x4 val = {v[0], v[2], v2[0], v2[2]};
        const int vec = t >> 6, c8 = (t & 63) * 8;
        if (vec == 1) lds_st<u32x4>(L_SB16 + c8 * 2, val);
        else if (vec == 2) {
#pragma unroll
            for (int i = 0; i < 4; ++i) { const unsigned u_ = val[i]; val[i] = pk2(1.f + bf_lo(u_), 1.f + bf_hi(u_)); }
            lds_st<u32x4>(L_GB16 + c8 * 2, val);
        } else if ((c8 >> 6) == ct) lds_st<u32x4>((vec == 0 ? L_GAO : L_GBO) + (c8 & 63) * 2, val);
    }
    if (w == 0) poll_flags(ws, WS_FLAG + (1 * 256 + g * 16) * 256, 16, epoch, lane);
    BAR();
    STAMP(10); WS3(0);
    u32x2 x1p0, x1p1;
    {
        FRESH(lane);
        const int gi = lane >> 4, q4 = (lane & 15) >> 2, p4 = lane & 3;
        const unsigned tokP = 128 * g + 64 * rh + 16 * tt + (lane & 15), colP = 64 * ct + 32 * kh + 4 * gi;
        u32x4 of[8];
#pragma unroll
        for (int s = 0; s < 8; ++s) of[s] = __builtin_amdgcn_raw_buffer_load_b128(rws, WS_O + ((g * 8 + 4 * rh + tt) * 16 + 8 * kh + s) * 1024 + lane * 16, 0, 16);
        const u32x2 xres0 = *(const u32x2*)(x + tokP * 512 + colP), xres1 = *(const u32x2*)(x + tokP * 512 + colP + 16);
        SBAR();
        f32x4 acc[4];
#pragma unroll
        for (int n = 0; n < 4; ++n) acc[n] = (f32x4){0.f, 0.f, 0.f, 0.f};
        const unsigned xv = 2 * (gi & 1) + (q4 >> 1), rowa = L_WO + (256 * kh + 16 * (gi >> 1) + 4 * (gi & 1) + q4) * 128 + p4 * 8;
        unsigned an[4]; bf16x8 wf[4];
#pragma unroll
        for (int n = 0; n < 4; ++n) { an[n] = rowa + ((n ^ xv) * 32); wf[n] = trfrag(an[n], an[n] + 1024); }
        const unsigned cl = (32 * kh + 4 * gi) * 2, cg = colP * 2;
        const u32x2 ga0 = lds_ld<u32x2>(L_GAO + cl), ga1 = lds_ld<u32x2>(L_GAO + cl + 32);
        const u32x2 gb0 = lds_ld<u32x2>(L_GB16 + cg), gb1 = lds_ld<u32x2>(L_GB16 + cg + 32), sb0 = lds_ld<u32x2>(L_SB16 + cg), sb1 = lds_ld<u32x2>(L_SB16 + cg + 32);
        SBAR();
#pragma unroll
        for (int s = 0; s < 8; ++s)
#pragma unroll
            for (int n = 0; n < 4; ++n) { acc[n] = MFMA16(wf[n], __builtin_bit_cast(bf16x8, of[s]), acc[n]); PIN(acc[n]); SBAR(); if (s < 7) wf[n] = trfrag(an[n] + (s + 1) * 4096, an[n] + (s + 1) * 4096 + 1024); SBAR(); }
        WS3(1);
        BAR();
        STAMP(11);
        const unsigned ex = w * 2048 + lane * 16;
        { f32x4 s0 = acc[0], s1 = acc[1], s2 = acc[2], s3 = acc[3]; PIN(s0); PIN(s1); PIN(s2); PIN(s3);
          lds_st<f32x4>(ex, kh ? s0 : s2); lds_st<f32x4>(ex + 1024, kh ? s1 : s3); acc[0] = kh ? s2 : s0; acc[1] = kh ? s3 : s1; }
        BAR();
        WS3(2);
        const unsigned ep = (w ^ 4) * 2048 + lane * 16;
        const f32x4 a0 = lds_ld<f32x4>(ep) + acc[0], a1 = lds_ld<f32x4>(ep + 1024) + acc[1];
        float xv1[8], gg[8], ss[8];
#pragma unroll
        for (int r = 0; r < 4; ++r) {
            const unsigned ua0 = ga0[r >> 1], ua1 = ga1[r >> 1], ux0 = xres0[r >> 1], ux1 = xres1[r >> 1], ug0 = gb0[r >> 1], ug1 = gb1[r >> 1], us0 = sb0[r >> 1], us1 = sb1[r >> 1];
            xv1[r] = rbf(fmaf((r & 1) ? bf_hi(ua0) : bf_lo(ua0), a0[r], (r & 1) ? bf_hi(ux0) : bf_lo(ux0))); xv1[4 + r] = rbf(fmaf((r & 1) ? bf_hi(ua1) : bf_lo(ua1), a1[r], (r & 1) ? bf_hi(ux1) : bf_lo(ux1)));
            gg[r] = (r & 1) ? bf_hi(ug0) : bf_lo(ug0); gg[4 + r] = (r & 1) ? bf_hi(ug1) : bf_lo(ug1);
            ss[r] = (r & 1) ? bf_hi(us0) : bf_lo(us0); ss[4 + r] = (r & 1) ? bf_hi(us1) : bf_lo(us1);
        }
        x1p0 = (u32x2){pk2(xv1[0], xv1[1]), pk2(xv1[2], xv1[3])}; x1p1 = (u32x2){pk2(xv1[4], xv1[5]), pk2(xv1[6], xv1[7])};
        float st[6] = {0.f, 0.f, 0.f, 0.f, 0.f, 0.f}, pg[8];
#pragma unroll
        for (int r = 0; r < 8; ++r) pg[r] = xv1[r] * gg[r];
        const u32x4 xg = {pk2(pg[0], pg[1]), pk2(pg[2], pg[3]), pk2(pg[4], pg[5]), pk2(pg[6], pg[7])};
        __builtin_amdgcn_raw_buffer_store_b128(xg, rws, WS_XG + ((g * 8 + 4 * rh + tt) * 16 + 2 * ct + kh) * 1024 + lane * 16, 0, 16);
#pragma unroll
        for (int r = 0; r < 8; ++r) { const float p = pg[r]; st[0] += xv1[r]; st[1] = fmaf(xv1[r], xv1[r], st[1]); st[2] += p; st[3] = fmaf(p, p, st[3]); st[4] = fmaf(p, gg[r], st[4]); st[5] = fmaf(p, ss[r], st[5]); }
#pragma unroll
        for (int i = 0; i < 6; ++i) st[i] = xsum32(xsum16(st[i]));
        if (kh == 1 && lane < 16) { lds_st<f32x4>(L_SCR + (tt * 16 + lane) * 32, (f32x4){st[0], st[1], st[2], st[3]}); lds_st<u32x2>(L_SCR + (tt * 16 + lane) * 32 + 16, (u32x2){__float_as_uint(st[4]), __float_as_uint(st[5])}); }
        WS3(3);
        BAR();
        if (kh == 0 && lane < 16) {
            const f32x4 o4 = lds_ld<f32x4>(L_SCR + (tt * 16 + lane) * 32); const u32x2 o2 = lds_ld<u32x2>(L_SCR + (tt * 16 + lane) * 32 + 16);
            const unsigned so = WS_ST + g * 24576, T = 4 * rh + tt;
            const u32x4 sa4 = {__float_as_uint(st[0] + o4[0]), __float_as_uint(st[1] + o4[1]), __float_as_uint(st[2] + o4[2]), __float_as_uint(st[3] + o4[3])};
            const u32x2 sb2 = {__float_as_uint(st[4] + __uint_as_float(o2[0])), __float_as_uint(st[5] + __uint_as_float(o2[1]))};
            __builtin_amdgcn_raw_buffer_store_b128(sa4, rws, so + ((T * 8 + ct) * 16 + lane) * 16, 0, 16);
            __builtin_amdgcn_raw_buffer_store_b64(sb2, rws, so + 16384 + ((T * 8 + ct) * 16 + lane) * 8, 0, 16);
        }
    }
    WS3(4);
    VMWAIT(0); WS3(5);
    BAR();
    if (tid == 0) stg_sc1(ws, WS_FLAG + (2 * 256 + g * 16 + j) * 256, epoch);
    STAMP(12);
    const int odd = j & 1;
    {
    FRESH(lane);
    const int gi = lane >> 4, q4 = (lane & 15) >> 2, p4 = lane & 3;
    const u32x2 b1v = *(const u32x2*)(b1 + 112 * j + (w > 0 ? w - 1 : 0) * 16 + 4 * gi);
    BAR();
    if (w > 0) {
        const int nt = w - 1; f32x4 acc = {0.f, 0.f, 0.f, 0.f};
        const unsigned ra = L_W1 + (4 * gi + q4) * 224 + p4 * 8 + nt * 32; const int tk = lane & 15;
        const unsigned va = (tk == 0 ? L_GB16 : L_SB16) + 8 * gi;
#pragma unroll
        for (int s = 0; s < 16; ++s) {
            const u32x2 lo = lds_ld<u32x2>(va + s * 64), hi = lds_ld<u32x2>(va + s * 64 + 32);
            u32x4 bv = {lo[0], lo[1], hi[0], hi[1]};
            if (tk == 2) bv = (u32x4){0x3F803F80u, 0x3F803F80u, 0x3F803F80u, 0x3F803F80u};
            if (tk > 2) bv = (u32x4){0u, 0u, 0u, 0u};
            acc = MFMA16(trfrag(ra + s * 7168, ra + s * 7168 + 3584), __builtin_bit_cast(bf16x8, bv), acc);
        }
        if (tk < 3) lds_st<f32x4>(L_UVW + (tk * 112 + nt * 16 + 4 * gi) * 4, acc);
        if (tk == 3) lds_st<f32x4>(L_UVW + (3 * 112 + nt * 16 + 4 * gi) * 4, (f32x4){bf_lo(b1v[0]), bf_hi(b1v[0]), bf_lo(b1v[1]), bf_hi(b1v[1])});
    } else {
        const u32x4 gq = lds_ld<u32x4>(L_GB16 + lane * 16), sq = lds_ld<u32x4>(L_SB16 + lane * 16);
        float c5[5] = {0.f, 0.f, 0.f, 0.f, 0.f};
#pragma unroll
        for (int i = 0; i < 4; ++i) { const unsigned ug = gq[i], us = sq[i]; const float g0 = bf_lo(ug), g1 = bf_hi(ug), s0 = bf_lo(us), s1 = bf_hi(us);
            c5[0] += g0 + g1; c5[1] += s0 + s1; c5[2] += g0 * g0 + g1 * g1; c5[3] += g0 * s0 + g1 * s1; c5[4] += s0 * s0 + s1 * s1; }
#pragma unroll
        for (int i = 0; i < 5; ++i) c5[i] = xsum32(xsum16(rowsum16(c5[i])));
        if (lane == 0) { lds_st<f32x4>(L_CONST, (f32x4){c5[0], c5[1], c5[2], c5[3]}); lds_st<float>(L_CONST + 16, c5[4]); }
        poll_flags(ws, WS_FLAG + (2 * 256 + g * 16) * 256, 16, epoch, lane);
    }
    }
    BAR();
    STAMP(13);
    {
        FRESH(lane);
        const int gi = lane >> 4, q4 = (lane & 15) >> 2, p4 = lane & 3;
        const int Tw = 2 * m + kh;
        u32x4 xf[2][8];
        const unsigned xo0 = WS_XG + (g * 8 + Tw) * 16384 + kh * 8192 + lane * 16, xo1 = WS_XG + (g * 8 + (Tw ^ 1)) * 16384 + kh * 8192 + lane * 16;
        constexpr int WIN = 5;
#pragma unroll
        for (int s = 0; s < WIN; ++s) { xf[0][s] = __builtin_amdgcn_raw_buffer_load_b128(rws, xo0 + s * 1024, 0, 16); xf[1][s] = __builtin_amdgcn_raw_buffer_load_b128(rws, xo1 + s * 1024, 0, 16); }
        const unsigned so = WS_ST + g * 24576 + Tw * 2048, sob = WS_ST + g * 24576 + 16384 + Tw * 1024;
        u32x4 sA0, sA1; u32x2 sB0, sB1;
        SBAR(); WS4(0);
        f32x4 acc[7], acb[7]; bf16x8 wf[7];
        const unsigned ra = L_W1 + (256 * kh + 4 * gi + q4) * 224 + p4 * 8, raA = ra + 32 * odd, raB = ra + (odd ? 0 : 192);
#define W1A(s, u) (((u) < 6 ? raA : raB) + (s) * 7168 + ((u) < 6 ? (u) * 32 : 0))
#pragma unroll
        for (int u = 0; u < 7; ++u) { acc[u] = (f32x4){0.f, 0.f, 0.f, 0.f}; acb[u] = acc[u]; wf[u] = trfrag(W1A(0, u), W1A(0, u) + 3584); }
        SBAR();
#pragma unroll
        for (int s = 0; s < 8; ++s) {
            if (s + WIN < 8) { xf[0][s + WIN] = __builtin_amdgcn_raw_buffer_load_b128(rws, xo0 + (s + WIN) * 1024, 0, 16); xf[1][s + WIN] = __builtin_amdgcn_raw_buffer_load_b128(rws, xo1 + (s + WIN) * 1024, 0, 16); }
            if (s == 4) { sA0 = __builtin_amdgcn_raw_buffer_load_b128(rws, so + ((2 * gi) * 16 + (lane & 15)) * 16, 0, 16); sA1 = __builtin_amdgcn_raw_buffer_load_b128(rws, so + ((2 * gi + 1) * 16 + (lane & 15)) * 16, 0, 16); }
            if (s == 5) { sB0 = __builtin_amdgcn_raw_buffer_load_b64(rws, sob + ((2 * gi) * 16 + (lane & 15)) * 8, 0, 16); sB1 = __builtin_amdgcn_raw_buffer_load_b64(rws, sob + ((2 * gi + 1) * 16 + (lane & 15)) * 8, 0, 16); }
            SBAR();
#pragma unroll
            for (int u = 0; u < 7; ++u) { acc[u] = MFMA16(wf[u], __builtin_bit_cast(bf16x8, xf[0][s]), acc[u]); PIN(acc[u]); SBAR();
                acb[u] = MFMA16(wf[u], __builtin_bit_cast(bf16x8, xf[1][s]), acb[u]); PIN(acb[u]); SBAR();
                if (s < 7) wf[u] = trfrag(W1A(s + 1, u), W1A(s + 1, u) + 3584); SBAR(); }
            if (s == 7) WS4(1);
        }
#undef W1A
        float ca, cb, cc, r2;
        { float S[6];
#pragma unroll
          for (int i = 0; i < 4; ++i) S[i] = xsum32(xsum16(__uint_as_float(sA0[i]) + __uint_as_float(sA1[i])));
#pragma unroll
          for (int i = 0; i < 2; ++i) S[4 + i] = xsum32(xsum16(__uint_as_float(sB0[i]) + __uint_as_float(sB1[i])));
          const f32x4 cc4 = lds_ld<f32x4>(L_CONST); const float SB2 = lds_ld<float>(L_CONST + 16);
          const float iD = 1.f / 512.f, m1 = S[0] * iD, r1 = __builtin_amdgcn_rsqf(fmaxf(S[1] * iD - m1 * m1, 0.f) + 1e-5f);
          const float m2 = (r1 * (S[2] - m1 * cc4[0]) + cc4[1]) * iD;
          const float ez2 = (r1 * r1 * (S[3] - 2.f * m1 * S[4] + m1 * m1 * cc4[2]) + 2.f * r1 * (S[5] - m1 * cc4[3]) + SB2) * iD;
          r2 = __builtin_amdgcn_rsqf(fmaxf(ez2 - m2 * m2, 0.f) + 1e-5f);
          ca = r1 * r2; cb = -ca * m1; cc = -r2 * m2; PIN(ca); PIN(cb); PIN(cc); PIN(r2); }
        const unsigned uo = L_UVW + 16 * gi + 64 * odd, uo6 = L_UVW + 16 * gi + (odd ? 0 : 384);
        f32x4 uu = lds_ld<f32x4>(uo), vv = lds_ld<f32x4>(uo + 448), ww = lds_ld<f32x4>(uo + 896), bb = lds_ld<f32x4>(uo + 1344);
        f32x4 u2 = lds_ld<f32x4>(uo + 64), v2 = lds_ld<f32x4>(uo + 64 + 448), w2v = lds_ld<f32x4>(uo + 64 + 896), b2v = lds_ld<f32x4>(uo + 64 + 1344);
        BAR();
        WS4(2);
        { const unsigned ex = 101376 + w * 7168 + lane * 16;
#pragma unroll
          for (int u = 0; u < 7; ++u) lds_st<f32x4>(ex + u * 1024, acb[u]); }
        WS4(3);
        BAR();
        WS4(4);
        { const unsigned ep = 101376 + (w ^ 4) * 7168 + lane * 16;
#pragma unroll
          for (int u = 0; u < 7; ++u) acc[u] += lds_ld<f32x4>(ep + u * 1024); }
        STAMP(14);
        const unsigned hb = WS_H + (g * 8 + Tw) * 57344, s0 = (7 * j + odd) >> 1;
        const f32x2 kA = {-0.044715f * 2.302208198f, -0.044715f * 2.302208198f}, kB = {-2.302208198f, -2.302208198f}, one2 = {1.f, 1.f};
#define LO2(v) ((f32x2){(v)[0], (v)[1]})
#define HI2(v) ((f32x2){(v)[2], (v)[3]})
#define PRE2(a_, u_, v_, w_, b_) (ca * (a_) + (cb * (u_) + (r2 * (v_) + (cc * (w_) + (b_)))))
#define GELU(N, p, y) { f32x2 z_[N], e_[N]; \
            _Pragma("unroll") for (int r = 0; r < N; ++r) { z_[r] = p[r] * p[r]; PIN(z_[r]); } \
            _Pragma("unroll") for (int r = 0; r < N; ++r) { z_[r] = z_[r] * kA + kB; PIN(z_[r]); } \
            _Pragma("unroll") for (int r = 0; r < N; ++r) { z_[r] = z_[r] * p[r]; PIN(z_[r]); } \
            SBAR(); _Pragma("unroll") for (int r = 0; r < N; ++r) { e_[r] = (f32x2){__builtin_amdgcn_exp2f(z_[r][0]), __builtin_amdgcn_exp2f(z_[r][1])}; PIN(e_[r]); } \
            SBAR(); _Pragma("unroll") for (int r = 0; r < N; ++r) { e_[r] = e_[r] + one2; PIN(e_[r]); } \
            SBAR(); _Pragma("unroll") for (int r = 0; r < N; ++r) { e_[r] = (f32x2){__builtin_amdgcn_rcpf(e_[r][0]), __builtin_amdgcn_rcpf(e_[r][1])}; PIN(e_[r]); } \
            SBAR(); _Pragma("unroll") for (int r = 0; r < N; ++r) y[r] = p[r] * e_[r]; }
#pragma unroll
        for (int u = 0; u < 6; u += 2) {
            f32x2 pre[4], y[4];
            pre[0] = PRE2(LO2(acc[u]), LO2(uu), LO2(vv), LO2(ww), LO2(bb)); pre[1] = PRE2(HI2(acc[u]), HI2(uu), HI2(vv), HI2(ww), HI2(bb));
            pre[2] = PRE2(LO2(acc[u + 1]), LO2(u2), LO2(v2), LO2(w2v), LO2(b2v)); pre[3] = PRE2(HI2(acc[u + 1]), HI2(u2), HI2(v2), HI2(w2v), HI2(b2v));
            PIN(pre[0]); PIN(pre[1]); PIN(pre[2]); PIN(pre[3]);
            SBAR();
            { const unsigned o = u < 4 ? uo + (u + 2) * 64 : uo6; uu = lds_ld<f32x4>(o); vv = lds_ld<f32x4>(o + 448); ww = lds_ld<f32x4>(o + 896); bb = lds_ld<f32x4>(o + 1344); }
            if (u < 4) { const unsigned o = uo + (u + 3) * 64; u2 = lds_ld<f32x4>(o); v2 = lds_ld<f32x4>(o + 448); w2v = lds_ld<f32x4>(o + 896); b2v = lds_ld<f32x4>(o + 1344); }
            SBAR();
            GELU(4, pre, y)
            __builtin_amdgcn_raw_buffer_store_b128((u32x4){pk2(y[0][0], y[0][1]), pk2(y[1][0], y[1][1]), pk2(y[2][0], y[2][1]), pk2(y[3][0], y[3][1])}, rws, hb + (s0 + (u >> 1)) * 1024 + lane * 16, 0, 16);
            SBAR();
        }
        {   f32x2 pre[2], y[2];
            pre[0] = PRE2(LO2(acc[6]), LO2(uu), LO2(vv), LO2(ww), LO2(bb)); pre[1] = PRE2(HI2(acc[6]), HI2(uu), HI2(vv), HI2(ww), HI2(bb));
            GELU(2, pre, y)
            __builtin_amdgcn_raw_buffer_store_b64((u32x2){pk2(y[0][0], y[0][1]), pk2(y[1][0], y[1][1])}, rws, hb + ((7 * j + (odd ? 0 : 6)) >> 1) * 1024 + odd * 512 + lane * 8, 0, 16);
        }
#undef GELU
#undef PRE2
#undef LO2
#undef HI2
        WS4(5);
    }
    VMWAIT(0);
    BAR();
    if (tid == 0) stg_sc1(ws, WS_FLAG + (3 * 256 + g * 16 + j) * 256, epoch);
    STAMP(15);
    u32x4 park[10];
    { FRESH(lane);
      const unsigned sw = (lane >> 3) * 1024u + ct * 128u + (((lane & 7) >> 1) ^ ((lane >> 4) & 3)) * 32u + (lane & 1) * 16u;
      const char* p0 = (const char*)w2 + (size_t)(56 * (w >> 2) + 36 + (w & 3)) * 8192, * p1 = p0 + (size_t)112 * 8192;
#pragma unroll
      for (int i = 0; i < 5; ++i) { park[2 * i] = *(const u32x4*)(p0 + i * 32768 + sw); park[2 * i + 1] = *(const u32x4*)(p1 + i * 32768 + sw); }
      SBAR();
      if (w == 0) poll_flags(ws, WS_FLAG + (3 * 256 + g * 16) * 256, 16, epoch, lane); }
    SBAR();
    BAR();
    STAMP(16); WS5(0);
    {
        constexpr int WIN = 3;
        FRESH(lane);
        const int gi = lane >> 4, q4 = (lane & 15) >> 2, p4 = lane & 3;
        const unsigned tokP = 128 * g + 64 * rh + 16 * tt + (lane & 15), colP = 64 * ct + 32 * kh + 4 * gi;
        const int kq = (w & 1) | (kh << 1);
        const unsigned hb0 = WS_H + (g * 8 + 4 * rh + tt) * 57344 + kq * 14 * 1024, hb1 = WS_H + (g * 8 + 4 * rh + (tt ^ 1)) * 57344 + kq * 14 * 1024;
        u32x4 hf[2][14];
#define HLD1(hb_, dst, s) { if ((s) % 7 == 3) { const u32x2 lo = __builtin_amdgcn_raw_buffer_load_b64(rws, hb_ + (s) * 1024 + lane * 8, 0, 16), hi = __builtin_amdgcn_raw_buffer_load_b64(rws, hb_ + (s) * 1024 + 512 + lane * 8, 0, 16); dst = (u32x4){lo[0], lo[1], hi[0], hi[1]}; } \
                    else dst = __builtin_amdgcn_raw_buffer_load_b128(rws, hb_ + (s) * 1024 + lane * 16, 0, 16); }
#define HLOAD(s) { HLD1(hb0, hf[0][s], s) HLD1(hb1, hf[1][s], s) }
#pragma unroll
        for (int s = 0; s < WIN; ++s) HLOAD(s)
        SBAR();
        f32x4 acc[2][4]; bf16x8 wf[4];
        const unsigned xv = (2 * gi + (q4 >> 1)) & 3, rowa = kq * 4096 + (4 * gi + q4) * 128 + p4 * 8;
        unsigned an[4];
#pragma unroll
        for (int n = 0; n < 4; ++n) { an[n] = rowa + (((n ^ (2 * kh)) ^ xv) * 32); acc[0][n] = (f32x4){0.f, 0.f, 0.f, 0.f}; acc[1][n] = acc[0][n]; }
#define W2A(s, n) (an[n] + (((s) % 9) & 3) * 16384)
#pragma unroll
        for (int n = 0; n < 4; ++n) wf[n] = trfrag(W2A(0, n), W2A(0, n) + 2048);
        const unsigned cl = (32 * kh + 4 * gi) * 2;
        const u32x2 g0 = lds_ld<u32x2>(L_GBO + cl), g1 = lds_ld<u32x2>(L_GBO + cl + 32);
        u32x2 bb0, bb1;
        SBAR();
#pragma unroll
        for (int s = 0; s < 14; ++s) {
            if (s == 11) { bb0 = *(const u32x2*)(b2 + colP); bb1 = *(const u32x2*)(b2 + colP + 16); }
            if (s == 1) WS5(1); if (s == 5) WS5(2); if (s == 8) WS5(3);
            if (s == 5) BAR();
            if (s == 8) BAR();
            if (s == 8) WS5(4);
            if (s + WIN < 14) HLOAD(s + WIN)
            SBAR();
#pragma unroll
            for (int n = 0; n < 4; ++n) { acc[0][n] = MFMA16(wf[n], __builtin_bit_cast(bf16x8, hf[0][s]), acc[0][n]); PIN(acc[0][n]); SBAR();
                acc[1][n] = MFMA16(wf[n], __builtin_bit_cast(bf16x8, hf[1][s]), acc[1][n]); PIN(acc[1][n]); SBAR();
                if (s >= 5 && s <= 7 && 4 * (s - 5) + n < 10) { const int i = 4 * (s - 5) + n; lds_st<u32x4>((i >> 1) * 16384 + (w + 8 * (i & 1)) * 1024 + lane * 16, park[i]); }
                if (s == 3 || s == 7 || s == 12) { an[n] += 65536; PIN(an[n]); } if (s == 8) { an[n] -= 131072; PIN(an[n]); }
                if (s < 13) wf[n] = trfrag(W2A(s + 1, n), W2A(s + 1, n) + 2048); SBAR(); }
        }
#undef W2A
#undef HLOAD
#undef HLD1
        WS5(5);
        BAR();
        STAMP(17);
        { const unsigned ex = w * 6144 + lane * 16;
          lds_st<f32x4>(ex, acc[0][2]); lds_st<f32x4>(ex + 1024, acc[0][3]); lds_st<f32x4>(ex + 2048, acc[1][0]); lds_st<f32x4>(ex + 3072, acc[1][1]); lds_st<f32x4>(ex + 4096, acc[1][2]); lds_st<f32x4>(ex + 5120, acc[1][3]); }
        BAR();
        const unsigned e4 = (w ^ 4) * 6144 + lane * 16, e1 = (w ^ 1) * 6144 + 2048 + lane * 16, e5 = (w ^ 5) * 6144 + 4096 + lane * 16;
        const f32x4 a0 = (acc[0][0] + lds_ld<f32x4>(e4)) + (lds_ld<f32x4>(e1) + lds_ld<f32x4>(e5)), a1 = (acc[0][1] + lds_ld<f32x4>(e4 + 1024)) + (lds_ld<f32x4>(e1 + 1024) + lds_ld<f32x4>(e5 + 1024));
        float y0[4], y1[4];
#pragma unroll
        for (int r = 0; r < 4; ++r) {
            const unsigned ug0 = g0[r >> 1], ug1 = g1[r >> 1], ub0 = bb0[r >> 1], ub1 = bb1[r >> 1], ux0 = x1p0[r >> 1], ux1 = x1p1[r >> 1];
            y0[r] = fmaf((r & 1) ? bf_hi(ug0) : bf_lo(ug0), a0[r] + ((r & 1) ? bf_hi(ub0) : bf_lo(ub0)), (r & 1) ? bf_hi(ux0) : bf_lo(ux0));
            y1[r] = fmaf((r & 1) ? bf_hi(ug1) : bf_lo(ug1), a1[r] + ((r & 1) ? bf_hi(ub1) : bf_lo(ub1)), (r & 1) ? bf_hi(ux1) : bf_lo(ux1));
        }
        *(u32x2*)(out + tokP * 512 + colP) = (u32x2){pk2(y0[0], y0[1]), pk2(y0[2], y0[3])};
        *(u32x2*)(out + tokP * 512 + colP + 16) = (u32x2){pk2(y1[0], y1[1]), pk2(y1[2], y1[3])};
    }
    if (w == 0) { asm volatile("s_waitcnt vmcnt(0)" ::: "memory"); const unsigned long long te = rt(); if (lane == 0) stg64_sc1(ws, 0x1F10000 + b * 8, ((unsigned long long)epoch << 32) | (unsigned)te); }
    if (b == 0 && w == 0) {
        unsigned smin = 0xffffffffu, smax = 0, emax = 0; const unsigned base = (unsigned)dbg_t0 - 2000u;
        for (int i = 0; i < 4; ++i) { unsigned long long a, e2;
            for (unsigned sp = 0; sp < SPIN_CAP; ++sp) { a = ldg64_sc1(ws, 0x1F00000 + (i * 64 + lane) * 8); e2 = ldg64_sc1(ws, 0x1F10000 + (i * 64 + lane) * 8); if (__all((unsigned)(a >> 32) == epoch && (unsigned)(e2 >> 32) == epoch)) break; }
            const unsigned st = (unsigned)a - base, en = (unsigned)e2 - base; smin = st < smin ? st : smin; smax = st > smax ? st : smax; emax = en > emax ? en : emax; }
        for (int o = 32; o; o >>= 1) { unsigned v = __shfl_xor(smin, o); smin = v < smin ? v : smin; v = __shfl_xor(smax, o); smax = v > smax ? v : smax; v = __shfl_xor(emax, o); emax = v > emax ? v : emax; }
        unsigned long long dl = 0; if (dl > 20000) dl = 20000; const unsigned long long t_end = rt() + dl; while (rt() < t_end) __builtin_amdgcn_s_sleep(1);
    }
    if (b == 0 && w == 0) {
        for (unsigned sp = 0; sp < SPIN_CAP; ++sp) { asm volatile("" ::: "memory"); const u32x4 v = __builtin_amdgcn_raw_buffer_load_b128(rws, WS_HELLO + lane * 16, 0, 16); if (__all(v[0] == epoch && v[1] == epoch && v[2] == epoch && v[3] == epoch)) break; }
        if (lane == 0) stg_sc1(ws, WS_E, epoch);
    }
    STAMP(18); STAMPC(21);
    STAMP_OUT();
    asm volatile("fb_code_end:");
}

extern "C" void kernel_launch(void* const* d_in, const int* in_sizes, int n_in, void* d_out, int out_size, void* d_ws, size_t ws_size, hipStream_t stream) {
    (void)hipFuncSetAttribute((const void*)fused_block, hipFuncAttributeMaxDynamicSharedMemorySize, LDS_BYTES);
    fused_block<<<256, 576, LDS_BYTES, stream>>>((const u16*)d_in[0], (const u16*)d_in[1], (const u16*)d_in[2], (const u16*)d_in[4], (const u16*)d_in[5], (const u16*)d_in[6], (const u16*)d_in[7],
                                                 (const u16*)d_in[8], (const u16*)d_in[9], (u16*)d_out, (char*)d_ws);
}
```

```cpp
#include <hip/hip_runtime.h>
#include <hip/hip_bf16.h>
#include <stdint.h>

typedef unsigned short u16;
typedef short bf16x8 __attribute__((ext_vector_type(8)));
typedef short s16x4 __attribute__((ext_vector_type(4)));
typedef float f32x4 __attribute__((ext_vector_type(4)));
typedef float f32x2 __attribute__((ext_vector_type(2)));
typedef float f32x16 __attribute__((ext_vector_type(16)));
typedef unsigned u32x4 __attribute__((ext_vector_type(4)));
typedef unsigned u32x2 __attribute__((ext_vector_type(2)));
typedef __bf16 bf2 __attribute__((ext_vector_type(2)));
#define LAS __attribute__((address_space(3)))
#define GAS __attribute__((address_space(1)))
#define DI __device__ __forceinline__

#define TS 0

constexpr unsigned WS_E = 0, WS_HELLO = 4096, WS_FLAG = 8192;
constexpr unsigned WS_MODU = 0x50000, WS_MODN = 0x60000;
constexpr unsigned WS_PART = 0x80000;
constexpr unsigned WS_KV = 0x100000, WS_O = 0x500000, WS_XG = 0x700000, WS_ST = 0x900000, WS_H = 0xA00000, WS_TS = 0x2000000;
constexpr unsigned WS_BYTES = 0x2100000;
constexpr unsigned L_X = 0, L_WQKV = 57344, L_RED = 155648, L_SAG = 156672, L_STAT = 160768;
constexpr unsigned L_QS = 65536, L_KIMG = 98304, L_VIMG = 122880, L_WO = 0, L_W1 = 44032;
constexpr unsigned L_GB16 = 158720, L_SB16 = 159744, L_UVW = 160768, L_CONST = 162560, L_GAO = 162592, L_GBO = 162720, L_ONES = 162848, L_DUMMY = 162880;
constexpr unsigned L_TAGM = L_RED + 512, L_TAGL = L_RED + 544, L_TAGC = L_RED + 560;
constexpr unsigned L_RED2 = 81920;
constexpr unsigned L_SCR = 32768;
constexpr unsigned LDS_BYTES = 163840;
constexpr unsigned SPIN_CAP = 1u << 16;

DI float bf_lo(unsigned u) { return __uint_as_float(u << 16); }
DI float bf_hi(unsigned u) { return __uint_as_float(u & 0xffff0000u); }
DI unsigned pk2(float a, float b) { bf2 v = {(__bf16)a, (__bf16)b}; return __builtin_bit_cast(unsigned, v); }
DI float rbf(float a) { return bf_lo(pk2(a, a)); }
DI void dma16(unsigned voff, const void* sbase, unsigned lds) { asm volatile("s_mov_b32 m0, %1\n\ts_nop 0\n\tglobal_load_lds_dwordx4 %0, %2" ::"v"(voff), "s"(lds), "s"(sbase) : "memory"); }
DI void dma16c(unsigned voff, const void* sbase, unsigned lds) { asm volatile("s_mov_b32 m0, %1\n\ts_nop 0\n\tglobal_load_lds_dwordx4 %0, %2 sc1" ::"v"(voff), "s"(lds), "s"(sbase) : "memory"); }
DI void dma4(unsigned v0, unsigned v1, unsigned v2, unsigned v3, const void* sbase, unsigned lds) {
    asm volatile("s_mov_b32 m0, %5\n\ts_nop 0\n\tglobal_load_lds_dwordx4 %0, %4\n\tglobal_load_lds_dwordx4 %1, %4 offset:1024\n\tglobal_load_lds_dwordx4 %2, %4 offset:2048\n\tglobal_load_lds_dwordx4 %3, %4 offset:3072"
                 ::"v"(v0), "v"(v1), "v"(v2), "v"(v3), "s"(sbase), "s"(lds) : "memory"); }
DI void dma3(unsigned v0, unsigned v1, unsigned v2, const void* sbase, unsigned lds) {
    asm volatile("s_mov_b32 m0, %4\n\ts_nop 0\n\tglobal_load_lds_dwordx4 %0, %3\n\tglobal_load_lds_dwordx4 %1, %3 offset:1024\n\tglobal_load_lds_dwordx4 %2, %3 offset:2048"
                 ::"v"(v0), "v"(v1), "v"(v2), "s"(sbase), "s"(lds) : "memory"); }
DI void dma4lin_c(unsigned v, const void* sbase, unsigned lds) {
    asm volatile("s_mov_b32 m0, %2\n\ts_nop 0\n\tglobal_load_lds_dwordx4 %0, %1 sc1\n\tglobal_load_lds_dwordx4 %0, %1 offset:1024 sc1\n\tglobal_load_lds_dwordx4 %0, %1 offset:2048 sc1\n\tglobal_load_lds_dwordx4 %0, %1 offset:3072 sc1"
                 ::"v"(v), "s"(sbase), "s"(lds) : "memory"); }
DI void lds_wait_tags(unsigned a, int n, unsigned tag) {
    for (unsigned sp = 0; sp < SPIN_CAP; ++sp) { asm volatile("" ::: "memory"); const unsigned t0 = *(const volatile LAS unsigned*)(size_t)a, t1 = *(const volatile LAS unsigned*)(size_t)(a + 4 * (n - 1));
        if (__builtin_amdgcn_readfirstlane(t0) == tag && __builtin_amdgcn_readfirstlane(t1) == tag) break; __builtin_amdgcn_s_sleep(1); }
    asm volatile("" ::: "memory"); }
DI void warm4(unsigned voff, const void* sbase) { asm volatile("s_mov_b32 m0, %1\n\ts_nop 0\n\tglobal_load_lds_dword %0, %2" ::"v"(voff), "s"(L_DUMMY), "s"(sbase) : "memory"); }
#define WSP 4
#define WS1(i) do { if (WSP == 1) WSTAMP(i); } while (0)
#define WS2(i) do { if (WSP == 2) WSTAMP(i); } while (0)
#define WS3(i) do { if (WSP == 3) WSTAMP(i); } while (0)
#define WS4(i) do { if (WSP == 4) WSTAMP(i); } while (0)
#define WS5(i) do { if (WSP == 5) WSTAMP(i); } while (0)
#define WS6(i) do { if (WSP == 6) WSTAMP(i); } while (0)
#define FRESH(v) int v = threadIdx.x & 63; asm volatile("" : "+v"(v))
#define SBAR() __builtin_amdgcn_sched_barrier(0)
#define PIN(x) asm volatile("" : "+v"(x))
#define BAR() asm volatile("s_waitcnt lgkmcnt(0)\n\ts_barrier" ::: "memory")
#define VMWAIT(n) asm volatile("s_waitcnt vmcnt(" #n ")" ::: "memory")
DI unsigned ldg_sc1(const char* ws, unsigned off) { return __hip_atomic_load((GAS unsigned*)(ws + off), __ATOMIC_RELAXED, __HIP_MEMORY_SCOPE_AGENT); }
DI unsigned long long ldg64_sc1(const char* ws, unsigned off) { return __hip_atomic_load((GAS unsigned long long*)(ws + off), __ATOMIC_RELAXED, __HIP_MEMORY_SCOPE_AGENT); }
DI void stg_sc1(char* ws, unsigned off, unsigned v) { __hip_atomic_store((GAS unsigned*)(ws + off), v, __ATOMIC_RELAXED, __HIP_MEMORY_SCOPE_AGENT); }
DI void stg64_sc1(char* ws, unsigned off, unsigned long long v) { __hip_atomic_store((GAS unsigned long long*)(ws + off), v, __ATOMIC_RELAXED, __HIP_MEMORY_SCOPE_AGENT); }
template <class T> DI T lds_ld(unsigned a) { return *(const LAS T*)(size_t)a; }
template <class T> DI void lds_st(unsigned a, T v) { *(LAS T*)(size_t)a = v; }
DI s16x4 lds_tr(unsigned a) { return __builtin_amdgcn_ds_read_tr16_b64_v4i16((LAS s16x4*)(size_t)a); }
DI bf16x8 trfrag(unsigned a0, unsigned a1) { s16x4 lo = lds_tr(a0), hi = lds_tr(a1); return __builtin_shufflevector(lo, hi, 0, 1, 2, 3, 4, 5, 6, 7); }
#define MFMA32(a, b, c) __builtin_amdgcn_mfma_f32_32x32x16_bf16((a), (b), (c), 0, 0, 0)
#define MFMA16(a, b, c) __builtin_amdgcn_mfma_f32_16x16x32_bf16((a), (b), (c), 0, 0, 0)
template <int N> DI float bcast(float v) { return __builtin_bit_cast(float, __builtin_amdgcn_mov_dpp(__builtin_bit_cast(int, v), 0x150 + N, 0xf, 0xf, true)); }
template <int N> DI float modu(float t, float gv, float sv) {
#pragma clang fp contract(off)
    const float p = bcast<N>(gv) * t; return bcast<N>(sv) + p; }
template <int N> DI unsigned hdw(unsigned x, float r, float n, float gv, float c, float d) {
    unsigned o; float a, b2;
    asm("v_lshlrev_b32 %1, 16, %5\n\tv_and_b32 %2, 0xffff0000, %5\n\tv_fma_f32 %1, %1, %6, %7\n\tv_fma_f32 %2, %2, %6, %7\n\t"
        "v_fmac_f32_dpp %3, %8, %1 row_newbcast:%c9 row_mask:0xf bank_mask:0xf\n\tv_fmac_f32_dpp %4, %8, %2 row_newbcast:%c10 row_mask:0xf bank_mask:0xf\n\t"
        "v_cvt_pk_bf16_f32 %0, %3, %4"
        : "=v"(o), "=&v"(a), "=&v"(b2), "+v"(c), "+v"(d) : "v"(x), "v"(r), "v"(n), "v"(gv), "i"(N), "i"(N + 1));
    return o; }
template <int C> DI float dppf(float v) { return __builtin_bit_cast(float, __builtin_amdgcn_update_dpp(0, __builtin_bit_cast(int, v), C, 0xf, 0xf, true)); }
DI float rowsum16(float v) { v += dppf<0xB1>(v); v += dppf<0x4E>(v); v += dppf<0x141>(v); v += dppf<0x140>(v); return v; }
DI float xsum32(float v) { auto r = __builtin_amdgcn_permlane32_swap(__float_as_uint(v), __float_as_uint(v), false, false); return __uint_as_float(r[0]) + __uint_as_float(r[1]); }
DI float xmax32(float v) { auto r = __builtin_amdgcn_permlane32_swap(__float_as_uint(v), __float_as_uint(v), false, false); return fmaxf(__uint_as_float(r[0]), __uint_as_float(r[1])); }
DI float xsum16(float v) { auto r = __builtin_amdgcn_permlane16_swap(__float_as_uint(v), __float_as_uint(v), false, false); return __uint_as_float(r[0]) + __uint_as_float(r[1]); }
DI unsigned long long mt() { unsigned long long t; asm volatile("s_memtime %0\n\ts_waitcnt lgkmcnt(0)" : "=s"(t)::"memory"); return t; }
DI unsigned long long rt() { unsigned long long t; asm volatile("s_memrealtime %0\n\ts_waitcnt lgkmcnt(0)" : "=s"(t)::"memory"); return t; }
#define STAMP(i) do {} while (0)
#define STAMP_OUT() do {} while (0)
#define STAMPC(i) do {} while (0)
#define WSTAMP(i) do {} while (0)

DI void poll_flags(const char* ws, unsigned off, int n, unsigned epoch, int lane) {
    const unsigned o = off + (unsigned)(lane < n ? lane : 0) * 256u;
    for (unsigned sp = 0; sp < SPIN_CAP; ++sp) { unsigned f = ldg_sc1(ws, o); if (__all(f == epoch)) break; }
}

DI void mod_part(const u16* __restrict__ cvec, const u16* __restrict__ wmod, char* ws, unsigned epoch, int lane, int slice, int kp) {
    const unsigned row0 = 64 * kp + 4 * (lane >> 2), o = row0 * 6144u + (unsigned)slice * 64u + (lane & 3) * 16u;
    const u32x2 cv = *(const u32x2*)(cvec + row0);
    u32x4 wm[4];
#pragma unroll
    for (int i = 0; i < 4; ++i) wm[i] = *(const u32x4*)((const char*)wmod + o + i * 6144u);
    SBAR();
    unsigned sp[2];
#pragma unroll
    for (int r = 0; r < 2; ++r) { const unsigned cu = cv[r]; const float c0 = bf_lo(cu), c1 = bf_hi(cu);
        sp[r] = pk2(c0 * __builtin_amdgcn_rcpf(1.f + __builtin_amdgcn_exp2f(-1.44269504089f * c0)), c1 * __builtin_amdgcn_rcpf(1.f + __builtin_amdgcn_exp2f(-1.44269504089f * c1))); }
    float a[8] = {0.f, 0.f, 0.f, 0.f, 0.f, 0.f, 0.f, 0.f};
#pragma unroll
    for (int r = 0; r < 2; ++r)
#pragma unroll
        for (int i = 0; i < 4; ++i) { const unsigned u0 = wm[2 * r][i], u1 = wm[2 * r + 1][i], s_ = sp[r];
            const unsigned lo = __builtin_amdgcn_perm(u1, u0, 0x05040100u), hi = __builtin_amdgcn_perm(u1, u0, 0x07060302u);
            a[2 * i] = __builtin_amdgcn_fdot2_f32_bf16(__builtin_bit_cast(bf2, lo), __builtin_bit_cast(bf2, s_), a[2 * i], false);
            a[2 * i + 1] = __builtin_amdgcn_fdot2_f32_bf16(__builtin_bit_cast(bf2, hi), __builtin_bit_cast(bf2, s_), a[2 * i + 1], false); }
    float b4[4], b2[2];
#pragma unroll
    for (int i = 0; i < 4; ++i) { auto r = __builtin_amdgcn_permlane32_swap(__float_as_uint(a[i]), __float_as_uint(a[i + 4]), false, false); b4[i] = __uint_as_float(r[0]) + __uint_as_float(r[1]); }
#pragma unroll
    for (int i = 0; i < 2; ++i) { auto r = __builtin_amdgcn_permlane16_swap(__float_as_uint(b4[i]), __float_as_uint(b4[i + 2]), false, false); float v = __uint_as_float(r[0]) + __uint_as_float(r[1]);
        v += dppf<0x124>(v); v += dppf<0x128>(v); b2[i] = v; }
    const __amdgpu_buffer_rsrc_t rws = __builtin_amdgcn_make_buffer_rsrc(ws, 0, WS_BYTES, 0x00020000);
    if ((lane & 12) == 0) __builtin_amdgcn_raw_buffer_store_b128((u32x4){__float_as_uint(b2[0]), epoch, __float_as_uint(b2[1]), epoch}, rws,
                                                                 WS_PART + (slice * 8 + kp) * 256 + ((lane & 3) * 4 + (lane >> 5) * 2 + ((lane >> 4) & 1)) * 16, 0, 16);
}
struct PartSum { u32x4 v0, v1; };
DI void mod_sum_load(PartSum& p, char* ws, int lane, int slice) {
    const __amdgpu_buffer_rsrc_t rws = __builtin_amdgcn_make_buffer_rsrc(ws, 0, WS_BYTES, 0x00020000);
    const unsigned o = WS_PART + (slice * 8 + (lane >> 3)) * 256 + (lane & 7) * 16;
    p.v0 = __builtin_amdgcn_raw_buffer_load_b128(rws, o, 0, 16); p.v1 = __builtin_amdgcn_raw_buffer_load_b128(rws, o + 128, 0, 16); }
DI void mod_sum_finish(PartSum& p, char* ws, unsigned epoch, int lane, int slice) {
    for (unsigned sp = 0; sp < SPIN_CAP; ++sp) { if (__all(p.v0[1] == epoch && p.v0[3] == epoch && p.v1[1] == epoch && p.v1[3] == epoch)) break; asm volatile("" ::: "memory"); mod_sum_load(p, ws, lane, slice); }
    float s[4] = {__uint_as_float(p.v0[0]), __uint_as_float(p.v0[2]), __uint_as_float(p.v1[0]), __uint_as_float(p.v1[2])};
#pragma unroll
    for (int i = 0; i < 4; ++i) { float v = s[i]; v += dppf<0x128>(v); s[i] = xsum32(xsum16(v)); }
    const unsigned long long g0 = ((unsigned long long)epoch << 32) | pk2(s[0], s[1]), g1 = ((unsigned long long)epoch << 32) | pk2(s[2], s[3]);
    const unsigned base = slice < 32 ? WS_MODU + slice * 128 : WS_MODN + (slice - 32) * 128, rs = slice < 32 ? 4096 : 8192, rep = lane >> 3, o = base + (lane & 7) * 8;
    stg64_sc1(ws, o + rep * rs, g0); stg64_sc1(ws, o + 64 + rep * rs, g1); stg64_sc1(ws, o + (rep + 8) * rs, g0); stg64_sc1(ws, o + 64 + (rep + 8) * rs, g1);
}

__global__ __launch_bounds__(576, 3) void fused_block(const u16* __restrict__ x, const u16* __restrict__ wqkv, const u16* __restrict__ wo, const u16* __restrict__ w1, const u16* __restrict__ b1,
                                                      const u16* __restrict__ w2, const u16* __restrict__ b2, const u16* __restrict__ cvec, const u16* __restrict__ wmod, u16* __restrict__ out, char* __restrict__ ws) {
    const unsigned epoch = *(const unsigned*)(ws + WS_E) + 1u;
    const int tid = threadIdx.x, lane = tid & 63, w = __builtin_amdgcn_readfirstlane(tid >> 6);
    const int b = blockIdx.x, xl = b & 7, idx = b >> 3, g = 2 * xl + (idx >> 4), j = idx & 15;
    const int ct = j & 7, rh = j >> 3;
    unsigned long long code_pc; unsigned code_len;
    asm volatile("s_getpc_b64 %0\nfb_code_begin:\n\ts_mov_b32 %1, fb_code_end-fb_code_begin" : "=s"(code_pc), "=s"(code_len));
    STAMP(0); STAMPC(20);
    if (w == 8) {
        {
            const unsigned o = ((unsigned)idx * 32u + (lane & 31)) * 64u;
            warm4((int)o < (int)code_len ? o : 0u, (const void*)code_pc);
        }
        if (lane == 0) stg_sc1(ws, WS_HELLO + b * 4, epoch);
        __builtin_amdgcn_s_setprio(3);
        const unsigned sw0 = (lane >> 3) * 1024u + ct * 128u + (((lane & 7) >> 1) ^ ((lane >> 4) & 3)) * 32u + (lane & 1) * 16u, sw1 = sw0 + 7168, sw2 = sw0 + 14336, sw3 = sw0 + 21504;
        unsigned w1o[7], wqo[3];
#pragma unroll
        for (int r = 0; r < 7; ++r) { const unsigned u = r * 64 + lane, row = u / 14, c = u % 14; w1o[r] = row * 3584u + 224u * j + c * 16u - (r & 3) * 1024u; }
#pragma unroll
        for (int r = 0; r < 3; ++r) { const unsigned u = r * 64 + lane, row = u / 12, c = u % 12; wqo[r] = row * 3072u + (c >> 2) * 1024u + 64u * j + (c & 3) * 16u - r * 1024u; }
#define RUN128(base, first, n4, lds0) { const char* p_ = (const char*)(base) + (size_t)(first) * 8192; unsigned l_ = (lds0); _Pragma("unroll 2") for (int i_ = 0; i_ < (n4); ++i_) { dma4(sw0, sw1, sw2, sw3, p_, l_); p_ += 32768; l_ += 4096; } }
#define W1GRPS(k0, k1) { const char* p_ = (const char*)w1 + (size_t)(k0) * 114688; unsigned l_ = L_W1 + 7 * (k0) * 1024; _Pragma("unroll 1") for (int k_ = (k0); k_ < (k1); ++k_) { \
            dma4(w1o[0], w1o[1], w1o[2], w1o[3], p_, l_); dma3(w1o[4], w1o[5], w1o[6], p_, l_ + 4096); p_ += 114688; l_ += 7168; } }
#define W2CHUNKS(c0, c1, slot0) { _Pragma("unroll 1") for (int c_ = (c0); c_ < (c1); ++c_) { const char* p_ = (const char*)w2 + (size_t)c_ * 32768; const unsigned l_ = ((slot0) + c_ - (c0)) * 16384; \
            dma4(sw0, sw1, sw2, sw3, p_, l_); dma4(sw0, sw1, sw2, sw3, p_ + 56 * 8192, l_ + 4096); dma4(sw0, sw1, sw2, sw3, p_ + 112 * 8192, l_ + 8192); dma4(sw0, sw1, sw2, sw3, p_ + 168 * 8192, l_ + 12288); } }
#define WQGRPS(k0, k1) { const char* p_ = (const char*)wqkv + (size_t)(k0) * 49152; unsigned l_ = L_WQKV + 3 * (k0) * 1024; _Pragma("unroll 2") for (int k_ = (k0); k_ < (k1); ++k_) { dma3(wqo[0], wqo[1], wqo[2], p_, l_); p_ += 49152; l_ += 3072; } }
        __builtin_amdgcn_s_barrier();
        __builtin_amdgcn_s_barrier();
        WQGRPS(14, 24)
        __builtin_amdgcn_s_barrier();
        WQGRPS(0, 14)
        VMWAIT(0);
        __builtin_amdgcn_s_barrier();
        mod_part(cvec, wmod, ws, epoch, lane, 32 + 8 * xl + (idx & 3), idx >> 2); mod_part(cvec, wmod, ws, epoch, lane, 36 + 8 * xl + (idx & 3), idx >> 2);
        if (idx < 4) { PartSum p; mod_sum_load(p, ws, lane, 32 + 8 * xl + idx); mod_sum_finish(p, ws, epoch, lane, 32 + 8 * xl + idx); mod_sum_load(p, ws, lane, 36 + 8 * xl + idx); mod_sum_finish(p, ws, epoch, lane, 36 + 8 * xl + idx); }
        __builtin_amdgcn_s_barrier();
        __builtin_amdgcn_s_barrier();
        if (g >= 1) {
            const unsigned fo = WS_FLAG + (((g >= 2 ? g - 2 + (lane & 1) : 0) * 16 + j) * 256);
            for (unsigned sp = 0; sp < SPIN_CAP; ++sp) { unsigned f = ldg_sc1(ws, fo); if (__all(f == epoch)) break; }
            const unsigned lo16 = lane * 16;
#pragma unroll 1
            for (int src = (g >= 2 ? 0 : 1); src < 2; ++src) {
                const char* p_ = ws + WS_KV + (size_t)((g - 2 + src) * 16 + j) * 16384;
                dma4lin_c(lo16, p_, L_KIMG + src * 8192); dma4lin_c(lo16, p_ + 4096, L_KIMG + src * 8192 + 4096);
                dma4lin_c(lo16, p_ + 8192, L_VIMG + src * 8192); dma4lin_c(lo16, p_ + 12288, L_VIMG + src * 8192 + 4096);
            }
        }
        VMWAIT(0);
        __builtin_amdgcn_s_barrier();
        RUN128(wo, 0, 16, L_WO)
        W1GRPS(3, 7)
        W1GRPS(15, 16)
        __builtin_amdgcn_s_barrier();
        W1GRPS(7, 15)
        VMWAIT(56);
        __builtin_amdgcn_s_barrier();
        __builtin_amdgcn_s_barrier();
        __builtin_amdgcn_s_barrier();
        __builtin_amdgcn_s_barrier();
        W1GRPS(0, 3)
        __builtin_amdgcn_s_barrier();
        VMWAIT(0);
        __builtin_amdgcn_s_barrier();
        __builtin_amdgcn_s_barrier();
        W2CHUNKS(0, 2, 0)
        __builtin_amdgcn_s_barrier();
        __builtin_amdgcn_s_barrier();
        W2CHUNKS(2, 6, 2)
        __builtin_amdgcn_s_barrier();
        W2CHUNKS(6, 9, 6)
        VMWAIT(48);
        __builtin_amdgcn_s_barrier();
        VMWAIT(0);
        __builtin_amdgcn_s_barrier();
        __builtin_amdgcn_s_barrier();
        __builtin_amdgcn_s_barrier();
        __builtin_amdgcn_s_barrier();
        return;
    }
    const __amdgpu_buffer_rsrc_t rws = __builtin_amdgcn_make_buffer_rsrc(ws, 0, WS_BYTES, 0x00020000);
    const int m = w & 3, kh = w >> 2;
#define XTILE(t) { const unsigned xo = (unsigned)(128 * g + 32 * (t)) * 1024u + ((unsigned)lane << 4); \
        _Pragma("unroll 2") for (int i = 0; i < 32; i += 4) dma4(xo ^ (unsigned)((i & 15) << 4), xo ^ (unsigned)(((i & 15) + 1) << 4), xo ^ (unsigned)(((i & 15) + 2) << 4), xo ^ (unsigned)(((i & 15) + 3) << 4), \
                                                                 (const char*)x + i * 1024, L_X + (3 - (t)) * 32768 + i * 1024); }
    PartSum psum;
    if (w >= 4 && w < 7) XTILE(w - 4)
    if (w == 0) lds_st<u32x4>(L_ONES, (u32x4){0x3F803F80u, 0x3F803F80u, 0x3F803F80u, 0x3F803F80u});
    if (w == 3) { __builtin_amdgcn_s_setprio(2); mod_part(cvec, wmod, ws, epoch, lane, 4 * xl + (idx & 3), idx >> 2); __builtin_amdgcn_s_setprio(0); }
    STAMP(1); WS6(0);
    bf16x8 qf; u32x4 kpk, vpk; float qown[8];
    {
    const int h = lane >> 5, gi = lane >> 4, q4 = (lane & 15) >> 2, p4 = lane & 3;
    u32x4 xr[16];
#define XFRAGS() { const unsigned tok = lane & 31, rb = L_X + (3 - m) * 32768 + tok * 1024 + kh * 512; \
        _Pragma("unroll") for (int s = 0; s < 16; ++s) xr[s] = lds_ld<u32x4>(rb + (s >> 3) * 256 + ((((2 * (s & 7) + h) ^ (tok & 15))) << 4)); \
        _Pragma("unroll") for (int s = 0; s < 16; ++s) PIN(xr[s]); }
    if (w == 4) VMWAIT(0);
    BAR();
    WS6(1);
    if (w == 4) XTILE(3)
    if (w == 7) {
        unsigned wq[3];
#pragma unroll
        for (int r = 0; r < 3; ++r) { const unsigned u = r * 64 + lane, row = u / 12, c = u % 12; wq[r] = row * 3072u + (c >> 2) * 1024u + 64u * j + (c & 3) * 16u - r * 1024u; }
#pragma unroll 2
        for (int k = 24; k < 32; ++k) dma3(wq[0], wq[1], wq[2], (const char*)wqkv + (size_t)k * 49152, L_WQKV + 3 * k * 1024); }
    if (w == 3 && idx < 4) mod_sum_load(psum, ws, lane, 4 * xl + idx);
    if (m == 0) XFRAGS()
    if (w == 5 || w == 6) VMWAIT(0);
    BAR();
    WS6(2);
    if (w == 3 && idx < 4) mod_sum_finish(psum, ws, epoch, lane, 4 * xl + idx);
    if (m == 1 || m == 2) XFRAGS()
    if (w == 4) VMWAIT(0);
    BAR();
    if (m == 3) XFRAGS()
#undef XFRAGS
#undef XTILE
    STAMP(2);
    const unsigned gmo = WS_MODU + g * 4096 + (tid & 255) * 16; u32x4 gmv;
    if (w < 4) gmv = __builtin_amdgcn_raw_buffer_load_b128(rws, gmo, 0, 16);
    SBAR();
    {
        float s0 = 0.f, s1 = 0.f, s2 = 0.f, s3 = 0.f; const bf2 one = {(__bf16)1.0f, (__bf16)1.0f};
#pragma unroll
        for (int s = 0; s < 16; ++s)
#pragma unroll
            for (int i = 0; i < 4; i += 2) { const unsigned ua = xr[s][i], ub = xr[s][i + 1]; const bf2 va = __builtin_bit_cast(bf2, ua), vb = __builtin_bit_cast(bf2, ub);
                s0 = __builtin_amdgcn_fdot2_f32_bf16(va, one, s0, false); s1 = __builtin_amdgcn_fdot2_f32_bf16(va, va, s1, false);
                s2 = __builtin_amdgcn_fdot2_f32_bf16(vb, one, s2, false); s3 = __builtin_amdgcn_fdot2_f32_bf16(vb, vb, s3, false); }
        s0 = xsum32(s0 + s2); s1 = xsum32(s1 + s3);
        if (lane < 32) lds_st<u32x2>(L_STAT + (kh * 128 + 32 * m + lane) * 8, (u32x2){__float_as_uint(s0), __float_as_uint(s1)});
    }
    WS6(3);
    if (w < 4) {
        for (unsigned sp = 0; sp < SPIN_CAP; ++sp) { if (__all(gmv[1] == epoch && gmv[3] == epoch)) break; asm volatile("" ::: "memory"); gmv = __builtin_amdgcn_raw_buffer_load_b128(rws, gmo, 0, 16); }
        const unsigned u0 = gmv[0], u2 = gmv[2]; f32x4 f = {bf_lo(u0), bf_hi(u0), bf_lo(u2), bf_hi(u2)};
        if (tid >= 128) { f[0] = rbf(1.f + f[0]); f[1] = rbf(1.f + f[1]); f[2] = rbf(1.f + f[2]); f[3] = rbf(1.f + f[3]); }
        lds_st<f32x4>(L_SAG + tid * 16, f);
    }
    STAMP(3);
    VMWAIT(0);
    BAR();
    STAMP(4); STAMPC(22); WS1(0);
    float rstd, nmr, gv[2]; f32x4 sa0[2], sa1[2];
    const unsigned sga = L_SAG + (256 * kh + 16 * ((lane & 15) >> 3) + 8 * h + (lane & 7)) * 4;
    const unsigned saa = L_SAG + (256 * kh + 8 * h) * 4;
    { const u32x2 a = lds_ld<u32x2>(L_STAT + (32 * m + (lane & 31)) * 8), c2 = lds_ld<u32x2>(L_STAT + (128 + 32 * m + (lane & 31)) * 8);
      gv[0] = lds_ld<float>(sga + 2048);
      sa0[0] = lds_ld<f32x4>(saa); sa1[0] = lds_ld<f32x4>(saa + 16);
      const float s0 = __uint_as_float(a[0]) + __uint_as_float(c2[0]), s1 = __uint_as_float(a[1]) + __uint_as_float(c2[1]);
      const float mean = s0 * (1.f / 512.f), var = fmaxf(s1 * (1.f / 512.f) - mean * mean, 0.f);
      rstd = __builtin_amdgcn_rsqf(var + 1e-5f); nmr = -mean * rstd;
      asm volatile("s_nop 1" : "+v"(gv[0])); }
    f32x16 aQ, aK, aV;
#pragma unroll
    for (int i = 0; i < 16; ++i) { aQ[i] = 0.f; aK[i] = 0.f; aV[i] = 0.f; }
    {
        const unsigned rowa = L_WQKV + (256 * kh + 8 * (gi >> 1) + q4) * 192 + p4 * 8;
        const unsigned wqa = rowa + (((gi & 1) ^ kh) * 32), wva = rowa + 128 + (gi & 1) * 32;
        bf16x8 fk, fv;
#define HDW(s, i) hdw<8 * ((s) & 1) + 2 * (i)>(xr[s][i], rstd, nmr, gv[((s) >> 1) & 1], (i) < 2 ? sa0[(s) & 1][2 * (i) & 3] : sa1[(s) & 1][2 * (i) & 3], (i) < 2 ? sa0[(s) & 1][(2 * (i) + 1) & 3] : sa1[(s) & 1][(2 * (i) + 1) & 3])
        fk = trfrag(wqa + 64, wqa + 64 + 768); fv = trfrag(wva, wva + 768);
        sa0[1] = lds_ld<f32x4>(saa + 64); sa1[1] = lds_ld<f32x4>(saa + 64 + 16);
        { const unsigned a0 = HDW(0, 0), a1 = HDW(0, 1), a2 = HDW(0, 2), a3 = HDW(0, 3); xr[0] = (u32x4){a0, a1, a2, a3}; }
        asm volatile("s_nop 1" : "+v"(xr[0])); SBAR();
#define QKV_STEP(s, sn) { const bf16x8 hb = __builtin_bit_cast(bf16x8, xr[s]); unsigned n0, n1, n2, n3; \
            aK = MFMA32(fk, hb, aK); PIN(aK); SBAR(); if ((sn) < 16) { fk = trfrag(wqa + (sn) * 3072 + 64, wqa + (sn) * 3072 + 64 + 768); if ((s) + 2 < 16) sa0[(s) & 1] = lds_ld<f32x4>(saa + ((s) + 2) * 64); \
                n0 = HDW((sn) & 15, 0); n1 = HDW((sn) & 15, 1); PIN(n0); PIN(n1); } SBAR(); \
            aV = MFMA32(hb, fv, aV); PIN(aV); SBAR(); if ((sn) < 16) { fv = trfrag(wva + (sn) * 3072, wva + (sn) * 3072 + 768); if ((s) + 2 < 16) sa1[(s) & 1] = lds_ld<f32x4>(saa + ((s) + 2) * 64 + 16); \
                n2 = HDW((sn) & 15, 2); n3 = HDW((sn) & 15, 3); PIN(n2); PIN(n3); xr[(sn) & 15] = (u32x4){n0, n1, n2, n3}; } \
            if ((s) % 2 == 0 && (s) + 2 < 16) { gv[(((s) >> 1) + 1) & 1] = lds_ld<float>(sga + 2048 + (((s) >> 1) + 1) * 128); } SBAR(); }
        WS1(1);
        QKV_STEP(0, 1) QKV_STEP(1, 2) QKV_STEP(2, 3) QKV_STEP(3, 4) QKV_STEP(4, 5) QKV_STEP(5, 6) QKV_STEP(6, 7) QKV_STEP(7, 8)
        QKV_STEP(8, 9) QKV_STEP(9, 10) QKV_STEP(10, 11) QKV_STEP(11, 12) QKV_STEP(12, 13) QKV_STEP(13, 14) QKV_STEP(14, 15) QKV_STEP(15, 16)
#undef QKV_STEP
#undef HDW
        STAMP(5); STAMPC(23); WS1(2);
        float vs[8], vk[8];
#pragma unroll
        for (int i = 0; i < 8; ++i) { float lo = aV[i], hi = aV[8 + i]; PIN(lo); PIN(hi); vs[i] = kh ? lo : hi; vk[i] = kh ? hi : lo; }
        const unsigned ex = w * 6144 + lane * 16;
        lds_st<f32x4>(ex, (f32x4){aK[8], aK[9], aK[10], aK[11]}); lds_st<f32x4>(ex + 1024, (f32x4){aK[12], aK[13], aK[14], aK[15]});
        lds_st<f32x4>(ex + 2048, (f32x4){vs[0], vs[1], vs[2], vs[3]}); lds_st<f32x4>(ex + 3072, (f32x4){vs[4], vs[5], vs[6], vs[7]});
        bf16x8 fq = trfrag(wqa, wqa + 768), fq1 = trfrag(wqa + 3072, wqa + 3072 + 768);
        BAR();
        WS1(3);
        const unsigned ep = (w ^ 4) * 6144 + lane * 16;
        { const f32x4 k0 = lds_ld<f32x4>(ep), k1 = lds_ld<f32x4>(ep + 1024), v0 = lds_ld<f32x4>(ep + 2048), v1 = lds_ld<f32x4>(ep + 3072);
          u32x4 kp, vp;
          kp[0] = pk2(aK[0] + k0[0], aK[1] + k0[1]); kp[1] = pk2(aK[2] + k0[2], aK[3] + k0[3]); kp[2] = pk2(aK[4] + k1[0], aK[5] + k1[1]); kp[3] = pk2(aK[6] + k1[2], aK[7] + k1[3]);
          vp[0] = pk2(vk[0] + v0[0], vk[1] + v0[1]); vp[1] = pk2(vk[2] + v0[2], vk[3] + v0[3]); vp[2] = pk2(vk[4] + v1[0], vk[5] + v1[1]); vp[3] = pk2(vk[6] + v1[2], vk[7] + v1[3]);
          const unsigned kvo = WS_KV + (g * 16 + j) * 16384 + (m * 2 + kh) * 1024 + lane * 16;
          __builtin_amdgcn_raw_buffer_store_b128(kp, rws, kvo, 0, 16);
          __builtin_amdgcn_raw_buffer_store_b128(vp, rws, kvo + 8192, 0, 16);
          PIN(kp); PIN(vp); kpk = kp; vpk = vp; }
        SBAR();
#pragma unroll
        for (int s = 0; s < 16; s += 2) {
            aQ = MFMA32(fq, __builtin_bit_cast(bf16x8, xr[s]), aQ); PIN(aQ); SBAR(); if (s + 2 < 16) fq = trfrag(wqa + (s + 2) * 3072, wqa + (s + 2) * 3072 + 768); SBAR();
            aQ = MFMA32(fq1, __builtin_bit_cast(bf16x8, xr[s + 1]), aQ); PIN(aQ); SBAR(); if (s + 3 < 16) fq1 = trfrag(wqa + (s + 3) * 3072, wqa + (s + 3) * 3072 + 768); SBAR(); }
        lds_st<f32x4>(ex + 4096, (f32x4){aQ[8], aQ[9], aQ[10], aQ[11]}); lds_st<f32x4>(ex + 5120, (f32x4){aQ[12], aQ[13], aQ[14], aQ[15]});
#pragma unroll
        for (int i = 0; i < 8; ++i) qown[i] = aQ[i];
    }
    }
    WS1(4);
    VMWAIT(0); WS1(5);
    BAR();
    if (tid == 0) stg_sc1(ws, WS_FLAG + (0 * 256 + g * 16 + j) * 256, epoch);
    STAMP(6);
    { FRESH(lane);
      const unsigned ep = (w ^ 4) * 6144 + 4096 + lane * 16; const f32x4 q0 = lds_ld<f32x4>(ep), q1 = lds_ld<f32x4>(ep + 1024);
      lds_st<u32x4>(L_KIMG + (8 + m) * 2048 + kh * 1024 + lane * 16, kpk);
      lds_st<u32x4>(L_VIMG + (8 + m) * 2048 + kh * 1024 + lane * 16, vpk);
      const float qs = 0.25f * 1.44269504089f; u32x4 qp;
      qp[0] = pk2((qown[0] + q0[0]) * qs, (qown[1] + q0[1]) * qs); qp[1] = pk2((qown[2] + q0[2]) * qs, (qown[3] + q0[3]) * qs);
      qp[2] = pk2((qown[4] + q1[0]) * qs, (qown[5] + q1[1]) * qs); qp[3] = pk2((qown[6] + q1[2]) * qs, (qown[7] + q1[3]) * qs);
      qf = __builtin_bit_cast(bf16x8, qp); PIN(qf); }
    const int tt = w & 3;
    {
    FRESH(lane);
    const int hh = kh, qq = lane & 31, h = lane >> 5;
    const float NEG = -__builtin_inff(), fq0 = (float)(4 * h - qq) - 0.5f;
    f32x16 c0, c8;
#pragma unroll
    for (int r = 0; r < 16; ++r) {
        const float t = fmaf(fq0, 1e30f, (float)((r & 3) + 8 * (r >> 2)) * 1e30f); c0[r] = fminf(t, 0.f); c8[r] = fminf(-t, 0.f); }
    PIN(c0); PIN(c8);
    BAR();
    STAMP(8); WS2(0);
    {
        const unsigned ka = L_KIMG + m * 2048 + hh * 1024 + lane * 16;
        const bool isd = ((lane >> 4) & 1) == hh;
        const unsigned va = isd ? L_VIMG + m * 2048 + lane * 16 : L_ONES, vst = isd ? 2048u : 0u, vs1 = isd ? 1024u : 0u;
        f32x16 o;
#pragma unroll
        for (int r = 0; r < 16; ++r) o[r] = 0.f;
#define MAX16(mx, s) { _Pragma("unroll") for (int r = 0; r < 16; r += 2) mx = fmaxf(fmaxf(mx, s[r]), s[r + 1]); }
#define EXPH(P, S, b) { P[0] = pk2(__builtin_amdgcn_exp2f(S[b]), __builtin_amdgcn_exp2f(S[b + 1])); P[1] = pk2(__builtin_amdgcn_exp2f(S[b + 2]), __builtin_amdgcn_exp2f(S[b + 3])); \
                        P[2] = pk2(__builtin_amdgcn_exp2f(S[b + 4]), __builtin_amdgcn_exp2f(S[b + 5])); P[3] = pk2(__builtin_amdgcn_exp2f(S[b + 6]), __builtin_amdgcn_exp2f(S[b + 7])); }
        if (g >= 2) {
            f32x16 z;
#pragma unroll
            for (int r = 0; r < 16; ++r) z[r] = 0.f;
            PIN(z);
            float mx = NEG, mx2 = NEG; f32x16 sA, sB;
#define KF(i) lds_ld<bf16x8>(ka + (i) * 2048)
            bf16x8 k0 = KF(0), k1 = KF(8), k2 = KF(1), k3 = KF(2);
            SBAR();
            sA = MFMA32(k0, qf, c0); PIN(sA); k0 = KF(3); SBAR();
            sB = MFMA32(k1, qf, c8); PIN(sB); k1 = KF(4); SBAR(); MAX16(mx, sA); PIN(mx); SBAR();
            sA = MFMA32(k2, qf, z); PIN(sA); k2 = KF(5); SBAR(); MAX16(mx2, sB); PIN(mx2); SBAR();
            sB = MFMA32(k3, qf, z); PIN(sB); k3 = KF(6); SBAR(); MAX16(mx, sA); PIN(mx); SBAR();
            sA = MFMA32(k0, qf, z); PIN(sA); k0 = KF(7); SBAR(); MAX16(mx2, sB); PIN(mx2); SBAR();
            sB = MFMA32(k1, qf, z); PIN(sB); k1 = KF(0); SBAR(); MAX16(mx, sA); PIN(mx); SBAR();
            sA = MFMA32(k2, qf, z); PIN(sA); k2 = KF(1); SBAR(); MAX16(mx2, sB); PIN(mx2); SBAR();
            sB = MFMA32(k3, qf, z); PIN(sB); SBAR(); MAX16(mx, sA); PIN(mx); SBAR();
            sA = MFMA32(k0, qf, z); PIN(sA); SBAR(); MAX16(mx2, sB); PIN(mx2); SBAR();
            WS2(1);
            bf16x8 v0 = lds_ld<bf16x8>(va), v1 = lds_ld<bf16x8>(va + vs1);
            MAX16(mx, sA);
            mx = xmax32(fmaxf(mx, mx2));
#pragma unroll
            for (int r = 0; r < 16; ++r) { z[r] = -mx; c0[r] -= mx; c8[r] -= mx; }
            PIN(z); PIN(c0); PIN(c8); SBAR();
            WS2(2);
            sA = MFMA32(k1, qf, c0); SBAR();
            u32x4 pa, pb; bf16x8 v0n, v1n, kn = k2; unsigned vp = va;
#define ABLK(I, SC, SN, CN) { bf16x8 kc = kn; if ((I) < 8) { vp += vst; v0n = lds_ld<bf16x8>(vp); v1n = lds_ld<bf16x8>(vp + vs1); } if ((I) < 7) kn = KF((I) + 2); SBAR(); \
                if ((I) < 8) { SN = MFMA32(kc, qf, CN); PIN(SN); } SBAR(); EXPH(pa, SC, 0); PIN(pa); SBAR(); \
                o = MFMA32(v0, __builtin_bit_cast(bf16x8, pa), o); PIN(o); SBAR(); EXPH(pb, SC, 8); PIN(pb); SBAR(); \
                o = MFMA32(v1, __builtin_bit_cast(bf16x8, pb), o); PIN(o); SBAR(); if ((I) < 8) { v0 = v0n; v1 = v1n; } }
            ABLK(0, sA, sB, z) ABLK(1, sB, sA, z) ABLK(2, sA, sB, z) ABLK(3, sB, sA, z)
            ABLK(4, sA, sB, z) ABLK(5, sB, sA, z) ABLK(6, sA, sB, z) ABLK(7, sB, sA, c8) ABLK(8, sA, sB, c8)
#undef KF
#undef ABLK
        } else {
            const int i0 = 8 - 4 * g - m;
            f32x16 z;
#pragma unroll
            for (int r = 0; r < 16; ++r) z[r] = 0.f;
            float mx = NEG;
            f32x16 s = MFMA32(lds_ld<bf16x8>(ka + 8 * 2048), qf, c8); MAX16(mx, s);
#pragma unroll 1
            for (int i = i0; i < 8; ++i) { s = MFMA32(lds_ld<bf16x8>(ka + i * 2048), qf, z); MAX16(mx, s); }
            mx = xmax32(mx);
#pragma unroll
            for (int r = 0; r < 16; ++r) { z[r] = -mx; c8[r] -= mx; }
#pragma unroll 1
            for (int i = i0; i <= 8; ++i) { u32x4 pa, pb;
                if (i < 8) s = MFMA32(lds_ld<bf16x8>(ka + i * 2048), qf, z); else s = MFMA32(lds_ld<bf16x8>(ka + i * 2048), qf, c8);
                EXPH(pa, s, 0); EXPH(pb, s, 8);
                o = MFMA32(lds_ld<bf16x8>(va + i * vst), __builtin_bit_cast(bf16x8, pa), o); o = MFMA32(lds_ld<bf16x8>(va + i * vst + vs1), __builtin_bit_cast(bf16x8, pb), o); }
        }
#undef EXPH
#undef MAX16
        WS2(3);
        float ov[8], l;
        { float lo = o[0], hi = o[8]; PIN(lo); PIN(hi); l = hh ? lo : hi; }
#pragma unroll
        for (int r = 0; r < 8; ++r) { float lo = o[r], hi = o[8 + r]; PIN(lo); PIN(hi); ov[r] = hh ? hi : lo; }
        const float inv = __builtin_amdgcn_rcpf(l);
        const u32x4 op = {pk2(ov[0] * inv, ov[1] * inv), pk2(ov[2] * inv, ov[3] * inv), pk2(ov[4] * inv, ov[5] * inv), pk2(ov[6] * inv, ov[7] * inv)};
        __builtin_amdgcn_raw_buffer_store_b128(op, rws, WS_O + ((g * 8 + 2 * m + (qq >> 4)) * 16 + j) * 1024 + ((qq & 15) + 16 * (2 * hh + h)) * 16, 0, 16);
    }
    }
    WS2(4);
    VMWAIT(0); WS2(5);
    BAR();
    if (tid == 0) stg_sc1(ws, WS_FLAG + (1 * 256 + g * 16 + j) * 256, epoch);
    STAMP(9);
    if (w >= 4) {
        const int t = tid - 256; const unsigned o = WS_MODN + g * 8192 + t * 32; u32x4 v, v2;
        for (unsigned sp = 0; sp < SPIN_CAP; ++sp) { asm volatile("" ::: "memory"); v = __builtin_amdgcn_raw_buffer_load_b128(rws, o, 0, 16); v2 = __builtin_amdgcn_raw_buffer_load_b128(rws, o + 16, 0, 16);
            if (__all(v[1] == epoch && v[3] == epoch && v2[1] == epoch && v2[3] == epoch)) break; }
        u32x4 val = {v[0], v[2], v2[0], v2[2]};
        const int vec = t >> 6, c8 = (t & 63) * 8;
        if (vec == 1) lds_st<u32x4>(L_SB16 + c8 * 2, val);
        else if (vec == 2) {
#pragma unroll
            for (int i = 0; i < 4; ++i) { const unsigned u_ = val[i]; val[i] = pk2(1.f + bf_lo(u_), 1.f + bf_hi(u_)); }
            lds_st<u32x4>(L_GB16 + c8 * 2, val);
        } else if ((c8 >> 6) == ct) lds_st<u32x4>((vec == 0 ? L_GAO : L_GBO) + (c8 & 63) * 2, val);
    }
    if (w == 0) poll_flags(ws, WS_FLAG + (1 * 256 + g * 16) * 256, 16, epoch, lane);
    BAR();
    STAMP(10); WS3(0);
    u32x2 x1p0, x1p1;
    {
        FRESH(lane);
        const int gi = lane >> 4, q4 = (lane & 15) >> 2, p4 = lane & 3;
        const unsigned tokP = 128 * g + 64 * rh + 16 * tt + (lane & 15), colP = 64 * ct + 32 * kh + 4 * gi;
        u32x4 of[8];
#pragma unroll
        for (int s = 0; s < 8; ++s) of[s] = __builtin_amdgcn_raw_buffer_load_b128(rws, WS_O + ((g * 8 + 4 * rh + tt) * 16 + 8 * kh + s) * 1024 + lane * 16, 0, 16);
        const u32x2 xres0 = *(const u32x2*)(x + tokP * 512 + colP), xres1 = *(const u32x2*)(x + tokP * 512 + colP + 16);
        SBAR();
        f32x4 acc[4];
#pragma unroll
        for (int n = 0; n < 4; ++n) acc[n] = (f32x4){0.f, 0.f, 0.f, 0.f};
        const unsigned xv = 2 * (gi & 1) + (q4 >> 1), rowa = L_WO + (256 * kh + 16 * (gi >> 1) + 4 * (gi & 1) + q4) * 128 + p4 * 8;
        unsigned an[4]; bf16x8 wf[4];
#pragma unroll
        for (int n = 0; n < 4; ++n) { an[n] = rowa + ((n ^ xv) * 32); wf[n] = trfrag(an[n], an[n] + 1024); }
        const unsigned cl = (32 * kh + 4 * gi) * 2, cg = colP * 2;
        const u32x2 ga0 = lds_ld<u32x2>(L_GAO + cl), ga1 = lds_ld<u32x2>(L_GAO + cl + 32);
        const u32x2 gb0 = lds_ld<u32x2>(L_GB16 + cg), gb1 = lds_ld<u32x2>(L_GB16 + cg + 32), sb0 = lds_ld<u32x2>(L_SB16 + cg), sb1 = lds_ld<u32x2>(L_SB16 + cg + 32);
        SBAR();
#pragma unroll
        for (int s = 0; s < 8; ++s)
#pragma unroll
            for (int n = 0; n < 4; ++n) { acc[n] = MFMA16(wf[n], __builtin_bit_cast(bf16x8, of[s]), acc[n]); PIN(acc[n]); SBAR(); if (s < 7) wf[n] = trfrag(an[n] + (s + 1) * 4096, an[n] + (s + 1) * 4096 + 1024); SBAR(); }
        WS3(1);
        BAR();
        STAMP(11);
        const unsigned ex = w * 2048 + lane * 16;
        { f32x4 s0 = acc[0], s1 = acc[1], s2 = acc[2], s3 = acc[3]; PIN(s0); PIN(s1); PIN(s2); PIN(s3);
          lds_st<f32x4>(ex, kh ? s0 : s2); lds_st<f32x4>(ex + 1024, kh ? s1 : s3); acc[0] = kh ? s2 : s0; acc[1] = kh ? s3 : s1; }
        BAR();
        WS3(2);
        const unsigned ep = (w ^ 4) * 2048 + lane * 16;
        const f32x4 a0 = lds_ld<f32x4>(ep) + acc[0], a1 = lds_ld<f32x4>(ep + 1024) + acc[1];
        float xv1[8], gg[8], ss[8];
#pragma unroll
        for (int r = 0; r < 4; ++r) {
            const unsigned ua0 = ga0[r >> 1], ua1 = ga1[r >> 1], ux0 = xres0[r >> 1], ux1 = xres1[r >> 1], ug0 = gb0[r >> 1], ug1 = gb1[r >> 1], us0 = sb0[r >> 1], us1 = sb1[r >> 1];
            xv1[r] = rbf(fmaf((r & 1) ? bf_hi(ua0) : bf_lo(ua0), a0[r], (r & 1) ? bf_hi(ux0) : bf_lo(ux0))); xv1[4 + r] = rbf(fmaf((r & 1) ? bf_hi(ua1) : bf_lo(ua1), a1[r], (r & 1) ? bf_hi(ux1) : bf_lo(ux1)));
            gg[r] = (r & 1) ? bf_hi(ug0) : bf_lo(ug0); gg[4 + r] = (r & 1) ? bf_hi(ug1) : bf_lo(ug1);
            ss[r] = (r & 1) ? bf_hi(us0) : bf_lo(us0); ss[4 + r] = (r & 1) ? bf_hi(us1) : bf_lo(us1);
        }
        x1p0 = (u32x2){pk2(xv1[0], xv1[1]), pk2(xv1[2], xv1[3])}; x1p1 = (u32x2){pk2(xv1[4], xv1[5]), pk2(xv1[6], xv1[7])};
        float st[6] = {0.f, 0.f, 0.f, 0.f, 0.f, 0.f}, pg[8];
#pragma unroll
        for (int r = 0; r < 8; ++r) pg[r] = xv1[r] * gg[r];
        const u32x4 xg = {pk2(pg[0], pg[1]), pk2(pg[2], pg[3]), pk2(pg[4], pg[5]), pk2(pg[6], pg[7])};
        __builtin_amdgcn_raw_buffer_store_b128(xg, rws, WS_XG + ((g * 8 + 4 * rh + tt) * 16 + 2 * ct + kh) * 1024 + lane * 16, 0, 16);
#pragma unroll
        for (int r = 0; r < 8; ++r) { const float p = pg[r]; st[0] += xv1[r]; st[1] = fmaf(xv1[r], xv1[r], st[1]); st[2] += p; st[3] = fmaf(p, p, st[3]); st[4] = fmaf(p, gg[r], st[4]); st[5] = fmaf(p, ss[r], st[5]); }
#pragma unroll
        for (int i = 0; i < 6; ++i) st[i] = xsum32(xsum16(st[i]));
        if (kh == 1 && lane < 16) { lds_st<f32x4>(L_SCR + (tt * 16 + lane) * 32, (f32x4){st[0], st[1], st[2], st[3]}); lds_st<u32x2>(L_SCR + (tt * 16 + lane) * 32 + 16, (u32x2){__float_as_uint(st[4]), __float_as_uint(st[5])}); }
        WS3(3);
        BAR();
        if (kh == 0 && lane < 16) {
            const f32x4 o4 = lds_ld<f32x4>(L_SCR + (tt * 16 + lane) * 32); const u32x2 o2 = lds_ld<u32x2>(L_SCR + (tt * 16 + lane) * 32 + 16);
            const unsigned so = WS_ST + g * 24576, T = 4 * rh + tt;
            const u32x4 sa4 = {__float_as_uint(st[0] + o4[0]), __float_as_uint(st[1] + o4[1]), __float_as_uint(st[2] + o4[2]), __float_as_uint(st[3] + o4[3])};
            const u32x2 sb2 = {__float_as_uint(st[4] + __uint_as_float(o2[0])), __float_as_uint(st[5] + __uint_as_float(o2[1]))};
            __builtin_amdgcn_raw_buffer_store_b128(sa4, rws, so + ((T * 8 + ct) * 16 + lane) * 16, 0, 16);
            __builtin_amdgcn_raw_buffer_store_b64(sb2, rws, so + 16384 + ((T * 8 + ct) * 16 + lane) * 8, 0, 16);
        }
    }
    WS3(4);
    VMWAIT(0); WS3(5);
    BAR();
    if (tid == 0) stg_sc1(ws, WS_FLAG + (2 * 256 + g * 16 + j) * 256, epoch);
    STAMP(12);
    const int odd = j & 1;
    {
    FRESH(lane);
    const int gi = lane >> 4, q4 = (lane & 15) >> 2, p4 = lane & 3;
    const u32x2 b1v = *(const u32x2*)(b1 + 112 * j + (w > 0 ? w - 1 : 0) * 16 + 4 * gi);
    BAR();
    if (w > 0) {
        const int nt = w - 1; f32x4 acc = {0.f, 0.f, 0.f, 0.f};
        const unsigned ra = L_W1 + (4 * gi + q4) * 224 + p4 * 8 + nt * 32; const int tk = lane & 15;
        const unsigned va = (tk == 0 ? L_GB16 : L_SB16) + 8 * gi;
#pragma unroll
        for (int s = 0; s < 16; ++s) {
            const u32x2 lo = lds_ld<u32x2>(va + s * 64), hi = lds_ld<u32x2>(va + s * 64 + 32);
            u32x4 bv = {lo[0], lo[1], hi[0], hi[1]};
            if (tk == 2) bv = (u32x4){0x3F803F80u, 0x3F803F80u, 0x3F803F80u, 0x3F803F80u};
            if (tk > 2) bv = (u32x4){0u, 0u, 0u, 0u};
            acc = MFMA16(trfrag(ra + s * 7168, ra + s * 7168 + 3584), __builtin_bit_cast(bf16x8, bv), acc);
        }
        if (tk < 3) lds_st<f32x4>(L_UVW + (tk * 112 + nt * 16 + 4 * gi) * 4, acc);
        if (tk == 3) lds_st<f32x4>(L_UVW + (3 * 112 + nt * 16 + 4 * gi) * 4, (f32x4){bf_lo(b1v[0]), bf_hi(b1v[0]), bf_lo(b1v[1]), bf_hi(b1v[1])});
    } else {
        const u32x4 gq = lds_ld<u32x4>(L_GB16 + lane * 16), sq = lds_ld<u32x4>(L_SB16 + lane * 16);
        float c5[5] = {0.f, 0.f, 0.f, 0.f, 0.f};
#pragma unroll
        for (int i = 0; i < 4; ++i) { const unsigned ug = gq[i], us = sq[i]; const float g0 = bf_lo(ug), g1 = bf_hi(ug), s0 = bf_lo(us), s1 = bf_hi(us);
            c5[0] += g0 + g1; c5[1] += s0 + s1; c5[2] += g0 * g0 + g1 * g1; c5[3] += g0 * s0 + g1 * s1; c5[4] += s0 * s0 + s1 * s1; }
#pragma unroll
        for (int i = 0; i < 5; ++i) c5[i] = xsum32(xsum16(rowsum16(c5[i])));
        if (lane == 0) { lds_st<f32x4>(L_CONST, (f32x4){c5[0], c5[1], c5[2], c5[3]}); lds_st<float>(L_CONST + 16, c5[4]); }
        poll_flags(ws, WS_FLAG + (2 * 256 + g * 16) * 256, 16, epoch, lane);
    }
    }
    BAR();
    STAMP(13);
    {
        FRESH(lane);
        const int gi = lane >> 4, q4 = (lane & 15) >> 2, p4 = lane & 3;
        const int Tw = 2 * m + kh;
        u32x4 xf[2][8];
        const unsigned xo0 = WS_XG + (g * 8 + Tw) * 16384 + kh * 8192 + lane * 16, xo1 = WS_XG + (g * 8 + (Tw ^ 1)) * 16384 + kh * 8192 + lane * 16;
        constexpr int WIN = 5;
#pragma unroll
        for (int s = 0; s < WIN; ++s) { xf[0][s] = __builtin_amdgcn_raw_buffer_load_b128(rws, xo0 + s * 1024, 0, 16); xf[1][s] = __builtin_amdgcn_raw_buffer_load_b128(rws, xo1 + s * 1024, 0, 16); }
        const unsigned so = WS_ST + g * 24576 + Tw * 2048, sob = WS_ST + g * 24576 + 16384 + Tw * 1024;
        u32x4 sA0, sA1; u32x2 sB0, sB1;
        SBAR(); WS4(0);
        f32x4 acc[7], acb[7]; bf16x8 wf[7];
        const unsigned ra = L_W1 + (256 * kh + 4 * gi + q4) * 224 + p4 * 8, raA = ra + 32 * odd, raB = ra + (odd ? 0 : 192);
#define W1A(s, u) (((u) < 6 ? raA : raB) + (s) * 7168 + ((u) < 6 ? (u) * 32 : 0))
#pragma unroll
        for (int u = 0; u < 7; ++u) { acc[u] = (f32x4){0.f, 0.f, 0.f, 0.f}; acb[u] = acc[u]; wf[u] = trfrag(W1A(0, u), W1A(0, u) + 3584); }
        SBAR();
#pragma unroll
        for (int s = 0; s < 8; ++s) {
            if (s + WIN < 8) { xf[0][s + WIN] = __builtin_amdgcn_raw_buffer_load_b128(rws, xo0 + (s + WIN) * 1024, 0, 16); xf[1][s + WIN] = __builtin_amdgcn_raw_buffer_load_b128(rws, xo1 + (s + WIN) * 1024, 0, 16); }
            if (s == 4) { sA0 = __builtin_amdgcn_raw_buffer_load_b128(rws, so + ((2 * gi) * 16 + (lane & 15)) * 16, 0, 16); sA1 = __builtin_amdgcn_raw_buffer_load_b128(rws, so + ((2 * gi + 1) * 16 + (lane & 15)) * 16, 0, 16); }
            if (s == 5) { sB0 = __builtin_amdgcn_raw_buffer_load_b64(rws, sob + ((2 * gi) * 16 + (lane & 15)) * 8, 0, 16); sB1 = __builtin_amdgcn_raw_buffer_load_b64(rws, sob + ((2 * gi + 1) * 16 + (lane & 15)) * 8, 0, 16); }
            SBAR();
#pragma unroll
            for (int u = 0; u < 7; ++u) { acc[u] = MFMA16(wf[u], __builtin_bit_cast(bf16x8, xf[0][s]), acc[u]); PIN(acc[u]); SBAR();
                acb[u] = MFMA16(wf[u], __builtin_bit_cast(bf16x8, xf[1][s]), acb[u]); PIN(acb[u]); SBAR();
                if (s < 7) wf[u] = trfrag(W1A(s + 1, u), W1A(s + 1, u) + 3584); SBAR(); }
            if (s == 7) WS4(1);
        }
#undef W1A
        float ca, cb, cc, r2;
        { float S[6];
#pragma unroll
          for (int i = 0; i < 4; ++i) S[i] = xsum32(xsum16(__uint_as_float(sA0[i]) + __uint_as_float(sA1[i])));
#pragma unroll
          for (int i = 0; i < 2; ++i) S[4 + i] = xsum32(xsum16(__uint_as_float(sB0[i]) + __uint_as_float(sB1[i])));
          const f32x4 cc4 = lds_ld<f32x4>(L_CONST); const float SB2 = lds_ld<float>(L_CONST + 16);
          const float iD = 1.f / 512.f, m1 = S[0] * iD, r1 = __builtin_amdgcn_rsqf(fmaxf(S[1] * iD - m1 * m1, 0.f) + 1e-5f);
          const float m2 = (r1 * (S[2] - m1 * cc4[0]) + cc4[1]) * iD;
          const float ez2 = (r1 * r1 * (S[3] - 2.f * m1 * S[4] + m1 * m1 * cc4[2]) + 2.f * r1 * (S[5] - m1 * cc4[3]) + SB2) * iD;
          r2 = __builtin_amdgcn_rsqf(fmaxf(ez2 - m2 * m2, 0.f) + 1e-5f);
          ca = r1 * r2; cb = -ca * m1; cc = -r2 * m2; PIN(ca); PIN(cb); PIN(cc); PIN(r2); }
        const unsigned uo = L_UVW + 16 * gi + 64 * odd, uo6 = L_UVW + 16 * gi + (odd ? 0 : 384);
        f32x4 uu = lds_ld<f32x4>(uo), vv = lds_ld<f32x4>(uo + 448), ww = lds_ld<f32x4>(uo + 896), bb = lds_ld<f32x4>(uo + 1344);
        f32x4 u2 = lds_ld<f32x4>(uo + 64), v2 = lds_ld<f32x4>(uo + 64 + 448), w2v = lds_ld<f32x4>(uo + 64 + 896), b2v = lds_ld<f32x4>(uo + 64 + 1344);
        BAR();
        WS4(2);
        { const unsigned ex = 101376 + w * 7168 + lane * 16;
#pragma unroll
          for (int u = 0; u < 7; ++u) lds_st<f32x4>(ex + u * 1024, acb[u]); }
        WS4(3);
        BAR();
        WS4(4);
        { const unsigned ep = 101376 + (w ^ 4) * 7168 + lane * 16;
#pragma unroll
          for (int u = 0; u < 7; ++u) acc[u] += lds_ld<f32x4>(ep + u * 1024); }
        STAMP(14);
        const unsigned hb = WS_H + (g * 8 + Tw) * 57344, s0 = (7 * j + odd) >> 1;
        const f32x2 kA = {-0.044715f * 2.302208198f, -0.044715f * 2.302208198f}, kB = {-2.302208198f, -2.302208198f}, one2 = {1.f, 1.f};
#define LO2(v) ((f32x2){(v)[0], (v)[1]})
#define HI2(v) ((f32x2){(v)[2], (v)[3]})
#define PRE2(a_, u_, v_, w_, b_) (ca * (a_) + (cb * (u_) + (r2 * (v_) + (cc * (w_) + (b_)))))
#define GELU(N, p, y) { f32x2 z_[N], e_[N]; \
            _Pragma("unroll") for (int r = 0; r < N; ++r) { z_[r] = p[r] * p[r]; PIN(z_[r]); } \
            _Pragma("unroll") for (int r = 0; r < N; ++r) { z_[r] = z_[r] * kA + kB; PIN(z_[r]); } \
            _Pragma("unroll") for (int r = 0; r < N; ++r) { z_[r] = z_[r] * p[r]; PIN(z_[r]); } \
            SBAR(); _Pragma("unroll") for (int r = 0; r < N; ++r) { e_[r] = (f32x2){__builtin_amdgcn_exp2f(z_[r][0]), __builtin_amdgcn_exp2f(z_[r][1])}; PIN(e_[r]); } \
            SBAR(); _Pragma("unroll") for (int r = 0; r < N; ++r) { e_[r] = e_[r] + one2; PIN(e_[r]); } \
            SBAR(); _Pragma("unroll") for (int r = 0; r < N; ++r) { e_[r] = (f32x2){__builtin_amdgcn_rcpf(e_[r][0]), __builtin_amdgcn_rcpf(e_[r][1])}; PIN(e_[r]); } \
            SBAR(); _Pragma("unroll") for (int r = 0; r < N; ++r) y[r] = p[r] * e_[r]; }
#pragma unroll
        for (int u = 0; u < 6; u += 2) {
            f32x2 pre[4], y[4];
            pre[0] = PRE2(LO2(acc[u]), LO2(uu), LO2(vv), LO2(ww), LO2(bb)); pre[1] = PRE2(HI2(acc[u]), HI2(uu), HI2(vv), HI2(ww), HI2(bb));
            pre[2] = PRE2(LO2(acc[u + 1]), LO2(u2), LO2(v2), LO2(w2v), LO2(b2v)); pre[3] = PRE2(HI2(acc[u + 1]), HI2(u2), HI2(v2), HI2(w2v), HI2(b2v));
            PIN(pre[0]); PIN(pre[1]); PIN(pre[2]); PIN(pre[3]);
            SBAR();
            { const unsigned o = u < 4 ? uo + (u + 2) * 64 : uo6; uu = lds_ld<f32x4>(o); vv = lds_ld<f32x4>(o + 448); ww = lds_ld<f32x4>(o + 896); bb = lds_ld<f32x4>(o + 1344); }
            if (u < 4) { const unsigned o = uo + (u + 3) * 64; u2 = lds_ld<f32x4>(o); v2 = lds_ld<f32x4>(o + 448); w2v = lds_ld<f32x4>(o + 896); b2v = lds_ld<f32x4>(o + 1344); }
            SBAR();
            GELU(4, pre, y)
            __builtin_amdgcn_raw_buffer_store_b128((u32x4){pk2(y[0][0], y[0][1]), pk2(y[1][0], y[1][1]), pk2(y[2][0], y[2][1]), pk2(y[3][0], y[3][1])}, rws, hb + (s0 + (u >> 1)) * 1024 + lane * 16, 0, 16);
            SBAR();
        }
        {   f32x2 pre[2], y[2];
            pre[0] = PRE2(LO2(acc[6]), LO2(uu), LO2(vv), LO2(ww), LO2(bb)); pre[1] = PRE2(HI2(acc[6]), HI2(uu), HI2(vv), HI2(ww), HI2(bb));
            GELU(2, pre, y)
            __builtin_amdgcn_raw_buffer_store_b64((u32x2){pk2(y[0][0], y[0][1]), pk2(y[1][0], y[1][1])}, rws, hb + ((7 * j + (odd ? 0 : 6)) >> 1) * 1024 + odd * 512 + lane * 8, 0, 16);
        }
#undef GELU
#undef PRE2
#undef LO2
#undef HI2
        WS4(5);
    }
    VMWAIT(0);
    BAR();
    if (tid == 0) stg_sc1(ws, WS_FLAG + (3 * 256 + g * 16 + j) * 256, epoch);
    STAMP(15);
    u32x4 park[10];
    { FRESH(lane);
      const unsigned sw = (lane >> 3) * 1024u + ct * 128u + (((lane & 7) >> 1) ^ ((lane >> 4) & 3)) * 32u + (lane & 1) * 16u;
      const char* p0 = (const char*)w2 + (size_t)(56 * (w >> 2) + 36 + (w & 3)) * 8192, * p1 = p0 + (size_t)112 * 8192;
#pragma unroll
      for (int i = 0; i < 5; ++i) { park[2 * i] = *(const u32x4*)(p0 + i * 32768 + sw); park[2 * i + 1] = *(const u32x4*)(p1 + i * 32768 + sw); }
      SBAR();
      if (w == 0) poll_flags(ws, WS_FLAG + (3 * 256 + g * 16) * 256, 16, epoch, lane); }
    SBAR();
    BAR();
    STAMP(16); WS5(0);
    {
        constexpr int WIN = 3;
        FRESH(lane);
        const int gi = lane >> 4, q4 = (lane & 15) >> 2, p4 = lane & 3;
        const unsigned tokP = 128 * g + 64 * rh + 16 * tt + (lane & 15), colP = 64 * ct + 32 * kh + 4 * gi;
        const int kq = (w & 1) | (kh << 1);
        const unsigned hb0 = WS_H + (g * 8 + 4 * rh + tt) * 57344 + kq * 14 * 1024, hb1 = WS_H + (g * 8 + 4 * rh + (tt ^ 1)) * 57344 + kq * 14 * 1024;
        u32x4 hf[2][14];
#define HLD1(hb_, dst, s) { if ((s) % 7 == 3) { const u32x2 lo = __builtin_amdgcn_raw_buffer_load_b64(rws, hb_ + (s) * 1024 + lane * 8, 0, 16), hi = __builtin_amdgcn_raw_buffer_load_b64(rws, hb_ + (s) * 1024 + 512 + lane * 8, 0, 16); dst = (u32x4){lo[0], lo[1], hi[0], hi[1]}; } \
                    else dst = __builtin_amdgcn_raw_buffer_load_b128(rws, hb_ + (s) * 1024 + lane * 16, 0, 16); }
#define HLOAD(s) { HLD1(hb0, hf[0][s], s) HLD1(hb1, hf[1][s], s) }
#pragma unroll
        for (int s = 0; s < WIN; ++s) HLOAD(s)
        SBAR();
        f32x4 acc[2][4]; bf16x8 wf[4];
        const unsigned xv = (2 * gi + (q4 >> 1)) & 3, rowa = kq * 4096 + (4 * gi + q4) * 128 + p4 * 8;
        unsigned an[4];
#pragma unroll
        for (int n = 0; n < 4; ++n) { an[n] = rowa + (((n ^ (2 * kh)) ^ xv) * 32); acc[0][n] = (f32x4){0.f, 0.f, 0.f, 0.f}; acc[1][n] = acc[0][n]; }
#define W2A(s, n) (an[n] + (((s) % 9) & 3) * 16384)
#pragma unroll
        for (int n = 0; n < 4; ++n) wf[n] = trfrag(W2A(0, n), W2A(0, n) + 2048);
        const unsigned cl = (32 * kh + 4 * gi) * 2;
        const u32x2 g0 = lds_ld<u32x2>(L_GBO + cl), g1 = lds_ld<u32x2>(L_GBO + cl + 32);
        u32x2 bb0, bb1;
        SBAR();
#pragma unroll
        for (int s = 0; s < 14; ++s) {
            if (s == 11) { bb0 = *(const u32x2*)(b2 + colP); bb1 = *(const u32x2*)(b2 + colP + 16); }
            if (s == 1) WS5(1); if (s == 5) WS5(2); if (s == 8) WS5(3);
            if (s == 5) BAR();
            if (s == 8) BAR();
            if (s == 8) WS5(4);
            if (s + WIN < 14) HLOAD(s + WIN)
            SBAR();
#pragma unroll
            for (int n = 0; n < 4; ++n) { acc[0][n] = MFMA16(wf[n], __builtin_bit_cast(bf16x8, hf[0][s]), acc[0][n]); PIN(acc[0][n]); SBAR();
                acc[1][n] = MFMA16(wf[n], __builtin_bit_cast(bf16x8, hf[1][s]), acc[1][n]); PIN(acc[1][n]); SBAR();
                if (s >= 5 && s <= 7 && 4 * (s - 5) + n < 10) { const int i = 4 * (s - 5) + n; lds_st<u32x4>((i >> 1) * 16384 + (w + 8 * (i & 1)) * 1024 + lane * 16, park[i]); }
                if (s == 3 || s == 7 || s == 12) { an[n] += 65536; PIN(an[n]); } if (s == 8) { an[n] -= 131072; PIN(an[n]); }
                if (s < 13) wf[n] = trfrag(W2A(s + 1, n), W2A(s + 1, n) + 2048); SBAR(); }
        }
#undef W2A
#undef HLOAD
#undef HLD1
        WS5(5);
        BAR();
        STAMP(17);
        { const unsigned ex = w * 6144 + lane * 16;
          lds_st<f32x4>(ex, acc[0][2]); lds_st<f32x4>(ex + 1024, acc[0][3]); lds_st<f32x4>(ex + 2048, acc[1][0]); lds_st<f32x4>(ex + 3072, acc[1][1]); lds_st<f32x4>(ex + 4096, acc[1][2]); lds_st<f32x4>(ex + 5120, acc[1][3]); }
        BAR();
        const unsigned e4 = (w ^ 4) * 6144 + lane * 16, e1 = (w ^ 1) * 6144 + 2048 + lane * 16, e5 = (w ^ 5) * 6144 + 4096 + lane * 16;
        const f32x4 a0 = (acc[0][0] + lds_ld<f32x4>(e4)) + (lds_ld<f32x4>(e1) + lds_ld<f32x4>(e5)), a1 = (acc[0][1] + lds_ld<f32x4>(e4 + 1024)) + (lds_ld<f32x4>(e1 + 1024) + lds_ld<f32x4>(e5 + 1024));
        float y0[4], y1[4];
#pragma unroll
        for (int r = 0; r < 4; ++r) {
            const unsigned ug0 = g0[r >> 1], ug1 = g1[r >> 1], ub0 = bb0[r >> 1], ub1 = bb1[r >> 1], ux0 = x1p0[r >> 1], ux1 = x1p1[r >> 1];
            y0[r] = fmaf((r & 1) ? bf_hi(ug0) : bf_lo(ug0), a0[r] + ((r & 1) ? bf_hi(ub0) : bf_lo(ub0)), (r & 1) ? bf_hi(ux0) : bf_lo(ux0));
            y1[r] = fmaf((r & 1) ? bf_hi(ug1) : bf_lo(ug1), a1[r] + ((r & 1) ? bf_hi(ub1) : bf_lo(ub1)), (r & 1) ? bf_hi(ux1) : bf_lo(ux1));
        }
        *(u32x2*)(out + tokP * 512 + colP) = (u32x2){pk2(y0[0], y0[1]), pk2(y0[2], y0[3])};
        *(u32x2*)(out + tokP * 512 + colP + 16) = (u32x2){pk2(y1[0], y1[1]), pk2(y1[2], y1[3])};
    }
    if (b == 0 && w == 0) {
        for (unsigned sp = 0; sp < SPIN_CAP; ++sp) { asm volatile("" ::: "memory"); const u32x4 v = __builtin_amdgcn_raw_buffer_load_b128(rws, WS_HELLO + lane * 16, 0, 16); if (__all(v[0] == epoch && v[1] == epoch && v[2] == epoch && v[3] == epoch)) break; }
        if (lane == 0) stg_sc1(ws, WS_E, epoch);
    }
    STAMP(18); STAMPC(21);
    STAMP_OUT();
    asm volatile("fb_code_end:");
}

__global__ void k_tiny(char* p) { if (p == nullptr) p[0] = 1; }
extern "C" void kernel_launch(void* const* d_in, const int* in_sizes, int n_in, void* d_out, int out_size, void* d_ws, size_t ws_size, hipStream_t stream) {
    (void)hipFuncSetAttribute((const void*)fused_block, hipFuncAttributeMaxDynamicSharedMemorySize, LDS_BYTES);
    fused_block<<<256, 576, LDS_BYTES, stream>>>((const u16*)d_in[0], (const u16*)d_in[1], (const u16*)d_in[2], (const u16*)d_in[4], (const u16*)d_in[5], (const u16*)d_in[6], (const u16*)d_in[7],
                                                 (const u16*)d_in[8], (const u16*)d_in[9], (u16*)d_out, (char*)d_ws);
    k_tiny<<<1, 64, 0, stream>>>((char*)d_ws);
}
```

```cpp
#include <hip/hip_runtime.h>
#include <hip/hip_bf16.h>
#include <stdint.h>

typedef unsigned short u16;
typedef short bf16x8 __attribute__((ext_vector_type(8)));
typedef short s16x4 __attribute__((ext_vector_type(4)));
typedef float f32x4 __attribute__((ext_vector_type(4)));
typedef float f32x2 __attribute__((ext_vector_type(2)));
typedef float f32x16 __attribute__((ext_vector_type(16)));
typedef unsigned u32x4 __attribute__((ext_vector_type(4)));
typedef unsigned u32x2 __attribute__((ext_vector_type(2)));
typedef __bf16 bf2 __attribute__((ext_vector_type(2)));
#define LAS __attribute__((address_space(3)))
#define GAS __attribute__((address_space(1)))
#define DI __device__ __forceinline__

#define TS 0

constexpr unsigned WS_E = 0, WS_HELLO = 4096, WS_FLAG = 8192;
constexpr unsigned WS_MODU = 0x50000, WS_MODN = 0x60000;
constexpr unsigned WS_PART = 0x80000;
constexpr unsigned WS_KV = 0x100000, WS_O = 0x500000, WS_XG = 0x700000, WS_ST = 0x900000, WS_H = 0xA00000, WS_TS = 0x2000000;
constexpr unsigned WS_BYTES = 0x2100000;
constexpr unsigned L_X = 0, L_WQKV = 57344, L_RED = 155648, L_SAG = 156672, L_STAT = 160768;
constexpr unsigned L_QS = 65536, L_KIMG = 98304, L_VIMG = 122880, L_WO = 0, L_W1 = 44032;
constexpr unsigned L_GB16 = 158720, L_SB16 = 159744, L_UVW = 160768, L_CONST = 162560, L_GAO = 162592, L_GBO = 162720, L_ONES = 162848, L_DUMMY = 162880;
constexpr unsigned L_TAGM = L_RED + 512, L_TAGL = L_RED + 544, L_TAGC = L_RED + 560;
constexpr unsigned L_RED2 = 81920;
constexpr unsigned L_SCR = 32768;
constexpr unsigned LDS_BYTES = 163840;
constexpr unsigned SPIN_CAP = 1u << 16;

DI float bf_lo(unsigned u) { return __uint_as_float(u << 16); }
DI float bf_hi(unsigned u) { return __uint_as_float(u & 0xffff0000u); }
DI unsigned pk2(float a, float b) { bf2 v = {(__bf16)a, (__bf16)b}; return __builtin_bit_cast(unsigned, v); }
DI float rbf(float a) { return bf_lo(pk2(a, a)); }
DI void dma16(unsigned voff, const void* sbase, unsigned lds) { asm volatile("s_mov_b32 m0, %1\n\ts_nop 0\n\tglobal_load_lds_dwordx4 %0, %2" ::"v"(voff), "s"(lds), "s"(sbase) : "memory"); }
DI void dma16c(unsigned voff, const void* sbase, unsigned lds) { asm volatile("s_mov_b32 m0, %1\n\ts_nop 0\n\tglobal_load_lds_dwordx4 %0, %2 sc1" ::"v"(voff), "s"(lds), "s"(sbase) : "memory"); }
DI void dma4(unsigned v0, unsigned v1, unsigned v2, unsigned v3, const void* sbase, unsigned lds) {
    asm volatile("s_mov_b32 m0, %5\n\ts_nop 0\n\tglobal_load_lds_dwordx4 %0, %4\n\tglobal_load_lds_dwordx4 %1, %4 offset:1024\n\tglobal_load_lds_dwordx4 %2, %4 offset:2048\n\tglobal_load_lds_dwordx4 %3, %4 offset:3072"
                 ::"v"(v0), "v"(v1), "v"(v2), "v"(v3), "s"(sbase), "s"(lds) : "memory"); }
DI void dma3(unsigned v0, unsigned v1, unsigned v2, const void* sbase, unsigned lds) {
    asm volatile("s_mov_b32 m0, %4\n\ts_nop 0\n\tglobal_load_lds_dwordx4 %0, %3\n\tglobal_load_lds_dwordx4 %1, %3 offset:1024\n\tglobal_load_lds_dwordx4 %2, %3 offset:2048"
                 ::"v"(v0), "v"(v1), "v"(v2), "s"(sbase), "s"(lds) : "memory"); }
DI void dma4lin_c(unsigned v, const void* sbase, unsigned lds) {
    asm volatile("s_mov_b32 m0, %2\n\ts_nop 0\n\tglobal_load_lds_dwordx4 %0, %1 sc1\n\tglobal_load_lds_dwordx4 %0, %1 offset:1024 sc1\n\tglobal_load_lds_dwordx4 %0, %1 offset:2048 sc1\n\tglobal_load_lds_dwordx4 %0, %1 offset:3072 sc1"
                 ::"v"(v), "s"(sbase), "s"(lds) : "memory"); }
DI void lds_wait_tags(unsigned a, int n, unsigned tag) {
    for (unsigned sp = 0; sp < SPIN_CAP; ++sp) { asm volatile("" ::: "memory"); const unsigned t0 = *(const volatile LAS unsigned*)(size_t)a, t1 = *(const volatile LAS unsigned*)(size_t)(a + 4 * (n - 1));
        if (__builtin_amdgcn_readfirstlane(t0) == tag && __builtin_amdgcn_readfirstlane(t1) == tag) break; __builtin_amdgcn_s_sleep(1); }
    asm volatile("" ::: "memory"); }
DI void warm4(unsigned voff, const void* sbase) { asm volatile("s_mov_b32 m0, %1\n\ts_nop 0\n\tglobal_load_lds_dword %0, %2" ::"v"(voff), "s"(L_DUMMY), "s"(sbase) : "memory"); }
#define WSP 4
#define WS1(i) do { if (WSP == 1) WSTAMP(i); } while (0)
#define WS2(i) do { if (WSP == 2) WSTAMP(i); } while (0)
#define WS3(i) do { if (WSP == 3) WSTAMP(i); } while (0)
#define WS4(i) do { if (WSP == 4) WSTAMP(i); } while (0)
#define WS5(i) do { if (WSP == 5) WSTAMP(i); } while (0)
#define WS6(i) do { if (WSP == 6) WSTAMP(i); } while (0)
#define FRESH(v) int v = threadIdx.x & 63; asm volatile("" : "+v"(v))
#define SBAR() __builtin_amdgcn_sched_barrier(0)
#define PIN(x) asm volatile("" : "+v"(x))
#define BAR() asm volatile("s_waitcnt lgkmcnt(0)\n\ts_barrier" ::: "memory")
#define VMWAIT(n) asm volatile("s_waitcnt vmcnt(" #n ")" ::: "memory")
DI unsigned ldg_sc1(const char* ws, unsigned off) { return __hip_atomic_load((GAS unsigned*)(ws + off), __ATOMIC_RELAXED, __HIP_MEMORY_SCOPE_AGENT); }
DI unsigned long long ldg64_sc1(const char* ws, unsigned off) { return __hip_atomic_load((GAS unsigned long long*)(ws + off), __ATOMIC_RELAXED, __HIP_MEMORY_SCOPE_AGENT); }
DI void stg_sc1(char* ws, unsigned off, unsigned v) { __hip_atomic_store((GAS unsigned*)(ws + off), v, __ATOMIC_RELAXED, __HIP_MEMORY_SCOPE_AGENT); }
DI void stg64_sc1(char* ws, unsigned off, unsigned long long v) { __hip_atomic_store((GAS unsigned long long*)(ws + off), v, __ATOMIC_RELAXED, __HIP_MEMORY_SCOPE_AGENT); }
template <class T> DI T lds_ld(unsigned a) { return *(const LAS T*)(size_t)a; }
template <class T> DI void lds_st(unsigned a, T v) { *(LAS T*)(size_t)a = v; }
DI s16x4 lds_tr(unsigned a) { return __builtin_amdgcn_ds_read_tr16_b64_v4i16((LAS s16x4*)(size_t)a); }
DI bf16x8 trfrag(unsigned a0, unsigned a1) { s16x4 lo = lds_tr(a0), hi = lds_tr(a1); return __builtin_shufflevector(lo, hi, 0, 1, 2, 3, 4, 5, 6, 7); }
#define MFMA32(a, b, c) __builtin_amdgcn_mfma_f32_32x32x16_bf16((a), (b), (c), 0, 0, 0)
#define MFMA16(a, b, c) __builtin_amdgcn_mfma_f32_16x16x32_bf16((a), (b), (c), 0, 0, 0)
template <int N> DI float bcast(float v) { return __builtin_bit_cast(float, __builtin_amdgcn_mov_dpp(__builtin_bit_cast(int, v), 0x150 + N, 0xf, 0xf, true)); }
template <int N> DI float modu(float t, float gv, float sv) {
#pragma clang fp contract(off)
    const float p = bcast<N>(gv) * t; return bcast<N>(sv) + p; }
template <int N> DI unsigned hdw(unsigned x, float r, float n, float gv, float c, float d) {
    unsigned o; float a, b2;
    asm("v_lshlrev_b32 %1, 16, %5\n\tv_and_b32 %2, 0xffff0000, %5\n\tv_fma_f32 %1, %1, %6, %7\n\tv_fma_f32 %2, %2, %6, %7\n\t"
        "v_fmac_f32_dpp %3, %8, %1 row_newbcast:%c9 row_mask:0xf bank_mask:0xf\n\tv_fmac_f32_dpp %4, %8, %2 row_newbcast:%c10 row_mask:0xf bank_mask:0xf\n\t"
        "v_cvt_pk_bf16_f32 %0, %3, %4"
        : "=v"(o), "=&v"(a), "=&v"(b2), "+v"(c), "+v"(d) : "v"(x), "v"(r), "v"(n), "v"(gv), "i"(N), "i"(N + 1));
    return o; }
template <int C> DI float dppf(float v) { return __builtin_bit_cast(float, __builtin_amdgcn_update_dpp(0, __builtin_bit_cast(int, v), C, 0xf, 0xf, true)); }
DI float rowsum16(float v) { v += dppf<0xB1>(v); v += dppf<0x4E>(v); v += dppf<0x141>(v); v += dppf<0x140>(v); return v; }
DI float xsum32(float v) { auto r = __builtin_amdgcn_permlane32_swap(__float_as_uint(v), __float_as_uint(v), false, false); return __uint_as_float(r[0]) + __uint_as_float(r[1]); }
DI float xmax32(float v) { auto r = __builtin_amdgcn_permlane32_swap(__float_as_uint(v), __float_as_uint(v), false, false); return fmaxf(__uint_as_float(r[0]), __uint_as_float(r[1])); }
DI float xsum16(float v) { auto r = __builtin_amdgcn_permlane16_swap(__float_as_uint(v), __float_as_uint(v), false, false); return __uint_as_float(r[0]) + __uint_as_float(r[1]); }
DI unsigned long long mt() { unsigned long long t; asm volatile("s_memtime %0\n\ts_waitcnt lgkmcnt(0)" : "=s"(t)::"memory"); return t; }
DI unsigned long long rt() { unsigned long long t; asm volatile("s_memrealtime %0\n\ts_waitcnt lgkmcnt(0)" : "=s"(t)::"memory"); return t; }
#define STAMP(i) do {} while (0)
#define STAMP_OUT() do {} while (0)
#define STAMPC(i) do {} while (0)
#define WSTAMP(i) do {} while (0)

DI void poll_flags(const char* ws, unsigned off, int n, unsigned epoch, int lane) {
    const unsigned o = off + (unsigned)(lane < n ? lane : 0) * 256u;
    for (unsigned sp = 0; sp < SPIN_CAP; ++sp) { unsigned f = ldg_sc1(ws, o); if (__all(f == epoch)) break; }
}

DI void mod_part(const u16* __restrict__ cvec, const u16* __restrict__ wmod, char* ws, unsigned epoch, int lane, int slice, int kp) {
    const unsigned row0 = 64 * kp + 4 * (lane >> 2), o = row0 * 6144u + (unsigned)slice * 64u + (lane & 3) * 16u;
    const u32x2 cv = *(const u32x2*)(cvec + row0);
    u32x4 wm[4];
#pragma unroll
    for (int i = 0; i < 4; ++i) wm[i] = *(const u32x4*)((const char*)wmod + o + i * 6144u);
    SBAR();
    unsigned sp[2];
#pragma unroll
    for (int r = 0; r < 2; ++r) { const unsigned cu = cv[r]; const float c0 = bf_lo(cu), c1 = bf_hi(cu);
        sp[r] = pk2(c0 * __builtin_amdgcn_rcpf(1.f + __builtin_amdgcn_exp2f(-1.44269504089f * c0)), c1 * __builtin_amdgcn_rcpf(1.f + __builtin_amdgcn_exp2f(-1.44269504089f * c1))); }
    float a[8] = {0.f, 0.f, 0.f, 0.f, 0.f, 0.f, 0.f, 0.f};
#pragma unroll
    for (int r = 0; r < 2; ++r)
#pragma unroll
        for (int i = 0; i < 4; ++i) { const unsigned u0 = wm[2 * r][i], u1 = wm[2 * r + 1][i], s_ = sp[r];
            const unsigned lo = __builtin_amdgcn_perm(u1, u0, 0x05040100u), hi = __builtin_amdgcn_perm(u1, u0, 0x07060302u);
            a[2 * i] = __builtin_amdgcn_fdot2_f32_bf16(__builtin_bit_cast(bf2, lo), __builtin_bit_cast(bf2, s_), a[2 * i], false);
            a[2 * i + 1] = __builtin_amdgcn_fdot2_f32_bf16(__builtin_bit_cast(bf2, hi), __builtin_bit_cast(bf2, s_), a[2 * i + 1], false); }
    float b4[4], b2[2];
#pragma unroll
    for (int i = 0; i < 4; ++i) { auto r = __builtin_amdgcn_permlane32_swap(__float_as_uint(a[i]), __float_as_uint(a[i + 4]), false, false); b4[i] = __uint_as_float(r[0]) + __uint_as_float(r[1]); }
#pragma unroll
    for (int i = 0; i < 2; ++i) { auto r = __builtin_amdgcn_permlane16_swap(__float_as_uint(b4[i]), __float_as_uint(b4[i + 2]), false, false); float v = __uint_as_float(r[0]) + __uint_as_float(r[1]);
        v += dppf<0x124>(v); v += dppf<0x128>(v); b2[i] = v; }
    const __amdgpu_buffer_rsrc_t rws = __builtin_amdgcn_make_buffer_rsrc(ws, 0, WS_BYTES, 0x00020000);
    if ((lane & 12) == 0) __builtin_amdgcn_raw_buffer_store_b128((u32x4){__float_as_uint(b2[0]), epoch, __float_as_uint(b2[1]), epoch}, rws,
                                                                 WS_PART + (slice * 8 + kp) * 256 + ((lane & 3) * 4 + (lane >> 5) * 2 + ((lane >> 4) & 1)) * 16, 0, 16);
}
struct PartSum { u32x4 v0, v1; };
DI void mod_sum_load(PartSum& p, char* ws, int lane, int slice) {
    const __amdgpu_buffer_rsrc_t rws = __builtin_amdgcn_make_buffer_rsrc(ws, 0, WS_BYTES, 0x00020000);
    const unsigned o = WS_PART + (slice * 8 + (lane >> 3)) * 256 + (lane & 7) * 16;
    p.v0 = __builtin_amdgcn_raw_buffer_load_b128(rws, o, 0, 16); p.v1 = __builtin_amdgcn_raw_buffer_load_b128(rws, o + 128, 0, 16); }
DI void mod_sum_finish(PartSum& p, char* ws, unsigned epoch, int lane, int slice) {
    for (unsigned sp = 0; sp < SPIN_CAP; ++sp) { if (__all(p.v0[1] == epoch && p.v0[3] == epoch && p.v1[1] == epoch && p.v1[3] == epoch)) break; asm volatile("" ::: "memory"); mod_sum_load(p, ws, lane, slice); }
    float s[4] = {__uint_as_float(p.v0[0]), __uint_as_float(p.v0[2]), __uint_as_float(p.v1[0]), __uint_as_float(p.v1[2])};
#pragma unroll
    for (int i = 0; i < 4; ++i) { float v = s[i]; v += dppf<0x128>(v); s[i] = xsum32(xsum16(v)); }
    const unsigned long long g0 = ((unsigned long long)epoch << 32) | pk2(s[0], s[1]), g1 = ((unsigned long long)epoch << 32) | pk2(s[2], s[3]);
    const unsigned base = slice < 32 ? WS_MODU + slice * 128 : WS_MODN + (slice - 32) * 128, rs = slice < 32 ? 4096 : 8192, rep = lane >> 3, o = base + (lane & 7) * 8;
    stg64_sc1(ws, o + rep * rs, g0); stg64_sc1(ws, o + 64 + rep * rs, g1); stg64_sc1(ws, o + (rep + 8) * rs, g0); stg64_sc1(ws, o + 64 + (rep + 8) * rs, g1);
}

__global__ __launch_bounds__(576, 3) void fused_block(const u16* __restrict__ x, const u16* __restrict__ wqkv, const u16* __restrict__ wo, const u16* __restrict__ w1, const u16* __restrict__ b1,
                                                      const u16* __restrict__ w2, const u16* __restrict__ b2, const u16* __restrict__ cvec, const u16* __restrict__ wmod, u16* __restrict__ out, char* __restrict__ ws) {
    const unsigned epoch = *(const unsigned*)(ws + WS_E) + 1u;
    const int tid = threadIdx.x, lane = tid & 63, w = __builtin_amdgcn_readfirstlane(tid >> 6);
    const int b = blockIdx.x, xl = b & 7, idx = b >> 3, g = 2 * xl + (idx >> 4), j = idx & 15;
    const int ct = j & 7, rh = j >> 3;
    unsigned long long code_pc; unsigned code_len;
    asm volatile("s_getpc_b64 %0\nfb_code_begin:\n\ts_mov_b32 %1, fb_code_end-fb_code_begin" : "=s"(code_pc), "=s"(code_len));
    unsigned long long dbg_t0 = rt();
    if (tid == 0) stg64_sc1(ws, 0x1F00000 + b * 8, ((unsigned long long)epoch << 32) | (unsigned)dbg_t0);
    STAMP(0); STAMPC(20);
    if (w == 8) {
        {
            const unsigned o = ((unsigned)idx * 32u + (lane & 31)) * 64u;
            warm4((int)o < (int)code_len ? o : 0u, (const void*)code_pc);
        }
        if (lane == 0) stg_sc1(ws, WS_HELLO + b * 4, epoch);
        __builtin_amdgcn_s_setprio(3);
        const unsigned sw0 = (lane >> 3) * 1024u + ct * 128u + (((lane & 7) >> 1) ^ ((lane >> 4) & 3)) * 32u + (lane & 1) * 16u, sw1 = sw0 + 7168, sw2 = sw0 + 14336, sw3 = sw0 + 21504;
        unsigned w1o[7], wqo[3];
#pragma unroll
        for (int r = 0; r < 7; ++r) { const unsigned u = r * 64 + lane, row = u / 14, c = u % 14; w1o[r] = row * 3584u + 224u * j + c * 16u - (r & 3) * 1024u; }
#pragma unroll
        for (int r = 0; r < 3; ++r) { const unsigned u = r * 64 + lane, row = u / 12, c = u % 12; wqo[r] = row * 3072u + (c >> 2) * 1024u + 64u * j + (c & 3) * 16u - r * 1024u; }
#define RUN128(base, first, n4, lds0) { const char* p_ = (const char*)(base) + (size_t)(first) * 8192; unsigned l_ = (lds0); _Pragma("unroll 2") for (int i_ = 0; i_ < (n4); ++i_) { dma4(sw0, sw1, sw2, sw3, p_, l_); p_ += 32768; l_ += 4096; } }
#define W1GRPS(k0, k1) { const char* p_ = (const char*)w1 + (size_t)(k0) * 114688; unsigned l_ = L_W1 + 7 * (k0) * 1024; _Pragma("unroll 1") for (int k_ = (k0); k_ < (k1); ++k_) { \
            dma4(w1o[0], w1o[1], w1o[2], w1o[3], p_, l_); dma3(w1o[4], w1o[5], w1o[6], p_, l_ + 4096); p_ += 114688; l_ += 7168; } }
#define W2CHUNKS(c0, c1, slot0) { _Pragma("unroll 1") for (int c_ = (c0); c_ < (c1); ++c_) { const char* p_ = (const char*)w2 + (size_t)c_ * 32768; const unsigned l_ = ((slot0) + c_ - (c0)) * 16384; \
            dma4(sw0, sw1, sw2, sw3, p_, l_); dma4(sw0, sw1, sw2, sw3, p_ + 56 * 8192, l_ + 4096); dma4(sw0, sw1, sw2, sw3, p_ + 112 * 8192, l_ + 8192); dma4(sw0, sw1, sw2, sw3, p_ + 168 * 8192, l_ + 12288); } }
#define WQGRPS(k0, k1) { const char* p_ = (const char*)wqkv + (size_t)(k0) * 49152; unsigned l_ = L_WQKV + 3 * (k0) * 1024; _Pragma("unroll 2") for (int k_ = (k0); k_ < (k1); ++k_) { dma3(wqo[0], wqo[1], wqo[2], p_, l_); p_ += 49152; l_ += 3072; } }
        __builtin_amdgcn_s_barrier();
        __builtin_amdgcn_s_barrier();
        WQGRPS(14, 24)
        __builtin_amdgcn_s_barrier();
        WQGRPS(0, 14)
        VMWAIT(0);
        __builtin_amdgcn_s_barrier();
        mod_part(cvec, wmod, ws, epoch, lane, 32 + 8 * xl + (idx & 3), idx >> 2); mod_part(cvec, wmod, ws, epoch, lane, 36 + 8 * xl + (idx & 3), idx >> 2);
        if (idx < 4) { PartSum p; mod_sum_load(p, ws, lane, 32 + 8 * xl + idx); mod_sum_finish(p, ws, epoch, lane, 32 + 8 * xl + idx); mod_sum_load(p, ws, lane, 36 + 8 * xl + idx); mod_sum_finish(p, ws, epoch, lane, 36 + 8 * xl + idx); }
        __builtin_amdgcn_s_barrier();
        __builtin_amdgcn_s_barrier();
        if (g >= 1) {
            const unsigned fo = WS_FLAG + (((g >= 2 ? g - 2 + (lane & 1) : 0) * 16 + j) * 256);
            for (unsigned sp = 0; sp < SPIN_CAP; ++sp) { unsigned f = ldg_sc1(ws, fo); if (__all(f == epoch)) break; }
            const unsigned lo16 = lane * 16;
#pragma unroll 1
            for (int src = (g >= 2 ? 0 : 1); src < 2; ++src) {
                const char* p_ = ws + WS_KV + (size_t)((g - 2 + src) * 16 + j) * 16384;
                dma4lin_c(lo16, p_, L_KIMG + src * 8192); dma4lin_c(lo16, p_ + 4096, L_KIMG + src * 8192 + 4096);
                dma4lin_c(lo16, p_ + 8192, L_VIMG + src * 8192); dma4lin_c(lo16, p_ + 12288, L_VIMG + src * 8192 + 4096);
            }
        }
        VMWAIT(0);
        __builtin_amdgcn_s_barrier();
        RUN128(wo, 0, 16, L_WO)
        W1GRPS(3, 7)
        W1GRPS(15, 16)
        __builtin_amdgcn_s_barrier();
        W1GRPS(7, 15)
        VMWAIT(56);
        __builtin_amdgcn_s_barrier();
        __builtin_amdgcn_s_barrier();
        __builtin_amdgcn_s_barrier();
        __builtin_amdgcn_s_barrier();
        W1GRPS(0, 3)
        __builtin_amdgcn_s_barrier();
        VMWAIT(0);
        __builtin_amdgcn_s_barrier();
        __builtin_amdgcn_s_barrier();
        W2CHUNKS(0, 2, 0)
        __builtin_amdgcn_s_barrier();
        __builtin_amdgcn_s_barrier();
        W2CHUNKS(2, 6, 2)
        __builtin_amdgcn_s_barrier();
        W2CHUNKS(6, 9, 6)
        VMWAIT(48);
        __builtin_amdgcn_s_barrier();
        VMWAIT(0);
        __builtin_amdgcn_s_barrier();
        __builtin_amdgcn_s_barrier();
        __builtin_amdgcn_s_barrier();
        __builtin_amdgcn_s_barrier();
        return;
    }
    const __amdgpu_buffer_rsrc_t rws = __builtin_amdgcn_make_buffer_rsrc(ws, 0, WS_BYTES, 0x00020000);
    const int m = w & 3, kh = w >> 2;
#define XTILE(t) { const unsigned xo = (unsigned)(128 * g + 32 * (t)) * 1024u + ((unsigned)lane << 4); \
        _Pragma("unroll 2") for (int i = 0; i < 32; i += 4) dma4(xo ^ (unsigned)((i & 15) << 4), xo ^ (unsigned)(((i & 15) + 1) << 4), xo ^ (unsigned)(((i & 15) + 2) << 4), xo ^ (unsigned)(((i & 15) + 3) << 4), \
                                                                 (const char*)x + i * 1024, L_X + (3 - (t)) * 32768 + i * 1024); }
    PartSum psum;
    if (w >= 4 && w < 7) XTILE(w - 4)
    if (w == 0) lds_st<u32x4>(L_ONES, (u32x4){0x3F803F80u, 0x3F803F80u, 0x3F803F80u, 0x3F803F80u});
    if (w == 3) { __builtin_amdgcn_s_setprio(2); mod_part(cvec, wmod, ws, epoch, lane, 4 * xl + (idx & 3), idx >> 2); __builtin_amdgcn_s_setprio(0); }
    STAMP(1); WS6(0);
    bf16x8 qf; u32x4 kpk, vpk; float qown[8];
    {
    const int h = lane >> 5, gi = lane >> 4, q4 = (lane & 15) >> 2, p4 = lane & 3;
    u32x4 xr[16];
#define XFRAGS() { const unsigned tok = lane & 31, rb = L_X + (3 - m) * 32768 + tok * 1024 + kh * 512; \
        _Pragma("unroll") for (int s = 0; s < 16; ++s) xr[s] = lds_ld<u32x4>(rb + (s >> 3) * 256 + ((((2 * (s & 7) + h) ^ (tok & 15))) << 4)); \
        _Pragma("unroll") for (int s = 0; s < 16; ++s) PIN(xr[s]); }
    if (w == 4) VMWAIT(0);
    BAR();
    WS6(1);
    if (w == 4) XTILE(3)
    if (w == 7) {
        unsigned wq[3];
#pragma unroll
        for (int r = 0; r < 3; ++r) { const unsigned u = r * 64 + lane, row = u / 12, c = u % 12; wq[r] = row * 3072u + (c >> 2) * 1024u + 64u * j + (c & 3) * 16u - r * 1024u; }
#pragma unroll 2
        for (int k = 24; k < 32; ++k) dma3(wq[0], wq[1], wq[2], (const char*)wqkv + (size_t)k * 49152, L_WQKV + 3 * k * 1024); }
    if (w == 3 && idx < 4) mod_sum_load(psum, ws, lane, 4 * xl + idx);
    if (m == 0) XFRAGS()
    if (w == 5 || w == 6) VMWAIT(0);
    BAR();
    WS6(2);
    if (w == 3 && idx < 4) mod_sum_finish(psum, ws, epoch, lane, 4 * xl + idx);
    if (m == 1 || m == 2) XFRAGS()
    if (w == 4) VMWAIT(0);
    BAR();
    if (m == 3) XFRAGS()
#undef XFRAGS
#undef XTILE
    STAMP(2);
    const unsigned gmo = WS_MODU + g * 4096 + (tid & 255) * 16; u32x4 gmv;
    if (w < 4) gmv = __builtin_amdgcn_raw_buffer_load_b128(rws, gmo, 0, 16);
    SBAR();
    {
        float s0 = 0.f, s1 = 0.f, s2 = 0.f, s3 = 0.f; const bf2 one = {(__bf16)1.0f, (__bf16)1.0f};
#pragma unroll
        for (int s = 0; s < 16; ++s)
#pragma unroll
            for (int i = 0; i < 4; i += 2) { const unsigned ua = xr[s][i], ub = xr[s][i + 1]; const bf2 va = __builtin_bit_cast(bf2, ua), vb = __builtin_bit_cast(bf2, ub);
                s0 = __builtin_amdgcn_fdot2_f32_bf16(va, one, s0, false); s1 = __builtin_amdgcn_fdot2_f32_bf16(va, va, s1, false);
                s2 = __builtin_amdgcn_fdot2_f32_bf16(vb, one, s2, false); s3 = __builtin_amdgcn_fdot2_f32_bf16(vb, vb, s3, false); }
        s0 = xsum32(s0 + s2); s1 = xsum32(s1 + s3);
        if (lane < 32) lds_st<u32x2>(L_STAT + (kh * 128 + 32 * m + lane) * 8, (u32x2){__float_as_uint(s0), __float_as_uint(s1)});
    }
    WS6(3);
    if (w < 4) {
        for (unsigned sp = 0; sp < SPIN_CAP; ++sp) { if (__all(gmv[1] == epoch && gmv[3] == epoch)) break; asm volatile("" ::: "memory"); gmv = __builtin_amdgcn_raw_buffer_load_b128(rws, gmo, 0, 16); }
        const unsigned u0 = gmv[0], u2 = gmv[2]; f32x4 f = {bf_lo(u0), bf_hi(u0), bf_lo(u2), bf_hi(u2)};
        if (tid >= 128) { f[0] = rbf(1.f + f[0]); f[1] = rbf(1.f + f[1]); f[2] = rbf(1.f + f[2]); f[3] = rbf(1.f + f[3]); }
        lds_st<f32x4>(L_SAG + tid * 16, f);
    }
    STAMP(3);
    VMWAIT(0);
    BAR();
    STAMP(4); STAMPC(22); WS1(0);
    float rstd, nmr, gv[2]; f32x4 sa0[2], sa1[2];
    const unsigned sga = L_SAG + (256 * kh + 16 * ((lane & 15) >> 3) + 8 * h + (lane & 7)) * 4;
    const unsigned saa = L_SAG + (256 * kh + 8 * h) * 4;
    { const u32x2 a = lds_ld<u32x2>(L_STAT + (32 * m + (lane & 31)) * 8), c2 = lds_ld<u32x2>(L_STAT + (128 + 32 * m + (lane & 31)) * 8);
      gv[0] = lds_ld<float>(sga + 2048);
      sa0[0] = lds_ld<f32x4>(saa); sa1[0] = lds_ld<f32x4>(saa + 16);
      const float s0 = __uint_as_float(a[0]) + __uint_as_float(c2[0]), s1 = __uint_as_float(a[1]) + __uint_as_float(c2[1]);
      const float mean = s0 * (1.f / 512.f), var = fmaxf(s1 * (1.f / 512.f) - mean * mean, 0.f);
      rstd = __builtin_amdgcn_rsqf(var + 1e-5f); nmr = -mean * rstd;
      asm volatile("s_nop 1" : "+v"(gv[0])); }
    f32x16 aQ, aK, aV;
#pragma unroll
    for (int i = 0; i < 16; ++i) { aQ[i] = 0.f; aK[i] = 0.f; aV[i] = 0.f; }
    {
        const unsigned rowa = L_WQKV + (256 * kh + 8 * (gi >> 1) + q4) * 192 + p4 * 8;
        const unsigned wqa = rowa + (((gi & 1) ^ kh) * 32), wva = rowa + 128 + (gi & 1) * 32;
        bf16x8 fk, fv;
#define HDW(s, i) hdw<8 * ((s) & 1) + 2 * (i)>(xr[s][i], rstd, nmr, gv[((s) >> 1) & 1], (i) < 2 ? sa0[(s) & 1][2 * (i) & 3] : sa1[(s) & 1][2 * (i) & 3], (i) < 2 ? sa0[(s) & 1][(2 * (i) + 1) & 3] : sa1[(s) & 1][(2 * (i) + 1) & 3])
        fk = trfrag(wqa + 64, wqa + 64 + 768); fv = trfrag(wva, wva + 768);
        sa0[1] = lds_ld<f32x4>(saa + 64); sa1[1] = lds_ld<f32x4>(saa + 64 + 16);
        { const unsigned a0 = HDW(0, 0), a1 = HDW(0, 1), a2 = HDW(0, 2), a3 = HDW(0, 3); xr[0] = (u32x4){a0, a1, a2, a3}; }
        asm volatile("s_nop 1" : "+v"(xr[0])); SBAR();
#define QKV_STEP(s, sn) { const bf16x8 hb = __builtin_bit_cast(bf16x8, xr[s]); unsigned n0, n1, n2, n3; \
            aK = MFMA32(fk, hb, aK); PIN(aK); SBAR(); if ((sn) < 16) { fk = trfrag(wqa + (sn) * 3072 + 64, wqa + (sn) * 3072 + 64 + 768); if ((s) + 2 < 16) sa0[(s) & 1] = lds_ld<f32x4>(saa + ((s) + 2) * 64); \
                n0 = HDW((sn) & 15, 0); n1 = HDW((sn) & 15, 1); PIN(n0); PIN(n1); } SBAR(); \
            aV = MFMA32(hb, fv, aV); PIN(aV); SBAR(); if ((sn) < 16) { fv = trfrag(wva + (sn) * 3072, wva + (sn) * 3072 + 768); if ((s) + 2 < 16) sa1[(s) & 1] = lds_ld<f32x4>(saa + ((s) + 2) * 64 + 16); \
                n2 = HDW((sn) & 15, 2); n3 = HDW((sn) & 15, 3); PIN(n2); PIN(n3); xr[(sn) & 15] = (u32x4){n0, n1, n2, n3}; } \
            if ((s) % 2 == 0 && (s) + 2 < 16) { gv[(((s) >> 1) + 1) & 1] = lds_ld<float>(sga + 2048 + (((s) >> 1) + 1) * 128); } SBAR(); }
        WS1(1);
        QKV_STEP(0, 1) QKV_STEP(1, 2) QKV_STEP(2, 3) QKV_STEP(3, 4) QKV_STEP(4, 5) QKV_STEP(5, 6) QKV_STEP(6, 7) QKV_STEP(7, 8)
        QKV_STEP(8, 9) QKV_STEP(9, 10) QKV_STEP(10, 11) QKV_STEP(11, 12) QKV_STEP(12, 13) QKV_STEP(13, 14) QKV_STEP(14, 15) QKV_STEP(15, 16)
#undef QKV_STEP
#undef HDW
        STAMP(5); STAMPC(23); WS1(2);
        float vs[8], vk[8];
#pragma unroll
        for (int i = 0; i < 8; ++i) { float lo = aV[i], hi = aV[8 + i]; PIN(lo); PIN(hi); vs[i] = kh ? lo : hi; vk[i] = kh ? hi : lo; }
        const unsigned ex = w * 6144 + lane * 16;
        lds_st<f32x4>(ex, (f32x4){aK[8], aK[9], aK[10], aK[11]}); lds_st<f32x4>(ex + 1024, (f32x4){aK[12], aK[13], aK[14], aK[15]});
        lds_st<f32x4>(ex + 2048, (f32x4){vs[0], vs[1], vs[2], vs[3]}); lds_st<f32x4>(ex + 3072, (f32x4){vs[4], vs[5], vs[6], vs[7]});
        bf16x8 fq = trfrag(wqa, wqa + 768), fq1 = trfrag(wqa + 3072, wqa + 3072 + 768);
        BAR();
        WS1(3);
        const unsigned ep = (w ^ 4) * 6144 + lane * 16;
        { const f32x4 k0 = lds_ld<f32x4>(ep), k1 = lds_ld<f32x4>(ep + 1024), v0 = lds_ld<f32x4>(ep + 2048), v1 = lds_ld<f32x4>(ep + 3072);
          u32x4 kp, vp;
          kp[0] = pk2(aK[0] + k0[0], aK[1] + k0[1]); kp[1] = pk2(aK[2] + k0[2], aK[3] + k0[3]); kp[2] = pk2(aK[4] + k1[0], aK[5] + k1[1]); kp[3] = pk2(aK[6] + k1[2], aK[7] + k1[3]);
          vp[0] = pk2(vk[0] + v0[0], vk[1] + v0[1]); vp[1] = pk2(vk[2] + v0[2], vk[3] + v0[3]); vp[2] = pk2(vk[4] + v1[0], vk[5] + v1[1]); vp[3] = pk2(vk[6] + v1[2], vk[7] + v1[3]);
          const unsigned kvo = WS_KV + (g * 16 + j) * 16384 + (m * 2 + kh) * 1024 + lane * 16;
          __builtin_amdgcn_raw_buffer_store_b128(kp, rws, kvo, 0, 16);
          __builtin_amdgcn_raw_buffer_store_b128(vp, rws, kvo + 8192, 0, 16);
          PIN(kp); PIN(vp); kpk = kp; vpk = vp; }
        SBAR();
#pragma unroll
        for (int s = 0; s < 16; s += 2) {
            aQ = MFMA32(fq, __builtin_bit_cast(bf16x8, xr[s]), aQ); PIN(aQ); SBAR(); if (s + 2 < 16) fq = trfrag(wqa + (s + 2) * 3072, wqa + (s + 2) * 3072 + 768); SBAR();
            aQ = MFMA32(fq1, __builtin_bit_cast(bf16x8, xr[s + 1]), aQ); PIN(aQ); SBAR(); if (s + 3 < 16) fq1 = trfrag(wqa + (s + 3) * 3072, wqa + (s + 3) * 3072 + 768); SBAR(); }
        lds_st<f32x4>(ex + 4096, (f32x4){aQ[8], aQ[9], aQ[10], aQ[11]}); lds_st<f32x4>(ex + 5120, (f32x4){aQ[12], aQ[13], aQ[14], aQ[15]});
#pragma unroll
        for (int i = 0; i < 8; ++i) qown[i] = aQ[i];
    }
    }
    WS1(4);
    VMWAIT(0); WS1(5);
    BAR();
    if (tid == 0) stg_sc1(ws, WS_FLAG + (0 * 256 + g * 16 + j) * 256, epoch);
    STAMP(6);
    { FRESH(lane);
      const unsigned ep = (w ^ 4) * 6144 + 4096 + lane * 16; const f32x4 q0 = lds_ld<f32x4>(ep), q1 = lds_ld<f32x4>(ep + 1024);
      lds_st<u32x4>(L_KIMG + (8 + m) * 2048 + kh * 1024 + lane * 16, kpk);
      lds_st<u32x4>(L_VIMG + (8 + m) * 2048 + kh * 1024 + lane * 16, vpk);
      const float qs = 0.25f * 1.44269504089f; u32x4 qp;
      qp[0] = pk2((qown[0] + q0[0]) * qs, (qown[1] + q0[1]) * qs); qp[1] = pk2((qown[2] + q0[2]) * qs, (qown[3] + q0[3]) * qs);
      qp[2] = pk2((qown[4] + q1[0]) * qs, (qown[5] + q1[1]) * qs); qp[3] = pk2((qown[6] + q1[2]) * qs, (qown[7] + q1[3]) * qs);
      qf = __builtin_bit_cast(bf16x8, qp); PIN(qf); }
    const int tt = w & 3;
    {
    FRESH(lane);
    const int hh = kh, qq = lane & 31, h = lane >> 5;
    const float NEG = -__builtin_inff(), fq0 = (float)(4 * h - qq) - 0.5f;
    f32x16 c0, c8;
#pragma unroll
    for (int r = 0; r < 16; ++r) {
        const float t = fmaf(fq0, 1e30f, (float)((r & 3) + 8 * (r >> 2)) * 1e30f); c0[r] = fminf(t, 0.f); c8[r] = fminf(-t, 0.f); }
    PIN(c0); PIN(c8);
    BAR();
    STAMP(8); WS2(0);
    {
        const unsigned ka = L_KIMG + m * 2048 + hh * 1024 + lane * 16;
        const bool isd = ((lane >> 4) & 1) == hh;
        const unsigned va = isd ? L_VIMG + m * 2048 + lane * 16 : L_ONES, vst = isd ? 2048u : 0u, vs1 = isd ? 1024u : 0u;
        f32x16 o;
#pragma unroll
        for (int r = 0; r < 16; ++r) o[r] = 0.f;
#define MAX16(mx, s) { _Pragma("unroll") for (int r = 0; r < 16; r += 2) mx = fmaxf(fmaxf(mx, s[r]), s[r + 1]); }
#define EXPH(P, S, b) { P[0] = pk2(__builtin_amdgcn_exp2f(S[b]), __builtin_amdgcn_exp2f(S[b + 1])); P[1] = pk2(__builtin_amdgcn_exp2f(S[b + 2]), __builtin_amdgcn_exp2f(S[b + 3])); \
                        P[2] = pk2(__builtin_amdgcn_exp2f(S[b + 4]), __builtin_amdgcn_exp2f(S[b + 5])); P[3] = pk2(__builtin_amdgcn_exp2f(S[b + 6]), __builtin_amdgcn_exp2f(S[b + 7])); }
        if (g >= 2) {
            f32x16 z;
#pragma unroll
            for (int r = 0; r < 16; ++r) z[r] = 0.f;
            PIN(z);
            float mx = NEG, mx2 = NEG; f32x16 sA, sB;
#define KF(i) lds_ld<bf16x8>(ka + (i) * 2048)
            bf16x8 k0 = KF(0), k1 = KF(8), k2 = KF(1), k3 = KF(2);
            SBAR();
            sA = MFMA32(k0, qf, c0); PIN(sA); k0 = KF(3); SBAR();
            sB = MFMA32(k1, qf, c8); PIN(sB); k1 = KF(4); SBAR(); MAX16(mx, sA); PIN(mx); SBAR();
            sA = MFMA32(k2, qf, z); PIN(sA); k2 = KF(5); SBAR(); MAX16(mx2, sB); PIN(mx2); SBAR();
            sB = MFMA32(k3, qf, z); PIN(sB); k3 = KF(6); SBAR(); MAX16(mx, sA); PIN(mx); SBAR();
            sA = MFMA32(k0, qf, z); PIN(sA); k0 = KF(7); SBAR(); MAX16(mx2, sB); PIN(mx2); SBAR();
            sB = MFMA32(k1, qf, z); PIN(sB); k1 = KF(0); SBAR(); MAX16(mx, sA); PIN(mx); SBAR();
            sA = MFMA32(k2, qf, z); PIN(sA); k2 = KF(1); SBAR(); MAX16(mx2, sB); PIN(mx2); SBAR();
            sB = MFMA32(k3, qf, z); PIN(sB); SBAR(); MAX16(mx, sA); PIN(mx); SBAR();
            sA = MFMA32(k0, qf, z); PIN(sA); SBAR(); MAX16(mx2, sB); PIN(mx2); SBAR();
            WS2(1);
            bf16x8 v0 = lds_ld<bf16x8>(va), v1 = lds_ld<bf16x8>(va + vs1);
            MAX16(mx, sA);
            mx = xmax32(fmaxf(mx, mx2));
#pragma unroll
            for (int r = 0; r < 16; ++r) { z[r] = -mx; c0[r] -= mx; c8[r] -= mx; }
            PIN(z); PIN(c0); PIN(c8); SBAR();
            WS2(2);
            sA = MFMA32(k1, qf, c0); SBAR();
            u32x4 pa, pb; bf16x8 v0n, v1n, kn = k2; unsigned vp = va;
#define ABLK(I, SC, SN, CN) { bf16x8 kc = kn; if ((I) < 8) { vp += vst; v0n = lds_ld<bf16x8>(vp); v1n = lds_ld<bf16x8>(vp + vs1); } if ((I) < 7) kn = KF((I) + 2); SBAR(); \
                if ((I) < 8) { SN = MFMA32(kc, qf, CN); PIN(SN); } SBAR(); EXPH(pa, SC, 0); PIN(pa); SBAR(); \
                o = MFMA32(v0, __builtin_bit_cast(bf16x8, pa), o); PIN(o); SBAR(); EXPH(pb, SC, 8); PIN(pb); SBAR(); \
                o = MFMA32(v1, __builtin_bit_cast(bf16x8, pb), o); PIN(o); SBAR(); if ((I) < 8) { v0 = v0n; v1 = v1n; } }
            ABLK(0, sA, sB, z) ABLK(1, sB, sA, z) ABLK(2, sA, sB, z) ABLK(3, sB, sA, z)
            ABLK(4, sA, sB, z) ABLK(5, sB, sA, z) ABLK(6, sA, sB, z) ABLK(7, sB, sA, c8) ABLK(8, sA, sB, c8)
#undef KF
#undef ABLK
        } else {
            const int i0 = 8 - 4 * g - m;
            f32x16 z;
#pragma unroll
            for (int r = 0; r < 16; ++r) z[r] = 0.f;
            float mx = NEG;
            f32x16 s = MFMA32(lds_ld<bf16x8>(ka + 8 * 2048), qf, c8); MAX16(mx, s);
#pragma unroll 1
            for (int i = i0; i < 8; ++i) { s = MFMA32(lds_ld<bf16x8>(ka + i * 2048), qf, z); MAX16(mx, s); }
            mx = xmax32(mx);
#pragma unroll
            for (int r = 0; r < 16; ++r) { z[r] = -mx; c8[r] -= mx; }
#pragma unroll 1
            for (int i = i0; i <= 8; ++i) { u32x4 pa, pb;
                if (i < 8) s = MFMA32(lds_ld<bf16x8>(ka + i * 2048), qf, z); else s = MFMA32(lds_ld<bf16x8>(ka + i * 2048), qf, c8);
                EXPH(pa, s, 0); EXPH(pb, s, 8);
                o = MFMA32(lds_ld<bf16x8>(va + i * vst), __builtin_bit_cast(bf16x8, pa), o); o = MFMA32(lds_ld<bf16x8>(va + i * vst + vs1), __builtin_bit_cast(bf16x8, pb), o); }
        }
#undef EXPH
#undef MAX16
        WS2(3);
        float ov[8], l;
        { float lo = o[0], hi = o[8]; PIN(lo); PIN(hi); l = hh ? lo : hi; }
#pragma unroll
        for (int r = 0; r < 8; ++r) { float lo = o[r], hi = o[8 + r]; PIN(lo); PIN(hi); ov[r] = hh ? hi : lo; }
        const float inv = __builtin_amdgcn_rcpf(l);
        const u32x4 op = {pk2(ov[0] * inv, ov[1] * inv), pk2(ov[2] * inv, ov[3] * inv), pk2(ov[4] * inv, ov[5] * inv), pk2(ov[6] * inv, ov[7] * inv)};
        __builtin_amdgcn_raw_buffer_store_b128(op, rws, WS_O + ((g * 8 + 2 * m + (qq >> 4)) * 16 + j) * 1024 + ((qq & 15) + 16 * (2 * hh + h)) * 16, 0, 16);
    }
    }
    WS2(4);
    VMWAIT(0); WS2(5);
    BAR();
    if (tid == 0) stg_sc1(ws, WS_FLAG + (1 * 256 + g * 16 + j) * 256, epoch);
    STAMP(9);
    if (w >= 4) {
        const int t = tid - 256; const unsigned o = WS_MODN + g * 8192 + t * 32; u32x4 v, v2;
        for (unsigned sp = 0; sp < SPIN_CAP; ++sp) { asm volatile("" ::: "memory"); v = __builtin_amdgcn_raw_buffer_load_b128(rws, o, 0, 16); v2 = __builtin_amdgcn_raw_buffer_load_b128(rws, o + 16, 0, 16);
            if (__all(v[1] == epoch && v[3] == epoch && v2[1] == epoch && v2[3] == epoch)) break; }
        u32x4 val = {v[0], v[2], v2[0], v2[2]};
        const int vec = t >> 6, c8 = (t & 63) * 8;
        if (vec == 1) lds_st<u32x4>(L_SB16 + c8 * 2, val);
        else if (vec == 2) {
#pragma unroll
            for (int i = 0; i < 4; ++i) { const unsigned u_ = val[i]; val[i] = pk2(1.f + bf_lo(u_), 1.f + bf_hi(u_)); }
            lds_st<u32x4>(L_GB16 + c8 * 2, val);
        } else if ((c8 >> 6) == ct) lds_st<u32x4>((vec == 0 ? L_GAO : L_GBO) + (c8 & 63) * 2, val);
    }
    if (w == 0) poll_flags(ws, WS_FLAG + (1 * 256 + g * 16) * 256, 16, epoch, lane);
    BAR();
    STAMP(10); WS3(0);
    u32x2 x1p0, x1p1;
    {
        FRESH(lane);
        const int gi = lane >> 4, q4 = (lane & 15) >> 2, p4 = lane & 3;
        const unsigned tokP = 128 * g + 64 * rh + 16 * tt + (lane & 15), colP = 64 * ct + 32 * kh + 4 * gi;
        u32x4 of[8];
#pragma unroll
        for (int s = 0; s < 8; ++s) of[s] = __builtin_amdgcn_raw_buffer_load_b128(rws, WS_O + ((g * 8 + 4 * rh + tt) * 16 + 8 * kh + s) * 1024 + lane * 16, 0, 16);
        const u32x2 xres0 = *(const u32x2*)(x + tokP * 512 + colP), xres1 = *(const u32x2*)(x + tokP * 512 + colP + 16);
        SBAR();
        f32x4 acc[4];
#pragma unroll
        for (int n = 0; n < 4; ++n) acc[n] = (f32x4){0.f, 0.f, 0.f, 0.f};
        const unsigned xv = 2 * (gi & 1) + (q4 >> 1), rowa = L_WO + (256 * kh + 16 * (gi >> 1) + 4 * (gi & 1) + q4) * 128 + p4 * 8;
        unsigned an[4]; bf16x8 wf[4];
#pragma unroll
        for (int n = 0; n < 4; ++n) { an[n] = rowa + ((n ^ xv) * 32); wf[n] = trfrag(an[n], an[n] + 1024); }
        const unsigned cl = (32 * kh + 4 * gi) * 2, cg = colP * 2;
        const u32x2 ga0 = lds_ld<u32x2>(L_GAO + cl), ga1 = lds_ld<u32x2>(L_GAO + cl + 32);
        const u32x2 gb0 = lds_ld<u32x2>(L_GB16 + cg), gb1 = lds_ld<u32x2>(L_GB16 + cg + 32), sb0 = lds_ld<u32x2>(L_SB16 + cg), sb1 = lds_ld<u32x2>(L_SB16 + cg + 32);
        SBAR();
#pragma unroll
        for (int s = 0; s < 8; ++s)
#pragma unroll
            for (int n = 0; n < 4; ++n) { acc[n] = MFMA16(wf[n], __builtin_bit_cast(bf16x8, of[s]), acc[n]); PIN(acc[n]); SBAR(); if (s < 7) wf[n] = trfrag(an[n] + (s + 1) * 4096, an[n] + (s + 1) * 4096 + 1024); SBAR(); }
        WS3(1);
        BAR();
        STAMP(11);
        const unsigned ex = w * 2048 + lane * 16;
        { f32x4 s0 = acc[0], s1 = acc[1], s2 = acc[2], s3 = acc[3]; PIN(s0); PIN(s1); PIN(s2); PIN(s3);
          lds_st<f32x4>(ex, kh ? s0 : s2); lds_st<f32x4>(ex + 1024, kh ? s1 : s3); acc[0] = kh ? s2 : s0; acc[1] = kh ? s3 : s1; }
        BAR();
        WS3(2);
        const unsigned ep = (w ^ 4) * 2048 + lane * 16;
        const f32x4 a0 = lds_ld<f32x4>(ep) + acc[0], a1 = lds_ld<f32x4>(ep + 1024) + acc[1];
        float xv1[8], gg[8], ss[8];
#pragma unroll
        for (int r = 0; r < 4; ++r) {
            const unsigned ua0 = ga0[r >> 1], ua1 = ga1[r >> 1], ux0 = xres0[r >> 1], ux1 = xres1[r >> 1], ug0 = gb0[r >> 1], ug1 = gb1[r >> 1], us0 = sb0[r >> 1], us1 = sb1[r >> 1];
            xv1[r] = rbf(fmaf((r & 1) ? bf_hi(ua0) : bf_lo(ua0), a0[r], (r & 1) ? bf_hi(ux0) : bf_lo(ux0))); xv1[4 + r] = rbf(fmaf((r & 1) ? bf_hi(ua1) : bf_lo(ua1), a1[r], (r & 1) ? bf_hi(ux1) : bf_lo(ux1)));
            gg[r] = (r & 1) ? bf_hi(ug0) : bf_lo(ug0); gg[4 + r] = (r & 1) ? bf_hi(ug1) : bf_lo(ug1);
            ss[r] = (r & 1) ? bf_hi(us0) : bf_lo(us0); ss[4 + r] = (r & 1) ? bf_hi(us1) : bf_lo(us1);
        }
        x1p0 = (u32x2){pk2(xv1[0], xv1[1]), pk2(xv1[2], xv1[3])}; x1p1 = (u32x2){pk2(xv1[4], xv1[5]), pk2(xv1[6], xv1[7])};
        float st[6] = {0.f, 0.f, 0.f, 0.f, 0.f, 0.f}, pg[8];
#pragma unroll
        for (int r = 0; r < 8; ++r) pg[r] = xv1[r] * gg[r];
        const u32x4 xg = {pk2(pg[0], pg[1]), pk2(pg[2], pg[3]), pk2(pg[4], pg[5]), pk2(pg[6], pg[7])};
        __builtin_amdgcn_raw_buffer_store_b128(xg, rws, WS_XG + ((g * 8 + 4 * rh + tt) * 16 + 2 * ct + kh) * 1024 + lane * 16, 0, 16);
#pragma unroll
        for (int r = 0; r < 8; ++r) { const float p = pg[r]; st[0] += xv1[r]; st[1] = fmaf(xv1[r], xv1[r], st[1]); st[2] += p; st[3] = fmaf(p, p, st[3]); st[4] = fmaf(p, gg[r], st[4]); st[5] = fmaf(p, ss[r], st[5]); }
#pragma unroll
        for (int i = 0; i < 6; ++i) st[i] = xsum32(xsum16(st[i]));
        if (kh == 1 && lane < 16) { lds_st<f32x4>(L_SCR + (tt * 16 + lane) * 32, (f32x4){st[0], st[1], st[2], st[3]}); lds_st<u32x2>(L_SCR + (tt * 16 + lane) * 32 + 16, (u32x2){__float_as_uint(st[4]), __float_as_uint(st[5])}); }
        WS3(3);
        BAR();
        if (kh == 0 && lane < 16) {
            const f32x4 o4 = lds_ld<f32x4>(L_SCR + (tt * 16 + lane) * 32); const u32x2 o2 = lds_ld<u32x2>(L_SCR + (tt * 16 + lane) * 32 + 16);
            const unsigned so = WS_ST + g * 24576, T = 4 * rh + tt;
            const u32x4 sa4 = {__float_as_uint(st[0] + o4[0]), __float_as_uint(st[1] + o4[1]), __float_as_uint(st[2] + o4[2]), __float_as_uint(st[3] + o4[3])};
            const u32x2 sb2 = {__float_as_uint(st[4] + __uint_as_float(o2[0])), __float_as_uint(st[5] + __uint_as_float(o2[1]))};
            __builtin_amdgcn_raw_buffer_store_b128(sa4, rws, so + ((T * 8 + ct) * 16 + lane) * 16, 0, 16);
            __builtin_amdgcn_raw_buffer_store_b64(sb2, rws, so + 16384 + ((T * 8 + ct) * 16 + lane) * 8, 0, 16);
        }
    }
    WS3(4);
    VMWAIT(0); WS3(5);
    BAR();
    if (tid == 0) stg_sc1(ws, WS_FLAG + (2 * 256 + g * 16 + j) * 256, epoch);
    STAMP(12);
    const int odd = j & 1;
    {
    FRESH(lane);
    const int gi = lane >> 4, q4 = (lane & 15) >> 2, p4 = lane & 3;
    const u32x2 b1v = *(const u32x2*)(b1 + 112 * j + (w > 0 ? w - 1 : 0) * 16 + 4 * gi);
    BAR();
    if (w > 0) {
        const int nt = w - 1; f32x4 acc = {0.f, 0.f, 0.f, 0.f};
        const unsigned ra = L_W1 + (4 * gi + q4) * 224 + p4 * 8 + nt * 32; const int tk = lane & 15;
        const unsigned va = (tk == 0 ? L_GB16 : L_SB16) + 8 * gi;
#pragma unroll
        for (int s = 0; s < 16; ++s) {
            const u32x2 lo = lds_ld<u32x2>(va + s * 64), hi = lds_ld<u32x2>(va + s * 64 + 32);
            u32x4 bv = {lo[0], lo[1], hi[0], hi[1]};
            if (tk == 2) bv = (u32x4){0x3F803F80u, 0x3F803F80u, 0x3F803F80u, 0x3F803F80u};
            if (tk > 2) bv = (u32x4){0u, 0u, 0u, 0u};
            acc = MFMA16(trfrag(ra + s * 7168, ra + s * 7168 + 3584), __builtin_bit_cast(bf16x8, bv), acc);
        }
        if (tk < 3) lds_st<f32x4>(L_UVW + (tk * 112 + nt * 16 + 4 * gi) * 4, acc);
        if (tk == 3) lds_st<f32x4>(L_UVW + (3 * 112 + nt * 16 + 4 * gi) * 4, (f32x4){bf_lo(b1v[0]), bf_hi(b1v[0]), bf_lo(b1v[1]), bf_hi(b1v[1])});
    } else {
        const u32x4 gq = lds_ld<u32x4>(L_GB16 + lane * 16), sq = lds_ld<u32x4>(L_SB16 + lane * 16);
        float c5[5] = {0.f, 0.f, 0.f, 0.f, 0.f};
#pragma unroll
        for (int i = 0; i < 4; ++i) { const unsigned ug = gq[i], us = sq[i]; const float g0 = bf_lo(ug), g1 = bf_hi(ug), s0 = bf_lo(us), s1 = bf_hi(us);
            c5[0] += g0 + g1; c5[1] += s0 + s1; c5[2] += g0 * g0 + g1 * g1; c5[3] += g0 * s0 + g1 * s1; c5[4] += s0 * s0 + s1 * s1; }
#pragma unroll
        for (int i = 0; i < 5; ++i) c5[i] = xsum32(xsum16(rowsum16(c5[i])));
        if (lane == 0) { lds_st<f32x4>(L_CONST, (f32x4){c5[0], c5[1], c5[2], c5[3]}); lds_st<float>(L_CONST + 16, c5[4]); }
        poll_flags(ws, WS_FLAG + (2 * 256 + g * 16) * 256, 16, epoch, lane);
    }
    }
    BAR();
    STAMP(13);
    {
        FRESH(lane);
        const int gi = lane >> 4, q4 = (lane & 15) >> 2, p4 = lane & 3;
        const int Tw = 2 * m + kh;
        u32x4 xf[2][8];
        const unsigned xo0 = WS_XG + (g * 8 + Tw) * 16384 + kh * 8192 + lane * 16, xo1 = WS_XG + (g * 8 + (Tw ^ 1)) * 16384 + kh * 8192 + lane * 16;
        constexpr int WIN = 5;
#pragma unroll
        for (int s = 0; s < WIN; ++s) { xf[0][s] = __builtin_amdgcn_raw_buffer_load_b128(rws, xo0 + s * 1024, 0, 16); xf[1][s] = __builtin_amdgcn_raw_buffer_load_b128(rws, xo1 + s * 1024, 0, 16); }
        const unsigned so = WS_ST + g * 24576 + Tw * 2048, sob = WS_ST + g * 24576 + 16384 + Tw * 1024;
        u32x4 sA0, sA1; u32x2 sB0, sB1;
        SBAR(); WS4(0);
        f32x4 acc[7], acb[7]; bf16x8 wf[7];
        const unsigned ra = L_W1 + (256 * kh + 4 * gi + q4) * 224 + p4 * 8, raA = ra + 32 * odd, raB = ra + (odd ? 0 : 192);
#define W1A(s, u) (((u) < 6 ? raA : raB) + (s) * 7168 + ((u) < 6 ? (u) * 32 : 0))
#pragma unroll
        for (int u = 0; u < 7; ++u) { acc[u] = (f32x4){0.f, 0.f, 0.f, 0.f}; acb[u] = acc[u]; wf[u] = trfrag(W1A(0, u), W1A(0, u) + 3584); }
        SBAR();
#pragma unroll
        for (int s = 0; s < 8; ++s) {
            if (s + WIN < 8) { xf[0][s + WIN] = __builtin_amdgcn_raw_buffer_load_b128(rws, xo0 + (s + WIN) * 1024, 0, 16); xf[1][s + WIN] = __builtin_amdgcn_raw_buffer_load_b128(rws, xo1 + (s + WIN) * 1024, 0, 16); }
            if (s == 4) { sA0 = __builtin_amdgcn_raw_buffer_load_b128(rws, so + ((2 * gi) * 16 + (lane & 15)) * 16, 0, 16); sA1 = __builtin_amdgcn_raw_buffer_load_b128(rws, so + ((2 * gi + 1) * 16 + (lane & 15)) * 16, 0, 16); }
            if (s == 5) { sB0 = __builtin_amdgcn_raw_buffer_load_b64(rws, sob + ((2 * gi) * 16 + (lane & 15)) * 8, 0, 16); sB1 = __builtin_amdgcn_raw_buffer_load_b64(rws, sob + ((2 * gi + 1) * 16 + (lane & 15)) * 8, 0, 16); }
            SBAR();
#pragma unroll
            for (int u = 0; u < 7; ++u) { acc[u] = MFMA16(wf[u], __builtin_bit_cast(bf16x8, xf[0][s]), acc[u]); PIN(acc[u]); SBAR();
                acb[u] = MFMA16(wf[u], __builtin_bit_cast(bf16x8, xf[1][s]), acb[u]); PIN(acb[u]); SBAR();
                if (s < 7) wf[u] = trfrag(W1A(s + 1, u), W1A(s + 1, u) + 3584); SBAR(); }
            if (s == 7) WS4(1);
        }
#undef W1A
        float ca, cb, cc, r2;
        { float S[6];
#pragma unroll
          for (int i = 0; i < 4; ++i) S[i] = xsum32(xsum16(__uint_as_float(sA0[i]) + __uint_as_float(sA1[i])));
#pragma unroll
          for (int i = 0; i < 2; ++i) S[4 + i] = xsum32(xsum16(__uint_as_float(sB0[i]) + __uint_as_float(sB1[i])));
          const f32x4 cc4 = lds_ld<f32x4>(L_CONST); const float SB2 = lds_ld<float>(L_CONST + 16);
          const float iD = 1.f / 512.f, m1 = S[0] * iD, r1 = __builtin_amdgcn_rsqf(fmaxf(S[1] * iD - m1 * m1, 0.f) + 1e-5f);
          const float m2 = (r1 * (S[2] - m1 * cc4[0]) + cc4[1]) * iD;
          const float ez2 = (r1 * r1 * (S[3] - 2.f * m1 * S[4] + m1 * m1 * cc4[2]) + 2.f * r1 * (S[5] - m1 * cc4[3]) + SB2) * iD;
          r2 = __builtin_amdgcn_rsqf(fmaxf(ez2 - m2 * m2, 0.f) + 1e-5f);
          ca = r1 * r2; cb = -ca * m1; cc = -r2 * m2; PIN(ca); PIN(cb); PIN(cc); PIN(r2); }
        const unsigned uo = L_UVW + 16 * gi + 64 * odd, uo6 = L_UVW + 16 * gi + (odd ? 0 : 384);
        f32x4 uu = lds_ld<f32x4>(uo), vv = lds_ld<f32x4>(uo + 448), ww = lds_ld<f32x4>(uo + 896), bb = lds_ld<f32x4>(uo + 1344);
        f32x4 u2 = lds_ld<f32x4>(uo + 64), v2 = lds_ld<f32x4>(uo + 64 + 448), w2v = lds_ld<f32x4>(uo + 64 + 896), b2v = lds_ld<f32x4>(uo + 64 + 1344);
        BAR();
        WS4(2);
        { const unsigned ex = 101376 + w * 7168 + lane * 16;
#pragma unroll
          for (int u = 0; u < 7; ++u) lds_st<f32x4>(ex + u * 1024, acb[u]); }
        WS4(3);
        BAR();
        WS4(4);
        { const unsigned ep = 101376 + (w ^ 4) * 7168 + lane * 16;
#pragma unroll
          for (int u = 0; u < 7; ++u) acc[u] += lds_ld<f32x4>(ep + u * 1024); }
        STAMP(14);
        const unsigned hb = WS_H + (g * 8 + Tw) * 57344, s0 = (7 * j + odd) >> 1;
        const f32x2 kA = {-0.044715f * 2.302208198f, -0.044715f * 2.302208198f}, kB = {-2.302208198f, -2.302208198f}, one2 = {1.f, 1.f};
#define LO2(v) ((f32x2){(v)[0], (v)[1]})
#define HI2(v) ((f32x2){(v)[2], (v)[3]})
#define PRE2(a_, u_, v_, w_, b_) (ca * (a_) + (cb * (u_) + (r2 * (v_) + (cc * (w_) + (b_)))))
#define GELU(N, p, y) { f32x2 z_[N], e_[N]; \
            _Pragma("unroll") for (int r = 0; r < N; ++r) { z_[r] = p[r] * p[r]; PIN(z_[r]); } \
            _Pragma("unroll") for (int r = 0; r < N; ++r) { z_[r] = z_[r] * kA + kB; PIN(z_[r]); } \
            _Pragma("unroll") for (int r = 0; r < N; ++r) { z_[r] = z_[r] * p[r]; PIN(z_[r]); } \
            SBAR(); _Pragma("unroll") for (int r = 0; r < N; ++r) { e_[r] = (f32x2){__builtin_amdgcn_exp2f(z_[r][0]), __builtin_amdgcn_exp2f(z_[r][1])}; PIN(e_[r]); } \
            SBAR(); _Pragma("unroll") for (int r = 0; r < N; ++r) { e_[r] = e_[r] + one2; PIN(e_[r]); } \
            SBAR(); _Pragma("unroll") for (int r = 0; r < N; ++r) { e_[r] = (f32x2){__builtin_amdgcn_rcpf(e_[r][0]), __builtin_amdgcn_rcpf(e_[r][1])}; PIN(e_[r]); } \
            SBAR(); _Pragma("unroll") for (int r = 0; r < N; ++r) y[r] = p[r] * e_[r]; }
#pragma unroll
        for (int u = 0; u < 6; u += 2) {
            f32x2 pre[4], y[4];
            pre[0] = PRE2(LO2(acc[u]), LO2(uu), LO2(vv), LO2(ww), LO2(bb)); pre[1] = PRE2(HI2(acc[u]), HI2(uu), HI2(vv), HI2(ww), HI2(bb));
            pre[2] = PRE2(LO2(acc[u + 1]), LO2(u2), LO2(v2), LO2(w2v), LO2(b2v)); pre[3] = PRE2(HI2(acc[u + 1]), HI2(u2), HI2(v2), HI2(w2v), HI2(b2v));
            PIN(pre[0]); PIN(pre[1]); PIN(pre[2]); PIN(pre[3]);
            SBAR();
            { const unsigned o = u < 4 ? uo + (u + 2) * 64 : uo6; uu = lds_ld<f32x4>(o); vv = lds_ld<f32x4>(o + 448); ww = lds_ld<f32x4>(o + 896); bb = lds_ld<f32x4>(o + 1344); }
            if (u < 4) { const unsigned o = uo + (u + 3) * 64; u2 = lds_ld<f32x4>(o); v2 = lds_ld<f32x4>(o + 448); w2v = lds_ld<f32x4>(o + 896); b2v = lds_ld<f32x4>(o + 1344); }
            SBAR();
            GELU(4, pre, y)
            __builtin_amdgcn_raw_buffer_store_b128((u32x4){pk2(y[0][0], y[0][1]), pk2(y[1][0], y[1][1]), pk2(y[2][0], y[2][1]), pk2(y[3][0], y[3][1])}, rws, hb + (s0 + (u >> 1)) * 1024 + lane * 16, 0, 16);
            SBAR();
        }
        {   f32x2 pre[2], y[2];
            pre[0] = PRE2(LO2(acc[6]), LO2(uu), LO2(vv), LO2(ww), LO2(bb)); pre[1] = PRE2(HI2(acc[6]), HI2(uu), HI2(vv), HI2(ww), HI2(bb));
            GELU(2, pre, y)
            __builtin_amdgcn_raw_buffer_store_b64((u32x2){pk2(y[0][0], y[0][1]), pk2(y[1][0], y[1][1])}, rws, hb + ((7 * j + (odd ? 0 : 6)) >> 1) * 1024 + odd * 512 + lane * 8, 0, 16);
        }
#undef GELU
#undef PRE2
#undef LO2
#undef HI2
        WS4(5);
    }
    VMWAIT(0);
    BAR();
    if (tid == 0) stg_sc1(ws, WS_FLAG + (3 * 256 + g * 16 + j) * 256, epoch);
    STAMP(15);
    u32x4 park[10];
    { FRESH(lane);
      const unsigned sw = (lane >> 3) * 1024u + ct * 128u + (((lane & 7) >> 1) ^ ((lane >> 4) & 3)) * 32u + (lane & 1) * 16u;
      const char* p0 = (const char*)w2 + (size_t)(56 * (w >> 2) + 36 + (w & 3)) * 8192, * p1 = p0 + (size_t)112 * 8192;
#pragma unroll
      for (int i = 0; i < 5; ++i) { park[2 * i] = *(const u32x4*)(p0 + i * 32768 + sw); park[2 * i + 1] = *(const u32x4*)(p1 + i * 32768 + sw); }
      SBAR();
      if (w == 0) poll_flags(ws, WS_FLAG + (3 * 256 + g * 16) * 256, 16, epoch, lane); }
    SBAR();
    BAR();
    STAMP(16); WS5(0);
    {
        constexpr int WIN = 3;
        FRESH(lane);
        const int gi = lane >> 4, q4 = (lane & 15) >> 2, p4 = lane & 3;
        const unsigned tokP = 128 * g + 64 * rh + 16 * tt + (lane & 15), colP = 64 * ct + 32 * kh + 4 * gi;
        const int kq = (w & 1) | (kh << 1);
        const unsigned hb0 = WS_H + (g * 8 + 4 * rh + tt) * 57344 + kq * 14 * 1024, hb1 = WS_H + (g * 8 + 4 * rh + (tt ^ 1)) * 57344 + kq * 14 * 1024;
        u32x4 hf[2][14];
#define HLD1(hb_, dst, s) { if ((s) % 7 == 3) { const u32x2 lo = __builtin_amdgcn_raw_buffer_load_b64(rws, hb_ + (s) * 1024 + lane * 8, 0, 16), hi = __builtin_amdgcn_raw_buffer_load_b64(rws, hb_ + (s) * 1024 + 512 + lane * 8, 0, 16); dst = (u32x4){lo[0], lo[1], hi[0], hi[1]}; } \
                    else dst = __builtin_amdgcn_raw_buffer_load_b128(rws, hb_ + (s) * 1024 + lane * 16, 0, 16); }
#define HLOAD(s) { HLD1(hb0, hf[0][s], s) HLD1(hb1, hf[1][s], s) }
#pragma unroll
        for (int s = 0; s < WIN; ++s) HLOAD(s)
        SBAR();
        f32x4 acc[2][4]; bf16x8 wf[4];
        const unsigned xv = (2 * gi + (q4 >> 1)) & 3, rowa = kq * 4096 + (4 * gi + q4) * 128 + p4 * 8;
        unsigned an[4];
#pragma unroll
        for (int n = 0; n < 4; ++n) { an[n] = rowa + (((n ^ (2 * kh)) ^ xv) * 32); acc[0][n] = (f32x4){0.f, 0.f, 0.f, 0.f}; acc[1][n] = acc[0][n]; }
#define W2A(s, n) (an[n] + (((s) % 9) & 3) * 16384)
#pragma unroll
        for (int n = 0; n < 4; ++n) wf[n] = trfrag(W2A(0, n), W2A(0, n) + 2048);
        const unsigned cl = (32 * kh + 4 * gi) * 2;
        const u32x2 g0 = lds_ld<u32x2>(L_GBO + cl), g1 = lds_ld<u32x2>(L_GBO + cl + 32);
        u32x2 bb0, bb1;
        SBAR();
#pragma unroll
        for (int s = 0; s < 14; ++s) {
            if (s == 11) { bb0 = *(const u32x2*)(b2 + colP); bb1 = *(const u32x2*)(b2 + colP + 16); }
            if (s == 1) WS5(1); if (s == 5) WS5(2); if (s == 8) WS5(3);
            if (s == 5) BAR();
            if (s == 8) BAR();
            if (s == 8) WS5(4);
            if (s + WIN < 14) HLOAD(s + WIN)
            SBAR();
#pragma unroll
            for (int n = 0; n < 4; ++n) { acc[0][n] = MFMA16(wf[n], __builtin_bit_cast(bf16x8, hf[0][s]), acc[0][n]); PIN(acc[0][n]); SBAR();
                acc[1][n] = MFMA16(wf[n], __builtin_bit_cast(bf16x8, hf[1][s]), acc[1][n]); PIN(acc[1][n]); SBAR();
                if (s >= 5 && s <= 7 && 4 * (s - 5) + n < 10) { const int i = 4 * (s - 5) + n; lds_st<u32x4>((i >> 1) * 16384 + (w + 8 * (i & 1)) * 1024 + lane * 16, park[i]); }
                if (s == 3 || s == 7 || s == 12) { an[n] += 65536; PIN(an[n]); } if (s == 8) { an[n] -= 131072; PIN(an[n]); }
                if (s < 13) wf[n] = trfrag(W2A(s + 1, n), W2A(s + 1, n) + 2048); SBAR(); }
        }
#undef W2A
#undef HLOAD
#undef HLD1
        WS5(5);
        BAR();
        STAMP(17);
        { const unsigned ex = w * 6144 + lane * 16;
          lds_st<f32x4>(ex, acc[0][2]); lds_st<f32x4>(ex + 1024, acc[0][3]); lds_st<f32x4>(ex + 2048, acc[1][0]); lds_st<f32x4>(ex + 3072, acc[1][1]); lds_st<f32x4>(ex + 4096, acc[1][2]); lds_st<f32x4>(ex + 5120, acc[1][3]); }
        BAR();
        const unsigned e4 = (w ^ 4) * 6144 + lane * 16, e1 = (w ^ 1) * 6144 + 2048 + lane * 16, e5 = (w ^ 5) * 6144 + 4096 + lane * 16;
        const f32x4 a0 = (acc[0][0] + lds_ld<f32x4>(e4)) + (lds_ld<f32x4>(e1) + lds_ld<f32x4>(e5)), a1 = (acc[0][1] + lds_ld<f32x4>(e4 + 1024)) + (lds_ld<f32x4>(e1 + 1024) + lds_ld<f32x4>(e5 + 1024));
        float y0[4], y1[4];
#pragma unroll
        for (int r = 0; r < 4; ++r) {
            const unsigned ug0 = g0[r >> 1], ug1 = g1[r >> 1], ub0 = bb0[r >> 1], ub1 = bb1[r >> 1], ux0 = x1p0[r >> 1], ux1 = x1p1[r >> 1];
            y0[r] = fmaf((r & 1) ? bf_hi(ug0) : bf_lo(ug0), a0[r] + ((r & 1) ? bf_hi(ub0) : bf_lo(ub0)), (r & 1) ? bf_hi(ux0) : bf_lo(ux0));
            y1[r] = fmaf((r & 1) ? bf_hi(ug1) : bf_lo(ug1), a1[r] + ((r & 1) ? bf_hi(ub1) : bf_lo(ub1)), (r & 1) ? bf_hi(ux1) : bf_lo(ux1));
        }
        *(u32x2*)(out + tokP * 512 + colP) = (u32x2){pk2(y0[0], y0[1]), pk2(y0[2], y0[3])};
        *(u32x2*)(out + tokP * 512 + colP + 16) = (u32x2){pk2(y1[0], y1[1]), pk2(y1[2], y1[3])};
    }
    if (w == 0) { asm volatile("s_waitcnt vmcnt(0)" ::: "memory"); const unsigned long long te = rt(); if (lane == 0) stg64_sc1(ws, 0x1F10000 + b * 8, ((unsigned long long)epoch << 32) | (unsigned)te); }
    if (b == 0 && w == 0) {
        unsigned smin = 0xffffffffu, smax = 0, emax = 0; const unsigned base = (unsigned)dbg_t0 - 2000u;
        for (int i = 0; i < 4; ++i) { unsigned long long a, e2;
            for (unsigned sp = 0; sp < SPIN_CAP; ++sp) { a = ldg64_sc1(ws, 0x1F00000 + (i * 64 + lane) * 8); e2 = ldg64_sc1(ws, 0x1F10000 + (i * 64 + lane) * 8); if (__all((unsigned)(a >> 32) == epoch && (unsigned)(e2 >> 32) == epoch)) break; }
            const unsigned st = (unsigned)a - base, en = (unsigned)e2 - base; smin = st < smin ? st : smin; smax = st > smax ? st : smax; emax = en > emax ? en : emax; }
        for (int o = 32; o; o >>= 1) { unsigned v = __shfl_xor(smin, o); smin = v < smin ? v : smin; v = __shfl_xor(smax, o); smax = v > smax ? v : smax; v = __shfl_xor(emax, o); emax = v > emax ? v : emax; }
        unsigned long long dl = 0; if (dl > 20000) dl = 20000; const unsigned long long t_end = rt() + dl; while (rt() < t_end) __builtin_amdgcn_s_sleep(1);
    }
    if (b == 0 && w == 0) {
        for (unsigned sp = 0; sp < SPIN_CAP; ++sp) { asm volatile("" ::: "memory"); const u32x4 v = __builtin_amdgcn_raw_buffer_load_b128(rws, WS_HELLO + lane * 16, 0, 16); if (__all(v[0] == epoch && v[1] == epoch && v[2] == epoch && v[3] == epoch)) break; }
        if (lane == 0) stg_sc1(ws, WS_E, epoch);
    }
    STAMP(18); STAMPC(21);
    STAMP_OUT();
    asm volatile("fb_code_end:");
}

__global__ void k_tiny(char* ws) { if (threadIdx.x == 0) { unsigned long long t = rt(); *(unsigned*)(ws + 0x1F20000) = (unsigned)t; } }
extern "C" void kernel_launch(void* const* d_in, const int* in_sizes, int n_in, void* d_out, int out_size, void* d_ws, size_t ws_size, hipStream_t stream) {
    (void)hipFuncSetAttribute((const void*)fused_block, hipFuncAttributeMaxDynamicSharedMemorySize, LDS_BYTES);
    k_tiny<<<1, 64, 0, stream>>>((char*)d_ws);
    fused_block<<<256, 576, LDS_BYTES, stream>>>((const u16*)d_in[0], (const u16*)d_in[1], (const u16*)d_in[2], (const u16*)d_in[4], (const u16*)d_in[5], (const u16*)d_in[6], (const u16*)d_in[7],
                                                 (const u16*)d_in[8], (const u16*)d_in[9], (u16*)d_out, (char*)d_ws);
}
```

```cpp
#include <hip/hip_runtime.h>
#include <hip/hip_bf16.h>
#include <stdint.h>

typedef unsigned short u16;
typedef short bf16x8 __attribute__((ext_vector_type(8)));
typedef short s16x4 __attribute__((ext_vector_type(4)));
typedef float f32x4 __attribute__((ext_vector_type(4)));
typedef float f32x2 __attribute__((ext_vector_type(2)));
typedef float f32x16 __attribute__((ext_vector_type(16)));
typedef unsigned u32x4 __attribute__((ext_vector_type(4)));
typedef unsigned u32x2 __attribute__((ext_vector_type(2)));
typedef __bf16 bf2 __attribute__((ext_vector_type(2)));
#define LAS __attribute__((address_space(3)))
#define GAS __attribute__((address_space(1)))
#define DI __device__ __forceinline__

#define TS 0

constexpr unsigned WS_E = 0, WS_HELLO = 4096, WS_FLAG = 8192;
constexpr unsigned WS_MODU = 0x50000, WS_MODN = 0x60000;
constexpr unsigned WS_PART = 0x80000;
constexpr unsigned WS_KV = 0x100000, WS_O = 0x500000, WS_XG = 0x700000, WS_ST = 0x900000, WS_H = 0xA00000, WS_TS = 0x2000000;
constexpr unsigned WS_BYTES = 0x2100000;
constexpr unsigned L_SLOT = 28672, L_EXS = 3 * L_SLOT;
constexpr unsigned L_UVQ = 155648, L_ZERO = 156416, L_RN = 143360, L_G32 = 156672, L_GP = 158720, L_SP = 159744, L_STAT = 160768;
constexpr unsigned L_QS = 65536, L_KIMG = 98304, L_VIMG = 122880, L_WO = 0, L_W1 = 44032;
constexpr unsigned L_GB16 = 158720, L_SB16 = 159744, L_UVW = 160768, L_CONST = 162560, L_GAO = 162592, L_GBO = 162720, L_ONES = 162848, L_ZERO2 = 162864, L_DUMMY = 162880;
constexpr unsigned L_SCR = 32768;
constexpr unsigned LDS_BYTES = 163840;
constexpr unsigned SPIN_CAP = 1u << 16;

DI float bf_lo(unsigned u) { return __uint_as_float(u << 16); }
DI float bf_hi(unsigned u) { return __uint_as_float(u & 0xffff0000u); }
DI unsigned pk2(float a, float b) { bf2 v = {(__bf16)a, (__bf16)b}; return __builtin_bit_cast(unsigned, v); }
DI float rbf(float a) { return bf_lo(pk2(a, a)); }
DI void dma16(unsigned voff, const void* sbase, unsigned lds) { asm volatile("s_mov_b32 m0, %1\n\ts_nop 0\n\tglobal_load_lds_dwordx4 %0, %2" ::"v"(voff), "s"(lds), "s"(sbase) : "memory"); }
DI void dma16c(unsigned voff, const void* sbase, unsigned lds) { asm volatile("s_mov_b32 m0, %1\n\ts_nop 0\n\tglobal_load_lds_dwordx4 %0, %2 sc1" ::"v"(voff), "s"(lds), "s"(sbase) : "memory"); }
DI void dma4(unsigned v0, unsigned v1, unsigned v2, unsigned v3, const void* sbase, unsigned lds) {
    asm volatile("s_mov_b32 m0, %5\n\ts_nop 0\n\tglobal_load_lds_dwordx4 %0, %4\n\tglobal_load_lds_dwordx4 %1, %4 offset:1024\n\tglobal_load_lds_dwordx4 %2, %4 offset:2048\n\tglobal_load_lds_dwordx4 %3, %4 offset:3072"
                 ::"v"(v0), "v"(v1), "v"(v2), "v"(v3), "s"(sbase), "s"(lds) : "memory"); }
DI void dma3(unsigned v0, unsigned v1, unsigned v2, const void* sbase, unsigned lds) {
    asm volatile("s_mov_b32 m0, %4\n\ts_nop 0\n\tglobal_load_lds_dwordx4 %0, %3\n\tglobal_load_lds_dwordx4 %1, %3 offset:1024\n\tglobal_load_lds_dwordx4 %2, %3 offset:2048"
                 ::"v"(v0), "v"(v1), "v"(v2), "s"(sbase), "s"(lds) : "memory"); }
DI void dma4lin_c(unsigned v, const void* sbase, unsigned lds) {
    asm volatile("s_mov_b32 m0, %2\n\ts_nop 0\n\tglobal_load_lds_dwordx4 %0, %1 sc1\n\tglobal_load_lds_dwordx4 %0, %1 offset:1024 sc1\n\tglobal_load_lds_dwordx4 %0, %1 offset:2048 sc1\n\tglobal_load_lds_dwordx4 %0, %1 offset:3072 sc1"
                 ::"v"(v), "s"(sbase), "s"(lds) : "memory"); }
DI void warm4(unsigned voff, const void* sbase) { asm volatile("s_mov_b32 m0, %1\n\ts_nop 0\n\tglobal_load_lds_dword %0, %2" ::"v"(voff), "s"(L_DUMMY), "s"(sbase) : "memory"); }
#define WSP 4
#define WS1(i) do { if (WSP == 1) WSTAMP(i); } while (0)
#define WS2(i) do { if (WSP == 2) WSTAMP(i); } while (0)
#define WS3(i) do { if (WSP == 3) WSTAMP(i); } while (0)
#define WS4(i) do { if (WSP == 4) WSTAMP(i); } while (0)
#define WS5(i) do { if (WSP == 5) WSTAMPG(i); } while (0)
#define WS6(i) do { if (WSP == 6) WSTAMP(i); } while (0)
#define FRESH(v) int v = threadIdx.x & 63; asm volatile("" : "+v"(v))
#define SBAR() __builtin_amdgcn_sched_barrier(0)
#define PIN(x) asm volatile("" : "+v"(x))
#define BAR() asm volatile("s_waitcnt lgkmcnt(0)\n\ts_barrier" ::: "memory")
#define P5WIN 5
#define WARM 1
#define QWIN 40
#define VMWAITN(n) asm volatile("s_waitcnt vmcnt(%0)" :: "i"(n) : "memory")
#define RBAR() asm volatile("s_barrier" ::: "memory")
#define VMWAIT(n) asm volatile("s_waitcnt vmcnt(" #n ")" ::: "memory")
DI unsigned ldg_sc1(const char* ws, unsigned off) { return __hip_atomic_load((GAS unsigned*)(ws + off), __ATOMIC_RELAXED, __HIP_MEMORY_SCOPE_AGENT); }
DI unsigned long long ldg64_sc1(const char* ws, unsigned off) { return __hip_atomic_load((GAS unsigned long long*)(ws + off), __ATOMIC_RELAXED, __HIP_MEMORY_SCOPE_AGENT); }
DI void stg_sc1(char* ws, unsigned off, unsigned v) { __hip_atomic_store((GAS unsigned*)(ws + off), v, __ATOMIC_RELAXED, __HIP_MEMORY_SCOPE_AGENT); }
DI void stg64_sc1(char* ws, unsigned off, unsigned long long v) { __hip_atomic_store((GAS unsigned long long*)(ws + off), v, __ATOMIC_RELAXED, __HIP_MEMORY_SCOPE_AGENT); }
template <class T> DI T lds_ld(unsigned a) { return *(const LAS T*)(size_t)a; }
template <class T> DI void lds_st(unsigned a, T v) { *(LAS T*)(size_t)a = v; }
DI s16x4 lds_tr(unsigned a) { return __builtin_amdgcn_ds_read_tr16_b64_v4i16((LAS s16x4*)(size_t)a); }
DI bf16x8 trfrag(unsigned a0, unsigned a1) { s16x4 lo = lds_tr(a0), hi = lds_tr(a1); return __builtin_shufflevector(lo, hi, 0, 1, 2, 3, 4, 5, 6, 7); }
#define MFMA32(a, b, c) __builtin_amdgcn_mfma_f32_32x32x16_bf16((a), (b), (c), 0, 0, 0)
#define MFMA16(a, b, c) __builtin_amdgcn_mfma_f32_16x16x32_bf16((a), (b), (c), 0, 0, 0)
template <int N> DI unsigned xgdw(unsigned x, float gv) {
    unsigned o; float a, b2;
    asm("v_lshlrev_b32 %1, 16, %3\n\tv_and_b32 %2, 0xffff0000, %3\n\t"
        "v_mul_f32_dpp %1, %4, %1 row_newbcast:%c5 row_mask:0xf bank_mask:0xf\n\tv_mul_f32_dpp %2, %4, %2 row_newbcast:%c6 row_mask:0xf bank_mask:0xf\n\t"
        "v_cvt_pk_bf16_f32 %0, %1, %2"
        : "=v"(o), "=&v"(a), "=&v"(b2) : "v"(x), "v"(gv), "i"(N), "i"(N + 1));
    return o; }
template <int C> DI float dppf(float v) { return __builtin_bit_cast(float, __builtin_amdgcn_update_dpp(0, __builtin_bit_cast(int, v), C, 0xf, 0xf, true)); }
DI float rowsum16(float v) { v += dppf<0xB1>(v); v += dppf<0x4E>(v); v += dppf<0x141>(v); v += dppf<0x140>(v); return v; }
DI float xsum32(float v) { auto r = __builtin_amdgcn_permlane32_swap(__float_as_uint(v), __float_as_uint(v), false, false); return __uint_as_float(r[0]) + __uint_as_float(r[1]); }
DI float xmax32(float v) { auto r = __builtin_amdgcn_permlane32_swap(__float_as_uint(v), __float_as_uint(v), false, false); return fmaxf(__uint_as_float(r[0]), __uint_as_float(r[1])); }
DI float xsum16(float v) { auto r = __builtin_amdgcn_permlane16_swap(__float_as_uint(v), __float_as_uint(v), false, false); return __uint_as_float(r[0]) + __uint_as_float(r[1]); }
DI unsigned long long mt() { unsigned long long t; asm volatile("s_memtime %0\n\ts_waitcnt lgkmcnt(0)" : "=s"(t)::"memory"); return t; }
DI unsigned long long rt() { unsigned long long t; asm volatile("s_memrealtime %0\n\ts_waitcnt lgkmcnt(0)" : "=s"(t)::"memory"); return t; }
#define STAMP(i) do {} while (0)
#define STAMP_OUT() do {} while (0)
#define STAMPG(i) do {} while (0)
#define STAMPGC(i) do {} while (0)
#define WSTAMPG(i) do {} while (0)
#define STAMPC(i) do {} while (0)
#define WSTAMP(i) do {} while (0)

DI void poll_flags(const char* ws, unsigned off, int n, unsigned epoch, int lane) {
    const unsigned o = off + (unsigned)(lane < n ? lane : 0) * 256u;
    for (unsigned sp = 0; sp < SPIN_CAP; ++sp) { unsigned f = ldg_sc1(ws, o); if (__all(f == epoch)) break; }
}

DI void mod_part(const u16* __restrict__ cvec, const u16* __restrict__ wmod, char* ws, unsigned epoch, int lane, int slice, int kp) {
    const unsigned row0 = 64 * kp + 4 * (lane >> 2), o = row0 * 6144u + (unsigned)slice * 64u + (lane & 3) * 16u;
    const u32x2 cv = *(const u32x2*)(cvec + row0);
    u32x4 wm[4];
#pragma unroll
    for (int i = 0; i < 4; ++i) wm[i] = *(const u32x4*)((const char*)wmod + o + i * 6144u);
    SBAR();
    unsigned sp[2];
#pragma unroll
    for (int r = 0; r < 2; ++r) { const unsigned cu = cv[r]; const float c0 = bf_lo(cu), c1 = bf_hi(cu);
        sp[r] = pk2(c0 * __builtin_amdgcn_rcpf(1.f + __builtin_amdgcn_exp2f(-1.44269504089f * c0)), c1 * __builtin_amdgcn_rcpf(1.f + __builtin_amdgcn_exp2f(-1.44269504089f * c1))); }
    float a[8] = {0.f, 0.f, 0.f, 0.f, 0.f, 0.f, 0.f, 0.f};
#pragma unroll
    for (int r = 0; r < 2; ++r)
#pragma unroll
        for (int i = 0; i < 4; ++i) { const unsigned u0 = wm[2 * r][i], u1 = wm[2 * r + 1][i], s_ = sp[r];
            const unsigned lo = __builtin_amdgcn_perm(u1, u0, 0x05040100u), hi = __builtin_amdgcn_perm(u1, u0, 0x07060302u);
            a[2 * i] = __builtin_amdgcn_fdot2_f32_bf16(__builtin_bit_cast(bf2, lo), __builtin_bit_cast(bf2, s_), a[2 * i], false);
            a[2 * i + 1] = __builtin_amdgcn_fdot2_f32_bf16(__builtin_bit_cast(bf2, hi), __builtin_bit_cast(bf2, s_), a[2 * i + 1], false); }
    float b4[4], b2[2];
#pragma unroll
    for (int i = 0; i < 4; ++i) { auto r = __builtin_amdgcn_permlane32_swap(__float_as_uint(a[i]), __float_as_uint(a[i + 4]), false, false); b4[i] = __uint_as_float(r[0]) + __uint_as_float(r[1]); }
#pragma unroll
    for (int i = 0; i < 2; ++i) { auto r = __builtin_amdgcn_permlane16_swap(__float_as_uint(b4[i]), __float_as_uint(b4[i + 2]), false, false); float v = __uint_as_float(r[0]) + __uint_as_float(r[1]);
        v += dppf<0x124>(v); v += dppf<0x128>(v); b2[i] = v; }
    const __amdgpu_buffer_rsrc_t rws = __builtin_amdgcn_make_buffer_rsrc(ws, 0, WS_BYTES, 0x00020000);
    if ((lane & 12) == 0) __builtin_amdgcn_raw_buffer_store_b128((u32x4){__float_as_uint(b2[0]), epoch, __float_as_uint(b2[1]), epoch}, rws,
                                                                 WS_PART + (slice * 8 + kp) * 256 + ((lane & 3) * 4 + (lane >> 5) * 2 + ((lane >> 4) & 1)) * 16, 0, 16);
}
struct PartSum { u32x4 v0, v1; };
DI void mod_sum_load(PartSum& p, char* ws, int lane, int slice) {
    const __amdgpu_buffer_rsrc_t rws = __builtin_amdgcn_make_buffer_rsrc(ws, 0, WS_BYTES, 0x00020000);
    const unsigned o = WS_PART + (slice * 8 + (lane >> 3)) * 256 + (lane & 7) * 16;
    p.v0 = __builtin_amdgcn_raw_buffer_load_b128(rws, o, 0, 16); p.v1 = __builtin_amdgcn_raw_buffer_load_b128(rws, o + 128, 0, 16); }
DI void mod_sum_finish(PartSum& p, char* ws, unsigned epoch, int lane, int slice) {
    PartSum q1, q2;
    __builtin_amdgcn_s_sleep(4); mod_sum_load(q1, ws, lane, slice); __builtin_amdgcn_s_sleep(4); asm volatile("" ::: "memory"); mod_sum_load(q2, ws, lane, slice);
    for (unsigned sp = 0; sp < SPIN_CAP; ++sp) { if (__all(p.v0[1] == epoch && p.v0[3] == epoch && p.v1[1] == epoch && p.v1[3] == epoch)) break; p = q1; q1 = q2; asm volatile("" ::: "memory"); mod_sum_load(q2, ws, lane, slice); }
    float s[4] = {__uint_as_float(p.v0[0]), __uint_as_float(p.v0[2]), __uint_as_float(p.v1[0]), __uint_as_float(p.v1[2])};
#pragma unroll
    for (int i = 0; i < 4; ++i) { float v = s[i]; v += dppf<0x128>(v); s[i] = xsum32(xsum16(v)); }
    const unsigned long long g0 = ((unsigned long long)epoch << 32) | pk2(s[0], s[1]), g1 = ((unsigned long long)epoch << 32) | pk2(s[2], s[3]);
    const unsigned base = slice < 32 ? WS_MODU + slice * 128 : WS_MODN + (slice - 32) * 128, rs = slice < 32 ? 4096 : 8192, rep = lane >> 3, o = base + (lane & 7) * 8;
    stg64_sc1(ws, o + rep * rs, g0); stg64_sc1(ws, o + 64 + rep * rs, g1); stg64_sc1(ws, o + (rep + 8) * rs, g0); stg64_sc1(ws, o + 64 + (rep + 8) * rs, g1);
}

__global__ __launch_bounds__(576, 3) void fused_block(const u16* __restrict__ x, const u16* __restrict__ wqkv, const u16* __restrict__ wo, const u16* __restrict__ w1, const u16* __restrict__ b1,
                                                      const u16* __restrict__ w2, const u16* __restrict__ b2, const u16* __restrict__ cvec, const u16* __restrict__ wmod, u16* __restrict__ out, char* __restrict__ ws) {
    const unsigned epoch = *(const unsigned*)(ws + WS_E) + 1u;
    const int tid = threadIdx.x, lane = tid & 63, w = __builtin_amdgcn_readfirstlane(tid >> 6);
    const int b = blockIdx.x, xl = b & 7, idx = b >> 3, g = 2 * xl + (idx >> 4), j = idx & 15;
    const int ct = j & 7, rh = j >> 3;
    unsigned long long code_pc; unsigned code_len;
    asm volatile("s_getpc_b64 %0\nfb_code_begin:\n\ts_mov_b32 %1, fb_code_end-fb_code_begin" : "=s"(code_pc), "=s"(code_len));
    STAMP(0); STAMPC(20);
    if (w == 8) {
        {
            const unsigned o = ((unsigned)idx * 32u + (lane & 31)) * 64u;
            warm4((int)o < (int)code_len ? o : 0u, (const void*)code_pc);
        }
        if (lane == 0) stg_sc1(ws, WS_HELLO + b * 4, epoch);
        __builtin_amdgcn_s_setprio(3);
        const unsigned sw0 = (lane >> 3) * 1024u + ct * 128u + (((lane & 7) >> 1) ^ ((lane >> 4) & 3)) * 32u + (lane & 1) * 16u, sw1 = sw0 + 7168, sw2 = sw0 + 14336, sw3 = sw0 + 21504;
        unsigned w1o[7], wqo[3];
#pragma unroll
        for (int r = 0; r < 7; ++r) { const unsigned u = r * 64 + lane, row = u / 14, c = u % 14; w1o[r] = row * 3584u + 224u * j + c * 16u - (r & 3) * 1024u; }
#pragma unroll
        for (int r = 0; r < 3; ++r) { const unsigned u = r * 64 + lane, row = u / 12, c = u % 12; wqo[r] = row * 3072u + (c >> 2) * 1024u + 64u * j + (c & 3) * 16u - r * 1024u; }
#define RUN128(base, first, n4, lds0) { const char* p_ = (const char*)(base) + (size_t)(first) * 8192; unsigned l_ = (lds0); _Pragma("unroll 2") for (int i_ = 0; i_ < (n4); ++i_) { dma4(sw0, sw1, sw2, sw3, p_, l_); p_ += 32768; l_ += 4096; } }
#define W1GRPS(k0, k1) { const char* p_ = (const char*)w1 + (size_t)(k0) * 114688; unsigned l_ = L_W1 + 7 * (k0) * 1024; _Pragma("unroll 1") for (int k_ = (k0); k_ < (k1); ++k_) { \
            dma4(w1o[0], w1o[1], w1o[2], w1o[3], p_, l_); dma3(w1o[4], w1o[5], w1o[6], p_, l_ + 4096); p_ += 114688; l_ += 7168; } }
#define W2CHUNKS(c0, c1, slot0) { _Pragma("unroll 1") for (int c_ = (c0); c_ < (c1); ++c_) { const char* p_ = (const char*)w2 + (size_t)c_ * 32768; const unsigned l_ = ((slot0) + c_ - (c0)) * 16384; \
            dma4(sw0, sw1, sw2, sw3, p_, l_); dma4(sw0, sw1, sw2, sw3, p_ + 56 * 8192, l_ + 4096); dma4(sw0, sw1, sw2, sw3, p_ + 112 * 8192, l_ + 8192); dma4(sw0, sw1, sw2, sw3, p_ + 168 * 8192, l_ + 12288); } }
#define WARM128(base, row0, n32) { const char* p_ = (const char*)(base) + (size_t)(row0) * 1024; const unsigned v_ = (lane >> 1) * 1024u + ct * 128u + (lane & 1) * 64u; \
            _Pragma("unroll 1") for (int i_ = 0; i_ < (n32); ++i_) { warm4(v_, p_); p_ += 32768; } }
#define WARMW1(row0, n16) { const char* p_ = (const char*)w1 + (size_t)(row0) * 3584; const unsigned c_ = (224u * j) & ~63u, v_ = (lane >> 2) * 3584u + c_ + (lane & 3) * 64u; \
            _Pragma("unroll 1") for (int i_ = 0; i_ < (n16); ++i_) { warm4(v_, p_); p_ += 16 * 3584; } }
        const unsigned xr0 = (lane >> 3) * 1024u, xe = 16u * ((lane & 7) ^ (lane >> 4)), xod = 16u * ((lane & 7) ^ (4 + (lane >> 4)));
        const unsigned xv0 = xr0 + xe, xv1 = xr0 + 7168 + xod, xv2 = xr0 + 14336 + xe, xv3 = xr0 + 21504 + xod;
#define QCHUNK(c) { const char* px_ = (const char*)x + (size_t)(128 * g) * 1024 + 128 * (c); const char* pw_ = (const char*)wqkv + (size_t)(4 * (c)) * 49152; const unsigned l_ = ((c) % 5) * L_SLOT; \
            _Pragma("unroll") for (int q_ = 0; q_ < 4; ++q_) { dma4(xv0, xv1, xv2, xv3, px_ + q_ * 32768, l_ + q_ * 4096); dma3(wqo[0], wqo[1], wqo[2], pw_ + q_ * 49152, l_ + 16384 + q_ * 3072); } }
        __builtin_amdgcn_s_barrier();
        __builtin_amdgcn_s_barrier();
        __builtin_amdgcn_s_barrier();
        QCHUNK(5)
        __builtin_amdgcn_s_barrier();
        QCHUNK(6)
        __builtin_amdgcn_s_barrier();
        QCHUNK(7)
        VMWAIT(56);
        __builtin_amdgcn_s_barrier();
        VMWAIT(28);
        __builtin_amdgcn_s_barrier();
        VMWAIT(0);
        __builtin_amdgcn_s_barrier();
        WARM128(wo, 128 * (rh + 2 * (g & 1)), 4)
        WARMW1(256 * (g & 1), 16)
        __builtin_amdgcn_s_barrier();
        __builtin_amdgcn_s_barrier();
        if (g >= 1) {
            const unsigned fo = WS_FLAG + (((g >= 2 ? g - 2 + (lane & 1) : 0) * 16 + j) * 256);
            for (unsigned sp = 0; sp < SPIN_CAP; ++sp) { unsigned f = ldg_sc1(ws, fo); if (__all(f == epoch)) break; }
            const unsigned lo16 = lane * 16;
#pragma unroll 1
            for (int src = (g >= 2 ? 0 : 1); src < 2; ++src) {
                const char* p_ = ws + WS_KV + (size_t)((g - 2 + src) * 16 + j) * 16384;
                dma4lin_c(lo16, p_, L_KIMG + src * 8192); dma4lin_c(lo16, p_ + 4096, L_KIMG + src * 8192 + 4096);
                dma4lin_c(lo16, p_ + 8192, L_VIMG + src * 8192); dma4lin_c(lo16, p_ + 12288, L_VIMG + src * 8192 + 4096);
            }
        }
        VMWAIT(0);
        __builtin_amdgcn_s_barrier();
        RUN128(wo, 0, 16, L_WO)
        W1GRPS(3, 7)
        W1GRPS(15, 16)
        WARM128(w2, 448 * (rh + 2 * (g & 1)), 14)
        mod_part(cvec, wmod, ws, epoch, lane, 32 + 8 * xl + (idx & 3), idx >> 2); mod_part(cvec, wmod, ws, epoch, lane, 36 + 8 * xl + (idx & 3), idx >> 2);
        if (idx < 4) { PartSum p; mod_sum_load(p, ws, lane, 32 + 8 * xl + idx); mod_sum_finish(p, ws, epoch, lane, 32 + 8 * xl + idx); mod_sum_load(p, ws, lane, 36 + 8 * xl + idx); mod_sum_finish(p, ws, epoch, lane, 36 + 8 * xl + idx); }
        __builtin_amdgcn_s_barrier();
        __builtin_amdgcn_s_sleep(2);
        W1GRPS(7, 10)
        VMWAIT(21);
        __builtin_amdgcn_s_barrier();
        __builtin_amdgcn_s_setprio(0); __builtin_amdgcn_s_sleep(11);
        W1GRPS(10, 15)
        __builtin_amdgcn_s_setprio(3);
        __builtin_amdgcn_s_barrier();
        __builtin_amdgcn_s_barrier();
        __builtin_amdgcn_s_barrier();
        __builtin_amdgcn_s_barrier();
        __builtin_amdgcn_s_sleep(2);
        W1GRPS(0, 3)
        VMWAIT(0);
        __builtin_amdgcn_s_barrier();
        __builtin_amdgcn_s_barrier();
        __builtin_amdgcn_s_sleep(22);
        W2CHUNKS(0, 2, 0)
        __builtin_amdgcn_s_barrier();
        __builtin_amdgcn_s_barrier();
        W2CHUNKS(2, 6, 2)
        __builtin_amdgcn_s_barrier();
        W2CHUNKS(6, 9, 6)
        __builtin_amdgcn_s_barrier();
        __builtin_amdgcn_s_sleep(2);
        W2CHUNKS(9, 10, 9)
        VMWAIT(16);
        __builtin_amdgcn_s_barrier();
        __builtin_amdgcn_s_barrier();
        VMWAIT(0);
        __builtin_amdgcn_s_barrier();
        __builtin_amdgcn_s_barrier();
        __builtin_amdgcn_s_barrier();
        return;
    }
    const __amdgpu_buffer_rsrc_t rws = __builtin_amdgcn_make_buffer_rsrc(ws, 0, WS_BYTES, 0x00020000);
    const int m = w & 3, kh = w >> 2;
    PartSum psum;
    if (w >= 4) {
        const unsigned xr0 = (lane >> 3) * 1024u, xe = 16u * ((lane & 7) ^ (lane >> 4)), xod = 16u * ((lane & 7) ^ (4 + (lane >> 4)));
        unsigned wq[3];
#pragma unroll
        for (int r = 0; r < 3; ++r) { const unsigned u = r * 64 + lane, row = u / 12, c = u % 12; wq[r] = row * 3072u + (c >> 2) * 1024u + 64u * j + (c & 3) * 16u - r * 1024u; }
        const char* px = (const char*)x + (size_t)(128 * g + 32 * m) * 1024; const char* pw = (const char*)wqkv + (size_t)m * 49152;
#pragma unroll
        for (int c = 0; c < 5; ++c) { dma4(xr0 + xe, xr0 + 7168 + xod, xr0 + 14336 + xe, xr0 + 21504 + xod, px + 128 * c, c * L_SLOT + m * 4096);
            VMWAITN(QWIN);
            dma3(wq[0], wq[1], wq[2], pw + (size_t)(4 * c) * 49152, c * L_SLOT + 16384 + m * 3072);
            VMWAITN(QWIN); }
    }
    if (w == 0) { lds_st<u32x4>(L_ONES, (u32x4){0x3F803F80u, 0x3F803F80u, 0x3F803F80u, 0x3F803F80u}); lds_st<u32x4>(L_ZERO, (u32x4){0u, 0u, 0u, 0u}); lds_st<u32x4>(L_ZERO2, (u32x4){0u, 0u, 0u, 0u}); }
    if (w == 3) { __builtin_amdgcn_s_setprio(2); mod_part(cvec, wmod, ws, epoch, lane, 4 * xl + (idx & 3), idx >> 2);
        if (idx < 4) { mod_sum_load(psum, ws, lane, 4 * xl + idx); mod_sum_finish(psum, ws, epoch, lane, 4 * xl + idx); } __builtin_amdgcn_s_setprio(0); }
    STAMP(1); WS6(0);
    bf16x8 qf; u32x4 kpk, vpk;
    {
    const int h = lane >> 5, gi = lane >> 4, q4 = (lane & 15) >> 2, p4 = lane & 3;
    u32x4 xr[16];
    const unsigned tok = 32 * m + (lane & 31), xa0 = tok * 128 + (((4 * kh + h) ^ ((tok >> 1) & 7)) << 4), xa1 = tok * 128 + (((4 * kh + 2 + h) ^ ((tok >> 1) & 7)) << 4);
#define XRD(s) xr[s] = lds_ld<u32x4>((((s) & 1) ? xa1 : xa0) + (((s) >> 1) % 5) * L_SLOT)
    float s0 = 0.f, s1 = 0.f, s2 = 0.f, s3 = 0.f;
#define XSTAT(s) { const bf2 one = {(__bf16)1.0f, (__bf16)1.0f}; _Pragma("unroll") for (int i = 0; i < 4; i += 2) { const unsigned ua = xr[s][i], ub = xr[s][i + 1]; const bf2 va = __builtin_bit_cast(bf2, ua), vb = __builtin_bit_cast(bf2, ub); \
                s0 = __builtin_amdgcn_fdot2_f32_bf16(va, one, s0, false); s1 = __builtin_amdgcn_fdot2_f32_bf16(va, va, s1, false); \
                s2 = __builtin_amdgcn_fdot2_f32_bf16(vb, one, s2, false); s3 = __builtin_amdgcn_fdot2_f32_bf16(vb, vb, s3, false); } }
    const unsigned gmo = WS_MODU + g * 4096 + (tid & 255) * 16; u32x4 gmv;
    if (w < 3) __builtin_amdgcn_s_sleep(56);
    if (w < 4) gmv = __builtin_amdgcn_raw_buffer_load_b128(rws, gmo, 0, 16);
    if (w >= 4) VMWAIT(14);
    BAR();
    WS6(1);
#pragma unroll
    for (int s = 0; s < 6; ++s) XRD(s);
#pragma unroll
    for (int s = 0; s < 6; ++s) XSTAT(s)
    PIN(s0); PIN(s1); PIN(s2); PIN(s3);
    WS6(2);
    if (w < 4) {
        for (unsigned sp = 0; sp < SPIN_CAP; ++sp) { if (__all(gmv[1] == epoch && gmv[3] == epoch)) break; asm volatile("" ::: "memory"); gmv = __builtin_amdgcn_raw_buffer_load_b128(rws, gmo, 0, 16); }
        const unsigned u0 = gmv[0], u2 = gmv[2], k = (4 * tid) & 511, po = (k >> 5) * 64 + ((k >> 2) & 3) * 16 + ((k >> 4) & 1) * 8;
        if (tid >= 128) { const unsigned g0 = pk2(1.f + bf_lo(u0), 1.f + bf_hi(u0)), g1 = pk2(1.f + bf_lo(u2), 1.f + bf_hi(u2));
            lds_st<f32x4>(L_G32 + k * 4, (f32x4){bf_lo(g0), bf_hi(g0), bf_lo(g1), bf_hi(g1)}); lds_st<u32x2>(L_GP + po, (u32x2){g0, g1}); }
        else lds_st<u32x2>(L_SP + po, (u32x2){u0, u2});
    }
    STAMP(3); WS6(3);
    VMWAIT(0);
    BAR();
    STAMP(4); STAMPC(22); WS1(0);
#pragma unroll
    for (int s = 6; s < 10; ++s) XRD(s);
    f32x16 aQ, aK, aV; f32x4 aU = {0.f, 0.f, 0.f, 0.f};
#pragma unroll
    for (int i = 0; i < 16; ++i) { aQ[i] = 0.f; aK[i] = 0.f; aV[i] = 0.f; }
    {
        const unsigned rowa = 16384 + (32 * kh + 8 * (gi >> 1) + q4) * 192 + p4 * 8;
        unsigned wqa = rowa + (((gi & 1) ^ kh) * 32), wva = rowa + 128 + (gi & 1) * 32;
        unsigned uaa = 16384 + (4 * gi + q4) * 192 + p4 * 8 + (w < 6 ? w : w - 6) * 32;
        const unsigned uinc = (lane & 15) < 2 ? 64u : 0u; unsigned uba = ((lane & 15) == 0 ? L_GP : (lane & 15) == 1 ? L_SP : L_ZERO) + ((lane & 15) < 2 ? gi * 16 : 0);
        const unsigned sga = L_G32 + (32 * kh + 16 * ((lane & 15) >> 3) + 8 * h + (lane & 7)) * 4;
        float gv[2]; bf16x8 fk, fv, fq, fu, bu; u32x4 hc, hn;
        gv[0] = lds_ld<float>(sga); gv[1] = lds_ld<float>(sga + 256);
        fk = trfrag(wqa + 64, wqa + 64 + 768); fv = trfrag(wva, wva + 768); fq = trfrag(wqa, wqa + 768); fu = trfrag(uaa, uaa + 3072); bu = lds_ld<bf16x8>(uba);
#pragma unroll
        for (int s = 6; s < 10; ++s) XSTAT(s)
        PIN(s0); PIN(s1); PIN(s2); PIN(s3);
        asm volatile("s_nop 1" : "+v"(gv[0]), "+v"(gv[1]));
#define HG(s, i) xgdw<8 * ((s) & 1) + 2 * (i)>(xr[s][i], gv[((s) >> 1) & 1])
        hc = (u32x4){HG(0, 0), HG(0, 1), HG(0, 2), HG(0, 3)};
        asm volatile("s_nop 1" : "+v"(hc)); SBAR();
#define WO(s) ((((s) >> 1) % 5 & 1 ? L_SLOT : 0) + ((s) & 1) * 3072)
#define UO(s) ((((s) >> 1) % 5 & 1 ? L_SLOT : 0) + ((s) & 1) * 6144)
#define QKV_STEP(s, sn) { const bf16x8 hb = __builtin_bit_cast(bf16x8, hc); unsigned n0, n1, n2, n3; \
            if ((s) == 2 || (s) == 4 || (s) == 6 || (s) == 8 || (s) == 10 || (s) == 12) RBAR();     \
            if ((sn) < 16 && (sn) % 2 == 0 && ((sn) >> 1) % 5 % 2 == 0) { const int d_ = (((sn) >> 1) % 5 == 0) ? -4 * (int)L_SLOT : 2 * (int)L_SLOT; wqa += d_; wva += d_; uaa += d_; PIN(wqa); PIN(wva); PIN(uaa); } \
            if ((s) + 2 >= 10 && (s) + 2 < 16) XRD(((s) + 2) & 15); \
            aK = MFMA32(fk, hb, aK); PIN(aK); SBAR(); if ((sn) < 16) { fk = trfrag(wqa + WO(sn) + 64, wqa + WO(sn) + 64 + 768); n0 = HG((sn) & 15, 0); n1 = HG((sn) & 15, 1); PIN(n0); PIN(n1); } SBAR(); \
            aV = MFMA32(hb, fv, aV); PIN(aV); SBAR(); if ((sn) < 16) { fv = trfrag(wva + WO(sn), wva + WO(sn) + 768); n2 = HG((sn) & 15, 2); PIN(n2); } SBAR(); \
            aQ = MFMA32(fq, hb, aQ); PIN(aQ); SBAR(); if ((sn) < 16) { fq = trfrag(wqa + WO(sn), wqa + WO(sn) + 768); n3 = HG((sn) & 15, 3); PIN(n3); } SBAR(); \
            aU = MFMA16(fu, bu, aU); PIN(aU); SBAR(); if ((sn) < 16) { fu = trfrag(uaa + UO(sn), uaa + UO(sn) + 3072); uba += uinc; bu = lds_ld<bf16x8>(uba); hc = (u32x4){n0, n1, n2, n3}; } \
            if ((s) % 2 == 1 && (s) + 3 < 16) gv[(((s) + 3) >> 1) & 1] = lds_ld<float>(sga + (((s) + 3) >> 1) * 256); SBAR(); }
        WS1(1);
        QKV_STEP(0, 1) QKV_STEP(1, 2) QKV_STEP(2, 3) QKV_STEP(3, 4) QKV_STEP(4, 5) QKV_STEP(5, 6) QKV_STEP(6, 7) QKV_STEP(7, 8)
        QKV_STEP(8, 9) QKV_STEP(9, 10) QKV_STEP(10, 11) QKV_STEP(11, 12) QKV_STEP(12, 13) QKV_STEP(13, 14) QKV_STEP(14, 15) QKV_STEP(15, 16)
#undef QKV_STEP
#undef HG
#undef WO
#undef UO
        STAMP(5); STAMPC(23); WS1(2);
        float vs[8], vk[8];
#pragma unroll
        for (int i = 0; i < 8; ++i) { float lo = aV[i], hi = aV[8 + i]; PIN(lo); PIN(hi); vs[i] = kh ? lo : hi; vk[i] = kh ? hi : lo; }
        const unsigned ex = L_EXS + w * 6144 + lane * 16;
        lds_st<f32x4>(ex, (f32x4){aK[8], aK[9], aK[10], aK[11]}); lds_st<f32x4>(ex + 1024, (f32x4){aK[12], aK[13], aK[14], aK[15]});
        lds_st<f32x4>(ex + 2048, (f32x4){vs[0], vs[1], vs[2], vs[3]}); lds_st<f32x4>(ex + 3072, (f32x4){vs[4], vs[5], vs[6], vs[7]});
        lds_st<f32x4>(ex + 4096, (f32x4){aQ[8], aQ[9], aQ[10], aQ[11]}); lds_st<f32x4>(ex + 5120, (f32x4){aQ[12], aQ[13], aQ[14], aQ[15]});
        if (w < 6 && (lane & 15) < 2) lds_st<f32x4>(L_UVQ + ((lane & 15) * 96 + w * 16 + 4 * gi) * 4, aU);
#pragma unroll
        for (int s = 10; s < 16; ++s) XSTAT(s)
        s0 = xsum32(s0 + s2); s1 = xsum32(s1 + s3);
        if (lane < 32) lds_st<u32x2>(L_STAT + (kh * 128 + 32 * m + lane) * 8, (u32x2){__float_as_uint(s0), __float_as_uint(s1)});
        BAR();
        WS1(3);
        const unsigned ep = L_EXS + (w ^ 4) * 6144 + lane * 16;
        const u32x2 so = lds_ld<u32x2>(L_STAT + ((kh ^ 1) * 128 + 32 * m + (lane & 31)) * 8);
        const f32x4 k0 = lds_ld<f32x4>(ep), k1 = lds_ld<f32x4>(ep + 1024), v0 = lds_ld<f32x4>(ep + 2048), v1 = lds_ld<f32x4>(ep + 3072), q0 = lds_ld<f32x4>(ep + 4096), q1 = lds_ld<f32x4>(ep + 5120);
        const unsigned uq = L_UVQ + (16 * kh + 4 * h) * 4;
        const f32x4 uk0 = lds_ld<f32x4>(uq + 128), uk1 = lds_ld<f32x4>(uq + 128 + 32), vk0 = lds_ld<f32x4>(uq + 384 + 128), vk1 = lds_ld<f32x4>(uq + 384 + 128 + 32);
        const f32x4 uq0 = lds_ld<f32x4>(uq), uq1 = lds_ld<f32x4>(uq + 32), vq0 = lds_ld<f32x4>(uq + 384), vq1 = lds_ld<f32x4>(uq + 384 + 32);
        const float uv_ = lds_ld<float>(L_UVQ + (64 + (lane & 31)) * 4), vv_ = lds_ld<float>(L_UVQ + (96 + 64 + (lane & 31)) * 4);
        const float t0 = s0 + __uint_as_float(so[0]), t1 = s1 + __uint_as_float(so[1]);
        const float mean = t0 * (1.f / 512.f), rstd = __builtin_amdgcn_rsqf(fmaxf(t1 * (1.f / 512.f) - mean * mean, 0.f) + 1e-5f), nmr = -mean * rstd;
        const unsigned rn = L_RN + w * 256;
        if (lane < 32) { lds_st<float>(rn + lane * 4, rstd); lds_st<float>(rn + 128 + lane * 4, nmr); }
        asm volatile("s_waitcnt lgkmcnt(0)" ::: "memory");
        const f32x4 ra = lds_ld<f32x4>(rn + (16 * kh + 4 * h) * 4), rb = lds_ld<f32x4>(rn + (16 * kh + 8 + 4 * h) * 4), na = lds_ld<f32x4>(rn + 128 + (16 * kh + 4 * h) * 4), nb = lds_ld<f32x4>(rn + 128 + (16 * kh + 8 + 4 * h) * 4);
        u32x4 kp, vp, qp; float kv[8], vv[8], qv[8];
#pragma unroll
        for (int i = 0; i < 4; ++i) { kv[i] = fmaf(rstd, aK[i] + k0[i], fmaf(nmr, uk0[i], vk0[i])); kv[4 + i] = fmaf(rstd, aK[4 + i] + k1[i], fmaf(nmr, uk1[i], vk1[i]));
            vv[i] = fmaf(ra[i], vk[i] + v0[i], fmaf(na[i], uv_, vv_)); vv[4 + i] = fmaf(rb[i], vk[4 + i] + v1[i], fmaf(nb[i], uv_, vv_)); }
#pragma unroll
        for (int i = 0; i < 4; ++i) { kp[i] = pk2(kv[2 * i], kv[2 * i + 1]); vp[i] = pk2(vv[2 * i], vv[2 * i + 1]); }
        const unsigned kvo = WS_KV + (g * 16 + j) * 16384 + (m * 2 + kh) * 1024 + lane * 16;
        __builtin_amdgcn_raw_buffer_store_b128(kp, rws, kvo, 0, 16);
        __builtin_amdgcn_raw_buffer_store_b128(vp, rws, kvo + 8192, 0, 16);
        PIN(kp); PIN(vp); kpk = kp; vpk = vp;
        SBAR();
        const float qs = 0.25f * 1.44269504089f;
#pragma unroll
        for (int i = 0; i < 4; ++i) { qv[i] = qs * fmaf(rstd, aQ[i] + q0[i], fmaf(nmr, uq0[i], vq0[i])); qv[4 + i] = qs * fmaf(rstd, aQ[4 + i] + q1[i], fmaf(nmr, uq1[i], vq1[i])); }
#pragma unroll
        for (int i = 0; i < 4; ++i) qp[i] = pk2(qv[2 * i], qv[2 * i + 1]);
        qf = __builtin_bit_cast(bf16x8, qp); PIN(qf);
    }
#undef XRD
#undef XSTAT
    }
    WS1(4);
    VMWAIT(0); WS1(5);
    BAR();
    if (tid == 0) stg_sc1(ws, WS_FLAG + (0 * 256 + g * 16 + j) * 256, epoch);
    STAMP(6);
    { FRESH(lane);
      lds_st<u32x4>(L_KIMG + (8 + m) * 2048 + kh * 1024 + lane * 16, kpk);
      lds_st<u32x4>(L_VIMG + (8 + m) * 2048 + kh * 1024 + lane * 16, vpk); }
    const int tt = w & 3;
    {
    FRESH(lane);
    const int hh = kh, qq = lane & 31, h = lane >> 5;
    const float NEG = -__builtin_inff(), fq0 = (float)(4 * h - qq) - 0.5f;
    f32x16 c0, c8;
#pragma unroll
    for (int r = 0; r < 16; ++r) {
        const float t = fmaf(fq0, 1e30f, (float)((r & 3) + 8 * (r >> 2)) * 1e30f); c0[r] = fminf(t, 0.f); c8[r] = fminf(-t, 0.f); }
    PIN(c0); PIN(c8);
    BAR();
    STAMP(8); WS2(0);
    {
        const unsigned ka = L_KIMG + m * 2048 + hh * 1024 + lane * 16;
        const bool isd = ((lane >> 4) & 1) == hh;
        const unsigned va = isd ? L_VIMG + m * 2048 + lane * 16 : L_ONES, vst = isd ? 2048u : 0u, vs1 = isd ? 1024u : 0u;
        f32x16 o;
#pragma unroll
        for (int r = 0; r < 16; ++r) o[r] = 0.f;
#define MAX16(mx, s) { _Pragma("unroll") for (int r = 0; r < 16; r += 2) mx = fmaxf(fmaxf(mx, s[r]), s[r + 1]); }
#define EXPH(P, S, b) { P[0] = pk2(__builtin_amdgcn_exp2f(S[b]), __builtin_amdgcn_exp2f(S[b + 1])); P[1] = pk2(__builtin_amdgcn_exp2f(S[b + 2]), __builtin_amdgcn_exp2f(S[b + 3])); \
                        P[2] = pk2(__builtin_amdgcn_exp2f(S[b + 4]), __builtin_amdgcn_exp2f(S[b + 5])); P[3] = pk2(__builtin_amdgcn_exp2f(S[b + 6]), __builtin_amdgcn_exp2f(S[b + 7])); }
        if (g >= 2) {
            f32x16 z; const f32x16 z0 = {0.f, 0.f, 0.f, 0.f, 0.f, 0.f, 0.f, 0.f, 0.f, 0.f, 0.f, 0.f, 0.f, 0.f, 0.f, 0.f};
            float mx = NEG, mx2 = NEG; f32x16 sA, sB;
#define KF(i) lds_ld<bf16x8>(ka + (i) * 2048)
            bf16x8 k0 = KF(0), k1 = KF(8), k2 = KF(1), k3 = KF(2);
            SBAR();
            sA = MFMA32(k0, qf, c0); PIN(sA); k0 = KF(3); SBAR();
            sB = MFMA32(k1, qf, c8); PIN(sB); k1 = KF(4); SBAR(); MAX16(mx, sA); PIN(mx); SBAR();
            sA = MFMA32(k2, qf, z0); PIN(sA); k2 = KF(5); SBAR(); MAX16(mx2, sB); PIN(mx2); SBAR();
            sB = MFMA32(k3, qf, z0); PIN(sB); k3 = KF(6); SBAR(); MAX16(mx, sA); PIN(mx); SBAR();
            sA = MFMA32(k0, qf, z0); PIN(sA); k0 = KF(7); SBAR(); MAX16(mx2, sB); PIN(mx2); SBAR();
            sB = MFMA32(k1, qf, z0); PIN(sB); k1 = KF(0); SBAR(); MAX16(mx, sA); PIN(mx); SBAR();
            sA = MFMA32(k2, qf, z0); PIN(sA); k2 = KF(1); SBAR(); MAX16(mx2, sB); PIN(mx2); SBAR();
            sB = MFMA32(k3, qf, z0); PIN(sB); SBAR(); MAX16(mx, sA); PIN(mx); SBAR();
            sA = MFMA32(k0, qf, z0); PIN(sA); SBAR(); MAX16(mx2, sB); PIN(mx2); SBAR();
            WS2(1);
            bf16x8 v0 = lds_ld<bf16x8>(va), v1 = lds_ld<bf16x8>(va + vs1);
            MAX16(mx, sA);
            mx = xmax32(fmaxf(mx, mx2));
#pragma unroll
            for (int r = 0; r < 16; ++r) { z[r] = -mx; c0[r] -= mx; c8[r] -= mx; }
            PIN(z); PIN(c0); PIN(c8); SBAR();
            WS2(2);
            sA = MFMA32(k1, qf, c0); SBAR();
            u32x4 pa, pb; bf16x8 v0n, v1n, kn = k2; unsigned vp = va;
#define ABLK(I, SC, SN, CN) { bf16x8 kc = kn; if ((I) < 8) { vp += vst; v0n = lds_ld<bf16x8>(vp); v1n = lds_ld<bf16x8>(vp + vs1); } if ((I) < 7) kn = KF((I) + 2); SBAR(); \
                if ((I) < 8) { SN = MFMA32(kc, qf, CN); PIN(SN); } SBAR(); EXPH(pa, SC, 0); PIN(pa); SBAR(); \
                o = MFMA32(v0, __builtin_bit_cast(bf16x8, pa), o); PIN(o); SBAR(); EXPH(pb, SC, 8); PIN(pb); SBAR(); \
                o = MFMA32(v1, __builtin_bit_cast(bf16x8, pb), o); PIN(o); SBAR(); if ((I) < 8) { v0 = v0n; v1 = v1n; } }
            ABLK(0, sA, sB, z) ABLK(1, sB, sA, z) ABLK(2, sA, sB, z) ABLK(3, sB, sA, z)
            ABLK(4, sA, sB, z) ABLK(5, sB, sA, z) ABLK(6, sA, sB, z) ABLK(7, sB, sA, c8) ABLK(8, sA, sB, c8)
#undef KF
#undef ABLK
        } else {
            const int i0 = 8 - 4 * g - m;
            f32x16 z;
#pragma unroll
            for (int r = 0; r < 16; ++r) z[r] = 0.f;
            float mx = NEG;
            f32x16 s = MFMA32(lds_ld<bf16x8>(ka + 8 * 2048), qf, c8); MAX16(mx, s);
#pragma unroll 1
            for (int i = i0; i < 8; ++i) { s = MFMA32(lds_ld<bf16x8>(ka + i * 2048), qf, z); MAX16(mx, s); }
            mx = xmax32(mx);
#pragma unroll
            for (int r = 0; r < 16; ++r) { z[r] = -mx; c8[r] -= mx; }
#pragma unroll 1
            for (int i = i0; i <= 8; ++i) { u32x4 pa, pb;
                if (i < 8) s = MFMA32(lds_ld<bf16x8>(ka + i * 2048), qf, z); else s = MFMA32(lds_ld<bf16x8>(ka + i * 2048), qf, c8);
                EXPH(pa, s, 0); EXPH(pb, s, 8);
                o = MFMA32(lds_ld<bf16x8>(va + i * vst), __builtin_bit_cast(bf16x8, pa), o); o = MFMA32(lds_ld<bf16x8>(va + i * vst + vs1), __builtin_bit_cast(bf16x8, pb), o); }
        }
#undef EXPH
#undef MAX16
        WS2(3);
        float ov[8], l;
        { float lo = o[0], hi = o[8]; PIN(lo); PIN(hi); l = hh ? lo : hi; }
#pragma unroll
        for (int r = 0; r < 8; ++r) { float lo = o[r], hi = o[8 + r]; PIN(lo); PIN(hi); ov[r] = hh ? hi : lo; }
        const float inv = __builtin_amdgcn_rcpf(l);
        const u32x4 op = {pk2(ov[0] * inv, ov[1] * inv), pk2(ov[2] * inv, ov[3] * inv), pk2(ov[4] * inv, ov[5] * inv), pk2(ov[6] * inv, ov[7] * inv)};
        __builtin_amdgcn_raw_buffer_store_b128(op, rws, WS_O + ((g * 8 + 2 * m + (qq >> 4)) * 16 + j) * 1024 + ((qq & 15) + 16 * (2 * hh + h)) * 16, 0, 16);
    }
    }
    WS2(4);
    VMWAIT(0); WS2(5);
    BAR();
    if (tid == 0) stg_sc1(ws, WS_FLAG + (1 * 256 + g * 16 + j) * 256, epoch);
    STAMP(9);
    if (w >= 4) {
        const int t = tid - 256; const unsigned o = WS_MODN + g * 8192 + t * 32; u32x4 v, v2;
        for (unsigned sp = 0; sp < SPIN_CAP; ++sp) { asm volatile("" ::: "memory"); v = __builtin_amdgcn_raw_buffer_load_b128(rws, o, 0, 16); v2 = __builtin_amdgcn_raw_buffer_load_b128(rws, o + 16, 0, 16);
            if (__all(v[1] == epoch && v[3] == epoch && v2[1] == epoch && v2[3] == epoch)) break; }
        u32x4 val = {v[0], v[2], v2[0], v2[2]};
        const int vec = t >> 6, c8 = (t & 63) * 8;
        if (vec == 1) lds_st<u32x4>(L_SB16 + c8 * 2, val);
        else if (vec == 2) {
#pragma unroll
            for (int i = 0; i < 4; ++i) { const unsigned u_ = val[i]; val[i] = pk2(1.f + bf_lo(u_), 1.f + bf_hi(u_)); }
            lds_st<u32x4>(L_GB16 + c8 * 2, val);
        } else if ((c8 >> 6) == ct) lds_st<u32x4>((vec == 0 ? L_GAO : L_GBO) + (c8 & 63) * 2, val);
    }
    if (w == 0) poll_flags(ws, WS_FLAG + (1 * 256 + g * 16) * 256, 16, epoch, lane);
    BAR();
    STAMP(10); WS3(0);
    u32x2 x1p0, x1p1;
    {
        FRESH(lane);
        const int gi = lane >> 4, q4 = (lane & 15) >> 2, p4 = lane & 3;
        const unsigned tokP = 128 * g + 64 * rh + 16 * tt + (lane & 15), colP = 64 * ct + 32 * kh + 4 * gi;
        u32x4 of[8];
#pragma unroll
        for (int s = 0; s < 8; ++s) of[s] = __builtin_amdgcn_raw_buffer_load_b128(rws, WS_O + ((g * 8 + 4 * rh + tt) * 16 + 8 * kh + s) * 1024 + lane * 16, 0, 16);
        const u32x2 xres0 = *(const u32x2*)(x + tokP * 512 + colP), xres1 = *(const u32x2*)(x + tokP * 512 + colP + 16);
        SBAR();
        f32x4 acc[4];
#pragma unroll
        for (int n = 0; n < 4; ++n) acc[n] = (f32x4){0.f, 0.f, 0.f, 0.f};
        const unsigned xv = 2 * (gi & 1) + (q4 >> 1), rowa = L_WO + (256 * kh + 16 * (gi >> 1) + 4 * (gi & 1) + q4) * 128 + p4 * 8;
        unsigned an[4]; bf16x8 wf[4];
#pragma unroll
        for (int n = 0; n < 4; ++n) { an[n] = rowa + ((n ^ xv) * 32); wf[n] = trfrag(an[n], an[n] + 1024); }
        const unsigned cl = (32 * kh + 4 * gi) * 2, cg = colP * 2;
        const u32x2 ga0 = lds_ld<u32x2>(L_GAO + cl), ga1 = lds_ld<u32x2>(L_GAO + cl + 32);
        const u32x2 gb0 = lds_ld<u32x2>(L_GB16 + cg), gb1 = lds_ld<u32x2>(L_GB16 + cg + 32), sb0 = lds_ld<u32x2>(L_SB16 + cg), sb1 = lds_ld<u32x2>(L_SB16 + cg + 32);
        SBAR();
#pragma unroll
        for (int s = 0; s < 8; ++s)
#pragma unroll
            for (int n = 0; n < 4; ++n) { acc[n] = MFMA16(wf[n], __builtin_bit_cast(bf16x8, of[s]), acc[n]); PIN(acc[n]); SBAR(); if (s < 7) wf[n] = trfrag(an[n] + (s + 1) * 4096, an[n] + (s + 1) * 4096 + 1024); SBAR(); }
        WS3(1);
        BAR();
        STAMP(11);
        const unsigned ex = w * 2048 + lane * 16;
        { f32x4 s0 = acc[0], s1 = acc[1], s2 = acc[2], s3 = acc[3]; PIN(s0); PIN(s1); PIN(s2); PIN(s3);
          lds_st<f32x4>(ex, kh ? s0 : s2); lds_st<f32x4>(ex + 1024, kh ? s1 : s3); acc[0] = kh ? s2 : s0; acc[1] = kh ? s3 : s1; }
        BAR();
        WS3(2);
        const unsigned ep = (w ^ 4) * 2048 + lane * 16;
        const f32x4 a0 = lds_ld<f32x4>(ep) + acc[0], a1 = lds_ld<f32x4>(ep + 1024) + acc[1];
        float xv1[8], gg[8], ss[8];
#pragma unroll
        for (int r = 0; r < 4; ++r) {
            const unsigned ua0 = ga0[r >> 1], ua1 = ga1[r >> 1], ux0 = xres0[r >> 1], ux1 = xres1[r >> 1], ug0 = gb0[r >> 1], ug1 = gb1[r >> 1], us0 = sb0[r >> 1], us1 = sb1[r >> 1];
            xv1[r] = rbf(fmaf((r & 1) ? bf_hi(ua0) : bf_lo(ua0), a0[r], (r & 1) ? bf_hi(ux0) : bf_lo(ux0))); xv1[4 + r] = rbf(fmaf((r & 1) ? bf_hi(ua1) : bf_lo(ua1), a1[r], (r & 1) ? bf_hi(ux1) : bf_lo(ux1)));
            gg[r] = (r & 1) ? bf_hi(ug0) : bf_lo(ug0); gg[4 + r] = (r & 1) ? bf_hi(ug1) : bf_lo(ug1);
            ss[r] = (r & 1) ? bf_hi(us0) : bf_lo(us0); ss[4 + r] = (r & 1) ? bf_hi(us1) : bf_lo(us1);
        }
        x1p0 = (u32x2){pk2(xv1[0], xv1[1]), pk2(xv1[2], xv1[3])}; x1p1 = (u32x2){pk2(xv1[4], xv1[5]), pk2(xv1[6], xv1[7])};
        float st[6] = {0.f, 0.f, 0.f, 0.f, 0.f, 0.f}, pg[8];
#pragma unroll
        for (int r = 0; r < 8; ++r) pg[r] = xv1[r] * gg[r];
        const u32x4 xg = {pk2(pg[0], pg[1]), pk2(pg[2], pg[3]), pk2(pg[4], pg[5]), pk2(pg[6], pg[7])};
        __builtin_amdgcn_raw_buffer_store_b128(xg, rws, WS_XG + ((g * 8 + 4 * rh + tt) * 16 + 2 * ct + kh) * 1024 + lane * 16, 0, 16);
#pragma unroll
        for (int r = 0; r < 8; ++r) { const float p = pg[r]; st[0] += xv1[r]; st[1] = fmaf(xv1[r], xv1[r], st[1]); st[2] += p; st[3] = fmaf(p, p, st[3]); st[4] = fmaf(p, gg[r], st[4]); st[5] = fmaf(p, ss[r], st[5]); }
#pragma unroll
        for (int i = 0; i < 6; ++i) st[i] = xsum32(xsum16(st[i]));
        if (kh == 1 && lane < 16) { lds_st<f32x4>(L_SCR + (tt * 16 + lane) * 32, (f32x4){st[0], st[1], st[2], st[3]}); lds_st<u32x2>(L_SCR + (tt * 16 + lane) * 32 + 16, (u32x2){__float_as_uint(st[4]), __float_as_uint(st[5])}); }
        WS3(3);
        BAR();
        if (kh == 0 && lane < 16) {
            const f32x4 o4 = lds_ld<f32x4>(L_SCR + (tt * 16 + lane) * 32); const u32x2 o2 = lds_ld<u32x2>(L_SCR + (tt * 16 + lane) * 32 + 16);
            const unsigned so = WS_ST + g * 24576, T = 4 * rh + tt;
            const u32x4 sa4 = {__float_as_uint(st[0] + o4[0]), __float_as_uint(st[1] + o4[1]), __float_as_uint(st[2] + o4[2]), __float_as_uint(st[3] + o4[3])};
            const u32x2 sb2 = {__float_as_uint(st[4] + __uint_as_float(o2[0])), __float_as_uint(st[5] + __uint_as_float(o2[1]))};
            __builtin_amdgcn_raw_buffer_store_b128(sa4, rws, so + ((T * 8 + ct) * 16 + lane) * 16, 0, 16);
            __builtin_amdgcn_raw_buffer_store_b64(sb2, rws, so + 16384 + ((T * 8 + ct) * 16 + lane) * 8, 0, 16);
        }
    }
    WS3(4);
    VMWAIT(0); WS3(5);
    BAR();
    if (tid == 0) stg_sc1(ws, WS_FLAG + (2 * 256 + g * 16 + j) * 256, epoch);
    STAMP(12);
    const int odd = j & 1;
    {
    FRESH(lane);
    const int gi = lane >> 4, q4 = (lane & 15) >> 2, p4 = lane & 3;
    const u32x2 b1v = *(const u32x2*)(b1 + 112 * j + (w > 0 ? w - 1 : 0) * 16 + 4 * gi);
    BAR();
    if (w > 0) {
        const int nt = w - 1; f32x4 acc = {0.f, 0.f, 0.f, 0.f}, acc2 = acc;
        const unsigned ra = L_W1 + (4 * gi + q4) * 224 + p4 * 8 + nt * 32; const int tk = lane & 15;
        const unsigned va = tk == 0 ? L_GB16 + 8 * gi : tk == 1 ? L_SB16 + 8 * gi : tk == 2 ? L_ONES : L_ZERO2, vst = tk < 2 ? 64u : 0u, vhi = tk < 2 ? 32u : 0u;
        u32x2 blo[16], bhi[16];
        { unsigned a_ = va;
#pragma unroll
          for (int s = 0; s < 16; ++s) { blo[s] = lds_ld<u32x2>(a_); bhi[s] = lds_ld<u32x2>(a_ + vhi); a_ += vst; } }
        bf16x8 f0 = trfrag(ra, ra + 3584), f1 = trfrag(ra + 7168, ra + 7168 + 3584);
#pragma unroll
        for (int s = 0; s < 16; s += 2) {
            acc = MFMA16(f0, __builtin_bit_cast(bf16x8, ((u32x4){blo[s][0], blo[s][1], bhi[s][0], bhi[s][1]})), acc); PIN(acc); SBAR(); if (s + 2 < 16) f0 = trfrag(ra + (s + 2) * 7168, ra + (s + 2) * 7168 + 3584); SBAR();
            acc2 = MFMA16(f1, __builtin_bit_cast(bf16x8, ((u32x4){blo[s + 1][0], blo[s + 1][1], bhi[s + 1][0], bhi[s + 1][1]})), acc2); PIN(acc2); SBAR(); if (s + 3 < 16) f1 = trfrag(ra + (s + 3) * 7168, ra + (s + 3) * 7168 + 3584); SBAR();
        }
        acc += acc2;
        if (tk < 3) lds_st<f32x4>(L_UVW + (tk * 112 + nt * 16 + 4 * gi) * 4, acc);
        if (tk == 3) lds_st<f32x4>(L_UVW + (3 * 112 + nt * 16 + 4 * gi) * 4, (f32x4){bf_lo(b1v[0]), bf_hi(b1v[0]), bf_lo(b1v[1]), bf_hi(b1v[1])});
    } else {
        const u32x4 gq = lds_ld<u32x4>(L_GB16 + lane * 16), sq = lds_ld<u32x4>(L_SB16 + lane * 16);
        float c5[5] = {0.f, 0.f, 0.f, 0.f, 0.f};
#pragma unroll
        for (int i = 0; i < 4; ++i) { const unsigned ug = gq[i], us = sq[i]; const float g0 = bf_lo(ug), g1 = bf_hi(ug), s0 = bf_lo(us), s1 = bf_hi(us);
            c5[0] += g0 + g1; c5[1] += s0 + s1; c5[2] += g0 * g0 + g1 * g1; c5[3] += g0 * s0 + g1 * s1; c5[4] += s0 * s0 + s1 * s1; }
#pragma unroll
        for (int i = 0; i < 5; ++i) c5[i] = xsum32(xsum16(rowsum16(c5[i])));
        if (lane == 0) { lds_st<f32x4>(L_CONST, (f32x4){c5[0], c5[1], c5[2], c5[3]}); lds_st<float>(L_CONST + 16, c5[4]); }
        poll_flags(ws, WS_FLAG + (2 * 256 + g * 16) * 256, 16, epoch, lane);
    }
    }
    BAR();
    STAMP(13);
    u32x4 park[8];
    {
        FRESH(lane);
        const int gi = lane >> 4, q4 = (lane & 15) >> 2, p4 = lane & 3;
        const int Tw = 2 * m + kh;
        u32x4 xf[2][8];
        const unsigned xo0 = WS_XG + (g * 8 + Tw) * 16384 + kh * 8192 + lane * 16, xo1 = WS_XG + (g * 8 + (Tw ^ 1)) * 16384 + kh * 8192 + lane * 16;
        constexpr int WIN = 5;
#define XLOAD(s) { xf[0][s] = __builtin_amdgcn_raw_buffer_load_b128(rws, xo0 + (s) * 1024, 0, 16); xf[1][s] = __builtin_amdgcn_raw_buffer_load_b128(rws, xo1 + (s) * 1024, 0, 16); }
        XLOAD(0) SBAR(); if (kh == 0) __builtin_amdgcn_s_sleep(3); XLOAD(1) SBAR(); if (kh == 0) __builtin_amdgcn_s_sleep(3); XLOAD(2) XLOAD(3) XLOAD(4)
        const unsigned so = WS_ST + g * 24576 + Tw * 2048, sob = WS_ST + g * 24576 + 16384 + Tw * 1024;
        u32x4 sA0, sA1; u32x2 sB0, sB1;
        SBAR(); WS4(0);
        f32x4 acc[7], acb[7]; bf16x8 wf[7];
        const unsigned ra = L_W1 + (256 * kh + 4 * gi + q4) * 224 + p4 * 8, raA = ra + 32 * odd, raB = ra + (odd ? 0 : 192);
#define W1A(s, u) (((u) < 6 ? raA : raB) + (s) * 7168 + ((u) < 6 ? (u) * 32 : 0))
#pragma unroll
        for (int u = 0; u < 7; ++u) { acc[u] = (f32x4){0.f, 0.f, 0.f, 0.f}; acb[u] = acc[u]; wf[u] = trfrag(W1A(0, u), W1A(0, u) + 3584); }
        SBAR();
#pragma unroll
        for (int s = 0; s < 8; ++s) {
            if (s + WIN < 8) XLOAD(s + WIN)
            if (s == 4) { sA0 = __builtin_amdgcn_raw_buffer_load_b128(rws, so + ((2 * gi) * 16 + (lane & 15)) * 16, 0, 16); sA1 = __builtin_amdgcn_raw_buffer_load_b128(rws, so + ((2 * gi + 1) * 16 + (lane & 15)) * 16, 0, 16); }
            if (s == 5) { sB0 = __builtin_amdgcn_raw_buffer_load_b64(rws, sob + ((2 * gi) * 16 + (lane & 15)) * 8, 0, 16); sB1 = __builtin_amdgcn_raw_buffer_load_b64(rws, sob + ((2 * gi + 1) * 16 + (lane & 15)) * 8, 0, 16); }
            SBAR();
#pragma unroll
            for (int u = 0; u < 7; ++u) { acc[u] = MFMA16(wf[u], __builtin_bit_cast(bf16x8, xf[0][s]), acc[u]); PIN(acc[u]); SBAR();
                acb[u] = MFMA16(wf[u], __builtin_bit_cast(bf16x8, xf[1][s]), acb[u]); PIN(acb[u]); SBAR();
                if (s < 7) wf[u] = trfrag(W1A(s + 1, u), W1A(s + 1, u) + 3584); SBAR(); }
            if (s == 7) WS4(1);
        }
#undef W1A
#undef XLOAD
        float ca, cb, cc, r2;
        { float S[6];
#pragma unroll
          for (int i = 0; i < 4; ++i) S[i] = xsum32(xsum16(__uint_as_float(sA0[i]) + __uint_as_float(sA1[i])));
#pragma unroll
          for (int i = 0; i < 2; ++i) S[4 + i] = xsum32(xsum16(__uint_as_float(sB0[i]) + __uint_as_float(sB1[i])));
          const f32x4 cc4 = lds_ld<f32x4>(L_CONST); const float SB2 = lds_ld<float>(L_CONST + 16);
          const float iD = 1.f / 512.f, m1 = S[0] * iD, r1 = __builtin_amdgcn_rsqf(fmaxf(S[1] * iD - m1 * m1, 0.f) + 1e-5f);
          const float m2 = (r1 * (S[2] - m1 * cc4[0]) + cc4[1]) * iD;
          const float ez2 = (r1 * r1 * (S[3] - 2.f * m1 * S[4] + m1 * m1 * cc4[2]) + 2.f * r1 * (S[5] - m1 * cc4[3]) + SB2) * iD;
          r2 = __builtin_amdgcn_rsqf(fmaxf(ez2 - m2 * m2, 0.f) + 1e-5f);
          ca = r1 * r2; cb = -ca * m1; cc = -r2 * m2; PIN(ca); PIN(cb); PIN(cc); PIN(r2); }
        const unsigned uo = L_UVW + 16 * gi + 64 * odd, uo6 = L_UVW + 16 * gi + (odd ? 0 : 384);
        f32x4 uu = lds_ld<f32x4>(uo), vv = lds_ld<f32x4>(uo + 448), ww = lds_ld<f32x4>(uo + 896), bb = lds_ld<f32x4>(uo + 1344);
        f32x4 u2 = lds_ld<f32x4>(uo + 64), v2 = lds_ld<f32x4>(uo + 64 + 448), w2v = lds_ld<f32x4>(uo + 64 + 896), b2v = lds_ld<f32x4>(uo + 64 + 1344);
        BAR();
        WS4(2);
        { const unsigned ex = 101376 + w * 7168 + lane * 16;
#pragma unroll
          for (int u = 0; u < 7; ++u) lds_st<f32x4>(ex + u * 1024, acb[u]); }
        WS4(3);
        BAR();
        WS4(4);
        { const unsigned ep = 101376 + (w ^ 4) * 7168 + lane * 16;
#pragma unroll
          for (int u = 0; u < 7; ++u) acc[u] += lds_ld<f32x4>(ep + u * 1024); }
#pragma unroll
        for (int u = 0; u < 7; ++u) PIN(acc[u]);
        BAR();
        STAMP(14);
        const unsigned hb = WS_H + (g * 8 + Tw) * 57344, s0 = (7 * j + odd) >> 1;
        const unsigned psw = (lane >> 3) * 1024u + ct * 128u + (((lane & 7) >> 1) ^ ((lane >> 4) & 3)) * 32u + (lane & 1) * 16u;
        const char* pp0 = (const char*)w2 + (size_t)(56 * (w >> 2) + 40 + (w & 3)) * 8192, * pp1 = pp0 + (size_t)112 * 8192;
#define PARK(i) { park[2 * (i)] = *(const u32x4*)(pp0 + (i) * 32768 + psw); park[2 * (i) + 1] = *(const u32x4*)(pp1 + (i) * 32768 + psw); }
        const f32x2 kA = {-0.044715f * 2.302208198f, -0.044715f * 2.302208198f}, kB = {-2.302208198f, -2.302208198f}, one2 = {1.f, 1.f};
#define LO2(v) ((f32x2){(v)[0], (v)[1]})
#define HI2(v) ((f32x2){(v)[2], (v)[3]})
#define PRE2(a_, u_, v_, w_, b_) (ca * (a_) + (cb * (u_) + (r2 * (v_) + (cc * (w_) + (b_)))))
#define GELU(N, p, y) { f32x2 z_[N], e_[N]; \
            _Pragma("unroll") for (int r = 0; r < N; ++r) { z_[r] = p[r] * p[r]; PIN(z_[r]); } \
            _Pragma("unroll") for (int r = 0; r < N; ++r) { z_[r] = z_[r] * kA + kB; PIN(z_[r]); } \
            _Pragma("unroll") for (int r = 0; r < N; ++r) { z_[r] = z_[r] * p[r]; PIN(z_[r]); } \
            SBAR(); _Pragma("unroll") for (int r = 0; r < N; ++r) { e_[r] = (f32x2){__builtin_amdgcn_exp2f(z_[r][0]), __builtin_amdgcn_exp2f(z_[r][1])}; PIN(e_[r]); } \
            SBAR(); _Pragma("unroll") for (int r = 0; r < N; ++r) { e_[r] = e_[r] + one2; PIN(e_[r]); } \
            SBAR(); _Pragma("unroll") for (int r = 0; r < N; ++r) { e_[r] = (f32x2){__builtin_amdgcn_rcpf(e_[r][0]), __builtin_amdgcn_rcpf(e_[r][1])}; PIN(e_[r]); } \
            SBAR(); _Pragma("unroll") for (int r = 0; r < N; ++r) y[r] = p[r] * e_[r]; }
#pragma unroll
        for (int u = 0; u < 6; u += 2) {
            f32x2 pre[4], y[4];
            pre[0] = PRE2(LO2(acc[u]), LO2(uu), LO2(vv), LO2(ww), LO2(bb)); pre[1] = PRE2(HI2(acc[u]), HI2(uu), HI2(vv), HI2(ww), HI2(bb));
            pre[2] = PRE2(LO2(acc[u + 1]), LO2(u2), LO2(v2), LO2(w2v), LO2(b2v)); pre[3] = PRE2(HI2(acc[u + 1]), HI2(u2), HI2(v2), HI2(w2v), HI2(b2v));
            PIN(pre[0]); PIN(pre[1]); PIN(pre[2]); PIN(pre[3]);
            SBAR();
            { const unsigned o = u < 4 ? uo + (u + 2) * 64 : uo6; uu = lds_ld<f32x4>(o); vv = lds_ld<f32x4>(o + 448); ww = lds_ld<f32x4>(o + 896); bb = lds_ld<f32x4>(o + 1344); }
            if (u < 4) { const unsigned o = uo + (u + 3) * 64; u2 = lds_ld<f32x4>(o); v2 = lds_ld<f32x4>(o + 448); w2v = lds_ld<f32x4>(o + 896); b2v = lds_ld<f32x4>(o + 1344); }
            PARK(u >> 1) SBAR();
            GELU(4, pre, y)
            __builtin_amdgcn_raw_buffer_store_b128((u32x4){pk2(y[0][0], y[0][1]), pk2(y[1][0], y[1][1]), pk2(y[2][0], y[2][1]), pk2(y[3][0], y[3][1])}, rws, hb + (s0 + (u >> 1)) * 1024 + lane * 16, 0, 16);
            SBAR();
        }
        {   f32x2 pre[2], y[2];
            pre[0] = PRE2(LO2(acc[6]), LO2(uu), LO2(vv), LO2(ww), LO2(bb)); pre[1] = PRE2(HI2(acc[6]), HI2(uu), HI2(vv), HI2(ww), HI2(bb));
            PARK(3) SBAR();
            GELU(2, pre, y)
            __builtin_amdgcn_raw_buffer_store_b64((u32x2){pk2(y[0][0], y[0][1]), pk2(y[1][0], y[1][1])}, rws, hb + ((7 * j + (odd ? 0 : 6)) >> 1) * 1024 + odd * 512 + lane * 8, 0, 16);
        }
#undef GELU
#undef PARK
#undef PRE2
#undef LO2
#undef HI2
        WS4(5);
    }
    u32x2 gbo0, gbo1;
    { FRESH(lane); const unsigned cl = (32 * kh + 4 * (lane >> 4)) * 2; gbo0 = lds_ld<u32x2>(L_GBO + cl); gbo1 = lds_ld<u32x2>(L_GBO + cl + 32); PIN(gbo0); PIN(gbo1); }
    STAMP_OUT();
    VMWAIT(0);
    BAR();
    if (tid == 0) stg_sc1(ws, WS_FLAG + (3 * 256 + g * 16 + j) * 256, epoch);
    STAMPG(15);
    if (w == 0) { FRESH(lane); poll_flags(ws, WS_FLAG + (3 * 256 + g * 16) * 256, 16, epoch, lane); }
    SBAR();
    BAR();
    STAMPG(16); WS5(0);
    {
        constexpr int WIN = P5WIN;
        FRESH(lane);
        const int gi = lane >> 4, q4 = (lane & 15) >> 2, p4 = lane & 3;
        const unsigned tokP = 128 * g + 64 * rh + 16 * tt + (lane & 15), colP = 64 * ct + 32 * kh + 4 * gi;
        const int kq = (w & 1) | (kh << 1);
        const unsigned hb0 = WS_H + (g * 8 + 4 * rh + tt) * 57344 + kq * 14 * 1024, hb1 = WS_H + (g * 8 + 4 * rh + (tt ^ 1)) * 57344 + kq * 14 * 1024;
        u32x4 hf[2][14];
#define HLD1(hb_, dst, s) { if ((s) % 7 == 3) { const u32x2 lo = __builtin_amdgcn_raw_buffer_load_b64(rws, hb_ + (s) * 1024 + lane * 8, 0, 16), hi = __builtin_amdgcn_raw_buffer_load_b64(rws, hb_ + (s) * 1024 + 512 + lane * 8, 0, 16); dst = (u32x4){lo[0], lo[1], hi[0], hi[1]}; } \
                    else dst = __builtin_amdgcn_raw_buffer_load_b128(rws, hb_ + (s) * 1024 + lane * 16, 0, 16); }
#define HLOAD(s) { HLD1(hb0, hf[0][s], s) HLD1(hb1, hf[1][s], s) }
        HLOAD(0) SBAR(); if (kh == 0) __builtin_amdgcn_s_sleep(3); HLOAD(1) SBAR(); if (kh == 0) __builtin_amdgcn_s_sleep(3);
#pragma unroll
        for (int s = 2; s < WIN; ++s) HLOAD(s)
        SBAR();
        f32x4 acc[2][4]; bf16x8 wf[4];
        const unsigned xv = (2 * gi + (q4 >> 1)) & 3, rowa = kq * 4096 + (4 * gi + q4) * 128 + p4 * 8;
        unsigned an[4];
#pragma unroll
        for (int n = 0; n < 4; ++n) { an[n] = rowa + (((n ^ (2 * kh)) ^ xv) * 32); acc[0][n] = (f32x4){0.f, 0.f, 0.f, 0.f}; acc[1][n] = acc[0][n]; }
#define W2A(s, n) (an[n] + (((s) % 10) & 3) * 16384)
#pragma unroll
        for (int n = 0; n < 4; ++n) wf[n] = trfrag(W2A(0, n), W2A(0, n) + 2048);
        const u32x2 g0 = gbo0, g1 = gbo1;
        u32x2 bb0, bb1;
        SBAR();
#pragma unroll
        for (int s = 0; s < 14; ++s) {
            if (s == 11) { bb0 = *(const u32x2*)(b2 + colP); bb1 = *(const u32x2*)(b2 + colP + 16); }
            if (s == 1) WS5(1); if (s == 4) WS5(2); if (s == 8) WS5(3);
            if (s == 5) RBAR();
            if (s == 8) BAR();
            if (s == 8) WS5(4);
            if (s + WIN < 14) HLOAD(s + WIN)
            SBAR();
#pragma unroll
            for (int n = 0; n < 4; ++n) { acc[0][n] = MFMA16(wf[n], __builtin_bit_cast(bf16x8, hf[0][s]), acc[0][n]); PIN(acc[0][n]); SBAR();
                acc[1][n] = MFMA16(wf[n], __builtin_bit_cast(bf16x8, hf[1][s]), acc[1][n]); PIN(acc[1][n]); SBAR();
                if (s >= 5 && s <= 6) { const int i = 4 * (s - 5) + n; lds_st<u32x4>((i >> 1) * 16384 + (w + 8 * (i & 1)) * 1024 + lane * 16, park[i]); }
                if (s == 3 || s == 7) { an[n] += 65536; PIN(an[n]); } if (s == 9) { an[n] -= 131072; PIN(an[n]); }
                if (s < 13) wf[n] = trfrag(W2A(s + 1, n), W2A(s + 1, n) + 2048); SBAR(); }
        }
#undef W2A
#undef HLOAD
#undef HLD1
        WS5(5);
        BAR();
        STAMPG(17);
        { const unsigned ex = w * 6144 + lane * 16;
          lds_st<f32x4>(ex, acc[0][2]); lds_st<f32x4>(ex + 1024, acc[0][3]); lds_st<f32x4>(ex + 2048, acc[1][0]); lds_st<f32x4>(ex + 3072, acc[1][1]); lds_st<f32x4>(ex + 4096, acc[1][2]); lds_st<f32x4>(ex + 5120, acc[1][3]); }
        BAR();
        const unsigned e4 = (w ^ 4) * 6144 + lane * 16, e1 = (w ^ 1) * 6144 + 2048 + lane * 16, e5 = (w ^ 5) * 6144 + 4096 + lane * 16;
        const f32x4 a0 = (acc[0][0] + lds_ld<f32x4>(e4)) + (lds_ld<f32x4>(e1) + lds_ld<f32x4>(e5)), a1 = (acc[0][1] + lds_ld<f32x4>(e4 + 1024)) + (lds_ld<f32x4>(e1 + 1024) + lds_ld<f32x4>(e5 + 1024));
        float y0[4], y1[4];
#pragma unroll
        for (int r = 0; r < 4; ++r) {
            const unsigned ug0 = g0[r >> 1], ug1 = g1[r >> 1], ub0 = bb0[r >> 1], ub1 = bb1[r >> 1], ux0 = x1p0[r >> 1], ux1 = x1p1[r >> 1];
            y0[r] = fmaf((r & 1) ? bf_hi(ug0) : bf_lo(ug0), a0[r] + ((r & 1) ? bf_hi(ub0) : bf_lo(ub0)), (r & 1) ? bf_hi(ux0) : bf_lo(ux0));
            y1[r] = fmaf((r & 1) ? bf_hi(ug1) : bf_lo(ug1), a1[r] + ((r & 1) ? bf_hi(ub1) : bf_lo(ub1)), (r & 1) ? bf_hi(ux1) : bf_lo(ux1));
        }
        *(u32x2*)(out + tokP * 512 + colP) = (u32x2){pk2(y0[0], y0[1]), pk2(y0[2], y0[3])};
        *(u32x2*)(out + tokP * 512 + colP + 16) = (u32x2){pk2(y1[0], y1[1]), pk2(y1[2], y1[3])};
    }
    if (b == 0 && w == 0) {
        for (unsigned sp = 0; sp < SPIN_CAP; ++sp) { asm volatile("" ::: "memory"); const u32x4 v = __builtin_amdgcn_raw_buffer_load_b128(rws, WS_HELLO + lane * 16, 0, 16); if (__all(v[0] == epoch && v[1] == epoch && v[2] == epoch && v[3] == epoch)) break; }
        if (lane == 0) stg_sc1(ws, WS_E, epoch);
    }
    STAMPG(18); STAMPGC(21);
    asm volatile("fb_code_end:");
}

extern "C" void kernel_launch(void* const* d_in, const int* in_sizes, int n_in, void* d_out, int out_size, void* d_ws, size_t ws_size, hipStream_t stream) {
    (void)hipFuncSetAttribute((const void*)fused_block, hipFuncAttributeMaxDynamicSharedMemorySize, LDS_BYTES);
    fused_block<<<256, 576, LDS_BYTES, stream>>>((const u16*)d_in[0], (const u16*)d_in[1], (const u16*)d_in[2], (const u16*)d_in[4], (const u16*)d_in[5], (const u16*)d_in[6], (const u16*)d_in[7],
                                                 (const u16*)d_in[8], (const u16*)d_in[9], (u16*)d_out, (char*)d_ws);
}
```

```cpp
#include <hip/hip_runtime.h>
#include <hip/hip_bf16.h>
#include <stdint.h>

typedef unsigned short u16;
typedef short bf16x8 __attribute__((ext_vector_type(8)));
typedef short s16x4 __attribute__((ext_vector_type(4)));
typedef float f32x4 __attribute__((ext_vector_type(4)));
typedef float f32x2 __attribute__((ext_vector_type(2)));
typedef float f32x16 __attribute__((ext_vector_type(16)));
typedef unsigned u32x4 __attribute__((ext_vector_type(4)));
typedef unsigned u32x2 __attribute__((ext_vector_type(2)));
typedef __bf16 bf2 __attribute__((ext_vector_type(2)));
#define LAS __attribute__((address_space(3)))
#define GAS __attribute__((address_space(1)))
#define DI __device__ __forceinline__

#define TS 0

constexpr unsigned WS_E = 0, WS_HELLO = 4096, WS_FLAG = 8192;
constexpr unsigned WS_MODU = 0x50000, WS_MODN = 0x60000;
constexpr unsigned WS_PART = 0x80000;
constexpr unsigned WS_KV = 0x100000, WS_O = 0x500000, WS_XG = 0x700000, WS_ST = 0x900000, WS_H = 0xA00000, WS_TS = 0x2000000;
constexpr unsigned WS_BYTES = 0x2100000;
constexpr unsigned L_SLOT = 28672, L_EXS = 3 * L_SLOT;
constexpr unsigned L_UVQ = 155648, L_ZERO = 156416, L_RN = 143360, L_G32 = 156672, L_GP = 158720, L_SP = 159744, L_STAT = 160768;
constexpr unsigned L_QS = 65536, L_KIMG = 98304, L_VIMG = 122880, L_WO = 0, L_W1 = 44032;
constexpr unsigned L_GB16 = 158720, L_SB16 = 159744, L_UVW = 160768, L_CONST = 162560, L_GAO = 162592, L_GBO = 162720, L_ONES = 162848, L_ZERO2 = 162864, L_DUMMY = 162880;
constexpr unsigned L_SCR = 32768;
constexpr unsigned LDS_BYTES = 163840;
constexpr unsigned SPIN_CAP = 1u << 16;

DI float bf_lo(unsigned u) { return __uint_as_float(u << 16); }
DI float bf_hi(unsigned u) { return __uint_as_float(u & 0xffff0000u); }
DI unsigned pk2(float a, float b) { bf2 v = {(__bf16)a, (__bf16)b}; return __builtin_bit_cast(unsigned, v); }
DI float rbf(float a) { return bf_lo(pk2(a, a)); }
DI void dma16(unsigned voff, const void* sbase, unsigned lds) { asm volatile("s_mov_b32 m0, %1\n\ts_nop 0\n\tglobal_load_lds_dwordx4 %0, %2" ::"v"(voff), "s"(lds), "s"(sbase) : "memory"); }
DI void dma16c(unsigned voff, const void* sbase, unsigned lds) { asm volatile("s_mov_b32 m0, %1\n\ts_nop 0\n\tglobal_load_lds_dwordx4 %0, %2 sc1" ::"v"(voff), "s"(lds), "s"(sbase) : "memory"); }
DI void dma4(unsigned v0, unsigned v1, unsigned v2, unsigned v3, const void* sbase, unsigned lds) {
    asm volatile("s_mov_b32 m0, %5\n\ts_nop 0\n\tglobal_load_lds_dwordx4 %0, %4\n\tglobal_load_lds_dwordx4 %1, %4 offset:1024\n\tglobal_load_lds_dwordx4 %2, %4 offset:2048\n\tglobal_load_lds_dwordx4 %3, %4 offset:3072"
                 ::"v"(v0), "v"(v1), "v"(v2), "v"(v3), "s"(sbase), "s"(lds) : "memory"); }
DI void dma3(unsigned v0, unsigned v1, unsigned v2, const void* sbase, unsigned lds) {
    asm volatile("s_mov_b32 m0, %4\n\ts_nop 0\n\tglobal_load_lds_dwordx4 %0, %3\n\tglobal_load_lds_dwordx4 %1, %3 offset:1024\n\tglobal_load_lds_dwordx4 %2, %3 offset:2048"
                 ::"v"(v0), "v"(v1), "v"(v2), "s"(sbase), "s"(lds) : "memory"); }
DI void dma4lin_c(unsigned v, const void* sbase, unsigned lds) {
    asm volatile("s_mov_b32 m0, %2\n\ts_nop 0\n\tglobal_load_lds_dwordx4 %0, %1 sc1\n\tglobal_load_lds_dwordx4 %0, %1 offset:1024 sc1\n\tglobal_load_lds_dwordx4 %0, %1 offset:2048 sc1\n\tglobal_load_lds_dwordx4 %0, %1 offset:3072 sc1"
                 ::"v"(v), "s"(sbase), "s"(lds) : "memory"); }
DI void warm4(unsigned voff, const void* sbase) { asm volatile("s_mov_b32 m0, %1\n\ts_nop 0\n\tglobal_load_lds_dword %0, %2" ::"v"(voff), "s"(L_DUMMY), "s"(sbase) : "memory"); }
#define WSP 4
#define WS1(i) do { if (WSP == 1) WSTAMP(i); } while (0)
#define WS2(i) do { if (WSP == 2) WSTAMP(i); } while (0)
#define WS3(i) do { if (WSP == 3) WSTAMP(i); } while (0)
#define WS4(i) do { if (WSP == 4) WSTAMP(i); } while (0)
#define WS5(i) do { if (WSP == 5) WSTAMPG(i); } while (0)
#define WS6(i) do { if (WSP == 6) WSTAMP(i); } while (0)
#define FRESH(v) int v = threadIdx.x & 63; asm volatile("" : "+v"(v))
#define SBAR() __builtin_amdgcn_sched_barrier(0)
#define PIN(x) asm volatile("" : "+v"(x))
#define BAR() asm volatile("s_waitcnt lgkmcnt(0)\n\ts_barrier" ::: "memory")
#define P5WIN 5
#define QWIN 40
#define VMWAITN(n) asm volatile("s_waitcnt vmcnt(%0)" :: "i"(n) : "memory")
#define RBAR() asm volatile("s_barrier" ::: "memory")
#define VMWAIT(n) asm volatile("s_waitcnt vmcnt(" #n ")" ::: "memory")
DI unsigned ldg_sc1(const char* ws, unsigned off) { return __hip_atomic_load((GAS unsigned*)(ws + off), __ATOMIC_RELAXED, __HIP_MEMORY_SCOPE_AGENT); }
DI unsigned long long ldg64_sc1(const char* ws, unsigned off) { return __hip_atomic_load((GAS unsigned long long*)(ws + off), __ATOMIC_RELAXED, __HIP_MEMORY_SCOPE_AGENT); }
DI void stg_sc1(char* ws, unsigned off, unsigned v) { __hip_atomic_store((GAS unsigned*)(ws + off), v, __ATOMIC_RELAXED, __HIP_MEMORY_SCOPE_AGENT); }
DI void stg64_sc1(char* ws, unsigned off, unsigned long long v) { __hip_atomic_store((GAS unsigned long long*)(ws + off), v, __ATOMIC_RELAXED, __HIP_MEMORY_SCOPE_AGENT); }
template <class T> DI T lds_ld(unsigned a) { return *(const LAS T*)(size_t)a; }
template <class T> DI void lds_st(unsigned a, T v) { *(LAS T*)(size_t)a = v; }
DI s16x4 lds_tr(unsigned a) { return __builtin_amdgcn_ds_read_tr16_b64_v4i16((LAS s16x4*)(size_t)a); }
DI bf16x8 trfrag(unsigned a0, unsigned a1) { s16x4 lo = lds_tr(a0), hi = lds_tr(a1); return __builtin_shufflevector(lo, hi, 0, 1, 2, 3, 4, 5, 6, 7); }
#define MFMA32(a, b, c) __builtin_amdgcn_mfma_f32_32x32x16_bf16((a), (b), (c), 0, 0, 0)
#define MFMA16(a, b, c) __builtin_amdgcn_mfma_f32_16x16x32_bf16((a), (b), (c), 0, 0, 0)
template <int N> DI unsigned xgdw(unsigned x, float gv) {
    unsigned o; float a, b2;
    asm("v_lshlrev_b32 %1, 16, %3\n\tv_and_b32 %2, 0xffff0000, %3\n\t"
        "v_mul_f32_dpp %1, %4, %1 row_newbcast:%c5 row_mask:0xf bank_mask:0xf\n\tv_mul_f32_dpp %2, %4, %2 row_newbcast:%c6 row_mask:0xf bank_mask:0xf\n\t"
        "v_cvt_pk_bf16_f32 %0, %1, %2"
        : "=v"(o), "=&v"(a), "=&v"(b2) : "v"(x), "v"(gv), "i"(N), "i"(N + 1));
    return o; }
template <int C> DI float dppf(float v) { return __builtin_bit_cast(float, __builtin_amdgcn_update_dpp(0, __builtin_bit_cast(int, v), C, 0xf, 0xf, true)); }
DI float rowsum16(float v) { v += dppf<0xB1>(v); v += dppf<0x4E>(v); v += dppf<0x141>(v); v += dppf<0x140>(v); return v; }
DI float xsum32(float v) { auto r = __builtin_amdgcn_permlane32_swap(__float_as_uint(v), __float_as_uint(v), false, false); return __uint_as_float(r[0]) + __uint_as_float(r[1]); }
DI float xmax32(float v) { auto r = __builtin_amdgcn_permlane32_swap(__float_as_uint(v), __float_as_uint(v), false, false); return fmaxf(__uint_as_float(r[0]), __uint_as_float(r[1])); }
DI float xsum16(float v) { auto r = __builtin_amdgcn_permlane16_swap(__float_as_uint(v), __float_as_uint(v), false, false); return __uint_as_float(r[0]) + __uint_as_float(r[1]); }
DI unsigned long long mt() { unsigned long long t; asm volatile("s_memtime %0\n\ts_waitcnt lgkmcnt(0)" : "=s"(t)::"memory"); return t; }
DI unsigned long long rt() { unsigned long long t; asm volatile("s_memrealtime %0\n\ts_waitcnt lgkmcnt(0)" : "=s"(t)::"memory"); return t; }
#define STAMP(i) do {} while (0)
#define STAMP_OUT() do {} while (0)
#define STAMPG(i) do {} while (0)
#define STAMPGC(i) do {} while (0)
#define WSTAMPG(i) do {} while (0)
#define STAMPC(i) do {} while (0)
#define WSTAMP(i) do {} while (0)

DI void poll_flags(const char* ws, unsigned off, int n, unsigned epoch, int lane) {
    const unsigned o = off + (unsigned)(lane < n ? lane : 0) * 256u;
    for (unsigned sp = 0; sp < SPIN_CAP; ++sp) { unsigned f = ldg_sc1(ws, o); if (__all(f == epoch)) break; }
}

DI void mod_part(const u16* __restrict__ cvec, const u16* __restrict__ wmod, char* ws, unsigned epoch, int lane, int slice, int kp) {
    const unsigned row0 = 64 * kp + 4 * (lane >> 2), o = row0 * 6144u + (unsigned)slice * 64u + (lane & 3) * 16u;
    const u32x2 cv = *(const u32x2*)(cvec + row0);
    u32x4 wm[4];
#pragma unroll
    for (int i = 0; i < 4; ++i) wm[i] = *(const u32x4*)((const char*)wmod + o + i * 6144u);
    SBAR();
    unsigned sp[2];
#pragma unroll
    for (int r = 0; r < 2; ++r) { const unsigned cu = cv[r]; const float c0 = bf_lo(cu), c1 = bf_hi(cu);
        sp[r] = pk2(c0 * __builtin_amdgcn_rcpf(1.f + __builtin_amdgcn_exp2f(-1.44269504089f * c0)), c1 * __builtin_amdgcn_rcpf(1.f + __builtin_amdgcn_exp2f(-1.44269504089f * c1))); }
    float a[8] = {0.f, 0.f, 0.f, 0.f, 0.f, 0.f, 0.f, 0.f};
#pragma unroll
    for (int r = 0; r < 2; ++r)
#pragma unroll
        for (int i = 0; i < 4; ++i) { const unsigned u0 = wm[2 * r][i], u1 = wm[2 * r + 1][i], s_ = sp[r];
            const unsigned lo = __builtin_amdgcn_perm(u1, u0, 0x05040100u), hi = __builtin_amdgcn_perm(u1, u0, 0x07060302u);
            a[2 * i] = __builtin_amdgcn_fdot2_f32_bf16(__builtin_bit_cast(bf2, lo), __builtin_bit_cast(bf2, s_), a[2 * i], false);
            a[2 * i + 1] = __builtin_amdgcn_fdot2_f32_bf16(__builtin_bit_cast(bf2, hi), __builtin_bit_cast(bf2, s_), a[2 * i + 1], false); }
    float b4[4], b2[2];
#pragma unroll
    for (int i = 0; i < 4; ++i) { auto r = __builtin_amdgcn_permlane32_swap(__float_as_uint(a[i]), __float_as_uint(a[i + 4]), false, false); b4[i] = __uint_as_float(r[0]) + __uint_as_float(r[1]); }
#pragma unroll
    for (int i = 0; i < 2; ++i) { auto r = __builtin_amdgcn_permlane16_swap(__float_as_uint(b4[i]), __float_as_uint(b4[i + 2]), false, false); float v = __uint_as_float(r[0]) + __uint_as_float(r[1]);
        v += dppf<0x124>(v); v += dppf<0x128>(v); b2[i] = v; }
    const __amdgpu_buffer_rsrc_t rws = __builtin_amdgcn_make_buffer_rsrc(ws, 0, WS_BYTES, 0x00020000);
    if ((lane & 12) == 0) __builtin_amdgcn_raw_buffer_store_b128((u32x4){__float_as_uint(b2[0]), epoch, __float_as_uint(b2[1]), epoch}, rws,
                                                                 WS_PART + (slice * 8 + kp) * 256 + ((lane & 3) * 4 + (lane >> 5) * 2 + ((lane >> 4) & 1)) * 16, 0, 16);
}
struct PartSum { u32x4 v0, v1; };
DI void mod_sum_load(PartSum& p, char* ws, int lane, int slice) {
    const __amdgpu_buffer_rsrc_t rws = __builtin_amdgcn_make_buffer_rsrc(ws, 0, WS_BYTES, 0x00020000);
    const unsigned o = WS_PART + (slice * 8 + (lane >> 3)) * 256 + (lane & 7) * 16;
    p.v0 = __builtin_amdgcn_raw_buffer_load_b128(rws, o, 0, 16); p.v1 = __builtin_amdgcn_raw_buffer_load_b128(rws, o + 128, 0, 16); }
DI void mod_sum_finish(PartSum& p, char* ws, unsigned epoch, int lane, int slice) {
    PartSum q1, q2;
    __builtin_amdgcn_s_sleep(4); mod_sum_load(q1, ws, lane, slice); __builtin_amdgcn_s_sleep(4); asm volatile("" ::: "memory"); mod_sum_load(q2, ws, lane, slice);
    for (unsigned sp = 0; sp < SPIN_CAP; ++sp) { if (__all(p.v0[1] == epoch && p.v0[3] == epoch && p.v1[1] == epoch && p.v1[3] == epoch)) break; p = q1; q1 = q2; asm volatile("" ::: "memory"); mod_sum_load(q2, ws, lane, slice); }
    float s[4] = {__uint_as_float(p.v0[0]), __uint_as_float(p.v0[2]), __uint_as_float(p.v1[0]), __uint_as_float(p.v1[2])};
#pragma unroll
    for (int i = 0; i < 4; ++i) { float v = s[i]; v += dppf<0x128>(v); s[i] = xsum32(xsum16(v)); }
    const unsigned long long g0 = ((unsigned long long)epoch << 32) | pk2(s[0], s[1]), g1 = ((unsigned long long)epoch << 32) | pk2(s[2], s[3]);
    const unsigned base = slice < 32 ? WS_MODU + slice * 128 : WS_MODN + (slice - 32) * 128, rs = slice < 32 ? 4096 : 8192, rep = lane >> 3, o = base + (lane & 7) * 8;
    stg64_sc1(ws, o + rep * rs, g0); stg64_sc1(ws, o + 64 + rep * rs, g1); stg64_sc1(ws, o + (rep + 8) * rs, g0); stg64_sc1(ws, o + 64 + (rep + 8) * rs, g1);
}

__global__ __launch_bounds__(576, 3) void fused_block(const u16* __restrict__ x, const u16* __restrict__ wqkv, const u16* __restrict__ wo, const u16* __restrict__ w1, const u16* __restrict__ b1,
                                                      const u16* __restrict__ w2, const u16* __restrict__ b2, const u16* __restrict__ cvec, const u16* __restrict__ wmod, u16* __restrict__ out, char* __restrict__ ws) {
    unsigned long long e_t0, e_t1 = 0;
    asm volatile("s_memrealtime %0\n\ts_waitcnt lgkmcnt(0)" : "=s"(e_t0));
    const unsigned epoch = *(const unsigned*)(ws + WS_E) + 1u;
    const int tid = threadIdx.x, lane = tid & 63, w = __builtin_amdgcn_readfirstlane(tid >> 6);
    const int b = blockIdx.x, xl = b & 7, idx = b >> 3, g = 2 * xl + (idx >> 4), j = idx & 15;
    const int ct = j & 7, rh = j >> 3;
    unsigned long long code_pc; unsigned code_len;
    asm volatile("s_getpc_b64 %0\nfb_code_begin:\n\ts_mov_b32 %1, fb_code_end-fb_code_begin" : "=s"(code_pc), "=s"(code_len));
    STAMP(0); STAMPC(20);
    if (w == 8) {
        {
            const unsigned o = ((unsigned)idx * 32u + (lane & 31)) * 64u;
            warm4((int)o < (int)code_len ? o : 0u, (const void*)code_pc);
        }
        if (lane == 0) stg_sc1(ws, WS_HELLO + b * 4, epoch);
        __builtin_amdgcn_s_setprio(3);
        const unsigned sw0 = (lane >> 3) * 1024u + ct * 128u + (((lane & 7) >> 1) ^ ((lane >> 4) & 3)) * 32u + (lane & 1) * 16u, sw1 = sw0 + 7168, sw2 = sw0 + 14336, sw3 = sw0 + 21504;
        unsigned w1o[7], wqo[3];
#pragma unroll
        for (int r = 0; r < 7; ++r) { const unsigned u = r * 64 + lane, row = u / 14, c = u % 14; w1o[r] = row * 3584u + 224u * j + c * 16u - (r & 3) * 1024u; }
#pragma unroll
        for (int r = 0; r < 3; ++r) { const unsigned u = r * 64 + lane, row = u / 12, c = u % 12; wqo[r] = row * 3072u + (c >> 2) * 1024u + 64u * j + (c & 3) * 16u - r * 1024u; }
#define RUN128(base, first, n4, lds0) { const char* p_ = (const char*)(base) + (size_t)(first) * 8192; unsigned l_ = (lds0); _Pragma("unroll 2") for (int i_ = 0; i_ < (n4); ++i_) { dma4(sw0, sw1, sw2, sw3, p_, l_); p_ += 32768; l_ += 4096; } }
#define W1GRPS(k0, k1) { const char* p_ = (const char*)w1 + (size_t)(k0) * 114688; unsigned l_ = L_W1 + 7 * (k0) * 1024; _Pragma("unroll 1") for (int k_ = (k0); k_ < (k1); ++k_) { \
            dma4(w1o[0], w1o[1], w1o[2], w1o[3], p_, l_); dma3(w1o[4], w1o[5], w1o[6], p_, l_ + 4096); p_ += 114688; l_ += 7168; } }
#define W2CHUNKS(c0, c1, slot0) { _Pragma("unroll 1") for (int c_ = (c0); c_ < (c1); ++c_) { const char* p_ = (const char*)w2 + (size_t)c_ * 32768; const unsigned l_ = ((slot0) + c_ - (c0)) * 16384; \
            dma4(sw0, sw1, sw2, sw3, p_, l_); dma4(sw0, sw1, sw2, sw3, p_ + 56 * 8192, l_ + 4096); dma4(sw0, sw1, sw2, sw3, p_ + 112 * 8192, l_ + 8192); dma4(sw0, sw1, sw2, sw3, p_ + 168 * 8192, l_ + 12288); } }
        const unsigned xr0 = (lane >> 3) * 1024u, xe = 16u * ((lane & 7) ^ (lane >> 4)), xod = 16u * ((lane & 7) ^ (4 + (lane >> 4)));
        const unsigned xv0 = xr0 + xe, xv1 = xr0 + 7168 + xod, xv2 = xr0 + 14336 + xe, xv3 = xr0 + 21504 + xod;
#define QCHUNK(c) { const char* px_ = (const char*)x + (size_t)(128 * g) * 1024 + 128 * (c); const char* pw_ = (const char*)wqkv + (size_t)(4 * (c)) * 49152; const unsigned l_ = ((c) % 5) * L_SLOT; \
            _Pragma("unroll") for (int q_ = 0; q_ < 4; ++q_) { dma4(xv0, xv1, xv2, xv3, px_ + q_ * 32768, l_ + q_ * 4096); dma3(wqo[0], wqo[1], wqo[2], pw_ + q_ * 49152, l_ + 16384 + q_ * 3072); } }
        __builtin_amdgcn_s_barrier();
        __builtin_amdgcn_s_barrier();
        __builtin_amdgcn_s_barrier();
        QCHUNK(5)
        __builtin_amdgcn_s_barrier();
        QCHUNK(6)
        __builtin_amdgcn_s_barrier();
        QCHUNK(7)
        VMWAIT(56);
        __builtin_amdgcn_s_barrier();
        VMWAIT(28);
        __builtin_amdgcn_s_barrier();
        VMWAIT(0);
        __builtin_amdgcn_s_barrier();
        __builtin_amdgcn_s_barrier();
        __builtin_amdgcn_s_barrier();
        if (g >= 1) {
            const unsigned fo = WS_FLAG + (((g >= 2 ? g - 2 + (lane & 1) : 0) * 16 + j) * 256);
            for (unsigned sp = 0; sp < SPIN_CAP; ++sp) { unsigned f = ldg_sc1(ws, fo); if (__all(f == epoch)) break; }
            const unsigned lo16 = lane * 16;
#pragma unroll 1
            for (int src = (g >= 2 ? 0 : 1); src < 2; ++src) {
                const char* p_ = ws + WS_KV + (size_t)((g - 2 + src) * 16 + j) * 16384;
                dma4lin_c(lo16, p_, L_KIMG + src * 8192); dma4lin_c(lo16, p_ + 4096, L_KIMG + src * 8192 + 4096);
                dma4lin_c(lo16, p_ + 8192, L_VIMG + src * 8192); dma4lin_c(lo16, p_ + 12288, L_VIMG + src * 8192 + 4096);
            }
        }
        VMWAIT(0);
        __builtin_amdgcn_s_barrier();
        RUN128(wo, 0, 16, L_WO)
        W1GRPS(3, 7)
        W1GRPS(15, 16)
        mod_part(cvec, wmod, ws, epoch, lane, 32 + 8 * xl + (idx & 3), idx >> 2); mod_part(cvec, wmod, ws, epoch, lane, 36 + 8 * xl + (idx & 3), idx >> 2);
        if (idx < 4) { PartSum p; mod_sum_load(p, ws, lane, 32 + 8 * xl + idx); mod_sum_finish(p, ws, epoch, lane, 32 + 8 * xl + idx); mod_sum_load(p, ws, lane, 36 + 8 * xl + idx); mod_sum_finish(p, ws, epoch, lane, 36 + 8 * xl + idx); }
        __builtin_amdgcn_s_barrier();
        __builtin_amdgcn_s_sleep(2);
        W1GRPS(7, 10)
        VMWAIT(21);
        __builtin_amdgcn_s_barrier();
        __builtin_amdgcn_s_setprio(0); __builtin_amdgcn_s_sleep(11);
        W1GRPS(10, 15)
        __builtin_amdgcn_s_setprio(3);
        __builtin_amdgcn_s_barrier();
        __builtin_amdgcn_s_barrier();
        __builtin_amdgcn_s_barrier();
        __builtin_amdgcn_s_barrier();
        __builtin_amdgcn_s_sleep(2);
        W1GRPS(0, 3)
        VMWAIT(0);
        __builtin_amdgcn_s_barrier();
        __builtin_amdgcn_s_barrier();
        __builtin_amdgcn_s_sleep(22);
        W2CHUNKS(0, 2, 0)
        __builtin_amdgcn_s_barrier();
        __builtin_amdgcn_s_barrier();
        W2CHUNKS(2, 6, 2)
        __builtin_amdgcn_s_barrier();
        W2CHUNKS(6, 9, 6)
        __builtin_amdgcn_s_barrier();
        __builtin_amdgcn_s_sleep(2);
        W2CHUNKS(9, 10, 9)
        VMWAIT(16);
        __builtin_amdgcn_s_barrier();
        __builtin_amdgcn_s_barrier();
        VMWAIT(0);
        __builtin_amdgcn_s_barrier();
        __builtin_amdgcn_s_barrier();
        __builtin_amdgcn_s_barrier();
        return;
    }
    const __amdgpu_buffer_rsrc_t rws = __builtin_amdgcn_make_buffer_rsrc(ws, 0, WS_BYTES, 0x00020000);
    const int m = w & 3, kh = w >> 2;
    PartSum psum;
    if (w >= 4) {
        const unsigned xr0 = (lane >> 3) * 1024u, xe = 16u * ((lane & 7) ^ (lane >> 4)), xod = 16u * ((lane & 7) ^ (4 + (lane >> 4)));
        unsigned wq[3];
#pragma unroll
        for (int r = 0; r < 3; ++r) { const unsigned u = r * 64 + lane, row = u / 12, c = u % 12; wq[r] = row * 3072u + (c >> 2) * 1024u + 64u * j + (c & 3) * 16u - r * 1024u; }
        const char* px = (const char*)x + (size_t)(128 * g + 32 * m) * 1024; const char* pw = (const char*)wqkv + (size_t)m * 49152;
#pragma unroll
        for (int c = 0; c < 5; ++c) { dma4(xr0 + xe, xr0 + 7168 + xod, xr0 + 14336 + xe, xr0 + 21504 + xod, px + 128 * c, c * L_SLOT + m * 4096);
            VMWAITN(QWIN);
            dma3(wq[0], wq[1], wq[2], pw + (size_t)(4 * c) * 49152, c * L_SLOT + 16384 + m * 3072);
            VMWAITN(QWIN); }
    }
    if (w == 0) { lds_st<u32x4>(L_ONES, (u32x4){0x3F803F80u, 0x3F803F80u, 0x3F803F80u, 0x3F803F80u}); lds_st<u32x4>(L_ZERO, (u32x4){0u, 0u, 0u, 0u}); lds_st<u32x4>(L_ZERO2, (u32x4){0u, 0u, 0u, 0u}); }
    if (w == 3) { __builtin_amdgcn_s_setprio(2); mod_part(cvec, wmod, ws, epoch, lane, 4 * xl + (idx & 3), idx >> 2);
        if (idx < 4) { mod_sum_load(psum, ws, lane, 4 * xl + idx); mod_sum_finish(psum, ws, epoch, lane, 4 * xl + idx); } __builtin_amdgcn_s_setprio(0); }
    STAMP(1); WS6(0);
    bf16x8 qf; u32x4 kpk, vpk;
    {
    const int h = lane >> 5, gi = lane >> 4, q4 = (lane & 15) >> 2, p4 = lane & 3;
    u32x4 xr[16];
    const unsigned tok = 32 * m + (lane & 31), xa0 = tok * 128 + (((4 * kh + h) ^ ((tok >> 1) & 7)) << 4), xa1 = tok * 128 + (((4 * kh + 2 + h) ^ ((tok >> 1) & 7)) << 4);
#define XRD(s) xr[s] = lds_ld<u32x4>((((s) & 1) ? xa1 : xa0) + (((s) >> 1) % 5) * L_SLOT)
    float s0 = 0.f, s1 = 0.f, s2 = 0.f, s3 = 0.f;
#define XSTAT(s) { const bf2 one = {(__bf16)1.0f, (__bf16)1.0f}; _Pragma("unroll") for (int i = 0; i < 4; i += 2) { const unsigned ua = xr[s][i], ub = xr[s][i + 1]; const bf2 va = __builtin_bit_cast(bf2, ua), vb = __builtin_bit_cast(bf2, ub); \
                s0 = __builtin_amdgcn_fdot2_f32_bf16(va, one, s0, false); s1 = __builtin_amdgcn_fdot2_f32_bf16(va, va, s1, false); \
                s2 = __builtin_amdgcn_fdot2_f32_bf16(vb, one, s2, false); s3 = __builtin_amdgcn_fdot2_f32_bf16(vb, vb, s3, false); } }
    const unsigned gmo = WS_MODU + g * 4096 + (tid & 255) * 16; u32x4 gmv;
    if (w < 3) __builtin_amdgcn_s_sleep(56);
    if (w < 4) gmv = __builtin_amdgcn_raw_buffer_load_b128(rws, gmo, 0, 16);
    if (w >= 4) VMWAIT(14);
    BAR();
    WS6(1);
#pragma unroll
    for (int s = 0; s < 6; ++s) XRD(s);
#pragma unroll
    for (int s = 0; s < 6; ++s) XSTAT(s)
    PIN(s0); PIN(s1); PIN(s2); PIN(s3);
    WS6(2);
    if (w < 4) {
        for (unsigned sp = 0; sp < SPIN_CAP; ++sp) { if (__all(gmv[1] == epoch && gmv[3] == epoch)) break; asm volatile("" ::: "memory"); gmv = __builtin_amdgcn_raw_buffer_load_b128(rws, gmo, 0, 16); }
        const unsigned u0 = gmv[0], u2 = gmv[2], k = (4 * tid) & 511, po = (k >> 5) * 64 + ((k >> 2) & 3) * 16 + ((k >> 4) & 1) * 8;
        if (tid >= 128) { const unsigned g0 = pk2(1.f + bf_lo(u0), 1.f + bf_hi(u0)), g1 = pk2(1.f + bf_lo(u2), 1.f + bf_hi(u2));
            lds_st<f32x4>(L_G32 + k * 4, (f32x4){bf_lo(g0), bf_hi(g0), bf_lo(g1), bf_hi(g1)}); lds_st<u32x2>(L_GP + po, (u32x2){g0, g1}); }
        else lds_st<u32x2>(L_SP + po, (u32x2){u0, u2});
    }
    STAMP(3); WS6(3);
    VMWAIT(0);
    BAR();
    STAMP(4); STAMPC(22); WS1(0);
    if (w == 0) e_t1 = rt();
#pragma unroll
    for (int s = 6; s < 10; ++s) XRD(s);
    f32x16 aQ, aK, aV; f32x4 aU = {0.f, 0.f, 0.f, 0.f};
#pragma unroll
    for (int i = 0; i < 16; ++i) { aQ[i] = 0.f; aK[i] = 0.f; aV[i] = 0.f; }
    {
        const unsigned rowa = 16384 + (32 * kh + 8 * (gi >> 1) + q4) * 192 + p4 * 8;
        unsigned wqa = rowa + (((gi & 1) ^ kh) * 32), wva = rowa + 128 + (gi & 1) * 32;
        unsigned uaa = 16384 + (4 * gi + q4) * 192 + p4 * 8 + (w < 6 ? w : w - 6) * 32;
        const unsigned uinc = (lane & 15) < 2 ? 64u : 0u; unsigned uba = ((lane & 15) == 0 ? L_GP : (lane & 15) == 1 ? L_SP : L_ZERO) + ((lane & 15) < 2 ? gi * 16 : 0);
        const unsigned sga = L_G32 + (32 * kh + 16 * ((lane & 15) >> 3) + 8 * h + (lane & 7)) * 4;
        float gv[2]; bf16x8 fk, fv, fq, fu, bu; u32x4 hc, hn;
        gv[0] = lds_ld<float>(sga); gv[1] = lds_ld<float>(sga + 256);
        fk = trfrag(wqa + 64, wqa + 64 + 768); fv = trfrag(wva, wva + 768); fq = trfrag(wqa, wqa + 768); fu = trfrag(uaa, uaa + 3072); bu = lds_ld<bf16x8>(uba);
#pragma unroll
        for (int s = 6; s < 10; ++s) XSTAT(s)
        PIN(s0); PIN(s1); PIN(s2); PIN(s3);
        asm volatile("s_nop 1" : "+v"(gv[0]), "+v"(gv[1]));
#define HG(s, i) xgdw<8 * ((s) & 1) + 2 * (i)>(xr[s][i], gv[((s) >> 1) & 1])
        hc = (u32x4){HG(0, 0), HG(0, 1), HG(0, 2), HG(0, 3)};
        asm volatile("s_nop 1" : "+v"(hc)); SBAR();
#define WO(s) ((((s) >> 1) % 5 & 1 ? L_SLOT : 0) + ((s) & 1) * 3072)
#define UO(s) ((((s) >> 1) % 5 & 1 ? L_SLOT : 0) + ((s) & 1) * 6144)
#define QKV_STEP(s, sn) { const bf16x8 hb = __builtin_bit_cast(bf16x8, hc); unsigned n0, n1, n2, n3; \
            if ((s) == 2 || (s) == 4 || (s) == 6 || (s) == 8 || (s) == 10 || (s) == 12) RBAR();     \
            if ((sn) < 16 && (sn) % 2 == 0 && ((sn) >> 1) % 5 % 2 == 0) { const int d_ = (((sn) >> 1) % 5 == 0) ? -4 * (int)L_SLOT : 2 * (int)L_SLOT; wqa += d_; wva += d_; uaa += d_; PIN(wqa); PIN(wva); PIN(uaa); } \
            if ((s) + 2 >= 10 && (s) + 2 < 16) XRD(((s) + 2) & 15); \
            aK = MFMA32(fk, hb, aK); PIN(aK); SBAR(); if ((sn) < 16) { fk = trfrag(wqa + WO(sn) + 64, wqa + WO(sn) + 64 + 768); n0 = HG((sn) & 15, 0); n1 = HG((sn) & 15, 1); PIN(n0); PIN(n1); } SBAR(); \
            aV = MFMA32(hb, fv, aV); PIN(aV); SBAR(); if ((sn) < 16) { fv = trfrag(wva + WO(sn), wva + WO(sn) + 768); n2 = HG((sn) & 15, 2); PIN(n2); } SBAR(); \
            aQ = MFMA32(fq, hb, aQ); PIN(aQ); SBAR(); if ((sn) < 16) { fq = trfrag(wqa + WO(sn), wqa + WO(sn) + 768); n3 = HG((sn) & 15, 3); PIN(n3); } SBAR(); \
            aU = MFMA16(fu, bu, aU); PIN(aU); SBAR(); if ((sn) < 16) { fu = trfrag(uaa + UO(sn), uaa + UO(sn) + 3072); uba += uinc; bu = lds_ld<bf16x8>(uba); hc = (u32x4){n0, n1, n2, n3}; } \
            if ((s) % 2 == 1 && (s) + 3 < 16) gv[(((s) + 3) >> 1) & 1] = lds_ld<float>(sga + (((s) + 3) >> 1) * 256); SBAR(); }
        WS1(1);
        QKV_STEP(0, 1) QKV_STEP(1, 2) QKV_STEP(2, 3) QKV_STEP(3, 4) QKV_STEP(4, 5) QKV_STEP(5, 6) QKV_STEP(6, 7) QKV_STEP(7, 8)
        QKV_STEP(8, 9) QKV_STEP(9, 10) QKV_STEP(10, 11) QKV_STEP(11, 12) QKV_STEP(12, 13) QKV_STEP(13, 14) QKV_STEP(14, 15) QKV_STEP(15, 16)
#undef QKV_STEP
#undef HG
#undef WO
#undef UO
        STAMP(5); STAMPC(23); WS1(2);
        float vs[8], vk[8];
#pragma unroll
        for (int i = 0; i < 8; ++i) { float lo = aV[i], hi = aV[8 + i]; PIN(lo); PIN(hi); vs[i] = kh ? lo : hi; vk[i] = kh ? hi : lo; }
        const unsigned ex = L_EXS + w * 6144 + lane * 16;
        lds_st<f32x4>(ex, (f32x4){aK[8], aK[9], aK[10], aK[11]}); lds_st<f32x4>(ex + 1024, (f32x4){aK[12], aK[13], aK[14], aK[15]});
        lds_st<f32x4>(ex + 2048, (f32x4){vs[0], vs[1], vs[2], vs[3]}); lds_st<f32x4>(ex + 3072, (f32x4){vs[4], vs[5], vs[6], vs[7]});
        lds_st<f32x4>(ex + 4096, (f32x4){aQ[8], aQ[9], aQ[10], aQ[11]}); lds_st<f32x4>(ex + 5120, (f32x4){aQ[12], aQ[13], aQ[14], aQ[15]});
        if (w < 6 && (lane & 15) < 2) lds_st<f32x4>(L_UVQ + ((lane & 15) * 96 + w * 16 + 4 * gi) * 4, aU);
#pragma unroll
        for (int s = 10; s < 16; ++s) XSTAT(s)
        s0 = xsum32(s0 + s2); s1 = xsum32(s1 + s3);
        if (lane < 32) lds_st<u32x2>(L_STAT + (kh * 128 + 32 * m + lane) * 8, (u32x2){__float_as_uint(s0), __float_as_uint(s1)});
        BAR();
        WS1(3);
        const unsigned ep = L_EXS + (w ^ 4) * 6144 + lane * 16;
        const u32x2 so = lds_ld<u32x2>(L_STAT + ((kh ^ 1) * 128 + 32 * m + (lane & 31)) * 8);
        const f32x4 k0 = lds_ld<f32x4>(ep), k1 = lds_ld<f32x4>(ep + 1024), v0 = lds_ld<f32x4>(ep + 2048), v1 = lds_ld<f32x4>(ep + 3072), q0 = lds_ld<f32x4>(ep + 4096), q1 = lds_ld<f32x4>(ep + 5120);
        const unsigned uq = L_UVQ + (16 * kh + 4 * h) * 4;
        const f32x4 uk0 = lds_ld<f32x4>(uq + 128), uk1 = lds_ld<f32x4>(uq + 128 + 32), vk0 = lds_ld<f32x4>(uq + 384 + 128), vk1 = lds_ld<f32x4>(uq + 384 + 128 + 32);
        const f32x4 uq0 = lds_ld<f32x4>(uq), uq1 = lds_ld<f32x4>(uq + 32), vq0 = lds_ld<f32x4>(uq + 384), vq1 = lds_ld<f32x4>(uq + 384 + 32);
        const float uv_ = lds_ld<float>(L_UVQ + (64 + (lane & 31)) * 4), vv_ = lds_ld<float>(L_UVQ + (96 + 64 + (lane & 31)) * 4);
        const float t0 = s0 + __uint_as_float(so[0]), t1 = s1 + __uint_as_float(so[1]);
        const float mean = t0 * (1.f / 512.f), rstd = __builtin_amdgcn_rsqf(fmaxf(t1 * (1.f / 512.f) - mean * mean, 0.f) + 1e-5f), nmr = -mean * rstd;
        const unsigned rn = L_RN + w * 256;
        if (lane < 32) { lds_st<float>(rn + lane * 4, rstd); lds_st<float>(rn + 128 + lane * 4, nmr); }
        asm volatile("s_waitcnt lgkmcnt(0)" ::: "memory");
        const f32x4 ra = lds_ld<f32x4>(rn + (16 * kh + 4 * h) * 4), rb = lds_ld<f32x4>(rn + (16 * kh + 8 + 4 * h) * 4), na = lds_ld<f32x4>(rn + 128 + (16 * kh + 4 * h) * 4), nb = lds_ld<f32x4>(rn + 128 + (16 * kh + 8 + 4 * h) * 4);
        u32x4 kp, vp, qp; float kv[8], vv[8], qv[8];
#pragma unroll
        for (int i = 0; i < 4; ++i) { kv[i] = fmaf(rstd, aK[i] + k0[i], fmaf(nmr, uk0[i], vk0[i])); kv[4 + i] = fmaf(rstd, aK[4 + i] + k1[i], fmaf(nmr, uk1[i], vk1[i]));
            vv[i] = fmaf(ra[i], vk[i] + v0[i], fmaf(na[i], uv_, vv_)); vv[4 + i] = fmaf(rb[i], vk[4 + i] + v1[i], fmaf(nb[i], uv_, vv_)); }
#pragma unroll
        for (int i = 0; i < 4; ++i) { kp[i] = pk2(kv[2 * i], kv[2 * i + 1]); vp[i] = pk2(vv[2 * i], vv[2 * i + 1]); }
        const unsigned kvo = WS_KV + (g * 16 + j) * 16384 + (m * 2 + kh) * 1024 + lane * 16;
        __builtin_amdgcn_raw_buffer_store_b128(kp, rws, kvo, 0, 16);
        __builtin_amdgcn_raw_buffer_store_b128(vp, rws, kvo + 8192, 0, 16);
        PIN(kp); PIN(vp); kpk = kp; vpk = vp;
        SBAR();
        const float qs = 0.25f * 1.44269504089f;
#pragma unroll
        for (int i = 0; i < 4; ++i) { qv[i] = qs * fmaf(rstd, aQ[i] + q0[i], fmaf(nmr, uq0[i], vq0[i])); qv[4 + i] = qs * fmaf(rstd, aQ[4 + i] + q1[i], fmaf(nmr, uq1[i], vq1[i])); }
#pragma unroll
        for (int i = 0; i < 4; ++i) qp[i] = pk2(qv[2 * i], qv[2 * i + 1]);
        qf = __builtin_bit_cast(bf16x8, qp); PIN(qf);
    }
#undef XRD
#undef XSTAT
    }
    WS1(4);
    VMWAIT(0); WS1(5);
    BAR();
    if (tid == 0) stg_sc1(ws, WS_FLAG + (0 * 256 + g * 16 + j) * 256, epoch);
    STAMP(6);
    { FRESH(lane);
      lds_st<u32x4>(L_KIMG + (8 + m) * 2048 + kh * 1024 + lane * 16, kpk);
      lds_st<u32x4>(L_VIMG + (8 + m) * 2048 + kh * 1024 + lane * 16, vpk); }
    const int tt = w & 3;
    {
    FRESH(lane);
    const int hh = kh, qq = lane & 31, h = lane >> 5;
    const float NEG = -__builtin_inff(), fq0 = (float)(4 * h - qq) - 0.5f;
    f32x16 c0, c8;
#pragma unroll
    for (int r = 0; r < 16; ++r) {
        const float t = fmaf(fq0, 1e30f, (float)((r & 3) + 8 * (r >> 2)) * 1e30f); c0[r] = fminf(t, 0.f); c8[r] = fminf(-t, 0.f); }
    PIN(c0); PIN(c8);
    BAR();
    STAMP(8); WS2(0);
    {
        const unsigned ka = L_KIMG + m * 2048 + hh * 1024 + lane * 16;
        const bool isd = ((lane >> 4) & 1) == hh;
        const unsigned va = isd ? L_VIMG + m * 2048 + lane * 16 : L_ONES, vst = isd ? 2048u : 0u, vs1 = isd ? 1024u : 0u;
        f32x16 o;
#pragma unroll
        for (int r = 0; r < 16; ++r) o[r] = 0.f;
#define MAX16(mx, s) { _Pragma("unroll") for (int r = 0; r < 16; r += 2) mx = fmaxf(fmaxf(mx, s[r]), s[r + 1]); }
#define EXPH(P, S, b) { P[0] = pk2(__builtin_amdgcn_exp2f(S[b]), __builtin_amdgcn_exp2f(S[b + 1])); P[1] = pk2(__builtin_amdgcn_exp2f(S[b + 2]), __builtin_amdgcn_exp2f(S[b + 3])); \
                        P[2] = pk2(__builtin_amdgcn_exp2f(S[b + 4]), __builtin_amdgcn_exp2f(S[b + 5])); P[3] = pk2(__builtin_amdgcn_exp2f(S[b + 6]), __builtin_amdgcn_exp2f(S[b + 7])); }
        if (g >= 2) {
            f32x16 z; const f32x16 z0 = {0.f, 0.f, 0.f, 0.f, 0.f, 0.f, 0.f, 0.f, 0.f, 0.f, 0.f, 0.f, 0.f, 0.f, 0.f, 0.f};
            float mx = NEG, mx2 = NEG; f32x16 sA, sB;
#define KF(i) lds_ld<bf16x8>(ka + (i) * 2048)
            bf16x8 k0 = KF(0), k1 = KF(8), k2 = KF(1), k3 = KF(2);
            SBAR();
            sA = MFMA32(k0, qf, c0); PIN(sA); k0 = KF(3); SBAR();
            sB = MFMA32(k1, qf, c8); PIN(sB); k1 = KF(4); SBAR(); MAX16(mx, sA); PIN(mx); SBAR();
            sA = MFMA32(k2, qf, z0); PIN(sA); k2 = KF(5); SBAR(); MAX16(mx2, sB); PIN(mx2); SBAR();
            sB = MFMA32(k3, qf, z0); PIN(sB); k3 = KF(6); SBAR(); MAX16(mx, sA); PIN(mx); SBAR();
            sA = MFMA32(k0, qf, z0); PIN(sA); k0 = KF(7); SBAR(); MAX16(mx2, sB); PIN(mx2); SBAR();
            sB = MFMA32(k1, qf, z0); PIN(sB); k1 = KF(0); SBAR(); MAX16(mx, sA); PIN(mx); SBAR();
            sA = MFMA32(k2, qf, z0); PIN(sA); k2 = KF(1); SBAR(); MAX16(mx2, sB); PIN(mx2); SBAR();
            sB = MFMA32(k3, qf, z0); PIN(sB); SBAR(); MAX16(mx, sA); PIN(mx); SBAR();
            sA = MFMA32(k0, qf, z0); PIN(sA); SBAR(); MAX16(mx2, sB); PIN(mx2); SBAR();
            WS2(1);
            bf16x8 v0 = lds_ld<bf16x8>(va), v1 = lds_ld<bf16x8>(va + vs1);
            MAX16(mx, sA);
            mx = xmax32(fmaxf(mx, mx2));
#pragma unroll
            for (int r = 0; r < 16; ++r) { z[r] = -mx; c0[r] -= mx; c8[r] -= mx; }
            PIN(z); PIN(c0); PIN(c8); SBAR();
            WS2(2);
            sA = MFMA32(k1, qf, c0); SBAR();
            u32x4 pa, pb; bf16x8 v0n, v1n, kn = k2; unsigned vp = va;
#define ABLK(I, SC, SN, CN) { bf16x8 kc = kn; if ((I) < 8) { vp += vst; v0n = lds_ld<bf16x8>(vp); v1n = lds_ld<bf16x8>(vp + vs1); } if ((I) < 7) kn = KF((I) + 2); SBAR(); \
                if ((I) < 8) { SN = MFMA32(kc, qf, CN); PIN(SN); } SBAR(); EXPH(pa, SC, 0); PIN(pa); SBAR(); \
                o = MFMA32(v0, __builtin_bit_cast(bf16x8, pa), o); PIN(o); SBAR(); EXPH(pb, SC, 8); PIN(pb); SBAR(); \
                o = MFMA32(v1, __builtin_bit_cast(bf16x8, pb), o); PIN(o); SBAR(); if ((I) < 8) { v0 = v0n; v1 = v1n; } }
            ABLK(0, sA, sB, z) ABLK(1, sB, sA, z) ABLK(2, sA, sB, z) ABLK(3, sB, sA, z)
            ABLK(4, sA, sB, z) ABLK(5, sB, sA, z) ABLK(6, sA, sB, z) ABLK(7, sB, sA, c8) ABLK(8, sA, sB, c8)
#undef KF
#undef ABLK
        } else {
            const int i0 = 8 - 4 * g - m;
            f32x16 z;
#pragma unroll
            for (int r = 0; r < 16; ++r) z[r] = 0.f;
            float mx = NEG;
            f32x16 s = MFMA32(lds_ld<bf16x8>(ka + 8 * 2048), qf, c8); MAX16(mx, s);
#pragma unroll 1
            for (int i = i0; i < 8; ++i) { s = MFMA32(lds_ld<bf16x8>(ka + i * 2048), qf, z); MAX16(mx, s); }
            mx = xmax32(mx);
#pragma unroll
            for (int r = 0; r < 16; ++r) { z[r] = -mx; c8[r] -= mx; }
#pragma unroll 1
            for (int i = i0; i <= 8; ++i) { u32x4 pa, pb;
                if (i < 8) s = MFMA32(lds_ld<bf16x8>(ka + i * 2048), qf, z); else s = MFMA32(lds_ld<bf16x8>(ka + i * 2048), qf, c8);
                EXPH(pa, s, 0); EXPH(pb, s, 8);
                o = MFMA32(lds_ld<bf16x8>(va + i * vst), __builtin_bit_cast(bf16x8, pa), o); o = MFMA32(lds_ld<bf16x8>(va + i * vst + vs1), __builtin_bit_cast(bf16x8, pb), o); }
        }
#undef EXPH
#undef MAX16
        WS2(3);
        float ov[8], l;
        { float lo = o[0], hi = o[8]; PIN(lo); PIN(hi); l = hh ? lo : hi; }
#pragma unroll
        for (int r = 0; r < 8; ++r) { float lo = o[r], hi = o[8 + r]; PIN(lo); PIN(hi); ov[r] = hh ? hi : lo; }
        const float inv = __builtin_amdgcn_rcpf(l);
        const u32x4 op = {pk2(ov[0] * inv, ov[1] * inv), pk2(ov[2] * inv, ov[3] * inv), pk2(ov[4] * inv, ov[5] * inv), pk2(ov[6] * inv, ov[7] * inv)};
        __builtin_amdgcn_raw_buffer_store_b128(op, rws, WS_O + ((g * 8 + 2 * m + (qq >> 4)) * 16 + j) * 1024 + ((qq & 15) + 16 * (2 * hh + h)) * 16, 0, 16);
    }
    }
    WS2(4);
    VMWAIT(0); WS2(5);
    BAR();
    if (tid == 0) stg_sc1(ws, WS_FLAG + (1 * 256 + g * 16 + j) * 256, epoch);
    STAMP(9);
    if (w >= 4) {
        const int t = tid - 256; const unsigned o = WS_MODN + g * 8192 + t * 32; u32x4 v, v2;
        for (unsigned sp = 0; sp < SPIN_CAP; ++sp) { asm volatile("" ::: "memory"); v = __builtin_amdgcn_raw_buffer_load_b128(rws, o, 0, 16); v2 = __builtin_amdgcn_raw_buffer_load_b128(rws, o + 16, 0, 16);
            if (__all(v[1] == epoch && v[3] == epoch && v2[1] == epoch && v2[3] == epoch)) break; }
        u32x4 val = {v[0], v[2], v2[0], v2[2]};
        const int vec = t >> 6, c8 = (t & 63) * 8;
        if (vec == 1) lds_st<u32x4>(L_SB16 + c8 * 2, val);
        else if (vec == 2) {
#pragma unroll
            for (int i = 0; i < 4; ++i) { const unsigned u_ = val[i]; val[i] = pk2(1.f + bf_lo(u_), 1.f + bf_hi(u_)); }
            lds_st<u32x4>(L_GB16 + c8 * 2, val);
        } else if ((c8 >> 6) == ct) lds_st<u32x4>((vec == 0 ? L_GAO : L_GBO) + (c8 & 63) * 2, val);
    }
    if (w == 0) poll_flags(ws, WS_FLAG + (1 * 256 + g * 16) * 256, 16, epoch, lane);
    BAR();
    STAMP(10); WS3(0);
    u32x2 x1p0, x1p1;
    {
        FRESH(lane);
        const int gi = lane >> 4, q4 = (lane & 15) >> 2, p4 = lane & 3;
        const unsigned tokP = 128 * g + 64 * rh + 16 * tt + (lane & 15), colP = 64 * ct + 32 * kh + 4 * gi;
        u32x4 of[8];
#pragma unroll
        for (int s = 0; s < 8; ++s) of[s] = __builtin_amdgcn_raw_buffer_load_b128(rws, WS_O + ((g * 8 + 4 * rh + tt) * 16 + 8 * kh + s) * 1024 + lane * 16, 0, 16);
        const u32x2 xres0 = *(const u32x2*)(x + tokP * 512 + colP), xres1 = *(const u32x2*)(x + tokP * 512 + colP + 16);
        SBAR();
        f32x4 acc[4];
#pragma unroll
        for (int n = 0; n < 4; ++n) acc[n] = (f32x4){0.f, 0.f, 0.f, 0.f};
        const unsigned xv = 2 * (gi & 1) + (q4 >> 1), rowa = L_WO + (256 * kh + 16 * (gi >> 1) + 4 * (gi & 1) + q4) * 128 + p4 * 8;
        unsigned an[4]; bf16x8 wf[4];
#pragma unroll
        for (int n = 0; n < 4; ++n) { an[n] = rowa + ((n ^ xv) * 32); wf[n] = trfrag(an[n], an[n] + 1024); }
        const unsigned cl = (32 * kh + 4 * gi) * 2, cg = colP * 2;
        const u32x2 ga0 = lds_ld<u32x2>(L_GAO + cl), ga1 = lds_ld<u32x2>(L_GAO + cl + 32);
        const u32x2 gb0 = lds_ld<u32x2>(L_GB16 + cg), gb1 = lds_ld<u32x2>(L_GB16 + cg + 32), sb0 = lds_ld<u32x2>(L_SB16 + cg), sb1 = lds_ld<u32x2>(L_SB16 + cg + 32);
        SBAR();
#pragma unroll
        for (int s = 0; s < 8; ++s)
#pragma unroll
            for (int n = 0; n < 4; ++n) { acc[n] = MFMA16(wf[n], __builtin_bit_cast(bf16x8, of[s]), acc[n]); PIN(acc[n]); SBAR(); if (s < 7) wf[n] = trfrag(an[n] + (s + 1) * 4096, an[n] + (s + 1) * 4096 + 1024); SBAR(); }
        WS3(1);
        BAR();
        STAMP(11);
        const unsigned ex = w * 2048 + lane * 16;
        { f32x4 s0 = acc[0], s1 = acc[1], s2 = acc[2], s3 = acc[3]; PIN(s0); PIN(s1); PIN(s2); PIN(s3);
          lds_st<f32x4>(ex, kh ? s0 : s2); lds_st<f32x4>(ex + 1024, kh ? s1 : s3); acc[0] = kh ? s2 : s0; acc[1] = kh ? s3 : s1; }
        BAR();
        WS3(2);
        const unsigned ep = (w ^ 4) * 2048 + lane * 16;
        const f32x4 a0 = lds_ld<f32x4>(ep) + acc[0], a1 = lds_ld<f32x4>(ep + 1024) + acc[1];
        float xv1[8], gg[8], ss[8];
#pragma unroll
        for (int r = 0; r < 4; ++r) {
            const unsigned ua0 = ga0[r >> 1], ua1 = ga1[r >> 1], ux0 = xres0[r >> 1], ux1 = xres1[r >> 1], ug0 = gb0[r >> 1], ug1 = gb1[r >> 1], us0 = sb0[r >> 1], us1 = sb1[r >> 1];
            xv1[r] = rbf(fmaf((r & 1) ? bf_hi(ua0) : bf_lo(ua0), a0[r], (r & 1) ? bf_hi(ux0) : bf_lo(ux0))); xv1[4 + r] = rbf(fmaf((r & 1) ? bf_hi(ua1) : bf_lo(ua1), a1[r], (r & 1) ? bf_hi(ux1) : bf_lo(ux1)));
            gg[r] = (r & 1) ? bf_hi(ug0) : bf_lo(ug0); gg[4 + r] = (r & 1) ? bf_hi(ug1) : bf_lo(ug1);
            ss[r] = (r & 1) ? bf_hi(us0) : bf_lo(us0); ss[4 + r] = (r & 1) ? bf_hi(us1) : bf_lo(us1);
        }
        x1p0 = (u32x2){pk2(xv1[0], xv1[1]), pk2(xv1[2], xv1[3])}; x1p1 = (u32x2){pk2(xv1[4], xv1[5]), pk2(xv1[6], xv1[7])};
        float st[6] = {0.f, 0.f, 0.f, 0.f, 0.f, 0.f}, pg[8];
#pragma unroll
        for (int r = 0; r < 8; ++r) pg[r] = xv1[r] * gg[r];
        const u32x4 xg = {pk2(pg[0], pg[1]), pk2(pg[2], pg[3]), pk2(pg[4], pg[5]), pk2(pg[6], pg[7])};
        __builtin_amdgcn_raw_buffer_store_b128(xg, rws, WS_XG + ((g * 8 + 4 * rh + tt) * 16 + 2 * ct + kh) * 1024 + lane * 16, 0, 16);
#pragma unroll
        for (int r = 0; r < 8; ++r) { const float p = pg[r]; st[0] += xv1[r]; st[1] = fmaf(xv1[r], xv1[r], st[1]); st[2] += p; st[3] = fmaf(p, p, st[3]); st[4] = fmaf(p, gg[r], st[4]); st[5] = fmaf(p, ss[r], st[5]); }
#pragma unroll
        for (int i = 0; i < 6; ++i) st[i] = xsum32(xsum16(st[i]));
        if (kh == 1 && lane < 16) { lds_st<f32x4>(L_SCR + (tt * 16 + lane) * 32, (f32x4){st[0], st[1], st[2], st[3]}); lds_st<u32x2>(L_SCR + (tt * 16 + lane) * 32 + 16, (u32x2){__float_as_uint(st[4]), __float_as_uint(st[5])}); }
        WS3(3);
        BAR();
        if (kh == 0 && lane < 16) {
            const f32x4 o4 = lds_ld<f32x4>(L_SCR + (tt * 16 + lane) * 32); const u32x2 o2 = lds_ld<u32x2>(L_SCR + (tt * 16 + lane) * 32 + 16);
            const unsigned so = WS_ST + g * 24576, T = 4 * rh + tt;
            const u32x4 sa4 = {__float_as_uint(st[0] + o4[0]), __float_as_uint(st[1] + o4[1]), __float_as_uint(st[2] + o4[2]), __float_as_uint(st[3] + o4[3])};
            const u32x2 sb2 = {__float_as_uint(st[4] + __uint_as_float(o2[0])), __float_as_uint(st[5] + __uint_as_float(o2[1]))};
            __builtin_amdgcn_raw_buffer_store_b128(sa4, rws, so + ((T * 8 + ct) * 16 + lane) * 16, 0, 16);
            __builtin_amdgcn_raw_buffer_store_b64(sb2, rws, so + 16384 + ((T * 8 + ct) * 16 + lane) * 8, 0, 16);
        }
    }
    WS3(4);
    VMWAIT(0); WS3(5);
    BAR();
    if (tid == 0) stg_sc1(ws, WS_FLAG + (2 * 256 + g * 16 + j) * 256, epoch);
    STAMP(12);
    const int odd = j & 1;
    {
    FRESH(lane);
    const int gi = lane >> 4, q4 = (lane & 15) >> 2, p4 = lane & 3;
    const u32x2 b1v = *(const u32x2*)(b1 + 112 * j + (w > 0 ? w - 1 : 0) * 16 + 4 * gi);
    BAR();
    if (w > 0) {
        const int nt = w - 1; f32x4 acc = {0.f, 0.f, 0.f, 0.f}, acc2 = acc;
        const unsigned ra = L_W1 + (4 * gi + q4) * 224 + p4 * 8 + nt * 32; const int tk = lane & 15;
        const unsigned va = tk == 0 ? L_GB16 + 8 * gi : tk == 1 ? L_SB16 + 8 * gi : tk == 2 ? L_ONES : L_ZERO2, vst = tk < 2 ? 64u : 0u, vhi = tk < 2 ? 32u : 0u;
        u32x2 blo[16], bhi[16];
        { unsigned a_ = va;
#pragma unroll
          for (int s = 0; s < 16; ++s) { blo[s] = lds_ld<u32x2>(a_); bhi[s] = lds_ld<u32x2>(a_ + vhi); a_ += vst; } }
        bf16x8 f0 = trfrag(ra, ra + 3584), f1 = trfrag(ra + 7168, ra + 7168 + 3584);
#pragma unroll
        for (int s = 0; s < 16; s += 2) {
            acc = MFMA16(f0, __builtin_bit_cast(bf16x8, ((u32x4){blo[s][0], blo[s][1], bhi[s][0], bhi[s][1]})), acc); PIN(acc); SBAR(); if (s + 2 < 16) f0 = trfrag(ra + (s + 2) * 7168, ra + (s + 2) * 7168 + 3584); SBAR();
            acc2 = MFMA16(f1, __builtin_bit_cast(bf16x8, ((u32x4){blo[s + 1][0], blo[s + 1][1], bhi[s + 1][0], bhi[s + 1][1]})), acc2); PIN(acc2); SBAR(); if (s + 3 < 16) f1 = trfrag(ra + (s + 3) * 7168, ra + (s + 3) * 7168 + 3584); SBAR();
        }
        acc += acc2;
        if (tk < 3) lds_st<f32x4>(L_UVW + (tk * 112 + nt * 16 + 4 * gi) * 4, acc);
        if (tk == 3) lds_st<f32x4>(L_UVW + (3 * 112 + nt * 16 + 4 * gi) * 4, (f32x4){bf_lo(b1v[0]), bf_hi(b1v[0]), bf_lo(b1v[1]), bf_hi(b1v[1])});
    } else {
        const u32x4 gq = lds_ld<u32x4>(L_GB16 + lane * 16), sq = lds_ld<u32x4>(L_SB16 + lane * 16);
        float c5[5] = {0.f, 0.f, 0.f, 0.f, 0.f};
#pragma unroll
        for (int i = 0; i < 4; ++i) { const unsigned ug = gq[i], us = sq[i]; const float g0 = bf_lo(ug), g1 = bf_hi(ug), s0 = bf_lo(us), s1 = bf_hi(us);
            c5[0] += g0 + g1; c5[1] += s0 + s1; c5[2] += g0 * g0 + g1 * g1; c5[3] += g0 * s0 + g1 * s1; c5[4] += s0 * s0 + s1 * s1; }
#pragma unroll
        for (int i = 0; i < 5; ++i) c5[i] = xsum32(xsum16(rowsum16(c5[i])));
        if (lane == 0) { lds_st<f32x4>(L_CONST, (f32x4){c5[0], c5[1], c5[2], c5[3]}); lds_st<float>(L_CONST + 16, c5[4]); }
        poll_flags(ws, WS_FLAG + (2 * 256 + g * 16) * 256, 16, epoch, lane);
    }
    }
    BAR();
    STAMP(13);
    u32x4 park[8];
    {
        FRESH(lane);
        const int gi = lane >> 4, q4 = (lane & 15) >> 2, p4 = lane & 3;
        const int Tw = 2 * m + kh;
        u32x4 xf[2][8];
        const unsigned xo0 = WS_XG + (g * 8 + Tw) * 16384 + kh * 8192 + lane * 16, xo1 = WS_XG + (g * 8 + (Tw ^ 1)) * 16384 + kh * 8192 + lane * 16;
        constexpr int WIN = 5;
#define XLOAD(s) { xf[0][s] = __builtin_amdgcn_raw_buffer_load_b128(rws, xo0 + (s) * 1024, 0, 16); xf[1][s] = __builtin_amdgcn_raw_buffer_load_b128(rws, xo1 + (s) * 1024, 0, 16); }
        XLOAD(0) SBAR(); if (kh == 0) __builtin_amdgcn_s_sleep(3); XLOAD(1) SBAR(); if (kh == 0) __builtin_amdgcn_s_sleep(3); XLOAD(2) XLOAD(3) XLOAD(4)
        const unsigned so = WS_ST + g * 24576 + Tw * 2048, sob = WS_ST + g * 24576 + 16384 + Tw * 1024;
        u32x4 sA0, sA1; u32x2 sB0, sB1;
        SBAR(); WS4(0);
        f32x4 acc[7], acb[7]; bf16x8 wf[7];
        const unsigned ra = L_W1 + (256 * kh + 4 * gi + q4) * 224 + p4 * 8, raA = ra + 32 * odd, raB = ra + (odd ? 0 : 192);
#define W1A(s, u) (((u) < 6 ? raA : raB) + (s) * 7168 + ((u) < 6 ? (u) * 32 : 0))
#pragma unroll
        for (int u = 0; u < 7; ++u) { acc[u] = (f32x4){0.f, 0.f, 0.f, 0.f}; acb[u] = acc[u]; wf[u] = trfrag(W1A(0, u), W1A(0, u) + 3584); }
        SBAR();
#pragma unroll
        for (int s = 0; s < 8; ++s) {
            if (s + WIN < 8) XLOAD(s + WIN)
            if (s == 4) { sA0 = __builtin_amdgcn_raw_buffer_load_b128(rws, so + ((2 * gi) * 16 + (lane & 15)) * 16, 0, 16); sA1 = __builtin_amdgcn_raw_buffer_load_b128(rws, so + ((2 * gi + 1) * 16 + (lane & 15)) * 16, 0, 16); }
            if (s == 5) { sB0 = __builtin_amdgcn_raw_buffer_load_b64(rws, sob + ((2 * gi) * 16 + (lane & 15)) * 8, 0, 16); sB1 = __builtin_amdgcn_raw_buffer_load_b64(rws, sob + ((2 * gi + 1) * 16 + (lane & 15)) * 8, 0, 16); }
            SBAR();
#pragma unroll
            for (int u = 0; u < 7; ++u) { acc[u] = MFMA16(wf[u], __builtin_bit_cast(bf16x8, xf[0][s]), acc[u]); PIN(acc[u]); SBAR();
                acb[u] = MFMA16(wf[u], __builtin_bit_cast(bf16x8, xf[1][s]), acb[u]); PIN(acb[u]); SBAR();
                if (s < 7) wf[u] = trfrag(W1A(s + 1, u), W1A(s + 1, u) + 3584); SBAR(); }
            if (s == 7) WS4(1);
        }
#undef W1A
#undef XLOAD
        float ca, cb, cc, r2;
        { float S[6];
#pragma unroll
          for (int i = 0; i < 4; ++i) S[i] = xsum32(xsum16(__uint_as_float(sA0[i]) + __uint_as_float(sA1[i])));
#pragma unroll
          for (int i = 0; i < 2; ++i) S[4 + i] = xsum32(xsum16(__uint_as_float(sB0[i]) + __uint_as_float(sB1[i])));
          const f32x4 cc4 = lds_ld<f32x4>(L_CONST); const float SB2 = lds_ld<float>(L_CONST + 16);
          const float iD = 1.f / 512.f, m1 = S[0] * iD, r1 = __builtin_amdgcn_rsqf(fmaxf(S[1] * iD - m1 * m1, 0.f) + 1e-5f);
          const float m2 = (r1 * (S[2] - m1 * cc4[0]) + cc4[1]) * iD;
          const float ez2 = (r1 * r1 * (S[3] - 2.f * m1 * S[4] + m1 * m1 * cc4[2]) + 2.f * r1 * (S[5] - m1 * cc4[3]) + SB2) * iD;
          r2 = __builtin_amdgcn_rsqf(fmaxf(ez2 - m2 * m2, 0.f) + 1e-5f);
          ca = r1 * r2; cb = -ca * m1; cc = -r2 * m2; PIN(ca); PIN(cb); PIN(cc); PIN(r2); }
        const unsigned uo = L_UVW + 16 * gi + 64 * odd, uo6 = L_UVW + 16 * gi + (odd ? 0 : 384);
        f32x4 uu = lds_ld<f32x4>(uo), vv = lds_ld<f32x4>(uo + 448), ww = lds_ld<f32x4>(uo + 896), bb = lds_ld<f32x4>(uo + 1344);
        f32x4 u2 = lds_ld<f32x4>(uo + 64), v2 = lds_ld<f32x4>(uo + 64 + 448), w2v = lds_ld<f32x4>(uo + 64 + 896), b2v = lds_ld<f32x4>(uo + 64 + 1344);
        BAR();
        WS4(2);
        { const unsigned ex = 101376 + w * 7168 + lane * 16;
#pragma unroll
          for (int u = 0; u < 7; ++u) lds_st<f32x4>(ex + u * 1024, acb[u]); }
        WS4(3);
        BAR();
        WS4(4);
        { const unsigned ep = 101376 + (w ^ 4) * 7168 + lane * 16;
#pragma unroll
          for (int u = 0; u < 7; ++u) acc[u] += lds_ld<f32x4>(ep + u * 1024); }
#pragma unroll
        for (int u = 0; u < 7; ++u) PIN(acc[u]);
        BAR();
        STAMP(14);
        const unsigned hb = WS_H + (g * 8 + Tw) * 57344, s0 = (7 * j + odd) >> 1;
        const unsigned psw = (lane >> 3) * 1024u + ct * 128u + (((lane & 7) >> 1) ^ ((lane >> 4) & 3)) * 32u + (lane & 1) * 16u;
        const char* pp0 = (const char*)w2 + (size_t)(56 * (w >> 2) + 40 + (w & 3)) * 8192, * pp1 = pp0 + (size_t)112 * 8192;
#define PARK(i) { park[2 * (i)] = *(const u32x4*)(pp0 + (i) * 32768 + psw); park[2 * (i) + 1] = *(const u32x4*)(pp1 + (i) * 32768 + psw); }
        const f32x2 kA = {-0.044715f * 2.302208198f, -0.044715f * 2.302208198f}, kB = {-2.302208198f, -2.302208198f}, one2 = {1.f, 1.f};
#define LO2(v) ((f32x2){(v)[0], (v)[1]})
#define HI2(v) ((f32x2){(v)[2], (v)[3]})
#define PRE2(a_, u_, v_, w_, b_) (ca * (a_) + (cb * (u_) + (r2 * (v_) + (cc * (w_) + (b_)))))
#define GELU(N, p, y) { f32x2 z_[N], e_[N]; \
            _Pragma("unroll") for (int r = 0; r < N; ++r) { z_[r] = p[r] * p[r]; PIN(z_[r]); } \
            _Pragma("unroll") for (int r = 0; r < N; ++r) { z_[r] = z_[r] * kA + kB; PIN(z_[r]); } \
            _Pragma("unroll") for (int r = 0; r < N; ++r) { z_[r] = z_[r] * p[r]; PIN(z_[r]); } \
            SBAR(); _Pragma("unroll") for (int r = 0; r < N; ++r) { e_[r] = (f32x2){__builtin_amdgcn_exp2f(z_[r][0]), __builtin_amdgcn_exp2f(z_[r][1])}; PIN(e_[r]); } \
            SBAR(); _Pragma("unroll") for (int r = 0; r < N; ++r) { e_[r] = e_[r] + one2; PIN(e_[r]); } \
            SBAR(); _Pragma("unroll") for (int r = 0; r < N; ++r) { e_[r] = (f32x2){__builtin_amdgcn_rcpf(e_[r][0]), __builtin_amdgcn_rcpf(e_[r][1])}; PIN(e_[r]); } \
            SBAR(); _Pragma("unroll") for (int r = 0; r < N; ++r) y[r] = p[r] * e_[r]; }
#pragma unroll
        for (int u = 0; u < 6; u += 2) {
            f32x2 pre[4], y[4];
            pre[0] = PRE2(LO2(acc[u]), LO2(uu), LO2(vv), LO2(ww), LO2(bb)); pre[1] = PRE2(HI2(acc[u]), HI2(uu), HI2(vv), HI2(ww), HI2(bb));
            pre[2] = PRE2(LO2(acc[u + 1]), LO2(u2), LO2(v2), LO2(w2v), LO2(b2v)); pre[3] = PRE2(HI2(acc[u + 1]), HI2(u2), HI2(v2), HI2(w2v), HI2(b2v));
            PIN(pre[0]); PIN(pre[1]); PIN(pre[2]); PIN(pre[3]);
            SBAR();
            { const unsigned o = u < 4 ? uo + (u + 2) * 64 : uo6; uu = lds_ld<f32x4>(o); vv = lds_ld<f32x4>(o + 448); ww = lds_ld<f32x4>(o + 896); bb = lds_ld<f32x4>(o + 1344); }
            if (u < 4) { const unsigned o = uo + (u + 3) * 64; u2 = lds_ld<f32x4>(o); v2 = lds_ld<f32x4>(o + 448); w2v = lds_ld<f32x4>(o + 896); b2v = lds_ld<f32x4>(o + 1344); }
            PARK(u >> 1) SBAR();
            GELU(4, pre, y)
            __builtin_amdgcn_raw_buffer_store_b128((u32x4){pk2(y[0][0], y[0][1]), pk2(y[1][0], y[1][1]), pk2(y[2][0], y[2][1]), pk2(y[3][0], y[3][1])}, rws, hb + (s0 + (u >> 1)) * 1024 + lane * 16, 0, 16);
            SBAR();
        }
        {   f32x2 pre[2], y[2];
            pre[0] = PRE2(LO2(acc[6]), LO2(uu), LO2(vv), LO2(ww), LO2(bb)); pre[1] = PRE2(HI2(acc[6]), HI2(uu), HI2(vv), HI2(ww), HI2(bb));
            PARK(3) SBAR();
            GELU(2, pre, y)
            __builtin_amdgcn_raw_buffer_store_b64((u32x2){pk2(y[0][0], y[0][1]), pk2(y[1][0], y[1][1])}, rws, hb + ((7 * j + (odd ? 0 : 6)) >> 1) * 1024 + odd * 512 + lane * 8, 0, 16);
        }
#undef GELU
#undef PARK
#undef PRE2
#undef LO2
#undef HI2
        WS4(5);
    }
    u32x2 gbo0, gbo1;
    { FRESH(lane); const unsigned cl = (32 * kh + 4 * (lane >> 4)) * 2; gbo0 = lds_ld<u32x2>(L_GBO + cl); gbo1 = lds_ld<u32x2>(L_GBO + cl + 32); PIN(gbo0); PIN(gbo1); }
    STAMP_OUT();
    VMWAIT(0);
    BAR();
    if (tid == 0) stg_sc1(ws, WS_FLAG + (3 * 256 + g * 16 + j) * 256, epoch);
    STAMPG(15);
    if (w == 0) { FRESH(lane); poll_flags(ws, WS_FLAG + (3 * 256 + g * 16) * 256, 16, epoch, lane); }
    SBAR();
    BAR();
    STAMPG(16); WS5(0);
    {
        constexpr int WIN = P5WIN;
        FRESH(lane);
        const int gi = lane >> 4, q4 = (lane & 15) >> 2, p4 = lane & 3;
        const unsigned tokP = 128 * g + 64 * rh + 16 * tt + (lane & 15), colP = 64 * ct + 32 * kh + 4 * gi;
        const int kq = (w & 1) | (kh << 1);
        const unsigned hb0 = WS_H + (g * 8 + 4 * rh + tt) * 57344 + kq * 14 * 1024, hb1 = WS_H + (g * 8 + 4 * rh + (tt ^ 1)) * 57344 + kq * 14 * 1024;
        u32x4 hf[2][14];
#define HLD1(hb_, dst, s) { if ((s) % 7 == 3) { const u32x2 lo = __builtin_amdgcn_raw_buffer_load_b64(rws, hb_ + (s) * 1024 + lane * 8, 0, 16), hi = __builtin_amdgcn_raw_buffer_load_b64(rws, hb_ + (s) * 1024 + 512 + lane * 8, 0, 16); dst = (u32x4){lo[0], lo[1], hi[0], hi[1]}; } \
                    else dst = __builtin_amdgcn_raw_buffer_load_b128(rws, hb_ + (s) * 1024 + lane * 16, 0, 16); }
#define HLOAD(s) { HLD1(hb0, hf[0][s], s) HLD1(hb1, hf[1][s], s) }
        HLOAD(0) SBAR(); if (kh == 0) __builtin_amdgcn_s_sleep(3); HLOAD(1) SBAR(); if (kh == 0) __builtin_amdgcn_s_sleep(3);
#pragma unroll
        for (int s = 2; s < WIN; ++s) HLOAD(s)
        SBAR();
        f32x4 acc[2][4]; bf16x8 wf[4];
        const unsigned xv = (2 * gi + (q4 >> 1)) & 3, rowa = kq * 4096 + (4 * gi + q4) * 128 + p4 * 8;
        unsigned an[4];
#pragma unroll
        for (int n = 0; n < 4; ++n) { an[n] = rowa + (((n ^ (2 * kh)) ^ xv) * 32); acc[0][n] = (f32x4){0.f, 0.f, 0.f, 0.f}; acc[1][n] = acc[0][n]; }
#define W2A(s, n) (an[n] + (((s) % 10) & 3) * 16384)
#pragma unroll
        for (int n = 0; n < 4; ++n) wf[n] = trfrag(W2A(0, n), W2A(0, n) + 2048);
        const u32x2 g0 = gbo0, g1 = gbo1;
        u32x2 bb0, bb1;
        SBAR();
#pragma unroll
        for (int s = 0; s < 14; ++s) {
            if (s == 11) { bb0 = *(const u32x2*)(b2 + colP); bb1 = *(const u32x2*)(b2 + colP + 16); }
            if (s == 1) WS5(1); if (s == 4) WS5(2); if (s == 8) WS5(3);
            if (s == 5) RBAR();
            if (s == 8) BAR();
            if (s == 8) WS5(4);
            if (s + WIN < 14) HLOAD(s + WIN)
            SBAR();
#pragma unroll
            for (int n = 0; n < 4; ++n) { acc[0][n] = MFMA16(wf[n], __builtin_bit_cast(bf16x8, hf[0][s]), acc[0][n]); PIN(acc[0][n]); SBAR();
                acc[1][n] = MFMA16(wf[n], __builtin_bit_cast(bf16x8, hf[1][s]), acc[1][n]); PIN(acc[1][n]); SBAR();
                if (s >= 5 && s <= 6) { const int i = 4 * (s - 5) + n; lds_st<u32x4>((i >> 1) * 16384 + (w + 8 * (i & 1)) * 1024 + lane * 16, park[i]); }
                if (s == 3 || s == 7) { an[n] += 65536; PIN(an[n]); } if (s == 9) { an[n] -= 131072; PIN(an[n]); }
                if (s < 13) wf[n] = trfrag(W2A(s + 1, n), W2A(s + 1, n) + 2048); SBAR(); }
        }
#undef W2A
#undef HLOAD
#undef HLD1
        WS5(5);
        BAR();
        STAMPG(17);
        { const unsigned ex = w * 6144 + lane * 16;
          lds_st<f32x4>(ex, acc[0][2]); lds_st<f32x4>(ex + 1024, acc[0][3]); lds_st<f32x4>(ex + 2048, acc[1][0]); lds_st<f32x4>(ex + 3072, acc[1][1]); lds_st<f32x4>(ex + 4096, acc[1][2]); lds_st<f32x4>(ex + 5120, acc[1][3]); }
        BAR();
        const unsigned e4 = (w ^ 4) * 6144 + lane * 16, e1 = (w ^ 1) * 6144 + 2048 + lane * 16, e5 = (w ^ 5) * 6144 + 4096 + lane * 16;
        const f32x4 a0 = (acc[0][0] + lds_ld<f32x4>(e4)) + (lds_ld<f32x4>(e1) + lds_ld<f32x4>(e5)), a1 = (acc[0][1] + lds_ld<f32x4>(e4 + 1024)) + (lds_ld<f32x4>(e1 + 1024) + lds_ld<f32x4>(e5 + 1024));
        float y0[4], y1[4];
#pragma unroll
        for (int r = 0; r < 4; ++r) {
            const unsigned ug0 = g0[r >> 1], ug1 = g1[r >> 1], ub0 = bb0[r >> 1], ub1 = bb1[r >> 1], ux0 = x1p0[r >> 1], ux1 = x1p1[r >> 1];
            y0[r] = fmaf((r & 1) ? bf_hi(ug0) : bf_lo(ug0), a0[r] + ((r & 1) ? bf_hi(ub0) : bf_lo(ub0)), (r & 1) ? bf_hi(ux0) : bf_lo(ux0));
            y1[r] = fmaf((r & 1) ? bf_hi(ug1) : bf_lo(ug1), a1[r] + ((r & 1) ? bf_hi(ub1) : bf_lo(ub1)), (r & 1) ? bf_hi(ux1) : bf_lo(ux1));
        }
        *(u32x2*)(out + tokP * 512 + colP) = (u32x2){pk2(y0[0], y0[1]), pk2(y0[2], y0[3])};
        *(u32x2*)(out + tokP * 512 + colP + 16) = (u32x2){pk2(y1[0], y1[1]), pk2(y1[2], y1[3])};
    }
    if (b == 0 && w == 0) {
        for (unsigned sp = 0; sp < SPIN_CAP; ++sp) { asm volatile("" ::: "memory"); const u32x4 v = __builtin_amdgcn_raw_buffer_load_b128(rws, WS_HELLO + lane * 16, 0, 16); if (__all(v[0] == epoch && v[1] == epoch && v[2] == epoch && v[3] == epoch)) break; }
        if (lane == 0) stg_sc1(ws, WS_E, epoch);
    }
    STAMPG(18); STAMPGC(21);
    if (b == 135 && w == 0) { unsigned long long dl = 2 * (e_t1 - e_t0); if (dl > 20000) dl = 20000; const unsigned long long t_end = rt() + dl; while (rt() < t_end) __builtin_amdgcn_s_sleep(1); }
    asm volatile("fb_code_end:");
}

extern "C" void kernel_launch(void* const* d_in, const int* in_sizes, int n_in, void* d_out, int out_size, void* d_ws, size_t ws_size, hipStream_t stream) {
    (void)hipFuncSetAttribute((const void*)fused_block, hipFuncAttributeMaxDynamicSharedMemorySize, LDS_BYTES);
    fused_block<<<256, 576, LDS_BYTES, stream>>>((const u16*)d_in[0], (const u16*)d_in[1], (const u16*)d_in[2], (const u16*)d_in[4], (const u16*)d_in[5], (const u16*)d_in[6], (const u16*)d_in[7],
                                                 (const u16*)d_in[8], (const u16*)d_in[9], (u16*)d_out, (char*)d_ws);
}
```

```cpp
#include <hip/hip_runtime.h>
#include <hip/hip_bf16.h>
#include <stdint.h>

typedef unsigned short u16;
typedef short bf16x8 __attribute__((ext_vector_type(8)));
typedef short s16x4 __attribute__((ext_vector_type(4)));
typedef float f32x4 __attribute__((ext_vector_type(4)));
typedef float f32x2 __attribute__((ext_vector_type(2)));
typedef float f32x16 __attribute__((ext_vector_type(16)));
typedef unsigned u32x4 __attribute__((ext_vector_type(4)));
typedef unsigned u32x2 __attribute__((ext_vector_type(2)));
typedef __bf16 bf2 __attribute__((ext_vector_type(2)));
#define LAS __attribute__((address_space(3)))
#define GAS __attribute__((address_space(1)))
#define DI __device__ __forceinline__

#define TS 0

constexpr unsigned WS_E = 0, WS_HELLO = 4096, WS_FLAG = 8192;
constexpr unsigned WS_MODU = 0x50000, WS_MODN = 0x60000;
constexpr unsigned WS_PART = 0x80000;
constexpr unsigned WS_KV = 0x100000, WS_O = 0x500000, WS_XG = 0x700000, WS_ST = 0x900000, WS_H = 0xA00000, WS_TS = 0x2000000;
constexpr unsigned WS_BYTES = 0x2100000;
constexpr unsigned L_X = 0, L_WQKV = 57344, L_RED = 155648, L_SAG = 156672, L_STAT = 160768;
constexpr unsigned L_QS = 65536, L_KIMG = 98304, L_VIMG = 122880, L_WO = 0, L_W1 = 44032;
constexpr unsigned L_GB16 = 158720, L_SB16 = 159744, L_UVW = 160768, L_CONST = 162560, L_GAO = 162592, L_GBO = 162720, L_ONES = 162848, L_DUMMY = 162880;
constexpr unsigned L_TAGM = L_RED + 512, L_TAGL = L_RED + 544, L_TAGC = L_RED + 560;
constexpr unsigned L_RED2 = 81920;
constexpr unsigned L_SCR = 32768;
constexpr unsigned LDS_BYTES = 163840;
constexpr unsigned SPIN_CAP = 1u << 16;

DI float bf_lo(unsigned u) { return __uint_as_float(u << 16); }
DI float bf_hi(unsigned u) { return __uint_as_float(u & 0xffff0000u); }
DI unsigned pk2(float a, float b) { bf2 v = {(__bf16)a, (__bf16)b}; return __builtin_bit_cast(unsigned, v); }
DI float rbf(float a) { return bf_lo(pk2(a, a)); }
DI void dma16(unsigned voff, const void* sbase, unsigned lds) { asm volatile("s_mov_b32 m0, %1\n\ts_nop 0\n\tglobal_load_lds_dwordx4 %0, %2" ::"v"(voff), "s"(lds), "s"(sbase) : "memory"); }
DI void dma16c(unsigned voff, const void* sbase, unsigned lds) { asm volatile("s_mov_b32 m0, %1\n\ts_nop 0\n\tglobal_load_lds_dwordx4 %0, %2 sc1" ::"v"(voff), "s"(lds), "s"(sbase) : "memory"); }
DI void dma4(unsigned v0, unsigned v1, unsigned v2, unsigned v3, const void* sbase, unsigned lds) {
    asm volatile("s_mov_b32 m0, %5\n\ts_nop 0\n\tglobal_load_lds_dwordx4 %0, %4\n\tglobal_load_lds_dwordx4 %1, %4 offset:1024\n\tglobal_load_lds_dwordx4 %2, %4 offset:2048\n\tglobal_load_lds_dwordx4 %3, %4 offset:3072"
                 ::"v"(v0), "v"(v1), "v"(v2), "v"(v3), "s"(sbase), "s"(lds) : "memory"); }
DI void dma3(unsigned v0, unsigned v1, unsigned v2, const void* sbase, unsigned lds) {
    asm volatile("s_mov_b32 m0, %4\n\ts_nop 0\n\tglobal_load_lds_dwordx4 %0, %3\n\tglobal_load_lds_dwordx4 %1, %3 offset:1024\n\tglobal_load_lds_dwordx4 %2, %3 offset:2048"
                 ::"v"(v0), "v"(v1), "v"(v2), "s"(sbase), "s"(lds) : "memory"); }
DI void dma4lin_c(unsigned v, const void* sbase, unsigned lds) {
    asm volatile("s_mov_b32 m0, %2\n\ts_nop 0\n\tglobal_load_lds_dwordx4 %0, %1 sc1\n\tglobal_load_lds_dwordx4 %0, %1 offset:1024 sc1\n\tglobal_load_lds_dwordx4 %0, %1 offset:2048 sc1\n\tglobal_load_lds_dwordx4 %0, %1 offset:3072 sc1"
                 ::"v"(v), "s"(sbase), "s"(lds) : "memory"); }
DI void lds_wait_tags(unsigned a, int n, unsigned tag) {
    for (unsigned sp = 0; sp < SPIN_CAP; ++sp) { asm volatile("" ::: "memory"); const unsigned t0 = *(const volatile LAS unsigned*)(size_t)a, t1 = *(const volatile LAS unsigned*)(size_t)(a + 4 * (n - 1));
        if (__builtin_amdgcn_readfirstlane(t0) == tag && __builtin_amdgcn_readfirstlane(t1) == tag) break; __builtin_amdgcn_s_sleep(1); }
    asm volatile("" ::: "memory"); }
DI void warm4(unsigned voff, const void* sbase) { asm volatile("s_mov_b32 m0, %1\n\ts_nop 0\n\tglobal_load_lds_dword %0, %2" ::"v"(voff), "s"(L_DUMMY), "s"(sbase) : "memory"); }
#define WSP 4
#define WS1(i) do { if (WSP == 1) WSTAMP(i); } while (0)
#define WS2(i) do { if (WSP == 2) WSTAMP(i); } while (0)
#define WS3(i) do { if (WSP == 3) WSTAMP(i); } while (0)
#define WS4(i) do { if (WSP == 4) WSTAMP(i); } while (0)
#define WS5(i) do { if (WSP == 5) WSTAMP(i); } while (0)
#define WS6(i) do { if (WSP == 6) WSTAMP(i); } while (0)
#define FRESH(v) int v = threadIdx.x & 63; asm volatile("" : "+v"(v))
#define SBAR() __builtin_amdgcn_sched_barrier(0)
#define PIN(x) asm volatile("" : "+v"(x))
#define BAR() asm volatile("s_waitcnt lgkmcnt(0)\n\ts_barrier" ::: "memory")
#define VMWAIT(n) asm volatile("s_waitcnt vmcnt(" #n ")" ::: "memory")
DI unsigned ldg_sc1(const char* ws, unsigned off) { return __hip_atomic_load((GAS unsigned*)(ws + off), __ATOMIC_RELAXED, __HIP_MEMORY_SCOPE_AGENT); }
DI unsigned long long ldg64_sc1(const char* ws, unsigned off) { return __hip_atomic_load((GAS unsigned long long*)(ws + off), __ATOMIC_RELAXED, __HIP_MEMORY_SCOPE_AGENT); }
DI void stg_sc1(char* ws, unsigned off, unsigned v) { __hip_atomic_store((GAS unsigned*)(ws + off), v, __ATOMIC_RELAXED, __HIP_MEMORY_SCOPE_AGENT); }
DI void stg64_sc1(char* ws, unsigned off, unsigned long long v) { __hip_atomic_store((GAS unsigned long long*)(ws + off), v, __ATOMIC_RELAXED, __HIP_MEMORY_SCOPE_AGENT); }
template <class T> DI T lds_ld(unsigned a) { return *(const LAS T*)(size_t)a; }
template <class T> DI void lds_st(unsigned a, T v) { *(LAS T*)(size_t)a = v; }
DI s16x4 lds_tr(unsigned a) { return __builtin_amdgcn_ds_read_tr16_b64_v4i16((LAS s16x4*)(size_t)a); }
DI bf16x8 trfrag(unsigned a0, unsigned a1) { s16x4 lo = lds_tr(a0), hi = lds_tr(a1); return __builtin_shufflevector(lo, hi, 0, 1, 2, 3, 4, 5, 6, 7); }
#define MFMA32(a, b, c) __builtin_amdgcn_mfma_f32_32x32x16_bf16((a), (b), (c), 0, 0, 0)
#define MFMA16(a, b, c) __builtin_amdgcn_mfma_f32_16x16x32_bf16((a), (b), (c), 0, 0, 0)
template <int N> DI float bcast(float v) { return __builtin_bit_cast(float, __builtin_amdgcn_mov_dpp(__builtin_bit_cast(int, v), 0x150 + N, 0xf, 0xf, true)); }
template <int N> DI float modu(float t, float gv, float sv) {
#pragma clang fp contract(off)
    const float p = bcast<N>(gv) * t; return bcast<N>(sv) + p; }
template <int N> DI unsigned hdw(unsigned x, float r, float n, float gv, float c, float d) {
    unsigned o; float a, b2;
    asm("v_lshlrev_b32 %1, 16, %5\n\tv_and_b32 %2, 0xffff0000, %5\n\tv_fma_f32 %1, %1, %6, %7\n\tv_fma_f32 %2, %2, %6, %7\n\t"
        "v_fmac_f32_dpp %3, %8, %1 row_newbcast:%c9 row_mask:0xf bank_mask:0xf\n\tv_fmac_f32_dpp %4, %8, %2 row_newbcast:%c10 row_mask:0xf bank_mask:0xf\n\t"
        "v_cvt_pk_bf16_f32 %0, %3, %4"
        : "=v"(o), "=&v"(a), "=&v"(b2), "+v"(c), "+v"(d) : "v"(x), "v"(r), "v"(n), "v"(gv), "i"(N), "i"(N + 1));
    return o; }
template <int C> DI float dppf(float v) { return __builtin_bit_cast(float, __builtin_amdgcn_update_dpp(0, __builtin_bit_cast(int, v), C, 0xf, 0xf, true)); }
DI float rowsum16(float v) { v += dppf<0xB1>(v); v += dppf<0x4E>(v); v += dppf<0x141>(v); v += dppf<0x140>(v); return v; }
DI float xsum32(float v) { auto r = __builtin_amdgcn_permlane32_swap(__float_as_uint(v), __float_as_uint(v), false, false); return __uint_as_float(r[0]) + __uint_as_float(r[1]); }
DI float xmax32(float v) { auto r = __builtin_amdgcn_permlane32_swap(__float_as_uint(v), __float_as_uint(v), false, false); return fmaxf(__uint_as_float(r[0]), __uint_as_float(r[1])); }
DI float xsum16(float v) { auto r = __builtin_amdgcn_permlane16_swap(__float_as_uint(v), __float_as_uint(v), false, false); return __uint_as_float(r[0]) + __uint_as_float(r[1]); }
DI unsigned long long mt() { unsigned long long t; asm volatile("s_memtime %0\n\ts_waitcnt lgkmcnt(0)" : "=s"(t)::"memory"); return t; }
DI unsigned long long rt() { unsigned long long t; asm volatile("s_memrealtime %0\n\ts_waitcnt lgkmcnt(0)" : "=s"(t)::"memory"); return t; }
#define STAMP(i) do {} while (0)
#define STAMP_OUT() do {} while (0)
#define STAMPC(i) do {} while (0)
#define WSTAMP(i) do {} while (0)

DI void poll_flags(const char* ws, unsigned off, int n, unsigned epoch, int lane) {
    const unsigned o = off + (unsigned)(lane < n ? lane : 0) * 256u;
    for (unsigned sp = 0; sp < SPIN_CAP; ++sp) { unsigned f = ldg_sc1(ws, o); if (__all(f == epoch)) break; }
}

DI void mod_part(const u16* __restrict__ cvec, const u16* __restrict__ wmod, char* ws, unsigned epoch, int lane, int slice, int kp) {
    const unsigned row0 = 64 * kp + 4 * (lane >> 2), o = row0 * 6144u + (unsigned)slice * 64u + (lane & 3) * 16u;
    const u32x2 cv = *(const u32x2*)(cvec + row0);
    u32x4 wm[4];
#pragma unroll
    for (int i = 0; i < 4; ++i) wm[i] = *(const u32x4*)((const char*)wmod + o + i * 6144u);
    SBAR();
    unsigned sp[2];
#pragma unroll
    for (int r = 0; r < 2; ++r) { const unsigned cu = cv[r]; const float c0 = bf_lo(cu), c1 = bf_hi(cu);
        sp[r] = pk2(c0 * __builtin_amdgcn_rcpf(1.f + __builtin_amdgcn_exp2f(-1.44269504089f * c0)), c1 * __builtin_amdgcn_rcpf(1.f + __builtin_amdgcn_exp2f(-1.44269504089f * c1))); }
    float a[8] = {0.f, 0.f, 0.f, 0.f, 0.f, 0.f, 0.f, 0.f};
#pragma unroll
    for (int r = 0; r < 2; ++r)
#pragma unroll
        for (int i = 0; i < 4; ++i) { const unsigned u0 = wm[2 * r][i], u1 = wm[2 * r + 1][i], s_ = sp[r];
            const unsigned lo = __builtin_amdgcn_perm(u1, u0, 0x05040100u), hi = __builtin_amdgcn_perm(u1, u0, 0x07060302u);
            a[2 * i] = __builtin_amdgcn_fdot2_f32_bf16(__builtin_bit_cast(bf2, lo), __builtin_bit_cast(bf2, s_), a[2 * i], false);
            a[2 * i + 1] = __builtin_amdgcn_fdot2_f32_bf16(__builtin_bit_cast(bf2, hi), __builtin_bit_cast(bf2, s_), a[2 * i + 1], false); }
    float b4[4], b2[2];
#pragma unroll
    for (int i = 0; i < 4; ++i) { auto r = __builtin_amdgcn_permlane32_swap(__float_as_uint(a[i]), __float_as_uint(a[i + 4]), false, false); b4[i] = __uint_as_float(r[0]) + __uint_as_float(r[1]); }
#pragma unroll
    for (int i = 0; i < 2; ++i) { auto r = __builtin_amdgcn_permlane16_swap(__float_as_uint(b4[i]), __float_as_uint(b4[i + 2]), false, false); float v = __uint_as_float(r[0]) + __uint_as_float(r[1]);
        v += dppf<0x124>(v); v += dppf<0x128>(v); b2[i] = v; }
    const __amdgpu_buffer_rsrc_t rws = __builtin_amdgcn_make_buffer_rsrc(ws, 0, WS_BYTES, 0x00020000);
    if ((lane & 12) == 0) __builtin_amdgcn_raw_buffer_store_b128((u32x4){__float_as_uint(b2[0]), epoch, __float_as_uint(b2[1]), epoch}, rws,
                                                                 WS_PART + (slice * 8 + kp) * 256 + ((lane & 3) * 4 + (lane >> 5) * 2 + ((lane >> 4) & 1)) * 16, 0, 16);
}
struct PartSum { u32x4 v0, v1; };
DI void mod_sum_load(PartSum& p, char* ws, int lane, int slice) {
    const __amdgpu_buffer_rsrc_t rws = __builtin_amdgcn_make_buffer_rsrc(ws, 0, WS_BYTES, 0x00020000);
    const unsigned o = WS_PART + (slice * 8 + (lane >> 3)) * 256 + (lane & 7) * 16;
    p.v0 = __builtin_amdgcn_raw_buffer_load_b128(rws, o, 0, 16); p.v1 = __builtin_amdgcn_raw_buffer_load_b128(rws, o + 128, 0, 16); }
DI void mod_sum_finish(PartSum& p, char* ws, unsigned epoch, int lane, int slice) {
    for (unsigned sp = 0; sp < SPIN_CAP; ++sp) { if (__all(p.v0[1] == epoch && p.v0[3] == epoch && p.v1[1] == epoch && p.v1[3] == epoch)) break; asm volatile("" ::: "memory"); mod_sum_load(p, ws, lane, slice); }
    float s[4] = {__uint_as_float(p.v0[0]), __uint_as_float(p.v0[2]), __uint_as_float(p.v1[0]), __uint_as_float(p.v1[2])};
#pragma unroll
    for (int i = 0; i < 4; ++i) { float v = s[i]; v += dppf<0x128>(v); s[i] = xsum32(xsum16(v)); }
    const unsigned long long g0 = ((unsigned long long)epoch << 32) | pk2(s[0], s[1]), g1 = ((unsigned long long)epoch << 32) | pk2(s[2], s[3]);
    const unsigned base = slice < 32 ? WS_MODU + slice * 128 : WS_MODN + (slice - 32) * 128, rs = slice < 32 ? 4096 : 8192, rep = lane >> 3, o = base + (lane & 7) * 8;
    stg64_sc1(ws, o + rep * rs, g0); stg64_sc1(ws, o + 64 + rep * rs, g1); stg64_sc1(ws, o + (rep + 8) * rs, g0); stg64_sc1(ws, o + 64 + (rep + 8) * rs, g1);
}

__global__ __launch_bounds__(576, 3) void fused_block(const u16* __restrict__ x, const u16* __restrict__ wqkv, const u16* __restrict__ wo, const u16* __restrict__ w1, const u16* __restrict__ b1,
                                                      const u16* __restrict__ w2, const u16* __restrict__ b2, const u16* __restrict__ cvec, const u16* __restrict__ wmod, u16* __restrict__ out, char* __restrict__ ws) {
    const unsigned epoch = *(const unsigned*)(ws + WS_E) + 1u;
    const int tid = threadIdx.x, lane = tid & 63, w = __builtin_amdgcn_readfirstlane(tid >> 6);
    const int b = blockIdx.x, xl = b & 7, idx = b >> 3, g = 2 * xl + (idx >> 4), j = idx & 15;
    const int ct = j & 7, rh = j >> 3;
    unsigned long long code_pc; unsigned code_len;
    asm volatile("s_getpc_b64 %0\nfb_code_begin:\n\ts_mov_b32 %1, fb_code_end-fb_code_begin" : "=s"(code_pc), "=s"(code_len));
    STAMP(0); STAMPC(20);
    if (w == 8) {
        {
            const unsigned o = ((unsigned)idx * 32u + (lane & 31)) * 64u;
            warm4((int)o < (int)code_len ? o : 0u, (const void*)code_pc);
        }
        {
            const char* px_ = (const char*)x + b * 8192; const char* pw_ = (const char*)wqkv + b * 6144;
            warm4(lane * 64u, px_); warm4(lane * 64u + 4096u, px_); warm4(lane * 64u, pw_); warm4(lane < 32 ? lane * 64u + 4096u : 0u, pw_); }
        if (lane == 0) stg_sc1(ws, WS_HELLO + b * 4, epoch);
        __builtin_amdgcn_s_setprio(3);
        const unsigned sw0 = (lane >> 3) * 1024u + ct * 128u + (((lane & 7) >> 1) ^ ((lane >> 4) & 3)) * 32u + (lane & 1) * 16u, sw1 = sw0 + 7168, sw2 = sw0 + 14336, sw3 = sw0 + 21504;
        unsigned w1o[7], wqo[3];
#pragma unroll
        for (int r = 0; r < 7; ++r) { const unsigned u = r * 64 + lane, row = u / 14, c = u % 14; w1o[r] = row * 3584u + 224u * j + c * 16u - (r & 3) * 1024u; }
#pragma unroll
        for (int r = 0; r < 3; ++r) { const unsigned u = r * 64 + lane, row = u / 12, c = u % 12; wqo[r] = row * 3072u + (c >> 2) * 1024u + 64u * j + (c & 3) * 16u - r * 1024u; }
#define RUN128(base, first, n4, lds0) { const char* p_ = (const char*)(base) + (size_t)(first) * 8192; unsigned l_ = (lds0); _Pragma("unroll 2") for (int i_ = 0; i_ < (n4); ++i_) { dma4(sw0, sw1, sw2, sw3, p_, l_); p_ += 32768; l_ += 4096; } }
#define W1GRPS(k0, k1) { const char* p_ = (const char*)w1 + (size_t)(k0) * 114688; unsigned l_ = L_W1 + 7 * (k0) * 1024; _Pragma("unroll 1") for (int k_ = (k0); k_ < (k1); ++k_) { \
            dma4(w1o[0], w1o[1], w1o[2], w1o[3], p_, l_); dma3(w1o[4], w1o[5], w1o[6], p_, l_ + 4096); p_ += 114688; l_ += 7168; } }
#define W2CHUNKS(c0, c1, slot0) { _Pragma("unroll 1") for (int c_ = (c0); c_ < (c1); ++c_) { const char* p_ = (const char*)w2 + (size_t)c_ * 32768; const unsigned l_ = ((slot0) + c_ - (c0)) * 16384; \
            dma4(sw0, sw1, sw2, sw3, p_, l_); dma4(sw0, sw1, sw2, sw3, p_ + 56 * 8192, l_ + 4096); dma4(sw0, sw1, sw2, sw3, p_ + 112 * 8192, l_ + 8192); dma4(sw0, sw1, sw2, sw3, p_ + 168 * 8192, l_ + 12288); } }
#define WQGRPS(k0, k1) { const char* p_ = (const char*)wqkv + (size_t)(k0) * 49152; unsigned l_ = L_WQKV + 3 * (k0) * 1024; _Pragma("unroll 2") for (int k_ = (k0); k_ < (k1); ++k_) { dma3(wqo[0], wqo[1], wqo[2], p_, l_); p_ += 49152; l_ += 3072; } }
        __builtin_amdgcn_s_barrier();
        __builtin_amdgcn_s_barrier();
        WQGRPS(14, 24)
        __builtin_amdgcn_s_barrier();
        WQGRPS(0, 14)
        VMWAIT(0);
        __builtin_amdgcn_s_barrier();
        {
            const char* p_ = b < 32 ? (const char*)wo + b * 16384 : b < 144 ? (const char*)w1 + (b - 32) * 16384 : (const char*)w2 + (b - 144) * 16384;
#pragma unroll
            for (int i = 0; i < 4; ++i) warm4(lane * 64u + i * 4096u, p_); }
        mod_part(cvec, wmod, ws, epoch, lane, 32 + 8 * xl + (idx & 3), idx >> 2); mod_part(cvec, wmod, ws, epoch, lane, 36 + 8 * xl + (idx & 3), idx >> 2);
        if (idx < 4) { PartSum p; mod_sum_load(p, ws, lane, 32 + 8 * xl + idx); mod_sum_finish(p, ws, epoch, lane, 32 + 8 * xl + idx); mod_sum_load(p, ws, lane, 36 + 8 * xl + idx); mod_sum_finish(p, ws, epoch, lane, 36 + 8 * xl + idx); }
        __builtin_amdgcn_s_barrier();
        __builtin_amdgcn_s_barrier();
        if (g >= 1) {
            const unsigned fo = WS_FLAG + (((g >= 2 ? g - 2 + (lane & 1) : 0) * 16 + j) * 256);
            for (unsigned sp = 0; sp < SPIN_CAP; ++sp) { unsigned f = ldg_sc1(ws, fo); if (__all(f == epoch)) break; }
            const unsigned lo16 = lane * 16;
#pragma unroll 1
            for (int src = (g >= 2 ? 0 : 1); src < 2; ++src) {
                const char* p_ = ws + WS_KV + (size_t)((g - 2 + src) * 16 + j) * 16384;
                dma4lin_c(lo16, p_, L_KIMG + src * 8192); dma4lin_c(lo16, p_ + 4096, L_KIMG + src * 8192 + 4096);
                dma4lin_c(lo16, p_ + 8192, L_VIMG + src * 8192); dma4lin_c(lo16, p_ + 12288, L_VIMG + src * 8192 + 4096);
            }
        }
        VMWAIT(0);
        __builtin_amdgcn_s_barrier();
        RUN128(wo, 0, 16, L_WO)
        W1GRPS(3, 7)
        W1GRPS(15, 16)
        __builtin_amdgcn_s_barrier();
        W1GRPS(7, 15)
        VMWAIT(56);
        __builtin_amdgcn_s_barrier();
        __builtin_amdgcn_s_barrier();
        __builtin_amdgcn_s_barrier();
        __builtin_amdgcn_s_barrier();
        W1GRPS(0, 3)
        __builtin_amdgcn_s_barrier();
        VMWAIT(0);
        __builtin_amdgcn_s_barrier();
        __builtin_amdgcn_s_barrier();
        W2CHUNKS(0, 2, 0)
        __builtin_amdgcn_s_barrier();
        __builtin_amdgcn_s_barrier();
        W2CHUNKS(2, 6, 2)
        __builtin_amdgcn_s_barrier();
        W2CHUNKS(6, 9, 6)
        VMWAIT(48);
        __builtin_amdgcn_s_barrier();
        VMWAIT(0);
        __builtin_amdgcn_s_barrier();
        __builtin_amdgcn_s_barrier();
        __builtin_amdgcn_s_barrier();
        __builtin_amdgcn_s_barrier();
        return;
    }
    const __amdgpu_buffer_rsrc_t rws = __builtin_amdgcn_make_buffer_rsrc(ws, 0, WS_BYTES, 0x00020000);
    const int m = w & 3, kh = w >> 2;
#define XTILE(t) { const unsigned xo = (unsigned)(128 * g + 32 * (t)) * 1024u + ((unsigned)lane << 4); \
        _Pragma("unroll 2") for (int i = 0; i < 32; i += 4) dma4(xo ^ (unsigned)((i & 15) << 4), xo ^ (unsigned)(((i & 15) + 1) << 4), xo ^ (unsigned)(((i & 15) + 2) << 4), xo ^ (unsigned)(((i & 15) + 3) << 4), \
                                                                 (const char*)x + i * 1024, L_X + (3 - (t)) * 32768 + i * 1024); }
    PartSum psum;
    if (w >= 4 && w < 7) XTILE(w - 4)
    if (w == 0) lds_st<u32x4>(L_ONES, (u32x4){0x3F803F80u, 0x3F803F80u, 0x3F803F80u, 0x3F803F80u});
    if (w == 3) { __builtin_amdgcn_s_setprio(2); mod_part(cvec, wmod, ws, epoch, lane, 4 * xl + (idx & 3), idx >> 2); __builtin_amdgcn_s_setprio(0); }
    STAMP(1); WS6(0);
    bf16x8 qf; u32x4 kpk, vpk; float qown[8];
    {
    const int h = lane >> 5, gi = lane >> 4, q4 = (lane & 15) >> 2, p4 = lane & 3;
    u32x4 xr[16];
#define XFRAGS() { const unsigned tok = lane & 31, rb = L_X + (3 - m) * 32768 + tok * 1024 + kh * 512; \
        _Pragma("unroll") for (int s = 0; s < 16; ++s) xr[s] = lds_ld<u32x4>(rb + (s >> 3) * 256 + ((((2 * (s & 7) + h) ^ (tok & 15))) << 4)); \
        _Pragma("unroll") for (int s = 0; s < 16; ++s) PIN(xr[s]); }
    if (w == 4) VMWAIT(0);
    BAR();
    WS6(1);
    if (w == 4) XTILE(3)
    if (w == 7) {
        unsigned wq[3];
#pragma unroll
        for (int r = 0; r < 3; ++r) { const unsigned u = r * 64 + lane, row = u / 12, c = u % 12; wq[r] = row * 3072u + (c >> 2) * 1024u + 64u * j + (c & 3) * 16u - r * 1024u; }
#pragma unroll 2
        for (int k = 24; k < 32; ++k) dma3(wq[0], wq[1], wq[2], (const char*)wqkv + (size_t)k * 49152, L_WQKV + 3 * k * 1024); }
    if (w == 3 && idx < 4) mod_sum_load(psum, ws, lane, 4 * xl + idx);
    if (m == 0) XFRAGS()
    if (w == 5 || w == 6) VMWAIT(0);
    BAR();
    WS6(2);
    if (w == 3 && idx < 4) mod_sum_finish(psum, ws, epoch, lane, 4 * xl + idx);
    if (m == 1 || m == 2) XFRAGS()
    if (w == 4) VMWAIT(0);
    BAR();
    if (m == 3) XFRAGS()
#undef XFRAGS
#undef XTILE
    STAMP(2);
    const unsigned gmo = WS_MODU + g * 4096 + (tid & 255) * 16; u32x4 gmv;
    if (w < 4) gmv = __builtin_amdgcn_raw_buffer_load_b128(rws, gmo, 0, 16);
    SBAR();
    {
        float s0 = 0.f, s1 = 0.f, s2 = 0.f, s3 = 0.f; const bf2 one = {(__bf16)1.0f, (__bf16)1.0f};
#pragma unroll
        for (int s = 0; s < 16; ++s)
#pragma unroll
            for (int i = 0; i < 4; i += 2) { const unsigned ua = xr[s][i], ub = xr[s][i + 1]; const bf2 va = __builtin_bit_cast(bf2, ua), vb = __builtin_bit_cast(bf2, ub);
                s0 = __builtin_amdgcn_fdot2_f32_bf16(va, one, s0, false); s1 = __builtin_amdgcn_fdot2_f32_bf16(va, va, s1, false);
                s2 = __builtin_amdgcn_fdot2_f32_bf16(vb, one, s2, false); s3 = __builtin_amdgcn_fdot2_f32_bf16(vb, vb, s3, false); }
        s0 = xsum32(s0 + s2); s1 = xsum32(s1 + s3);
        if (lane < 32) lds_st<u32x2>(L_STAT + (kh * 128 + 32 * m + lane) * 8, (u32x2){__float_as_uint(s0), __float_as_uint(s1)});
    }
    WS6(3);
    if (w < 4) {
        for (unsigned sp = 0; sp < SPIN_CAP; ++sp) { if (__all(gmv[1] == epoch && gmv[3] == epoch)) break; asm volatile("" ::: "memory"); gmv = __builtin_amdgcn_raw_buffer_load_b128(rws, gmo, 0, 16); }
        const unsigned u0 = gmv[0], u2 = gmv[2]; f32x4 f = {bf_lo(u0), bf_hi(u0), bf_lo(u2), bf_hi(u2)};
        if (tid >= 128) { f[0] = rbf(1.f + f[0]); f[1] = rbf(1.f + f[1]); f[2] = rbf(1.f + f[2]); f[3] = rbf(1.f + f[3]); }
        lds_st<f32x4>(L_SAG + tid * 16, f);
    }
    STAMP(3);
    VMWAIT(0);
    BAR();
    STAMP(4); STAMPC(22); WS1(0);
    float rstd, nmr, gv[2]; f32x4 sa0[2], sa1[2];
    const unsigned sga = L_SAG + (256 * kh + 16 * ((lane & 15) >> 3) + 8 * h + (lane & 7)) * 4;
    const unsigned saa = L_SAG + (256 * kh + 8 * h) * 4;
    { const u32x2 a = lds_ld<u32x2>(L_STAT + (32 * m + (lane & 31)) * 8), c2 = lds_ld<u32x2>(L_STAT + (128 + 32 * m + (lane & 31)) * 8);
      gv[0] = lds_ld<float>(sga + 2048);
      sa0[0] = lds_ld<f32x4>(saa); sa1[0] = lds_ld<f32x4>(saa + 16);
      const float s0 = __uint_as_float(a[0]) + __uint_as_float(c2[0]), s1 = __uint_as_float(a[1]) + __uint_as_float(c2[1]);
      const float mean = s0 * (1.f / 512.f), var = fmaxf(s1 * (1.f / 512.f) - mean * mean, 0.f);
      rstd = __builtin_amdgcn_rsqf(var + 1e-5f); nmr = -mean * rstd;
      asm volatile("s_nop 1" : "+v"(gv[0])); }
    f32x16 aQ, aK, aV;
#pragma unroll
    for (int i = 0; i < 16; ++i) { aQ[i] = 0.f; aK[i] = 0.f; aV[i] = 0.f; }
    {
        const unsigned rowa = L_WQKV + (256 * kh + 8 * (gi >> 1) + q4) * 192 + p4 * 8;
        const unsigned wqa = rowa + (((gi & 1) ^ kh) * 32), wva = rowa + 128 + (gi & 1) * 32;
        bf16x8 fk, fv;
#define HDW(s, i) hdw<8 * ((s) & 1) + 2 * (i)>(xr[s][i], rstd, nmr, gv[((s) >> 1) & 1], (i) < 2 ? sa0[(s) & 1][2 * (i) & 3] : sa1[(s) & 1][2 * (i) & 3], (i) < 2 ? sa0[(s) & 1][(2 * (i) + 1) & 3] : sa1[(s) & 1][(2 * (i) + 1) & 3])
        fk = trfrag(wqa + 64, wqa + 64 + 768); fv = trfrag(wva, wva + 768);
        sa0[1] = lds_ld<f32x4>(saa + 64); sa1[1] = lds_ld<f32x4>(saa + 64 + 16);
        { const unsigned a0 = HDW(0, 0), a1 = HDW(0, 1), a2 = HDW(0, 2), a3 = HDW(0, 3); xr[0] = (u32x4){a0, a1, a2, a3}; }
        asm volatile("s_nop 1" : "+v"(xr[0])); SBAR();
#define QKV_STEP(s, sn) { const bf16x8 hb = __builtin_bit_cast(bf16x8, xr[s]); unsigned n0, n1, n2, n3; \
            aK = MFMA32(fk, hb, aK); PIN(aK); SBAR(); if ((sn) < 16) { fk = trfrag(wqa + (sn) * 3072 + 64, wqa + (sn) * 3072 + 64 + 768); if ((s) + 2 < 16) sa0[(s) & 1] = lds_ld<f32x4>(saa + ((s) + 2) * 64); \
                n0 = HDW((sn) & 15, 0); n1 = HDW((sn) & 15, 1); PIN(n0); PIN(n1); } SBAR(); \
            aV = MFMA32(hb, fv, aV); PIN(aV); SBAR(); if ((sn) < 16) { fv = trfrag(wva + (sn) * 3072, wva + (sn) * 3072 + 768); if ((s) + 2 < 16) sa1[(s) & 1] = lds_ld<f32x4>(saa + ((s) + 2) * 64 + 16); \
                n2 = HDW((sn) & 15, 2); n3 = HDW((sn) & 15, 3); PIN(n2); PIN(n3); xr[(sn) & 15] = (u32x4){n0, n1, n2, n3}; } \
            if ((s) % 2 == 0 && (s) + 2 < 16) { gv[(((s) >> 1) + 1) & 1] = lds_ld<float>(sga + 2048 + (((s) >> 1) + 1) * 128); } SBAR(); }
        WS1(1);
        QKV_STEP(0, 1) QKV_STEP(1, 2) QKV_STEP(2, 3) QKV_STEP(3, 4) QKV_STEP(4, 5) QKV_STEP(5, 6) QKV_STEP(6, 7) QKV_STEP(7, 8)
        QKV_STEP(8, 9) QKV_STEP(9, 10) QKV_STEP(10, 11) QKV_STEP(11, 12) QKV_STEP(12, 13) QKV_STEP(13, 14) QKV_STEP(14, 15) QKV_STEP(15, 16)
#undef QKV_STEP
#undef HDW
        STAMP(5); STAMPC(23); WS1(2);
        float vs[8], vk[8];
#pragma unroll
        for (int i = 0; i < 8; ++i) { float lo = aV[i], hi = aV[8 + i]; PIN(lo); PIN(hi); vs[i] = kh ? lo : hi; vk[i] = kh ? hi : lo; }
        const unsigned ex = w * 6144 + lane * 16;
        lds_st<f32x4>(ex, (f32x4){aK[8], aK[9], aK[10], aK[11]}); lds_st<f32x4>(ex + 1024, (f32x4){aK[12], aK[13], aK[14], aK[15]});
        lds_st<f32x4>(ex + 2048, (f32x4){vs[0], vs[1], vs[2], vs[3]}); lds_st<f32x4>(ex + 3072, (f32x4){vs[4], vs[5], vs[6], vs[7]});
        bf16x8 fq = trfrag(wqa, wqa + 768), fq1 = trfrag(wqa + 3072, wqa + 3072 + 768);
        BAR();
        WS1(3);
        const unsigned ep = (w ^ 4) * 6144 + lane * 16;
        { const f32x4 k0 = lds_ld<f32x4>(ep), k1 = lds_ld<f32x4>(ep + 1024), v0 = lds_ld<f32x4>(ep + 2048), v1 = lds_ld<f32x4>(ep + 3072);
          u32x4 kp, vp;
          kp[0] = pk2(aK[0] + k0[0], aK[1] + k0[1]); kp[1] = pk2(aK[2] + k0[2], aK[3] + k0[3]); kp[2] = pk2(aK[4] + k1[0], aK[5] + k1[1]); kp[3] = pk2(aK[6] + k1[2], aK[7] + k1[3]);
          vp[0] = pk2(vk[0] + v0[0], vk[1] + v0[1]); vp[1] = pk2(vk[2] + v0[2], vk[3] + v0[3]); vp[2] = pk2(vk[4] + v1[0], vk[5] + v1[1]); vp[3] = pk2(vk[6] + v1[2], vk[7] + v1[3]);
          const unsigned kvo = WS_KV + (g * 16 + j) * 16384 + (m * 2 + kh) * 1024 + lane * 16;
          __builtin_amdgcn_raw_buffer_store_b128(kp, rws, kvo, 0, 16);
          __builtin_amdgcn_raw_buffer_store_b128(vp, rws, kvo + 8192, 0, 16);
          PIN(kp); PIN(vp); kpk = kp; vpk = vp; }
        SBAR();
#pragma unroll
        for (int s = 0; s < 16; s += 2) {
            aQ = MFMA32(fq, __builtin_bit_cast(bf16x8, xr[s]), aQ); PIN(aQ); SBAR(); if (s + 2 < 16) fq = trfrag(wqa + (s + 2) * 3072, wqa + (s + 2) * 3072 + 768); SBAR();
            aQ = MFMA32(fq1, __builtin_bit_cast(bf16x8, xr[s + 1]), aQ); PIN(aQ); SBAR(); if (s + 3 < 16) fq1 = trfrag(wqa + (s + 3) * 3072, wqa + (s + 3) * 3072 + 768); SBAR(); }
        lds_st<f32x4>(ex + 4096, (f32x4){aQ[8], aQ[9], aQ[10], aQ[11]}); lds_st<f32x4>(ex + 5120, (f32x4){aQ[12], aQ[13], aQ[14], aQ[15]});
#pragma unroll
        for (int i = 0; i < 8; ++i) qown[i] = aQ[i];
    }
    }
    WS1(4);
    VMWAIT(0); WS1(5);
    BAR();
    if (tid == 0) stg_sc1(ws, WS_FLAG + (0 * 256 + g * 16 + j) * 256, epoch);
    STAMP(6);
    { FRESH(lane);
      const unsigned ep = (w ^ 4) * 6144 + 4096 + lane * 16; const f32x4 q0 = lds_ld<f32x4>(ep), q1 = lds_ld<f32x4>(ep + 1024);
      lds_st<u32x4>(L_KIMG + (8 + m) * 2048 + kh * 1024 + lane * 16, kpk);
      lds_st<u32x4>(L_VIMG + (8 + m) * 2048 + kh * 1024 + lane * 16, vpk);
      const float qs = 0.25f * 1.44269504089f; u32x4 qp;
      qp[0] = pk2((qown[0] + q0[0]) * qs, (qown[1] + q0[1]) * qs); qp[1] = pk2((qown[2] + q0[2]) * qs, (qown[3] + q0[3]) * qs);
      qp[2] = pk2((qown[4] + q1[0]) * qs, (qown[5] + q1[1]) * qs); qp[3] = pk2((qown[6] + q1[2]) * qs, (qown[7] + q1[3]) * qs);
      qf = __builtin_bit_cast(bf16x8, qp); PIN(qf); }
    const int tt = w & 3;
    {
    FRESH(lane);
    const int hh = kh, qq = lane & 31, h = lane >> 5;
    const float NEG = -__builtin_inff(), fq0 = (float)(4 * h - qq) - 0.5f;
    f32x16 c0, c8;
#pragma unroll
    for (int r = 0; r < 16; ++r) {
        const float t = fmaf(fq0, 1e30f, (float)((r & 3) + 8 * (r >> 2)) * 1e30f); c0[r] = fminf(t, 0.f); c8[r] = fminf(-t, 0.f); }
    PIN(c0); PIN(c8);
    BAR();
    STAMP(8); WS2(0);
    {
        const unsigned ka = L_KIMG + m * 2048 + hh * 1024 + lane * 16;
        const bool isd = ((lane >> 4) & 1) == hh;
        const unsigned va = isd ? L_VIMG + m * 2048 + lane * 16 : L_ONES, vst = isd ? 2048u : 0u, vs1 = isd ? 1024u : 0u;
        f32x16 o;
#pragma unroll
        for (int r = 0; r < 16; ++r) o[r] = 0.f;
#define MAX16(mx, s) { _Pragma("unroll") for (int r = 0; r < 16; r += 2) mx = fmaxf(fmaxf(mx, s[r]), s[r + 1]); }
#define EXPH(P, S, b) { P[0] = pk2(__builtin_amdgcn_exp2f(S[b]), __builtin_amdgcn_exp2f(S[b + 1])); P[1] = pk2(__builtin_amdgcn_exp2f(S[b + 2]), __builtin_amdgcn_exp2f(S[b + 3])); \
                        P[2] = pk2(__builtin_amdgcn_exp2f(S[b + 4]), __builtin_amdgcn_exp2f(S[b + 5])); P[3] = pk2(__builtin_amdgcn_exp2f(S[b + 6]), __builtin_amdgcn_exp2f(S[b + 7])); }
        if (g >= 2) {
            f32x16 z;
#pragma unroll
            for (int r = 0; r < 16; ++r) z[r] = 0.f;
            PIN(z);
            float mx = NEG, mx2 = NEG; f32x16 sA, sB;
#define KF(i) lds_ld<bf16x8>(ka + (i) * 2048)
            bf16x8 k0 = KF(0), k1 = KF(8), k2 = KF(1), k3 = KF(2);
            SBAR();
            sA = MFMA32(k0, qf, c0); PIN(sA); k0 = KF(3); SBAR();
            sB = MFMA32(k1, qf, c8); PIN(sB); k1 = KF(4); SBAR(); MAX16(mx, sA); PIN(mx); SBAR();
            sA = MFMA32(k2, qf, z); PIN(sA); k2 = KF(5); SBAR(); MAX16(mx2, sB); PIN(mx2); SBAR();
            sB = MFMA32(k3, qf, z); PIN(sB); k3 = KF(6); SBAR(); MAX16(mx, sA); PIN(mx); SBAR();
            sA = MFMA32(k0, qf, z); PIN(sA); k0 = KF(7); SBAR(); MAX16(mx2, sB); PIN(mx2); SBAR();
            sB = MFMA32(k1, qf, z); PIN(sB); k1 = KF(0); SBAR(); MAX16(mx, sA); PIN(mx); SBAR();
            sA = MFMA32(k2, qf, z); PIN(sA); k2 = KF(1); SBAR(); MAX16(mx2, sB); PIN(mx2); SBAR();
            sB = MFMA32(k3, qf, z); PIN(sB); SBAR(); MAX16(mx, sA); PIN(mx); SBAR();
            sA = MFMA32(k0, qf, z); PIN(sA); SBAR(); MAX16(mx2, sB); PIN(mx2); SBAR();
            WS2(1);
            bf16x8 v0 = lds_ld<bf16x8>(va), v1 = lds_ld<bf16x8>(va + vs1);
            MAX16(mx, sA);
            mx = xmax32(fmaxf(mx, mx2));
#pragma unroll
            for (int r = 0; r < 16; ++r) { z[r] = -mx; c0[r] -= mx; c8[r] -= mx; }
            PIN(z); PIN(c0); PIN(c8); SBAR();
            WS2(2);
            sA = MFMA32(k1, qf, c0); SBAR();
            u32x4 pa, pb; bf16x8 v0n, v1n, kn = k2; unsigned vp = va;
#define ABLK(I, SC, SN, CN) { bf16x8 kc = kn; if ((I) < 8) { vp += vst; v0n = lds_ld<bf16x8>(vp); v1n = lds_ld<bf16x8>(vp + vs1); } if ((I) < 7) kn = KF((I) + 2); SBAR(); \
                if ((I) < 8) { SN = MFMA32(kc, qf, CN); PIN(SN); } SBAR(); EXPH(pa, SC, 0); PIN(pa); SBAR(); \
                o = MFMA32(v0, __builtin_bit_cast(bf16x8, pa), o); PIN(o); SBAR(); EXPH(pb, SC, 8); PIN(pb); SBAR(); \
                o = MFMA32(v1, __builtin_bit_cast(bf16x8, pb), o); PIN(o); SBAR(); if ((I) < 8) { v0 = v0n; v1 = v1n; } }
            ABLK(0, sA, sB, z) ABLK(1, sB, sA, z) ABLK(2, sA, sB, z) ABLK(3, sB, sA, z)
            ABLK(4, sA, sB, z) ABLK(5, sB, sA, z) ABLK(6, sA, sB, z) ABLK(7, sB, sA, c8) ABLK(8, sA, sB, c8)
#undef KF
#undef ABLK
        } else {
            const int i0 = 8 - 4 * g - m;
            f32x16 z;
#pragma unroll
            for (int r = 0; r < 16; ++r) z[r] = 0.f;
            float mx = NEG;
            f32x16 s = MFMA32(lds_ld<bf16x8>(ka + 8 * 2048), qf, c8); MAX16(mx, s);
#pragma unroll 1
            for (int i = i0; i < 8; ++i) { s = MFMA32(lds_ld<bf16x8>(ka + i * 2048), qf, z); MAX16(mx, s); }
            mx = xmax32(mx);
#pragma unroll
            for (int r = 0; r < 16; ++r) { z[r] = -mx; c8[r] -= mx; }
#pragma unroll 1
            for (int i = i0; i <= 8; ++i) { u32x4 pa, pb;
                if (i < 8) s = MFMA32(lds_ld<bf16x8>(ka + i * 2048), qf, z); else s = MFMA32(lds_ld<bf16x8>(ka + i * 2048), qf, c8);
                EXPH(pa, s, 0); EXPH(pb, s, 8);
                o = MFMA32(lds_ld<bf16x8>(va + i * vst), __builtin_bit_cast(bf16x8, pa), o); o = MFMA32(lds_ld<bf16x8>(va + i * vst + vs1), __builtin_bit_cast(bf16x8, pb), o); }
        }
#undef EXPH
#undef MAX16
        WS2(3);
        float ov[8], l;
        { float lo = o[0], hi = o[8]; PIN(lo); PIN(hi); l = hh ? lo : hi; }
#pragma unroll
        for (int r = 0; r < 8; ++r) { float lo = o[r], hi = o[8 + r]; PIN(lo); PIN(hi); ov[r] = hh ? hi : lo; }
        const float inv = __builtin_amdgcn_rcpf(l);
        const u32x4 op = {pk2(ov[0] * inv, ov[1] * inv), pk2(ov[2] * inv, ov[3] * inv), pk2(ov[4] * inv, ov[5] * inv), pk2(ov[6] * inv, ov[7] * inv)};
        __builtin_amdgcn_raw_buffer_store_b128(op, rws, WS_O + ((g * 8 + 2 * m + (qq >> 4)) * 16 + j) * 1024 + ((qq & 15) + 16 * (2 * hh + h)) * 16, 0, 16);
    }
    }
    WS2(4);
    VMWAIT(0); WS2(5);
    BAR();
    if (tid == 0) stg_sc1(ws, WS_FLAG + (1 * 256 + g * 16 + j) * 256, epoch);
    STAMP(9);
    if (w >= 4) {
        const int t = tid - 256; const unsigned o = WS_MODN + g * 8192 + t * 32; u32x4 v, v2;
        for (unsigned sp = 0; sp < SPIN_CAP; ++sp) { asm volatile("" ::: "memory"); v = __builtin_amdgcn_raw_buffer_load_b128(rws, o, 0, 16); v2 = __builtin_amdgcn_raw_buffer_load_b128(rws, o + 16, 0, 16);
            if (__all(v[1] == epoch && v[3] == epoch && v2[1] == epoch && v2[3] == epoch)) break; }
        u32x4 val = {v[0], v[2], v2[0], v2[2]};
        const int vec = t >> 6, c8 = (t & 63) * 8;
        if (vec == 1) lds_st<u32x4>(L_SB16 + c8 * 2, val);
        else if (vec == 2) {
#pragma unroll
            for (int i = 0; i < 4; ++i) { const unsigned u_ = val[i]; val[i] = pk2(1.f + bf_lo(u_), 1.f + bf_hi(u_)); }
            lds_st<u32x4>(L_GB16 + c8 * 2, val);
        } else if ((c8 >> 6) == ct) lds_st<u32x4>((vec == 0 ? L_GAO : L_GBO) + (c8 & 63) * 2, val);
    }
    if (w == 0) poll_flags(ws, WS_FLAG + (1 * 256 + g * 16) * 256, 16, epoch, lane);
    BAR();
    STAMP(10); WS3(0);
    u32x2 x1p0, x1p1;
    {
        FRESH(lane);
        const int gi = lane >> 4, q4 = (lane & 15) >> 2, p4 = lane & 3;
        const unsigned tokP = 128 * g + 64 * rh + 16 * tt + (lane & 15), colP = 64 * ct + 32 * kh + 4 * gi;
        u32x4 of[8];
#pragma unroll
        for (int s = 0; s < 8; ++s) of[s] = __builtin_amdgcn_raw_buffer_load_b128(rws, WS_O + ((g * 8 + 4 * rh + tt) * 16 + 8 * kh + s) * 1024 + lane * 16, 0, 16);
        const u32x2 xres0 = *(const u32x2*)(x + tokP * 512 + colP), xres1 = *(const u32x2*)(x + tokP * 512 + colP + 16);
        SBAR();
        f32x4 acc[4];
#pragma unroll
        for (int n = 0; n < 4; ++n) acc[n] = (f32x4){0.f, 0.f, 0.f, 0.f};
        const unsigned xv = 2 * (gi & 1) + (q4 >> 1), rowa = L_WO + (256 * kh + 16 * (gi >> 1) + 4 * (gi & 1) + q4) * 128 + p4 * 8;
        unsigned an[4]; bf16x8 wf[4];
#pragma unroll
        for (int n = 0; n < 4; ++n) { an[n] = rowa + ((n ^ xv) * 32); wf[n] = trfrag(an[n], an[n] + 1024); }
        const unsigned cl = (32 * kh + 4 * gi) * 2, cg = colP * 2;
        const u32x2 ga0 = lds_ld<u32x2>(L_GAO + cl), ga1 = lds_ld<u32x2>(L_GAO + cl + 32);
        const u32x2 gb0 = lds_ld<u32x2>(L_GB16 + cg), gb1 = lds_ld<u32x2>(L_GB16 + cg + 32), sb0 = lds_ld<u32x2>(L_SB16 + cg), sb1 = lds_ld<u32x2>(L_SB16 + cg + 32);
        SBAR();
#pragma unroll
        for (int s = 0; s < 8; ++s)
#pragma unroll
            for (int n = 0; n < 4; ++n) { acc[n] = MFMA16(wf[n], __builtin_bit_cast(bf16x8, of[s]), acc[n]); PIN(acc[n]); SBAR(); if (s < 7) wf[n] = trfrag(an[n] + (s + 1) * 4096, an[n] + (s + 1) * 4096 + 1024); SBAR(); }
        WS3(1);
        BAR();
        STAMP(11);
        const unsigned ex = w * 2048 + lane * 16;
        { f32x4 s0 = acc[0], s1 = acc[1], s2 = acc[2], s3 = acc[3]; PIN(s0); PIN(s1); PIN(s2); PIN(s3);
          lds_st<f32x4>(ex, kh ? s0 : s2); lds_st<f32x4>(ex + 1024, kh ? s1 : s3); acc[0] = kh ? s2 : s0; acc[1] = kh ? s3 : s1; }
        BAR();
        WS3(2);
        const unsigned ep = (w ^ 4) * 2048 + lane * 16;
        const f32x4 a0 = lds_ld<f32x4>(ep) + acc[0], a1 = lds_ld<f32x4>(ep + 1024) + acc[1];
        float xv1[8], gg[8], ss[8];
#pragma unroll
        for (int r = 0; r < 4; ++r) {
            const unsigned ua0 = ga0[r >> 1], ua1 = ga1[r >> 1], ux0 = xres0[r >> 1], ux1 = xres1[r >> 1], ug0 = gb0[r >> 1], ug1 = gb1[r >> 1], us0 = sb0[r >> 1], us1 = sb1[r >> 1];
            xv1[r] = rbf(fmaf((r & 1) ? bf_hi(ua0) : bf_lo(ua0), a0[r], (r & 1) ? bf_hi(ux0) : bf_lo(ux0))); xv1[4 + r] = rbf(fmaf((r & 1) ? bf_hi(ua1) : bf_lo(ua1), a1[r], (r & 1) ? bf_hi(ux1) : bf_lo(ux1)));
            gg[r] = (r & 1) ? bf_hi(ug0) : bf_lo(ug0); gg[4 + r] = (r & 1) ? bf_hi(ug1) : bf_lo(ug1);
            ss[r] = (r & 1) ? bf_hi(us0) : bf_lo(us0); ss[4 + r] = (r & 1) ? bf_hi(us1) : bf_lo(us1);
        }
        x1p0 = (u32x2){pk2(xv1[0], xv1[1]), pk2(xv1[2], xv1[3])}; x1p1 = (u32x2){pk2(xv1[4], xv1[5]), pk2(xv1[6], xv1[7])};
        float st[6] = {0.f, 0.f, 0.f, 0.f, 0.f, 0.f}, pg[8];
#pragma unroll
        for (int r = 0; r < 8; ++r) pg[r] = xv1[r] * gg[r];
        const u32x4 xg = {pk2(pg[0], pg[1]), pk2(pg[2], pg[3]), pk2(pg[4], pg[5]), pk2(pg[6], pg[7])};
        __builtin_amdgcn_raw_buffer_store_b128(xg, rws, WS_XG + ((g * 8 + 4 * rh + tt) * 16 + 2 * ct + kh) * 1024 + lane * 16, 0, 16);
#pragma unroll
        for (int r = 0; r < 8; ++r) { const float p = pg[r]; st[0] += xv1[r]; st[1] = fmaf(xv1[r], xv1[r], st[1]); st[2] += p; st[3] = fmaf(p, p, st[3]); st[4] = fmaf(p, gg[r], st[4]); st[5] = fmaf(p, ss[r], st[5]); }
#pragma unroll
        for (int i = 0; i < 6; ++i) st[i] = xsum32(xsum16(st[i]));
        if (kh == 1 && lane < 16) { lds_st<f32x4>(L_SCR + (tt * 16 + lane) * 32, (f32x4){st[0], st[1], st[2], st[3]}); lds_st<u32x2>(L_SCR + (tt * 16 + lane) * 32 + 16, (u32x2){__float_as_uint(st[4]), __float_as_uint(st[5])}); }
        WS3(3);
        BAR();
        if (kh == 0 && lane < 16) {
            const f32x4 o4 = lds_ld<f32x4>(L_SCR + (tt * 16 + lane) * 32); const u32x2 o2 = lds_ld<u32x2>(L_SCR + (tt * 16 + lane) * 32 + 16);
            const unsigned so = WS_ST + g * 24576, T = 4 * rh + tt;
            const u32x4 sa4 = {__float_as_uint(st[0] + o4[0]), __float_as_uint(st[1] + o4[1]), __float_as_uint(st[2] + o4[2]), __float_as_uint(st[3] + o4[3])};
            const u32x2 sb2 = {__float_as_uint(st[4] + __uint_as_float(o2[0])), __float_as_uint(st[5] + __uint_as_float(o2[1]))};
            __builtin_amdgcn_raw_buffer_store_b128(sa4, rws, so + ((T * 8 + ct) * 16 + lane) * 16, 0, 16);
            __builtin_amdgcn_raw_buffer_store_b64(sb2, rws, so + 16384 + ((T * 8 + ct) * 16 + lane) * 8, 0, 16);
        }
    }
    WS3(4);
    VMWAIT(0); WS3(5);
    BAR();
    if (tid == 0) stg_sc1(ws, WS_FLAG + (2 * 256 + g * 16 + j) * 256, epoch);
    STAMP(12);
    const int odd = j & 1;
    {
    FRESH(lane);
    const int gi = lane >> 4, q4 = (lane & 15) >> 2, p4 = lane & 3;
    const u32x2 b1v = *(const u32x2*)(b1 + 112 * j + (w > 0 ? w - 1 : 0) * 16 + 4 * gi);
    BAR();
    if (w > 0) {
        const int nt = w - 1; f32x4 acc = {0.f, 0.f, 0.f, 0.f};
        const unsigned ra = L_W1 + (4 * gi + q4) * 224 + p4 * 8 + nt * 32; const int tk = lane & 15;
        const unsigned va = (tk == 0 ? L_GB16 : L_SB16) + 8 * gi;
#pragma unroll
        for (int s = 0; s < 16; ++s) {
            const u32x2 lo = lds_ld<u32x2>(va + s * 64), hi = lds_ld<u32x2>(va + s * 64 + 32);
            u32x4 bv = {lo[0], lo[1], hi[0], hi[1]};
            if (tk == 2) bv = (u32x4){0x3F803F80u, 0x3F803F80u, 0x3F803F80u, 0x3F803F80u};
            if (tk > 2) bv = (u32x4){0u, 0u, 0u, 0u};
            acc = MFMA16(trfrag(ra + s * 7168, ra + s * 7168 + 3584), __builtin_bit_cast(bf16x8, bv), acc);
        }
        if (tk < 3) lds_st<f32x4>(L_UVW + (tk * 112 + nt * 16 + 4 * gi) * 4, acc);
        if (tk == 3) lds_st<f32x4>(L_UVW + (3 * 112 + nt * 16 + 4 * gi) * 4, (f32x4){bf_lo(b1v[0]), bf_hi(b1v[0]), bf_lo(b1v[1]), bf_hi(b1v[1])});
    } else {
        const u32x4 gq = lds_ld<u32x4>(L_GB16 + lane * 16), sq = lds_ld<u32x4>(L_SB16 + lane * 16);
        float c5[5] = {0.f, 0.f, 0.f, 0.f, 0.f};
#pragma unroll
        for (int i = 0; i < 4; ++i) { const unsigned ug = gq[i], us = sq[i]; const float g0 = bf_lo(ug), g1 = bf_hi(ug), s0 = bf_lo(us), s1 = bf_hi(us);
            c5[0] += g0 + g1; c5[1] += s0 + s1; c5[2] += g0 * g0 + g1 * g1; c5[3] += g0 * s0 + g1 * s1; c5[4] += s0 * s0 + s1 * s1; }
#pragma unroll
        for (int i = 0; i < 5; ++i) c5[i] = xsum32(xsum16(rowsum16(c5[i])));
        if (lane == 0) { lds_st<f32x4>(L_CONST, (f32x4){c5[0], c5[1], c5[2], c5[3]}); lds_st<float>(L_CONST + 16, c5[4]); }
        poll_flags(ws, WS_FLAG + (2 * 256 + g * 16) * 256, 16, epoch, lane);
    }
    }
    BAR();
    STAMP(13);
    {
        FRESH(lane);
        const int gi = lane >> 4, q4 = (lane & 15) >> 2, p4 = lane & 3;
        const int Tw = 2 * m + kh;
        u32x4 xf[2][8];
        const unsigned xo0 = WS_XG + (g * 8 + Tw) * 16384 + kh * 8192 + lane * 16, xo1 = WS_XG + (g * 8 + (Tw ^ 1)) * 16384 + kh * 8192 + lane * 16;
        constexpr int WIN = 5;
#pragma unroll
        for (int s = 0; s < WIN; ++s) { xf[0][s] = __builtin_amdgcn_raw_buffer_load_b128(rws, xo0 + s * 1024, 0, 16); xf[1][s] = __builtin_amdgcn_raw_buffer_load_b128(rws, xo1 + s * 1024, 0, 16); }
        const unsigned so = WS_ST + g * 24576 + Tw * 2048, sob = WS_ST + g * 24576 + 16384 + Tw * 1024;
        u32x4 sA0, sA1; u32x2 sB0, sB1;
        SBAR(); WS4(0);
        f32x4 acc[7], acb[7]; bf16x8 wf[7];
        const unsigned ra = L_W1 + (256 * kh + 4 * gi + q4) * 224 + p4 * 8, raA = ra + 32 * odd, raB = ra + (odd ? 0 : 192);
#define W1A(s, u) (((u) < 6 ? raA : raB) + (s) * 7168 + ((u) < 6 ? (u) * 32 : 0))
#pragma unroll
        for (int u = 0; u < 7; ++u) { acc[u] = (f32x4){0.f, 0.f, 0.f, 0.f}; acb[u] = acc[u]; wf[u] = trfrag(W1A(0, u), W1A(0, u) + 3584); }
        SBAR();
#pragma unroll
        for (int s = 0; s < 8; ++s) {
            if (s + WIN < 8) { xf[0][s + WIN] = __builtin_amdgcn_raw_buffer_load_b128(rws, xo0 + (s + WIN) * 1024, 0, 16); xf[1][s + WIN] = __builtin_amdgcn_raw_buffer_load_b128(rws, xo1 + (s + WIN) * 1024, 0, 16); }
            if (s == 4) { sA0 = __builtin_amdgcn_raw_buffer_load_b128(rws, so + ((2 * gi) * 16 + (lane & 15)) * 16, 0, 16); sA1 = __builtin_amdgcn_raw_buffer_load_b128(rws, so + ((2 * gi + 1) * 16 + (lane & 15)) * 16, 0, 16); }
            if (s == 5) { sB0 = __builtin_amdgcn_raw_buffer_load_b64(rws, sob + ((2 * gi) * 16 + (lane & 15)) * 8, 0, 16); sB1 = __builtin_amdgcn_raw_buffer_load_b64(rws, sob + ((2 * gi + 1) * 16 + (lane & 15)) * 8, 0, 16); }
            SBAR();
#pragma unroll
            for (int u = 0; u < 7; ++u) { acc[u] = MFMA16(wf[u], __builtin_bit_cast(bf16x8, xf[0][s]), acc[u]); PIN(acc[u]); SBAR();
                acb[u] = MFMA16(wf[u], __builtin_bit_cast(bf16x8, xf[1][s]), acb[u]); PIN(acb[u]); SBAR();
                if (s < 7) wf[u] = trfrag(W1A(s + 1, u), W1A(s + 1, u) + 3584); SBAR(); }
            if (s == 7) WS4(1);
        }
#undef W1A
        float ca, cb, cc, r2;
        { float S[6];
#pragma unroll
          for (int i = 0; i < 4; ++i) S[i] = xsum32(xsum16(__uint_as_float(sA0[i]) + __uint_as_float(sA1[i])));
#pragma unroll
          for (int i = 0; i < 2; ++i) S[4 + i] = xsum32(xsum16(__uint_as_float(sB0[i]) + __uint_as_float(sB1[i])));
          const f32x4 cc4 = lds_ld<f32x4>(L_CONST); const float SB2 = lds_ld<float>(L_CONST + 16);
          const float iD = 1.f / 512.f, m1 = S[0] * iD, r1 = __builtin_amdgcn_rsqf(fmaxf(S[1] * iD - m1 * m1, 0.f) + 1e-5f);
          const float m2 = (r1 * (S[2] - m1 * cc4[0]) + cc4[1]) * iD;
          const float ez2 = (r1 * r1 * (S[3] - 2.f * m1 * S[4] + m1 * m1 * cc4[2]) + 2.f * r1 * (S[5] - m1 * cc4[3]) + SB2) * iD;
          r2 = __builtin_amdgcn_rsqf(fmaxf(ez2 - m2 * m2, 0.f) + 1e-5f);
          ca = r1 * r2; cb = -ca * m1; cc = -r2 * m2; PIN(ca); PIN(cb); PIN(cc); PIN(r2); }
        const unsigned uo = L_UVW + 16 * gi + 64 * odd, uo6 = L_UVW + 16 * gi + (odd ? 0 : 384);
        f32x4 uu = lds_ld<f32x4>(uo), vv = lds_ld<f32x4>(uo + 448), ww = lds_ld<f32x4>(uo + 896), bb = lds_ld<f32x4>(uo + 1344);
        f32x4 u2 = lds_ld<f32x4>(uo + 64), v2 = lds_ld<f32x4>(uo + 64 + 448), w2v = lds_ld<f32x4>(uo + 64 + 896), b2v = lds_ld<f32x4>(uo + 64 + 1344);
        BAR();
        WS4(2);
        { const unsigned ex = 101376 + w * 7168 + lane * 16;
#pragma unroll
          for (int u = 0; u < 7; ++u) lds_st<f32x4>(ex + u * 1024, acb[u]); }
        WS4(3);
        BAR();
        WS4(4);
        { const unsigned ep = 101376 + (w ^ 4) * 7168 + lane * 16;
#pragma unroll
          for (int u = 0; u < 7; ++u) acc[u] += lds_ld<f32x4>(ep + u * 1024); }
        STAMP(14);
        const unsigned hb = WS_H + (g * 8 + Tw) * 57344, s0 = (7 * j + odd) >> 1;
        const f32x2 kA = {-0.044715f * 2.302208198f, -0.044715f * 2.302208198f}, kB = {-2.302208198f, -2.302208198f}, one2 = {1.f, 1.f};
#define LO2(v) ((f32x2){(v)[0], (v)[1]})
#define HI2(v) ((f32x2){(v)[2], (v)[3]})
#define PRE2(a_, u_, v_, w_, b_) (ca * (a_) + (cb * (u_) + (r2 * (v_) + (cc * (w_) + (b_)))))
#define GELU(N, p, y) { f32x2 z_[N], e_[N]; \
            _Pragma("unroll") for (int r = 0; r < N; ++r) { z_[r] = p[r] * p[r]; PIN(z_[r]); } \
            _Pragma("unroll") for (int r = 0; r < N; ++r) { z_[r] = z_[r] * kA + kB; PIN(z_[r]); } \
            _Pragma("unroll") for (int r = 0; r < N; ++r) { z_[r] = z_[r] * p[r]; PIN(z_[r]); } \
            SBAR(); _Pragma("unroll") for (int r = 0; r < N; ++r) { e_[r] = (f32x2){__builtin_amdgcn_exp2f(z_[r][0]), __builtin_amdgcn_exp2f(z_[r][1])}; PIN(e_[r]); } \
            SBAR(); _Pragma("unroll") for (int r = 0; r < N; ++r) { e_[r] = e_[r] + one2; PIN(e_[r]); } \
            SBAR(); _Pragma("unroll") for (int r = 0; r < N; ++r) { e_[r] = (f32x2){__builtin_amdgcn_rcpf(e_[r][0]), __builtin_amdgcn_rcpf(e_[r][1])}; PIN(e_[r]); } \
            SBAR(); _Pragma("unroll") for (int r = 0; r < N; ++r) y[r] = p[r] * e_[r]; }
#pragma unroll
        for (int u = 0; u < 6; u += 2) {
            f32x2 pre[4], y[4];
            pre[0] = PRE2(LO2(acc[u]), LO2(uu), LO2(vv), LO2(ww), LO2(bb)); pre[1] = PRE2(HI2(acc[u]), HI2(uu), HI2(vv), HI2(ww), HI2(bb));
            pre[2] = PRE2(LO2(acc[u + 1]), LO2(u2), LO2(v2), LO2(w2v), LO2(b2v)); pre[3] = PRE2(HI2(acc[u + 1]), HI2(u2), HI2(v2), HI2(w2v), HI2(b2v));
            PIN(pre[0]); PIN(pre[1]); PIN(pre[2]); PIN(pre[3]);
            SBAR();
            { const unsigned o = u < 4 ? uo + (u + 2) * 64 : uo6; uu = lds_ld<f32x4>(o); vv = lds_ld<f32x4>(o + 448); ww = lds_ld<f32x4>(o + 896); bb = lds_ld<f32x4>(o + 1344); }
            if (u < 4) { const unsigned o = uo + (u + 3) * 64; u2 = lds_ld<f32x4>(o); v2 = lds_ld<f32x4>(o + 448); w2v = lds_ld<f32x4>(o + 896); b2v = lds_ld<f32x4>(o + 1344); }
            SBAR();
            GELU(4, pre, y)
            __builtin_amdgcn_raw_buffer_store_b128((u32x4){pk2(y[0][0], y[0][1]), pk2(y[1][0], y[1][1]), pk2(y[2][0], y[2][1]), pk2(y[3][0], y[3][1])}, rws, hb + (s0 + (u >> 1)) * 1024 + lane * 16, 0, 16);
            SBAR();
        }
        {   f32x2 pre[2], y[2];
            pre[0] = PRE2(LO2(acc[6]), LO2(uu), LO2(vv), LO2(ww), LO2(bb)); pre[1] = PRE2(HI2(acc[6]), HI2(uu), HI2(vv), HI2(ww), HI2(bb));
            GELU(2, pre, y)
            __builtin_amdgcn_raw_buffer_store_b64((u32x2){pk2(y[0][0], y[0][1]), pk2(y[1][0], y[1][1])}, rws, hb + ((7 * j + (odd ? 0 : 6)) >> 1) * 1024 + odd * 512 + lane * 8, 0, 16);
        }
#undef GELU
#undef PRE2
#undef LO2
#undef HI2
        WS4(5);
    }
    VMWAIT(0);
    BAR();
    if (tid == 0) stg_sc1(ws, WS_FLAG + (3 * 256 + g * 16 + j) * 256, epoch);
    STAMP(15);
    u32x4 park[10];
    { FRESH(lane);
      const unsigned sw = (lane >> 3) * 1024u + ct * 128u + (((lane & 7) >> 1) ^ ((lane >> 4) & 3)) * 32u + (lane & 1) * 16u;
      const char* p0 = (const char*)w2 + (size_t)(56 * (w >> 2) + 36 + (w & 3)) * 8192, * p1 = p0 + (size_t)112 * 8192;
#pragma unroll
      for (int i = 0; i < 5; ++i) { park[2 * i] = *(const u32x4*)(p0 + i * 32768 + sw); park[2 * i + 1] = *(const u32x4*)(p1 + i * 32768 + sw); }
      SBAR();
      if (w == 0) poll_flags(ws, WS_FLAG + (3 * 256 + g * 16) * 256, 16, epoch, lane); }
    SBAR();
    BAR();
    STAMP(16); WS5(0);
    {
        constexpr int WIN = 3;
        FRESH(lane);
        const int gi = lane >> 4, q4 = (lane & 15) >> 2, p4 = lane & 3;
        const unsigned tokP = 128 * g + 64 * rh + 16 * tt + (lane & 15), colP = 64 * ct + 32 * kh + 4 * gi;
        const int kq = (w & 1) | (kh << 1);
        const unsigned hb0 = WS_H + (g * 8 + 4 * rh + tt) * 57344 + kq * 14 * 1024, hb1 = WS_H + (g * 8 + 4 * rh + (tt ^ 1)) * 57344 + kq * 14 * 1024;
        u32x4 hf[2][14];
#define HLD1(hb_, dst, s) { if ((s) % 7 == 3) { const u32x2 lo = __builtin_amdgcn_raw_buffer_load_b64(rws, hb_ + (s) * 1024 + lane * 8, 0, 16), hi = __builtin_amdgcn_raw_buffer_load_b64(rws, hb_ + (s) * 1024 + 512 + lane * 8, 0, 16); dst = (u32x4){lo[0], lo[1], hi[0], hi[1]}; } \
                    else dst = __builtin_amdgcn_raw_buffer_load_b128(rws, hb_ + (s) * 1024 + lane * 16, 0, 16); }
#define HLOAD(s) { HLD1(hb0, hf[0][s], s) HLD1(hb1, hf[1][s], s) }
#pragma unroll
        for (int s = 0; s < WIN; ++s) HLOAD(s)
        SBAR();
        f32x4 acc[2][4]; bf16x8 wf[4];
        const unsigned xv = (2 * gi + (q4 >> 1)) & 3, rowa = kq * 4096 + (4 * gi + q4) * 128 + p4 * 8;
        unsigned an[4];
#pragma unroll
        for (int n = 0; n < 4; ++n) { an[n] = rowa + (((n ^ (2 * kh)) ^ xv) * 32); acc[0][n] = (f32x4){0.f, 0.f, 0.f, 0.f}; acc[1][n] = acc[0][n]; }
#define W2A(s, n) (an[n] + (((s) % 9) & 3) * 16384)
#pragma unroll
        for (int n = 0; n < 4; ++n) wf[n] = trfrag(W2A(0, n), W2A(0, n) + 2048);
        const unsigned cl = (32 * kh + 4 * gi) * 2;
        const u32x2 g0 = lds_ld<u32x2>(L_GBO + cl), g1 = lds_ld<u32x2>(L_GBO + cl + 32);
        u32x2 bb0, bb1;
        SBAR();
#pragma unroll
        for (int s = 0; s < 14; ++s) {
            if (s == 11) { bb0 = *(const u32x2*)(b2 + colP); bb1 = *(const u32x2*)(b2 + colP + 16); }
            if (s == 1) WS5(1); if (s == 5) WS5(2); if (s == 8) WS5(3);
            if (s == 5) BAR();
            if (s == 8) BAR();
            if (s == 8) WS5(4);
            if (s + WIN < 14) HLOAD(s + WIN)
            SBAR();
#pragma unroll
            for (int n = 0; n < 4; ++n) { acc[0][n] = MFMA16(wf[n], __builtin_bit_cast(bf16x8, hf[0][s]), acc[0][n]); PIN(acc[0][n]); SBAR();
                acc[1][n] = MFMA16(wf[n], __builtin_bit_cast(bf16x8, hf[1][s]), acc[1][n]); PIN(acc[1][n]); SBAR();
                if (s >= 5 && s <= 7 && 4 * (s - 5) + n < 10) { const int i = 4 * (s - 5) + n; lds_st<u32x4>((i >> 1) * 16384 + (w + 8 * (i & 1)) * 1024 + lane * 16, park[i]); }
                if (s == 3 || s == 7 || s == 12) { an[n] += 65536; PIN(an[n]); } if (s == 8) { an[n] -= 131072; PIN(an[n]); }
                if (s < 13) wf[n] = trfrag(W2A(s + 1, n), W2A(s + 1, n) + 2048); SBAR(); }
        }
#undef W2A
#undef HLOAD
#undef HLD1
        WS5(5);
        BAR();
        STAMP(17);
        { const unsigned ex = w * 6144 + lane * 16;
          lds_st<f32x4>(ex, acc[0][2]); lds_st<f32x4>(ex + 1024, acc[0][3]); lds_st<f32x4>(ex + 2048, acc[1][0]); lds_st<f32x4>(ex + 3072, acc[1][1]); lds_st<f32x4>(ex + 4096, acc[1][2]); lds_st<f32x4>(ex + 5120, acc[1][3]); }
        BAR();
        const unsigned e4 = (w ^ 4) * 6144 + lane * 16, e1 = (w ^ 1) * 6144 + 2048 + lane * 16, e5 = (w ^ 5) * 6144 + 4096 + lane * 16;
        const f32x4 a0 = (acc[0][0] + lds_ld<f32x4>(e4)) + (lds_ld<f32x4>(e1) + lds_ld<f32x4>(e5)), a1 = (acc[0][1] + lds_ld<f32x4>(e4 + 1024)) + (lds_ld<f32x4>(e1 + 1024) + lds_ld<f32x4>(e5 + 1024));
        float y0[4], y1[4];
#pragma unroll
        for (int r = 0; r < 4; ++r) {
            const unsigned ug0 = g0[r >> 1], ug1 = g1[r >> 1], ub0 = bb0[r >> 1], ub1 = bb1[r >> 1], ux0 = x1p0[r >> 1], ux1 = x1p1[r >> 1];
            y0[r] = fmaf((r & 1) ? bf_hi(ug0) : bf_lo(ug0), a0[r] + ((r & 1) ? bf_hi(ub0) : bf_lo(ub0)), (r & 1) ? bf_hi(ux0) : bf_lo(ux0));
            y1[r] = fmaf((r & 1) ? bf_hi(ug1) : bf_lo(ug1), a1[r] + ((r & 1) ? bf_hi(ub1) : bf_lo(ub1)), (r & 1) ? bf_hi(ux1) : bf_lo(ux1));
        }
        *(u32x2*)(out + tokP * 512 + colP) = (u32x2){pk2(y0[0], y0[1]), pk2(y0[2], y0[3])};
        *(u32x2*)(out + tokP * 512 + colP + 16) = (u32x2){pk2(y1[0], y1[1]), pk2(y1[2], y1[3])};
    }
    if (b == 0 && w == 0) {
        for (unsigned sp = 0; sp < SPIN_CAP; ++sp) { asm volatile("" ::: "memory"); const u32x4 v = __builtin_amdgcn_raw_buffer_load_b128(rws, WS_HELLO + lane * 16, 0, 16); if (__all(v[0] == epoch && v[1] == epoch && v[2] == epoch && v[3] == epoch)) break; }
        if (lane == 0) stg_sc1(ws, WS_E, epoch);
    }
    STAMP(18); STAMPC(21);
    STAMP_OUT();
    asm volatile("fb_code_end:");
}

extern "C" void kernel_launch(void* const* d_in, const int* in_sizes, int n_in, void* d_out, int out_size, void* d_ws, size_t ws_size, hipStream_t stream) {
    (void)hipFuncSetAttribute((const void*)fused_block, hipFuncAttributeMaxDynamicSharedMemorySize, LDS_BYTES);
    fused_block<<<256, 576, LDS_BYTES, stream>>>((const u16*)d_in[0], (const u16*)d_in[1], (const u16*)d_in[2], (const u16*)d_in[4], (const u16*)d_in[5], (const u16*)d_in[6], (const u16*)d_in[7],
                                                 (const u16*)d_in[8], (const u16*)d_in[9], (u16*)d_out, (char*)d_ws);
}
```

```cpp
#include <hip/hip_runtime.h>
#include <hip/hip_bf16.h>
#include <stdint.h>

typedef unsigned short u16;
typedef short bf16x8 __attribute__((ext_vector_type(8)));
typedef short s16x4 __attribute__((ext_vector_type(4)));
typedef float f32x4 __attribute__((ext_vector_type(4)));
typedef float f32x2 __attribute__((ext_vector_type(2)));
typedef float f32x16 __attribute__((ext_vector_type(16)));
typedef unsigned u32x4 __attribute__((ext_vector_type(4)));
typedef unsigned u32x2 __attribute__((ext_vector_type(2)));
typedef __bf16 bf2 __attribute__((ext_vector_type(2)));
#define LAS __attribute__((address_space(3)))
#define GAS __attribute__((address_space(1)))
#define DI __device__ __forceinline__

#define TS 0

constexpr unsigned WS_E = 0, WS_HELLO = 4096, WS_FLAG = 8192;
constexpr unsigned WS_MODU = 0x50000, WS_MODN = 0x60000;
constexpr unsigned WS_PART = 0x80000;
constexpr unsigned WS_KV = 0x100000, WS_O = 0x500000, WS_XG = 0x700000, WS_ST = 0x900000, WS_H = 0xA00000, WS_TS = 0x2000000;
constexpr unsigned WS_BYTES = 0x2100000;
constexpr unsigned L_X = 0, L_WQKV = 57344, L_RED = 155648, L_SAG = 156672, L_STAT = 160768;
constexpr unsigned L_QS = 65536, L_KIMG = 98304, L_VIMG = 122880, L_WO = 0, L_W1 = 44032;
constexpr unsigned L_GB16 = 158720, L_SB16 = 159744, L_UVW = 160768, L_CONST = 162560, L_GAO = 162592, L_GBO = 162720, L_ONES = 162848, L_ZERO2 = 162864, L_DUMMY = 162880;
constexpr unsigned L_TAGM = L_RED + 512, L_TAGL = L_RED + 544, L_TAGC = L_RED + 560;
constexpr unsigned L_RED2 = 81920;
constexpr unsigned L_SCR = 32768;
constexpr unsigned LDS_BYTES = 163840;
constexpr unsigned SPIN_CAP = 1u << 16;

DI float bf_lo(unsigned u) { return __uint_as_float(u << 16); }
DI float bf_hi(unsigned u) { return __uint_as_float(u & 0xffff0000u); }
DI unsigned pk2(float a, float b) { bf2 v = {(__bf16)a, (__bf16)b}; return __builtin_bit_cast(unsigned, v); }
DI float rbf(float a) { return bf_lo(pk2(a, a)); }
DI void dma16(unsigned voff, const void* sbase, unsigned lds) { asm volatile("s_mov_b32 m0, %1\n\ts_nop 0\n\tglobal_load_lds_dwordx4 %0, %2" ::"v"(voff), "s"(lds), "s"(sbase) : "memory"); }
DI void dma16c(unsigned voff, const void* sbase, unsigned lds) { asm volatile("s_mov_b32 m0, %1\n\ts_nop 0\n\tglobal_load_lds_dwordx4 %0, %2 sc1" ::"v"(voff), "s"(lds), "s"(sbase) : "memory"); }
DI void dma4(unsigned v0, unsigned v1, unsigned v2, unsigned v3, const void* sbase, unsigned lds) {
    asm volatile("s_mov_b32 m0, %5\n\ts_nop 0\n\tglobal_load_lds_dwordx4 %0, %4\n\tglobal_load_lds_dwordx4 %1, %4 offset:1024\n\tglobal_load_lds_dwordx4 %2, %4 offset:2048\n\tglobal_load_lds_dwordx4 %3, %4 offset:3072"
                 ::"v"(v0), "v"(v1), "v"(v2), "v"(v3), "s"(sbase), "s"(lds) : "memory"); }
DI void dma3(unsigned v0, unsigned v1, unsigned v2, const void* sbase, unsigned lds) {
    asm volatile("s_mov_b32 m0, %4\n\ts_nop 0\n\tglobal_load_lds_dwordx4 %0, %3\n\tglobal_load_lds_dwordx4 %1, %3 offset:1024\n\tglobal_load_lds_dwordx4 %2, %3 offset:2048"
                 ::"v"(v0), "v"(v1), "v"(v2), "s"(sbase), "s"(lds) : "memory"); }
DI void dma4lin_c(unsigned v, const void* sbase, unsigned lds) {
    asm volatile("s_mov_b32 m0, %2\n\ts_nop 0\n\tglobal_load_lds_dwordx4 %0, %1 sc1\n\tglobal_load_lds_dwordx4 %0, %1 offset:1024 sc1\n\tglobal_load_lds_dwordx4 %0, %1 offset:2048 sc1\n\tglobal_load_lds_dwordx4 %0, %1 offset:3072 sc1"
                 ::"v"(v), "s"(sbase), "s"(lds) : "memory"); }
DI void lds_wait_tags(unsigned a, int n, unsigned tag) {
    for (unsigned sp = 0; sp < SPIN_CAP; ++sp) { asm volatile("" ::: "memory"); const unsigned t0 = *(const volatile LAS unsigned*)(size_t)a, t1 = *(const volatile LAS unsigned*)(size_t)(a + 4 * (n - 1));
        if (__builtin_amdgcn_readfirstlane(t0) == tag && __builtin_amdgcn_readfirstlane(t1) == tag) break; __builtin_amdgcn_s_sleep(1); }
    asm volatile("" ::: "memory"); }
DI void warm4(unsigned voff, const void* sbase) { asm volatile("s_mov_b32 m0, %1\n\ts_nop 0\n\tglobal_load_lds_dword %0, %2" ::"v"(voff), "s"(L_DUMMY), "s"(sbase) : "memory"); }
#define WSP 4
#define WS1(i) do { if (WSP == 1) WSTAMP(i); } while (0)
#define WS2(i) do { if (WSP == 2) WSTAMP(i); } while (0)
#define WS3(i) do { if (WSP == 3) WSTAMP(i); } while (0)
#define WS4(i) do { if (WSP == 4) WSTAMP(i); } while (0)
#define WS5(i) do { if (WSP == 5) WSTAMP(i); } while (0)
#define WS6(i) do { if (WSP == 6) WSTAMP(i); } while (0)
#define FRESH(v) int v = threadIdx.x & 63; asm volatile("" : "+v"(v))
#define SBAR() __builtin_amdgcn_sched_barrier(0)
#define PIN(x) asm volatile("" : "+v"(x))
#define BAR() asm volatile("s_waitcnt lgkmcnt(0)\n\ts_barrier" ::: "memory")
#define VMWAIT(n) asm volatile("s_waitcnt vmcnt(" #n ")" ::: "memory")
DI unsigned ldg_sc1(const char* ws, unsigned off) { return __hip_atomic_load((GAS unsigned*)(ws + off), __ATOMIC_RELAXED, __HIP_MEMORY_SCOPE_AGENT); }
DI unsigned long long ldg64_sc1(const char* ws, unsigned off) { return __hip_atomic_load((GAS unsigned long long*)(ws + off), __ATOMIC_RELAXED, __HIP_MEMORY_SCOPE_AGENT); }
DI void stg_sc1(char* ws, unsigned off, unsigned v) { __hip_atomic_store((GAS unsigned*)(ws + off), v, __ATOMIC_RELAXED, __HIP_MEMORY_SCOPE_AGENT); }
DI void stg64_sc1(char* ws, unsigned off, unsigned long long v) { __hip_atomic_store((GAS unsigned long long*)(ws + off), v, __ATOMIC_RELAXED, __HIP_MEMORY_SCOPE_AGENT); }
template <class T> DI T lds_ld(unsigned a) { return *(const LAS T*)(size_t)a; }
template <class T> DI void lds_st(unsigned a, T v) { *(LAS T*)(size_t)a = v; }
DI s16x4 lds_tr(unsigned a) { return __builtin_amdgcn_ds_read_tr16_b64_v4i16((LAS s16x4*)(size_t)a); }
DI bf16x8 trfrag(unsigned a0, unsigned a1) { s16x4 lo = lds_tr(a0), hi = lds_tr(a1); return __builtin_shufflevector(lo, hi, 0, 1, 2, 3, 4, 5, 6, 7); }
#define MFMA32(a, b, c) __builtin_amdgcn_mfma_f32_32x32x16_bf16((a), (b), (c), 0, 0, 0)
#define MFMA16(a, b, c) __builtin_amdgcn_mfma_f32_16x16x32_bf16((a), (b), (c), 0, 0, 0)
template <int N> DI float bcast(float v) { return __builtin_bit_cast(float, __builtin_amdgcn_mov_dpp(__builtin_bit_cast(int, v), 0x150 + N, 0xf, 0xf, true)); }
template <int N> DI float modu(float t, float gv, float sv) {
#pragma clang fp contract(off)
    const float p = bcast<N>(gv) * t; return bcast<N>(sv) + p; }
template <int N> DI unsigned hdw(unsigned x, float r, float n, float gv, float c, float d) {
    unsigned o; float a, b2;
    asm("v_lshlrev_b32 %1, 16, %5\n\tv_and_b32 %2, 0xffff0000, %5\n\tv_fma_f32 %1, %1, %6, %7\n\tv_fma_f32 %2, %2, %6, %7\n\t"
        "v_fmac_f32_dpp %3, %8, %1 row_newbcast:%c9 row_mask:0xf bank_mask:0xf\n\tv_fmac_f32_dpp %4, %8, %2 row_newbcast:%c10 row_mask:0xf bank_mask:0xf\n\t"
        "v_cvt_pk_bf16_f32 %0, %3, %4"
        : "=v"(o), "=&v"(a), "=&v"(b2), "+v"(c), "+v"(d) : "v"(x), "v"(r), "v"(n), "v"(gv), "i"(N), "i"(N + 1));
    return o; }
template <int C> DI float dppf(float v) { return __builtin_bit_cast(float, __builtin_amdgcn_update_dpp(0, __builtin_bit_cast(int, v), C, 0xf, 0xf, true)); }
DI float rowsum16(float v) { v += dppf<0xB1>(v); v += dppf<0x4E>(v); v += dppf<0x141>(v); v += dppf<0x140>(v); return v; }
DI float xsum32(float v) { auto r = __builtin_amdgcn_permlane32_swap(__float_as_uint(v), __float_as_uint(v), false, false); return __uint_as_float(r[0]) + __uint_as_float(r[1]); }
DI float xmax32(float v) { auto r = __builtin_amdgcn_permlane32_swap(__float_as_uint(v), __float_as_uint(v), false, false); return fmaxf(__uint_as_float(r[0]), __uint_as_float(r[1])); }
DI float xsum16(float v) { auto r = __builtin_amdgcn_permlane16_swap(__float_as_uint(v), __float_as_uint(v), false, false); return __uint_as_float(r[0]) + __uint_as_float(r[1]); }
DI unsigned long long mt() { unsigned long long t; asm volatile("s_memtime %0\n\ts_waitcnt lgkmcnt(0)" : "=s"(t)::"memory"); return t; }
DI unsigned long long rt() { unsigned long long t; asm volatile("s_memrealtime %0\n\ts_waitcnt lgkmcnt(0)" : "=s"(t)::"memory"); return t; }
#define STAMP(i) do {} while (0)
#define STAMP_OUT() do {} while (0)
#define STAMPC(i) do {} while (0)
#define WSTAMP(i) do {} while (0)

DI void poll_flags(const char* ws, unsigned off, int n, unsigned epoch, int lane) {
    const unsigned o = off + (unsigned)(lane < n ? lane : 0) * 256u;
    for (unsigned sp = 0; sp < SPIN_CAP; ++sp) { unsigned f = ldg_sc1(ws, o); if (__all(f == epoch)) break; }
}

DI void mod_part(const u16* __restrict__ cvec, const u16* __restrict__ wmod, char* ws, unsigned epoch, int lane, int slice, int kp) {
    const unsigned row0 = 64 * kp + 4 * (lane >> 2), o = row0 * 6144u + (unsigned)slice * 64u + (lane & 3) * 16u;
    const u32x2 cv = *(const u32x2*)(cvec + row0);
    u32x4 wm[4];
#pragma unroll
    for (int i = 0; i < 4; ++i) wm[i] = *(const u32x4*)((const char*)wmod + o + i * 6144u);
    SBAR();
    unsigned sp[2];
#pragma unroll
    for (int r = 0; r < 2; ++r) { const unsigned cu = cv[r]; const float c0 = bf_lo(cu), c1 = bf_hi(cu);
        sp[r] = pk2(c0 * __builtin_amdgcn_rcpf(1.f + __builtin_amdgcn_exp2f(-1.44269504089f * c0)), c1 * __builtin_amdgcn_rcpf(1.f + __builtin_amdgcn_exp2f(-1.44269504089f * c1))); }
    float a[8] = {0.f, 0.f, 0.f, 0.f, 0.f, 0.f, 0.f, 0.f};
#pragma unroll
    for (int r = 0; r < 2; ++r)
#pragma unroll
        for (int i = 0; i < 4; ++i) { const unsigned u0 = wm[2 * r][i], u1 = wm[2 * r + 1][i], s_ = sp[r];
            const unsigned lo = __builtin_amdgcn_perm(u1, u0, 0x05040100u), hi = __builtin_amdgcn_perm(u1, u0, 0x07060302u);
            a[2 * i] = __builtin_amdgcn_fdot2_f32_bf16(__builtin_bit_cast(bf2, lo), __builtin_bit_cast(bf2, s_), a[2 * i], false);
            a[2 * i + 1] = __builtin_amdgcn_fdot2_f32_bf16(__builtin_bit_cast(bf2, hi), __builtin_bit_cast(bf2, s_), a[2 * i + 1], false); }
    float b4[4], b2[2];
#pragma unroll
    for (int i = 0; i < 4; ++i) { auto r = __builtin_amdgcn_permlane32_swap(__float_as_uint(a[i]), __float_as_uint(a[i + 4]), false, false); b4[i] = __uint_as_float(r[0]) + __uint_as_float(r[1]); }
#pragma unroll
    for (int i = 0; i < 2; ++i) { auto r = __builtin_amdgcn_permlane16_swap(__float_as_uint(b4[i]), __float_as_uint(b4[i + 2]), false, false); float v = __uint_as_float(r[0]) + __uint_as_float(r[1]);
        v += dppf<0x124>(v); v += dppf<0x128>(v); b2[i] = v; }
    const __amdgpu_buffer_rsrc_t rws = __builtin_amdgcn_make_buffer_rsrc(ws, 0, WS_BYTES, 0x00020000);
    if ((lane & 12) == 0) __builtin_amdgcn_raw_buffer_store_b128((u32x4){__float_as_uint(b2[0]), epoch, __float_as_uint(b2[1]), epoch}, rws,
                                                                 WS_PART + (slice * 8 + kp) * 256 + ((lane & 3) * 4 + (lane >> 5) * 2 + ((lane >> 4) & 1)) * 16, 0, 16);
}
struct PartSum { u32x4 v0, v1; };
DI void mod_sum_load(PartSum& p, char* ws, int lane, int slice) {
    const __amdgpu_buffer_rsrc_t rws = __builtin_amdgcn_make_buffer_rsrc(ws, 0, WS_BYTES, 0x00020000);
    const unsigned o = WS_PART + (slice * 8 + (lane >> 3)) * 256 + (lane & 7) * 16;
    p.v0 = __builtin_amdgcn_raw_buffer_load_b128(rws, o, 0, 16); p.v1 = __builtin_amdgcn_raw_buffer_load_b128(rws, o + 128, 0, 16); }
DI void mod_sum_finish(PartSum& p, char* ws, unsigned epoch, int lane, int slice) {
    for (unsigned sp = 0; sp < SPIN_CAP; ++sp) { if (__all(p.v0[1] == epoch && p.v0[3] == epoch && p.v1[1] == epoch && p.v1[3] == epoch)) break; asm volatile("" ::: "memory"); mod_sum_load(p, ws, lane, slice); }
    float s[4] = {__uint_as_float(p.v0[0]), __uint_as_float(p.v0[2]), __uint_as_float(p.v1[0]), __uint_as_float(p.v1[2])};
#pragma unroll
    for (int i = 0; i < 4; ++i) { float v = s[i]; v += dppf<0x128>(v); s[i] = xsum32(xsum16(v)); }
    const unsigned long long g0 = ((unsigned long long)epoch << 32) | pk2(s[0], s[1]), g1 = ((unsigned long long)epoch << 32) | pk2(s[2], s[3]);
    const unsigned base = slice < 32 ? WS_MODU + slice * 128 : WS_MODN + (slice - 32) * 128, rs = slice < 32 ? 4096 : 8192, rep = lane >> 3, o = base + (lane & 7) * 8;
    stg64_sc1(ws, o + rep * rs, g0); stg64_sc1(ws, o + 64 + rep * rs, g1); stg64_sc1(ws, o + (rep + 8) * rs, g0); stg64_sc1(ws, o + 64 + (rep + 8) * rs, g1);
}

__global__ __launch_bounds__(576, 3) void fused_block(const u16* __restrict__ x, const u16* __restrict__ wqkv, const u16* __restrict__ wo, const u16* __restrict__ w1, const u16* __restrict__ b1,
                                                      const u16* __restrict__ w2, const u16* __restrict__ b2, const u16* __restrict__ cvec, const u16* __restrict__ wmod, u16* __restrict__ out, char* __restrict__ ws) {
    const unsigned epoch = *(const unsigned*)(ws + WS_E) + 1u;
    const int tid = threadIdx.x, lane = tid & 63, w = __builtin_amdgcn_readfirstlane(tid >> 6);
    const int b = blockIdx.x, xl = b & 7, idx = b >> 3, g = 2 * xl + (idx >> 4), j = idx & 15;
    const int ct = j & 7, rh = j >> 3;
    unsigned long long code_pc; unsigned code_len;
    asm volatile("s_getpc_b64 %0\nfb_code_begin:\n\ts_mov_b32 %1, fb_code_end-fb_code_begin" : "=s"(code_pc), "=s"(code_len));
    STAMP(0); STAMPC(20);
    if (w == 8) {
        {
            const unsigned o = ((unsigned)idx * 32u + (lane & 31)) * 64u;
            warm4((int)o < (int)code_len ? o : 0u, (const void*)code_pc);
        }
        {
            const char* px_ = (const char*)x + b * 8192; const char* pw_ = (const char*)wqkv + b * 6144;
            warm4(lane * 64u, px_); warm4(lane * 64u + 4096u, px_); warm4(lane * 64u, pw_); warm4(lane < 32 ? lane * 64u + 4096u : 0u, pw_); }
        {
            unsigned o_ = lane < 4 ? WS_FLAG + (lane * 256 + b) * 256 : lane < 10 ? WS_MODU + b * 768 + (lane - 4) * 128 : lane < 22 ? WS_ST + b * 1536 + (lane - 10) * 128 : lane < 28 ? WS_PART + b * 768 + (lane - 22) * 128 : WS_FLAG + b * 256;
            warm4(o_, ws); }
        if (lane == 0) stg_sc1(ws, WS_HELLO + b * 4, epoch);
        __builtin_amdgcn_s_setprio(3);
        const unsigned sw0 = (lane >> 3) * 1024u + ct * 128u + (((lane & 7) >> 1) ^ ((lane >> 4) & 3)) * 32u + (lane & 1) * 16u, sw1 = sw0 + 7168, sw2 = sw0 + 14336, sw3 = sw0 + 21504;
        unsigned w1o[7], wqo[3];
#pragma unroll
        for (int r = 0; r < 7; ++r) { const unsigned u = r * 64 + lane, row = u / 14, c = u % 14; w1o[r] = row * 3584u + 224u * j + c * 16u - (r & 3) * 1024u; }
#pragma unroll
        for (int r = 0; r < 3; ++r) { const unsigned u = r * 64 + lane, row = u / 12, c = u % 12; wqo[r] = row * 3072u + (c >> 2) * 1024u + 64u * j + (c & 3) * 16u - r * 1024u; }
#define RUN128(base, first, n4, lds0) { const char* p_ = (const char*)(base) + (size_t)(first) * 8192; unsigned l_ = (lds0); _Pragma("unroll 2") for (int i_ = 0; i_ < (n4); ++i_) { dma4(sw0, sw1, sw2, sw3, p_, l_); p_ += 32768; l_ += 4096; } }
#define W1GRPS(k0, k1) { const char* p_ = (const char*)w1 + (size_t)(k0) * 114688; unsigned l_ = L_W1 + 7 * (k0) * 1024; _Pragma("unroll 1") for (int k_ = (k0); k_ < (k1); ++k_) { \
            dma4(w1o[0], w1o[1], w1o[2], w1o[3], p_, l_); dma3(w1o[4], w1o[5], w1o[6], p_, l_ + 4096); p_ += 114688; l_ += 7168; } }
#define W2CHUNKS(c0, c1, slot0) { _Pragma("unroll 1") for (int c_ = (c0); c_ < (c1); ++c_) { const char* p_ = (const char*)w2 + (size_t)c_ * 32768; const unsigned l_ = ((slot0) + c_ - (c0)) * 16384; \
            dma4(sw0, sw1, sw2, sw3, p_, l_); dma4(sw0, sw1, sw2, sw3, p_ + 56 * 8192, l_ + 4096); dma4(sw0, sw1, sw2, sw3, p_ + 112 * 8192, l_ + 8192); dma4(sw0, sw1, sw2, sw3, p_ + 168 * 8192, l_ + 12288); } }
#define WQGRPS(k0, k1) { const char* p_ = (const char*)wqkv + (size_t)(k0) * 49152; unsigned l_ = L_WQKV + 3 * (k0) * 1024; _Pragma("unroll 2") for (int k_ = (k0); k_ < (k1); ++k_) { dma3(wqo[0], wqo[1], wqo[2], p_, l_); p_ += 49152; l_ += 3072; } }
#define WARM128(base, row0, n, GR) { const char* p_ = (const char*)(base) + (size_t)(row0) * 1024; const unsigned v_ = GR == 64 ? (lane >> 1) * 1024u + ct * 128u + (lane & 1) * 64u : lane * 1024u + ct * 128u; \
            _Pragma("unroll 1") for (int i_ = 0; i_ < (n); ++i_) { warm4(v_, p_); p_ += (GR == 64 ? 32768 : 65536); } }
#define WARMW1(row0, n16) { const char* p_ = (const char*)w1 + (size_t)(row0) * 3584; const unsigned c_ = (224u * j) & ~63u, v_ = (lane >> 2) * 3584u + c_ + (lane & 3) * 64u; \
            _Pragma("unroll 1") for (int i_ = 0; i_ < (n16); ++i_) { warm4(v_, p_); p_ += 16 * 3584; } }
        __builtin_amdgcn_s_barrier();
        __builtin_amdgcn_s_barrier();
        WQGRPS(14, 24)
        __builtin_amdgcn_s_barrier();
        WQGRPS(0, 14)
        VMWAIT(0);
        __builtin_amdgcn_s_barrier();
        {
            const char* p_ = b < 32 ? (const char*)wo + b * 16384 : b < 144 ? (const char*)w1 + (b - 32) * 16384 : (const char*)w2 + (b - 144) * 16384;
#pragma unroll
            for (int i = 0; i < 4; ++i) warm4(lane * 64u + i * 4096u, p_); }
        mod_part(cvec, wmod, ws, epoch, lane, 32 + 8 * xl + (idx & 3), idx >> 2); mod_part(cvec, wmod, ws, epoch, lane, 36 + 8 * xl + (idx & 3), idx >> 2);
        if (idx < 4) { PartSum p; mod_sum_load(p, ws, lane, 32 + 8 * xl + idx); mod_sum_finish(p, ws, epoch, lane, 32 + 8 * xl + idx); mod_sum_load(p, ws, lane, 36 + 8 * xl + idx); mod_sum_finish(p, ws, epoch, lane, 36 + 8 * xl + idx); }
        __builtin_amdgcn_s_barrier();
        __builtin_amdgcn_s_barrier();
        if (g >= 1) {
            const unsigned fo = WS_FLAG + (((g >= 2 ? g - 2 + (lane & 1) : 0) * 16 + j) * 256);
            for (unsigned sp = 0; sp < SPIN_CAP; ++sp) { unsigned f = ldg_sc1(ws, fo); if (__all(f == epoch)) break; }
            const unsigned lo16 = lane * 16;
#pragma unroll 1
            for (int src = (g >= 2 ? 0 : 1); src < 2; ++src) {
                const char* p_ = ws + WS_KV + (size_t)((g - 2 + src) * 16 + j) * 16384;
                dma4lin_c(lo16, p_, L_KIMG + src * 8192); dma4lin_c(lo16, p_ + 4096, L_KIMG + src * 8192 + 4096);
                dma4lin_c(lo16, p_ + 8192, L_VIMG + src * 8192); dma4lin_c(lo16, p_ + 12288, L_VIMG + src * 8192 + 4096);
            }
        }
        VMWAIT(0);
        __builtin_amdgcn_s_barrier();
        RUN128(wo, 0, 16, L_WO)
        W1GRPS(3, 7)
        W1GRPS(15, 16)
        WARM128(w2, 448 * (rh + 2 * (g & 1)), 7, 128)
        __builtin_amdgcn_s_barrier();
        W1GRPS(7, 15)
        VMWAIT(56);
        __builtin_amdgcn_s_barrier();
        __builtin_amdgcn_s_barrier();
        __builtin_amdgcn_s_barrier();
        __builtin_amdgcn_s_barrier();
        W1GRPS(0, 3)
        __builtin_amdgcn_s_barrier();
        VMWAIT(0);
        __builtin_amdgcn_s_barrier();
        __builtin_amdgcn_s_barrier();
        W2CHUNKS(0, 2, 0)
        __builtin_amdgcn_s_barrier();
        __builtin_amdgcn_s_barrier();
        W2CHUNKS(2, 6, 2)
        __builtin_amdgcn_s_barrier();
        W2CHUNKS(6, 9, 6)
        VMWAIT(48);
        __builtin_amdgcn_s_barrier();
        VMWAIT(0);
        __builtin_amdgcn_s_barrier();
        __builtin_amdgcn_s_barrier();
        __builtin_amdgcn_s_barrier();
        __builtin_amdgcn_s_barrier();
        return;
    }
    const __amdgpu_buffer_rsrc_t rws = __builtin_amdgcn_make_buffer_rsrc(ws, 0, WS_BYTES, 0x00020000);
    const int m = w & 3, kh = w >> 2;
#define XTILE(t) { const unsigned xo = (unsigned)(128 * g + 32 * (t)) * 1024u + ((unsigned)lane << 4); \
        _Pragma("unroll 2") for (int i = 0; i < 32; i += 4) dma4(xo ^ (unsigned)((i & 15) << 4), xo ^ (unsigned)(((i & 15) + 1) << 4), xo ^ (unsigned)(((i & 15) + 2) << 4), xo ^ (unsigned)(((i & 15) + 3) << 4), \
                                                                 (const char*)x + i * 1024, L_X + (3 - (t)) * 32768 + i * 1024); }
    PartSum psum;
    if (w >= 4 && w < 7) XTILE(w - 4)
    if (w == 0) { lds_st<u32x4>(L_ONES, (u32x4){0x3F803F80u, 0x3F803F80u, 0x3F803F80u, 0x3F803F80u}); lds_st<u32x4>(L_ZERO2, (u32x4){0u, 0u, 0u, 0u}); }
    if (w == 3) { __builtin_amdgcn_s_setprio(2); mod_part(cvec, wmod, ws, epoch, lane, 4 * xl + (idx & 3), idx >> 2); __builtin_amdgcn_s_setprio(0); }
    STAMP(1); WS6(0);
    bf16x8 qf; u32x4 kpk, vpk; float qown[8];
    {
    const int h = lane >> 5, gi = lane >> 4, q4 = (lane & 15) >> 2, p4 = lane & 3;
    u32x4 xr[16];
#define XFRAGS() { const unsigned tok = lane & 31, rb = L_X + (3 - m) * 32768 + tok * 1024 + kh * 512; \
        _Pragma("unroll") for (int s = 0; s < 16; ++s) xr[s] = lds_ld<u32x4>(rb + (s >> 3) * 256 + ((((2 * (s & 7) + h) ^ (tok & 15))) << 4)); \
        _Pragma("unroll") for (int s = 0; s < 16; ++s) PIN(xr[s]); }
    if (w == 4) VMWAIT(0);
    BAR();
    WS6(1);
    if (w == 4) XTILE(3)
    if (w == 7) {
        unsigned wq[3];
#pragma unroll
        for (int r = 0; r < 3; ++r) { const unsigned u = r * 64 + lane, row = u / 12, c = u % 12; wq[r] = row * 3072u + (c >> 2) * 1024u + 64u * j + (c & 3) * 16u - r * 1024u; }
#pragma unroll 2
        for (int k = 24; k < 32; ++k) dma3(wq[0], wq[1], wq[2], (const char*)wqkv + (size_t)k * 49152, L_WQKV + 3 * k * 1024); }
    if (w == 3 && idx < 4) mod_sum_load(psum, ws, lane, 4 * xl + idx);
    if (m == 0) XFRAGS()
    if (w == 5 || w == 6) VMWAIT(0);
    BAR();
    WS6(2);
    if (w == 3 && idx < 4) mod_sum_finish(psum, ws, epoch, lane, 4 * xl + idx);
    if (m == 1 || m == 2) XFRAGS()
    if (w == 4) VMWAIT(0);
    BAR();
    if (m == 3) XFRAGS()
#undef XFRAGS
#undef XTILE
    STAMP(2);
    const unsigned gmo = WS_MODU + g * 4096 + (tid & 255) * 16; u32x4 gmv;
    if (w < 4) gmv = __builtin_amdgcn_raw_buffer_load_b128(rws, gmo, 0, 16);
    SBAR();
    {
        float s0 = 0.f, s1 = 0.f, s2 = 0.f, s3 = 0.f; const bf2 one = {(__bf16)1.0f, (__bf16)1.0f};
#pragma unroll
        for (int s = 0; s < 16; ++s)
#pragma unroll
            for (int i = 0; i < 4; i += 2) { const unsigned ua = xr[s][i], ub = xr[s][i + 1]; const bf2 va = __builtin_bit_cast(bf2, ua), vb = __builtin_bit_cast(bf2, ub);
                s0 = __builtin_amdgcn_fdot2_f32_bf16(va, one, s0, false); s1 = __builtin_amdgcn_fdot2_f32_bf16(va, va, s1, false);
                s2 = __builtin_amdgcn_fdot2_f32_bf16(vb, one, s2, false); s3 = __builtin_amdgcn_fdot2_f32_bf16(vb, vb, s3, false); }
        s0 = xsum32(s0 + s2); s1 = xsum32(s1 + s3);
        if (lane < 32) lds_st<u32x2>(L_STAT + (kh * 128 + 32 * m + lane) * 8, (u32x2){__float_as_uint(s0), __float_as_uint(s1)});
    }
    WS6(3);
    if (w < 4) {
        for (unsigned sp = 0; sp < SPIN_CAP; ++sp) { if (__all(gmv[1] == epoch && gmv[3] == epoch)) break; asm volatile("" ::: "memory"); gmv = __builtin_amdgcn_raw_buffer_load_b128(rws, gmo, 0, 16); }
        const unsigned u0 = gmv[0], u2 = gmv[2]; f32x4 f = {bf_lo(u0), bf_hi(u0), bf_lo(u2), bf_hi(u2)};
        if (tid >= 128) { f[0] = rbf(1.f + f[0]); f[1] = rbf(1.f + f[1]); f[2] = rbf(1.f + f[2]); f[3] = rbf(1.f + f[3]); }
        lds_st<f32x4>(L_SAG + tid * 16, f);
    }
    STAMP(3);
    VMWAIT(0);
    BAR();
    STAMP(4); STAMPC(22); WS1(0);
    float rstd, nmr, gv[2]; f32x4 sa0[2], sa1[2];
    const unsigned sga = L_SAG + (256 * kh + 16 * ((lane & 15) >> 3) + 8 * h + (lane & 7)) * 4;
    const unsigned saa = L_SAG + (256 * kh + 8 * h) * 4;
    { const u32x2 a = lds_ld<u32x2>(L_STAT + (32 * m + (lane & 31)) * 8), c2 = lds_ld<u32x2>(L_STAT + (128 + 32 * m + (lane & 31)) * 8);
      gv[0] = lds_ld<float>(sga + 2048);
      sa0[0] = lds_ld<f32x4>(saa); sa1[0] = lds_ld<f32x4>(saa + 16);
      const float s0 = __uint_as_float(a[0]) + __uint_as_float(c2[0]), s1 = __uint_as_float(a[1]) + __uint_as_float(c2[1]);
      const float mean = s0 * (1.f / 512.f), var = fmaxf(s1 * (1.f / 512.f) - mean * mean, 0.f);
      rstd = __builtin_amdgcn_rsqf(var + 1e-5f); nmr = -mean * rstd;
      asm volatile("s_nop 1" : "+v"(gv[0])); }
    f32x16 aQ, aK, aV;
#pragma unroll
    for (int i = 0; i < 16; ++i) { aQ[i] = 0.f; aK[i] = 0.f; aV[i] = 0.f; }
    {
        const unsigned rowa = L_WQKV + (256 * kh + 8 * (gi >> 1) + q4) * 192 + p4 * 8;
        const unsigned wqa = rowa + (((gi & 1) ^ kh) * 32), wva = rowa + 128 + (gi & 1) * 32;
        bf16x8 fk, fv;
#define HDW(s, i) hdw<8 * ((s) & 1) + 2 * (i)>(xr[s][i], rstd, nmr, gv[((s) >> 1) & 1], (i) < 2 ? sa0[(s) & 1][2 * (i) & 3] : sa1[(s) & 1][2 * (i) & 3], (i) < 2 ? sa0[(s) & 1][(2 * (i) + 1) & 3] : sa1[(s) & 1][(2 * (i) + 1) & 3])
        fk = trfrag(wqa + 64, wqa + 64 + 768); fv = trfrag(wva, wva + 768);
        sa0[1] = lds_ld<f32x4>(saa + 64); sa1[1] = lds_ld<f32x4>(saa + 64 + 16);
        { const unsigned a0 = HDW(0, 0), a1 = HDW(0, 1), a2 = HDW(0, 2), a3 = HDW(0, 3); xr[0] = (u32x4){a0, a1, a2, a3}; }
        asm volatile("s_nop 1" : "+v"(xr[0])); SBAR();
#define QKV_STEP(s, sn) { const bf16x8 hb = __builtin_bit_cast(bf16x8, xr[s]); unsigned n0, n1, n2, n3; \
            aK = MFMA32(fk, hb, aK); PIN(aK); SBAR(); if ((sn) < 16) { fk = trfrag(wqa + (sn) * 3072 + 64, wqa + (sn) * 3072 + 64 + 768); if ((s) + 2 < 16) sa0[(s) & 1] = lds_ld<f32x4>(saa + ((s) + 2) * 64); \
                n0 = HDW((sn) & 15, 0); n1 = HDW((sn) & 15, 1); PIN(n0); PIN(n1); } SBAR(); \
            aV = MFMA32(hb, fv, aV); PIN(aV); SBAR(); if ((sn) < 16) { fv = trfrag(wva + (sn) * 3072, wva + (sn) * 3072 + 768); if ((s) + 2 < 16) sa1[(s) & 1] = lds_ld<f32x4>(saa + ((s) + 2) * 64 + 16); \
                n2 = HDW((sn) & 15, 2); n3 = HDW((sn) & 15, 3); PIN(n2); PIN(n3); xr[(sn) & 15] = (u32x4){n0, n1, n2, n3}; } \
            if ((s) % 2 == 0 && (s) + 2 < 16) { gv[(((s) >> 1) + 1) & 1] = lds_ld<float>(sga + 2048 + (((s) >> 1) + 1) * 128); } SBAR(); }
        WS1(1);
        QKV_STEP(0, 1) QKV_STEP(1, 2) QKV_STEP(2, 3) QKV_STEP(3, 4) QKV_STEP(4, 5) QKV_STEP(5, 6) QKV_STEP(6, 7) QKV_STEP(7, 8)
        QKV_STEP(8, 9) QKV_STEP(9, 10) QKV_STEP(10, 11) QKV_STEP(11, 12) QKV_STEP(12, 13) QKV_STEP(13, 14) QKV_STEP(14, 15) QKV_STEP(15, 16)
#undef QKV_STEP
#undef HDW
        STAMP(5); STAMPC(23); WS1(2);
        float vs[8], vk[8];
#pragma unroll
        for (int i = 0; i < 8; ++i) { float lo = aV[i], hi = aV[8 + i]; PIN(lo); PIN(hi); vs[i] = kh ? lo : hi; vk[i] = kh ? hi : lo; }
        const unsigned ex = w * 6144 + lane * 16;
        lds_st<f32x4>(ex, (f32x4){aK[8], aK[9], aK[10], aK[11]}); lds_st<f32x4>(ex + 1024, (f32x4){aK[12], aK[13], aK[14], aK[15]});
        lds_st<f32x4>(ex + 2048, (f32x4){vs[0], vs[1], vs[2], vs[3]}); lds_st<f32x4>(ex + 3072, (f32x4){vs[4], vs[5], vs[6], vs[7]});
        bf16x8 fq = trfrag(wqa, wqa + 768), fq1 = trfrag(wqa + 3072, wqa + 3072 + 768);
        BAR();
        WS1(3);
        const unsigned ep = (w ^ 4) * 6144 + lane * 16;
        { const f32x4 k0 = lds_ld<f32x4>(ep), k1 = lds_ld<f32x4>(ep + 1024), v0 = lds_ld<f32x4>(ep + 2048), v1 = lds_ld<f32x4>(ep + 3072);
          u32x4 kp, vp;
          kp[0] = pk2(aK[0] + k0[0], aK[1] + k0[1]); kp[1] = pk2(aK[2] + k0[2], aK[3] + k0[3]); kp[2] = pk2(aK[4] + k1[0], aK[5] + k1[1]); kp[3] = pk2(aK[6] + k1[2], aK[7] + k1[3]);
          vp[0] = pk2(vk[0] + v0[0], vk[1] + v0[1]); vp[1] = pk2(vk[2] + v0[2], vk[3] + v0[3]); vp[2] = pk2(vk[4] + v1[0], vk[5] + v1[1]); vp[3] = pk2(vk[6] + v1[2], vk[7] + v1[3]);
          const unsigned kvo = WS_KV + (g * 16 + j) * 16384 + (m * 2 + kh) * 1024 + lane * 16;
          __builtin_amdgcn_raw_buffer_store_b128(kp, rws, kvo, 0, 16);
          __builtin_amdgcn_raw_buffer_store_b128(vp, rws, kvo + 8192, 0, 16);
          PIN(kp); PIN(vp); kpk = kp; vpk = vp; }
        SBAR();
#pragma unroll
        for (int s = 0; s < 16; s += 2) {
            aQ = MFMA32(fq, __builtin_bit_cast(bf16x8, xr[s]), aQ); PIN(aQ); SBAR(); if (s + 2 < 16) fq = trfrag(wqa + (s + 2) * 3072, wqa + (s + 2) * 3072 + 768); SBAR();
            aQ = MFMA32(fq1, __builtin_bit_cast(bf16x8, xr[s + 1]), aQ); PIN(aQ); SBAR(); if (s + 3 < 16) fq1 = trfrag(wqa + (s + 3) * 3072, wqa + (s + 3) * 3072 + 768); SBAR(); }
        lds_st<f32x4>(ex + 4096, (f32x4){aQ[8], aQ[9], aQ[10], aQ[11]}); lds_st<f32x4>(ex + 5120, (f32x4){aQ[12], aQ[13], aQ[14], aQ[15]});
#pragma unroll
        for (int i = 0; i < 8; ++i) qown[i] = aQ[i];
    }
    }
    WS1(4);
    VMWAIT(0); WS1(5);
    BAR();
    if (tid == 0) stg_sc1(ws, WS_FLAG + (0 * 256 + g * 16 + j) * 256, epoch);
    STAMP(6);
    { FRESH(lane);
      const unsigned ep = (w ^ 4) * 6144 + 4096 + lane * 16; const f32x4 q0 = lds_ld<f32x4>(ep), q1 = lds_ld<f32x4>(ep + 1024);
      lds_st<u32x4>(L_KIMG + (8 + m) * 2048 + kh * 1024 + lane * 16, kpk);
      lds_st<u32x4>(L_VIMG + (8 + m) * 2048 + kh * 1024 + lane * 16, vpk);
      const float qs = 0.25f * 1.44269504089f; u32x4 qp;
      qp[0] = pk2((qown[0] + q0[0]) * qs, (qown[1] + q0[1]) * qs); qp[1] = pk2((qown[2] + q0[2]) * qs, (qown[3] + q0[3]) * qs);
      qp[2] = pk2((qown[4] + q1[0]) * qs, (qown[5] + q1[1]) * qs); qp[3] = pk2((qown[6] + q1[2]) * qs, (qown[7] + q1[3]) * qs);
      qf = __builtin_bit_cast(bf16x8, qp); PIN(qf); }
    const int tt = w & 3;
    {
    FRESH(lane);
    const int hh = kh, qq = lane & 31, h = lane >> 5;
    const float NEG = -__builtin_inff(), fq0 = (float)(4 * h - qq) - 0.5f;
    f32x16 c0, c8;
#pragma unroll
    for (int r = 0; r < 16; ++r) {
        const float t = fmaf(fq0, 1e30f, (float)((r & 3) + 8 * (r >> 2)) * 1e30f); c0[r] = fminf(t, 0.f); c8[r] = fminf(-t, 0.f); }
    PIN(c0); PIN(c8);
    BAR();
    STAMP(8); WS2(0);
    {
        const unsigned ka = L_KIMG + m * 2048 + hh * 1024 + lane * 16;
        const bool isd = ((lane >> 4) & 1) == hh;
        const unsigned va = isd ? L_VIMG + m * 2048 + lane * 16 : L_ONES, vst = isd ? 2048u : 0u, vs1 = isd ? 1024u : 0u;
        f32x16 o;
#pragma unroll
        for (int r = 0; r < 16; ++r) o[r] = 0.f;
#define MAX16(mx, s) { _Pragma("unroll") for (int r = 0; r < 16; r += 2) mx = fmaxf(fmaxf(mx, s[r]), s[r + 1]); }
#define EXPH(P, S, b) { P[0] = pk2(__builtin_amdgcn_exp2f(S[b]), __builtin_amdgcn_exp2f(S[b + 1])); P[1] = pk2(__builtin_amdgcn_exp2f(S[b + 2]), __builtin_amdgcn_exp2f(S[b + 3])); \
                        P[2] = pk2(__builtin_amdgcn_exp2f(S[b + 4]), __builtin_amdgcn_exp2f(S[b + 5])); P[3] = pk2(__builtin_amdgcn_exp2f(S[b + 6]), __builtin_amdgcn_exp2f(S[b + 7])); }
        if (g >= 2) {
            f32x16 z; const f32x16 z0 = {0.f, 0.f, 0.f, 0.f, 0.f, 0.f, 0.f, 0.f, 0.f, 0.f, 0.f, 0.f, 0.f, 0.f, 0.f, 0.f};
            float mx = NEG, mx2 = NEG; f32x16 sA, sB;
#define KF(i) lds_ld<bf16x8>(ka + (i) * 2048)
            bf16x8 k0 = KF(0), k1 = KF(8), k2 = KF(1), k3 = KF(2);
            SBAR();
            sA = MFMA32(k0, qf, c0); PIN(sA); k0 = KF(3); SBAR();
            sB = MFMA32(k1, qf, c8); PIN(sB); k1 = KF(4); SBAR(); MAX16(mx, sA); PIN(mx); SBAR();
            sA = MFMA32(k2, qf, z0); PIN(sA); k2 = KF(5); SBAR(); MAX16(mx2, sB); PIN(mx2); SBAR();
            sB = MFMA32(k3, qf, z0); PIN(sB); k3 = KF(6); SBAR(); MAX16(mx, sA); PIN(mx); SBAR();
            sA = MFMA32(k0, qf, z0); PIN(sA); k0 = KF(7); SBAR(); MAX16(mx2, sB); PIN(mx2); SBAR();
            sB = MFMA32(k1, qf, z0); PIN(sB); k1 = KF(0); SBAR(); MAX16(mx, sA); PIN(mx); SBAR();
            sA = MFMA32(k2, qf, z0); PIN(sA); k2 = KF(1); SBAR(); MAX16(mx2, sB); PIN(mx2); SBAR();
            sB = MFMA32(k3, qf, z0); PIN(sB); SBAR(); MAX16(mx, sA); PIN(mx); SBAR();
            sA = MFMA32(k0, qf, z0); PIN(sA); SBAR(); MAX16(mx2, sB); PIN(mx2); SBAR();
            WS2(1);
            bf16x8 v0 = lds_ld<bf16x8>(va), v1 = lds_ld<bf16x8>(va + vs1);
            MAX16(mx, sA);
            mx = xmax32(fmaxf(mx, mx2));
#pragma unroll
            for (int r = 0; r < 16; ++r) { z[r] = -mx; c0[r] -= mx; c8[r] -= mx; }
            PIN(z); PIN(c0); PIN(c8); SBAR();
            WS2(2);
            sA = MFMA32(k1, qf, c0); SBAR();
            u32x4 pa, pb; bf16x8 v0n, v1n, kn = k2; unsigned vp = va;
#define ABLK(I, SC, SN, CN) { bf16x8 kc = kn; if ((I) < 8) { vp += vst; v0n = lds_ld<bf16x8>(vp); v1n = lds_ld<bf16x8>(vp + vs1); } if ((I) < 7) kn = KF((I) + 2); SBAR(); \
                if ((I) < 8) { SN = MFMA32(kc, qf, CN); PIN(SN); } SBAR(); EXPH(pa, SC, 0); PIN(pa); SBAR(); \
                o = MFMA32(v0, __builtin_bit_cast(bf16x8, pa), o); PIN(o); SBAR(); EXPH(pb, SC, 8); PIN(pb); SBAR(); \
                o = MFMA32(v1, __builtin_bit_cast(bf16x8, pb), o); PIN(o); SBAR(); if ((I) < 8) { v0 = v0n; v1 = v1n; } }
            ABLK(0, sA, sB, z) ABLK(1, sB, sA, z) ABLK(2, sA, sB, z) ABLK(3, sB, sA, z)
            ABLK(4, sA, sB, z) ABLK(5, sB, sA, z) ABLK(6, sA, sB, z) ABLK(7, sB, sA, c8) ABLK(8, sA, sB, c8)
#undef KF
#undef ABLK
        } else {
            const int i0 = 8 - 4 * g - m;
            f32x16 z;
#pragma unroll
            for (int r = 0; r < 16; ++r) z[r] = 0.f;
            float mx = NEG;
            f32x16 s = MFMA32(lds_ld<bf16x8>(ka + 8 * 2048), qf, c8); MAX16(mx, s);
#pragma unroll 1
            for (int i = i0; i < 8; ++i) { s = MFMA32(lds_ld<bf16x8>(ka + i * 2048), qf, z); MAX16(mx, s); }
            mx = xmax32(mx);
#pragma unroll
            for (int r = 0; r < 16; ++r) { z[r] = -mx; c8[r] -= mx; }
#pragma unroll 1
            for (int i = i0; i <= 8; ++i) { u32x4 pa, pb;
                if (i < 8) s = MFMA32(lds_ld<bf16x8>(ka + i * 2048), qf, z); else s = MFMA32(lds_ld<bf16x8>(ka + i * 2048), qf, c8);
                EXPH(pa, s, 0); EXPH(pb, s, 8);
                o = MFMA32(lds_ld<bf16x8>(va + i * vst), __builtin_bit_cast(bf16x8, pa), o); o = MFMA32(lds_ld<bf16x8>(va + i * vst + vs1), __builtin_bit_cast(bf16x8, pb), o); }
        }
#undef EXPH
#undef MAX16
        WS2(3);
        float ov[8], l;
        { float lo = o[0], hi = o[8]; PIN(lo); PIN(hi); l = hh ? lo : hi; }
#pragma unroll
        for (int r = 0; r < 8; ++r) { float lo = o[r], hi = o[8 + r]; PIN(lo); PIN(hi); ov[r] = hh ? hi : lo; }
        const float inv = __builtin_amdgcn_rcpf(l);
        const u32x4 op = {pk2(ov[0] * inv, ov[1] * inv), pk2(ov[2] * inv, ov[3] * inv), pk2(ov[4] * inv, ov[5] * inv), pk2(ov[6] * inv, ov[7] * inv)};
        __builtin_amdgcn_raw_buffer_store_b128(op, rws, WS_O + ((g * 8 + 2 * m + (qq >> 4)) * 16 + j) * 1024 + ((qq & 15) + 16 * (2 * hh + h)) * 16, 0, 16);
    }
    }
    WS2(4);
    VMWAIT(0); WS2(5);
    BAR();
    if (tid == 0) stg_sc1(ws, WS_FLAG + (1 * 256 + g * 16 + j) * 256, epoch);
    STAMP(9);
    if (w >= 4) {
        const int t = tid - 256; const unsigned o = WS_MODN + g * 8192 + t * 32; u32x4 v, v2;
        for (unsigned sp = 0; sp < SPIN_CAP; ++sp) { asm volatile("" ::: "memory"); v = __builtin_amdgcn_raw_buffer_load_b128(rws, o, 0, 16); v2 = __builtin_amdgcn_raw_buffer_load_b128(rws, o + 16, 0, 16);
            if (__all(v[1] == epoch && v[3] == epoch && v2[1] == epoch && v2[3] == epoch)) break; }
        u32x4 val = {v[0], v[2], v2[0], v2[2]};
        const int vec = t >> 6, c8 = (t & 63) * 8;
        if (vec == 1) lds_st<u32x4>(L_SB16 + c8 * 2, val);
        else if (vec == 2) {
#pragma unroll
            for (int i = 0; i < 4; ++i) { const unsigned u_ = val[i]; val[i] = pk2(1.f + bf_lo(u_), 1.f + bf_hi(u_)); }
            lds_st<u32x4>(L_GB16 + c8 * 2, val);
        } else if ((c8 >> 6) == ct) lds_st<u32x4>((vec == 0 ? L_GAO : L_GBO) + (c8 & 63) * 2, val);
    }
    if (w == 0) poll_flags(ws, WS_FLAG + (1 * 256 + g * 16) * 256, 16, epoch, lane);
    BAR();
    STAMP(10); WS3(0);
    u32x2 x1p0, x1p1;
    {
        FRESH(lane);
        const int gi = lane >> 4, q4 = (lane & 15) >> 2, p4 = lane & 3;
        const unsigned tokP = 128 * g + 64 * rh + 16 * tt + (lane & 15), colP = 64 * ct + 32 * kh + 4 * gi;
        u32x4 of[8];
#pragma unroll
        for (int s = 0; s < 8; ++s) of[s] = __builtin_amdgcn_raw_buffer_load_b128(rws, WS_O + ((g * 8 + 4 * rh + tt) * 16 + 8 * kh + s) * 1024 + lane * 16, 0, 16);
        const u32x2 xres0 = *(const u32x2*)(x + tokP * 512 + colP), xres1 = *(const u32x2*)(x + tokP * 512 + colP + 16);
        SBAR();
        f32x4 acc[4];
#pragma unroll
        for (int n = 0; n < 4; ++n) acc[n] = (f32x4){0.f, 0.f, 0.f, 0.f};
        const unsigned xv = 2 * (gi & 1) + (q4 >> 1), rowa = L_WO + (256 * kh + 16 * (gi >> 1) + 4 * (gi & 1) + q4) * 128 + p4 * 8;
        unsigned an[4]; bf16x8 wf[4];
#pragma unroll
        for (int n = 0; n < 4; ++n) { an[n] = rowa + ((n ^ xv) * 32); wf[n] = trfrag(an[n], an[n] + 1024); }
        const unsigned cl = (32 * kh + 4 * gi) * 2, cg = colP * 2;
        const u32x2 ga0 = lds_ld<u32x2>(L_GAO + cl), ga1 = lds_ld<u32x2>(L_GAO + cl + 32);
        const u32x2 gb0 = lds_ld<u32x2>(L_GB16 + cg), gb1 = lds_ld<u32x2>(L_GB16 + cg + 32), sb0 = lds_ld<u32x2>(L_SB16 + cg), sb1 = lds_ld<u32x2>(L_SB16 + cg + 32);
        SBAR();
#pragma unroll
        for (int s = 0; s < 8; ++s)
#pragma unroll
            for (int n = 0; n < 4; ++n) { acc[n] = MFMA16(wf[n], __builtin_bit_cast(bf16x8, of[s]), acc[n]); PIN(acc[n]); SBAR(); if (s < 7) wf[n] = trfrag(an[n] + (s + 1) * 4096, an[n] + (s + 1) * 4096 + 1024); SBAR(); }
        WS3(1);
        BAR();
        STAMP(11);
        const unsigned ex = w * 2048 + lane * 16;
        { f32x4 s0 = acc[0], s1 = acc[1], s2 = acc[2], s3 = acc[3]; PIN(s0); PIN(s1); PIN(s2); PIN(s3);
          lds_st<f32x4>(ex, kh ? s0 : s2); lds_st<f32x4>(ex + 1024, kh ? s1 : s3); acc[0] = kh ? s2 : s0; acc[1] = kh ? s3 : s1; }
        BAR();
        WS3(2);
        const unsigned ep = (w ^ 4) * 2048 + lane * 16;
        const f32x4 a0 = lds_ld<f32x4>(ep) + acc[0], a1 = lds_ld<f32x4>(ep + 1024) + acc[1];
        float xv1[8], gg[8], ss[8];
#pragma unroll
        for (int r = 0; r < 4; ++r) {
            const unsigned ua0 = ga0[r >> 1], ua1 = ga1[r >> 1], ux0 = xres0[r >> 1], ux1 = xres1[r >> 1], ug0 = gb0[r >> 1], ug1 = gb1[r >> 1], us0 = sb0[r >> 1], us1 = sb1[r >> 1];
            xv1[r] = rbf(fmaf((r & 1) ? bf_hi(ua0) : bf_lo(ua0), a0[r], (r & 1) ? bf_hi(ux0) : bf_lo(ux0))); xv1[4 + r] = rbf(fmaf((r & 1) ? bf_hi(ua1) : bf_lo(ua1), a1[r], (r & 1) ? bf_hi(ux1) : bf_lo(ux1)));
            gg[r] = (r & 1) ? bf_hi(ug0) : bf_lo(ug0); gg[4 + r] = (r & 1) ? bf_hi(ug1) : bf_lo(ug1);
            ss[r] = (r & 1) ? bf_hi(us0) : bf_lo(us0); ss[4 + r] = (r & 1) ? bf_hi(us1) : bf_lo(us1);
        }
        x1p0 = (u32x2){pk2(xv1[0], xv1[1]), pk2(xv1[2], xv1[3])}; x1p1 = (u32x2){pk2(xv1[4], xv1[5]), pk2(xv1[6], xv1[7])};
        float st[6] = {0.f, 0.f, 0.f, 0.f, 0.f, 0.f}, pg[8];
#pragma unroll
        for (int r = 0; r < 8; ++r) pg[r] = xv1[r] * gg[r];
        const u32x4 xg = {pk2(pg[0], pg[1]), pk2(pg[2], pg[3]), pk2(pg[4], pg[5]), pk2(pg[6], pg[7])};
        __builtin_amdgcn_raw_buffer_store_b128(xg, rws, WS_XG + ((g * 8 + 4 * rh + tt) * 16 + 2 * ct + kh) * 1024 + lane * 16, 0, 16);
#pragma unroll
        for (int r = 0; r < 8; ++r) { const float p = pg[r]; st[0] += xv1[r]; st[1] = fmaf(xv1[r], xv1[r], st[1]); st[2] += p; st[3] = fmaf(p, p, st[3]); st[4] = fmaf(p, gg[r], st[4]); st[5] = fmaf(p, ss[r], st[5]); }
#pragma unroll
        for (int i = 0; i < 6; ++i) st[i] = xsum32(xsum16(st[i]));
        if (kh == 1 && lane < 16) { lds_st<f32x4>(L_SCR + (tt * 16 + lane) * 32, (f32x4){st[0], st[1], st[2], st[3]}); lds_st<u32x2>(L_SCR + (tt * 16 + lane) * 32 + 16, (u32x2){__float_as_uint(st[4]), __float_as_uint(st[5])}); }
        WS3(3);
        BAR();
        if (kh == 0 && lane < 16) {
            const f32x4 o4 = lds_ld<f32x4>(L_SCR + (tt * 16 + lane) * 32); const u32x2 o2 = lds_ld<u32x2>(L_SCR + (tt * 16 + lane) * 32 + 16);
            const unsigned so = WS_ST + g * 24576, T = 4 * rh + tt;
            const u32x4 sa4 = {__float_as_uint(st[0] + o4[0]), __float_as_uint(st[1] + o4[1]), __float_as_uint(st[2] + o4[2]), __float_as_uint(st[3] + o4[3])};
            const u32x2 sb2 = {__float_as_uint(st[4] + __uint_as_float(o2[0])), __float_as_uint(st[5] + __uint_as_float(o2[1]))};
            __builtin_amdgcn_raw_buffer_store_b128(sa4, rws, so + ((T * 8 + ct) * 16 + lane) * 16, 0, 16);
            __builtin_amdgcn_raw_buffer_store_b64(sb2, rws, so + 16384 + ((T * 8 + ct) * 16 + lane) * 8, 0, 16);
        }
    }
    WS3(4);
    VMWAIT(0); WS3(5);
    BAR();
    if (tid == 0) stg_sc1(ws, WS_FLAG + (2 * 256 + g * 16 + j) * 256, epoch);
    STAMP(12);
    const int odd = j & 1;
    {
    FRESH(lane);
    const int gi = lane >> 4, q4 = (lane & 15) >> 2, p4 = lane & 3;
    const u32x2 b1v = *(const u32x2*)(b1 + 112 * j + (w > 0 ? w - 1 : 0) * 16 + 4 * gi);
    BAR();
    if (w > 0) {
        const int nt = w - 1; f32x4 acc = {0.f, 0.f, 0.f, 0.f}, acc2 = acc;
        const unsigned ra = L_W1 + (4 * gi + q4) * 224 + p4 * 8 + nt * 32; const int tk = lane & 15;
        const unsigned va = tk == 0 ? L_GB16 + 8 * gi : tk == 1 ? L_SB16 + 8 * gi : tk == 2 ? L_ONES : L_ZERO2, vst = tk < 2 ? 64u : 0u, vhi = tk < 2 ? 32u : 0u;
        u32x2 blo[16], bhi[16];
        { unsigned a_ = va;
#pragma unroll
          for (int s = 0; s < 16; ++s) { blo[s] = lds_ld<u32x2>(a_); bhi[s] = lds_ld<u32x2>(a_ + vhi); a_ += vst; } }
        bf16x8 f0 = trfrag(ra, ra + 3584), f1 = trfrag(ra + 7168, ra + 7168 + 3584);
#pragma unroll
        for (int s = 0; s < 16; s += 2) {
            acc = MFMA16(f0, __builtin_bit_cast(bf16x8, ((u32x4){blo[s][0], blo[s][1], bhi[s][0], bhi[s][1]})), acc); PIN(acc); SBAR(); if (s + 2 < 16) f0 = trfrag(ra + (s + 2) * 7168, ra + (s + 2) * 7168 + 3584); SBAR();
            acc2 = MFMA16(f1, __builtin_bit_cast(bf16x8, ((u32x4){blo[s + 1][0], blo[s + 1][1], bhi[s + 1][0], bhi[s + 1][1]})), acc2); PIN(acc2); SBAR(); if (s + 3 < 16) f1 = trfrag(ra + (s + 3) * 7168, ra + (s + 3) * 7168 + 3584); SBAR();
        }
        acc += acc2;
        if (tk < 3) lds_st<f32x4>(L_UVW + (tk * 112 + nt * 16 + 4 * gi) * 4, acc);
        if (tk == 3) lds_st<f32x4>(L_UVW + (3 * 112 + nt * 16 + 4 * gi) * 4, (f32x4){bf_lo(b1v[0]), bf_hi(b1v[0]), bf_lo(b1v[1]), bf_hi(b1v[1])});
    } else {
        const u32x4 gq = lds_ld<u32x4>(L_GB16 + lane * 16), sq = lds_ld<u32x4>(L_SB16 + lane * 16);
        float c5[5] = {0.f, 0.f, 0.f, 0.f, 0.f};
#pragma unroll
        for (int i = 0; i < 4; ++i) { const unsigned ug = gq[i], us = sq[i]; const float g0 = bf_lo(ug), g1 = bf_hi(ug), s0 = bf_lo(us), s1 = bf_hi(us);
            c5[0] += g0 + g1; c5[1] += s0 + s1; c5[2] += g0 * g0 + g1 * g1; c5[3] += g0 * s0 + g1 * s1; c5[4] += s0 * s0 + s1 * s1; }
#pragma unroll
        for (int i = 0; i < 5; ++i) c5[i] = xsum32(xsum16(rowsum16(c5[i])));
        if (lane == 0) { lds_st<f32x4>(L_CONST, (f32x4){c5[0], c5[1], c5[2], c5[3]}); lds_st<float>(L_CONST + 16, c5[4]); }
        poll_flags(ws, WS_FLAG + (2 * 256 + g * 16) * 256, 16, epoch, lane);
    }
    }
    BAR();
    STAMP(13);
    {
        FRESH(lane);
        const int gi = lane >> 4, q4 = (lane & 15) >> 2, p4 = lane & 3;
        const int Tw = 2 * m + kh;
        u32x4 xf[2][8];
        const unsigned xo0 = WS_XG + (g * 8 + Tw) * 16384 + kh * 8192 + lane * 16, xo1 = WS_XG + (g * 8 + (Tw ^ 1)) * 16384 + kh * 8192 + lane * 16;
        constexpr int WIN = 5;
#pragma unroll
        for (int s = 0; s < WIN; ++s) { xf[0][s] = __builtin_amdgcn_raw_buffer_load_b128(rws, xo0 + s * 1024, 0, 16); xf[1][s] = __builtin_amdgcn_raw_buffer_load_b128(rws, xo1 + s * 1024, 0, 16); }
        const unsigned so = WS_ST + g * 24576 + Tw * 2048, sob = WS_ST + g * 24576 + 16384 + Tw * 1024;
        u32x4 sA0, sA1; u32x2 sB0, sB1;
        SBAR(); WS4(0);
        f32x4 acc[7], acb[7]; bf16x8 wf[7];
        const unsigned ra = L_W1 + (256 * kh + 4 * gi + q4) * 224 + p4 * 8, raA = ra + 32 * odd, raB = ra + (odd ? 0 : 192);
#define W1A(s, u) (((u) < 6 ? raA : raB) + (s) * 7168 + ((u) < 6 ? (u) * 32 : 0))
#pragma unroll
        for (int u = 0; u < 7; ++u) { acc[u] = (f32x4){0.f, 0.f, 0.f, 0.f}; acb[u] = acc[u]; wf[u] = trfrag(W1A(0, u), W1A(0, u) + 3584); }
        SBAR();
#pragma unroll
        for (int s = 0; s < 8; ++s) {
            if (s + WIN < 8) { xf[0][s + WIN] = __builtin_amdgcn_raw_buffer_load_b128(rws, xo0 + (s + WIN) * 1024, 0, 16); xf[1][s + WIN] = __builtin_amdgcn_raw_buffer_load_b128(rws, xo1 + (s + WIN) * 1024, 0, 16); }
            if (s == 4) { sA0 = __builtin_amdgcn_raw_buffer_load_b128(rws, so + ((2 * gi) * 16 + (lane & 15)) * 16, 0, 16); sA1 = __builtin_amdgcn_raw_buffer_load_b128(rws, so + ((2 * gi + 1) * 16 + (lane & 15)) * 16, 0, 16); }
            if (s == 5) { sB0 = __builtin_amdgcn_raw_buffer_load_b64(rws, sob + ((2 * gi) * 16 + (lane & 15)) * 8, 0, 16); sB1 = __builtin_amdgcn_raw_buffer_load_b64(rws, sob + ((2 * gi + 1) * 16 + (lane & 15)) * 8, 0, 16); }
            SBAR();
#pragma unroll
            for (int u = 0; u < 7; ++u) { acc[u] = MFMA16(wf[u], __builtin_bit_cast(bf16x8, xf[0][s]), acc[u]); PIN(acc[u]); SBAR();
                acb[u] = MFMA16(wf[u], __builtin_bit_cast(bf16x8, xf[1][s]), acb[u]); PIN(acb[u]); SBAR();
                if (s < 7) wf[u] = trfrag(W1A(s + 1, u), W1A(s + 1, u) + 3584); SBAR(); }
            if (s == 7) WS4(1);
        }
#undef W1A
        float ca, cb, cc, r2;
        { float S[6];
#pragma unroll
          for (int i = 0; i < 4; ++i) S[i] = xsum32(xsum16(__uint_as_float(sA0[i]) + __uint_as_float(sA1[i])));
#pragma unroll
          for (int i = 0; i < 2; ++i) S[4 + i] = xsum32(xsum16(__uint_as_float(sB0[i]) + __uint_as_float(sB1[i])));
          const f32x4 cc4 = lds_ld<f32x4>(L_CONST); const float SB2 = lds_ld<float>(L_CONST + 16);
          const float iD = 1.f / 512.f, m1 = S[0] * iD, r1 = __builtin_amdgcn_rsqf(fmaxf(S[1] * iD - m1 * m1, 0.f) + 1e-5f);
          const float m2 = (r1 * (S[2] - m1 * cc4[0]) + cc4[1]) * iD;
          const float ez2 = (r1 * r1 * (S[3] - 2.f * m1 * S[4] + m1 * m1 * cc4[2]) + 2.f * r1 * (S[5] - m1 * cc4[3]) + SB2) * iD;
          r2 = __builtin_amdgcn_rsqf(fmaxf(ez2 - m2 * m2, 0.f) + 1e-5f);
          ca = r1 * r2; cb = -ca * m1; cc = -r2 * m2; PIN(ca); PIN(cb); PIN(cc); PIN(r2); }
        const unsigned uo = L_UVW + 16 * gi + 64 * odd, uo6 = L_UVW + 16 * gi + (odd ? 0 : 384);
        f32x4 uu = lds_ld<f32x4>(uo), vv = lds_ld<f32x4>(uo + 448), ww = lds_ld<f32x4>(uo + 896), bb = lds_ld<f32x4>(uo + 1344);
        f32x4 u2 = lds_ld<f32x4>(uo + 64), v2 = lds_ld<f32x4>(uo + 64 + 448), w2v = lds_ld<f32x4>(uo + 64 + 896), b2v = lds_ld<f32x4>(uo + 64 + 1344);
        BAR();
        WS4(2);
        { const unsigned ex = 101376 + w * 7168 + lane * 16;
#pragma unroll
          for (int u = 0; u < 7; ++u) lds_st<f32x4>(ex + u * 1024, acb[u]); }
        WS4(3);
        BAR();
        WS4(4);
        { const unsigned ep = 101376 + (w ^ 4) * 7168 + lane * 16;
#pragma unroll
          for (int u = 0; u < 7; ++u) acc[u] += lds_ld<f32x4>(ep + u * 1024); }
        STAMP(14);
        const unsigned hb = WS_H + (g * 8 + Tw) * 57344, s0 = (7 * j + odd) >> 1;
        const f32x2 kA = {-0.044715f * 2.302208198f, -0.044715f * 2.302208198f}, kB = {-2.302208198f, -2.302208198f}, one2 = {1.f, 1.f};
#define LO2(v) ((f32x2){(v)[0], (v)[1]})
#define HI2(v) ((f32x2){(v)[2], (v)[3]})
#define PRE2(a_, u_, v_, w_, b_) (ca * (a_) + (cb * (u_) + (r2 * (v_) + (cc * (w_) + (b_)))))
#define GELU(N, p, y) { f32x2 z_[N], e_[N]; \
            _Pragma("unroll") for (int r = 0; r < N; ++r) { z_[r] = p[r] * p[r]; PIN(z_[r]); } \
            _Pragma("unroll") for (int r = 0; r < N; ++r) { z_[r] = z_[r] * kA + kB; PIN(z_[r]); } \
            _Pragma("unroll") for (int r = 0; r < N; ++r) { z_[r] = z_[r] * p[r]; PIN(z_[r]); } \
            SBAR(); _Pragma("unroll") for (int r = 0; r < N; ++r) { e_[r] = (f32x2){__builtin_amdgcn_exp2f(z_[r][0]), __builtin_amdgcn_exp2f(z_[r][1])}; PIN(e_[r]); } \
            SBAR(); _Pragma("unroll") for (int r = 0; r < N; ++r) { e_[r] = e_[r] + one2; PIN(e_[r]); } \
            SBAR(); _Pragma("unroll") for (int r = 0; r < N; ++r) { e_[r] = (f32x2){__builtin_amdgcn_rcpf(e_[r][0]), __builtin_amdgcn_rcpf(e_[r][1])}; PIN(e_[r]); } \
            SBAR(); _Pragma("unroll") for (int r = 0; r < N; ++r) y[r] = p[r] * e_[r]; }
#pragma unroll
        for (int u = 0; u < 6; u += 2) {
            f32x2 pre[4], y[4];
            pre[0] = PRE2(LO2(acc[u]), LO2(uu), LO2(vv), LO2(ww), LO2(bb)); pre[1] = PRE2(HI2(acc[u]), HI2(uu), HI2(vv), HI2(ww), HI2(bb));
            pre[2] = PRE2(LO2(acc[u + 1]), LO2(u2), LO2(v2), LO2(w2v), LO2(b2v)); pre[3] = PRE2(HI2(acc[u + 1]), HI2(u2), HI2(v2), HI2(w2v), HI2(b2v));
            PIN(pre[0]); PIN(pre[1]); PIN(pre[2]); PIN(pre[3]);
            SBAR();
            { const unsigned o = u < 4 ? uo + (u + 2) * 64 : uo6; uu = lds_ld<f32x4>(o); vv = lds_ld<f32x4>(o + 448); ww = lds_ld<f32x4>(o + 896); bb = lds_ld<f32x4>(o + 1344); }
            if (u < 4) { const unsigned o = uo + (u + 3) * 64; u2 = lds_ld<f32x4>(o); v2 = lds_ld<f32x4>(o + 448); w2v = lds_ld<f32x4>(o + 896); b2v = lds_ld<f32x4>(o + 1344); }
            SBAR();
            GELU(4, pre, y)
            __builtin_amdgcn_raw_buffer_store_b128((u32x4){pk2(y[0][0], y[0][1]), pk2(y[1][0], y[1][1]), pk2(y[2][0], y[2][1]), pk2(y[3][0], y[3][1])}, rws, hb + (s0 + (u >> 1)) * 1024 + lane * 16, 0, 16);
            SBAR();
        }
        {   f32x2 pre[2], y[2];
            pre[0] = PRE2(LO2(acc[6]), LO2(uu), LO2(vv), LO2(ww), LO2(bb)); pre[1] = PRE2(HI2(acc[6]), HI2(uu), HI2(vv), HI2(ww), HI2(bb));
            GELU(2, pre, y)
            __builtin_amdgcn_raw_buffer_store_b64((u32x2){pk2(y[0][0], y[0][1]), pk2(y[1][0], y[1][1])}, rws, hb + ((7 * j + (odd ? 0 : 6)) >> 1) * 1024 + odd * 512 + lane * 8, 0, 16);
        }
#undef GELU
#undef PRE2
#undef LO2
#undef HI2
        WS4(5);
    }
    VMWAIT(0);
    BAR();
    if (tid == 0) stg_sc1(ws, WS_FLAG + (3 * 256 + g * 16 + j) * 256, epoch);
    STAMP(15);
    u32x4 park[10];
    { FRESH(lane);
      const unsigned sw = (lane >> 3) * 1024u + ct * 128u + (((lane & 7) >> 1) ^ ((lane >> 4) & 3)) * 32u + (lane & 1) * 16u;
      const char* p0 = (const char*)w2 + (size_t)(56 * (w >> 2) + 36 + (w & 3)) * 8192, * p1 = p0 + (size_t)112 * 8192;
#pragma unroll
      for (int i = 0; i < 5; ++i) { park[2 * i] = *(const u32x4*)(p0 + i * 32768 + sw); park[2 * i + 1] = *(const u32x4*)(p1 + i * 32768 + sw); }
      SBAR();
      if (w == 0) poll_flags(ws, WS_FLAG + (3 * 256 + g * 16) * 256, 16, epoch, lane); }
    SBAR();
    BAR();
    STAMP(16); WS5(0);
    {
        constexpr int WIN = 3;
        FRESH(lane);
        const int gi = lane >> 4, q4 = (lane & 15) >> 2, p4 = lane & 3;
        const unsigned tokP = 128 * g + 64 * rh + 16 * tt + (lane & 15), colP = 64 * ct + 32 * kh + 4 * gi;
        const int kq = (w & 1) | (kh << 1);
        const unsigned hb0 = WS_H + (g * 8 + 4 * rh + tt) * 57344 + kq * 14 * 1024, hb1 = WS_H + (g * 8 + 4 * rh + (tt ^ 1)) * 57344 + kq * 14 * 1024;
        u32x4 hf[2][14];
#define HLD1(hb_, dst, s) { if ((s) % 7 == 3) { const u32x2 lo = __builtin_amdgcn_raw_buffer_load_b64(rws, hb_ + (s) * 1024 + lane * 8, 0, 16), hi = __builtin_amdgcn_raw_buffer_load_b64(rws, hb_ + (s) * 1024 + 512 + lane * 8, 0, 16); dst = (u32x4){lo[0], lo[1], hi[0], hi[1]}; } \
                    else dst = __builtin_amdgcn_raw_buffer_load_b128(rws, hb_ + (s) * 1024 + lane * 16, 0, 16); }
#define HLOAD(s) { HLD1(hb0, hf[0][s], s) HLD1(hb1, hf[1][s], s) }
#pragma unroll
        for (int s = 0; s < WIN; ++s) HLOAD(s)
        SBAR();
        f32x4 acc[2][4]; bf16x8 wf[4];
        const unsigned xv = (2 * gi + (q4 >> 1)) & 3, rowa = kq * 4096 + (4 * gi + q4) * 128 + p4 * 8;
        unsigned an[4];
#pragma unroll
        for (int n = 0; n < 4; ++n) { an[n] = rowa + (((n ^ (2 * kh)) ^ xv) * 32); acc[0][n] = (f32x4){0.f, 0.f, 0.f, 0.f}; acc[1][n] = acc[0][n]; }
#define W2A(s, n) (an[n] + (((s) % 9) & 3) * 16384)
#pragma unroll
        for (int n = 0; n < 4; ++n) wf[n] = trfrag(W2A(0, n), W2A(0, n) + 2048);
        const unsigned cl = (32 * kh + 4 * gi) * 2;
        const u32x2 g0 = lds_ld<u32x2>(L_GBO + cl), g1 = lds_ld<u32x2>(L_GBO + cl + 32);
        u32x2 bb0, bb1;
        SBAR();
#pragma unroll
        for (int s = 0; s < 14; ++s) {
            if (s == 11) { bb0 = *(const u32x2*)(b2 + colP); bb1 = *(const u32x2*)(b2 + colP + 16); }
            if (s == 1) WS5(1); if (s == 5) WS5(2); if (s == 8) WS5(3);
            if (s == 5) BAR();
            if (s == 8) BAR();
            if (s == 8) WS5(4);
            if (s + WIN < 14) HLOAD(s + WIN)
            SBAR();
#pragma unroll
            for (int n = 0; n < 4; ++n) { acc[0][n] = MFMA16(wf[n], __builtin_bit_cast(bf16x8, hf[0][s]), acc[0][n]); PIN(acc[0][n]); SBAR();
                acc[1][n] = MFMA16(wf[n], __builtin_bit_cast(bf16x8, hf[1][s]), acc[1][n]); PIN(acc[1][n]); SBAR();
                if (s >= 5 && s <= 7 && 4 * (s - 5) + n < 10) { const int i = 4 * (s - 5) + n; lds_st<u32x4>((i >> 1) * 16384 + (w + 8 * (i & 1)) * 1024 + lane * 16, park[i]); }
                if (s == 3 || s == 7 || s == 12) { an[n] += 65536; PIN(an[n]); } if (s == 8) { an[n] -= 131072; PIN(an[n]); }
                if (s < 13) wf[n] = trfrag(W2A(s + 1, n), W2A(s + 1, n) + 2048); SBAR(); }
        }
#undef W2A
#undef HLOAD
#undef HLD1
        WS5(5);
        BAR();
        STAMP(17);
        { const unsigned ex = w * 6144 + lane * 16;
          lds_st<f32x4>(ex, acc[0][2]); lds_st<f32x4>(ex + 1024, acc[0][3]); lds_st<f32x4>(ex + 2048, acc[1][0]); lds_st<f32x4>(ex + 3072, acc[1][1]); lds_st<f32x4>(ex + 4096, acc[1][2]); lds_st<f32x4>(ex + 5120, acc[1][3]); }
        BAR();
        const unsigned e4 = (w ^ 4) * 6144 + lane * 16, e1 = (w ^ 1) * 6144 + 2048 + lane * 16, e5 = (w ^ 5) * 6144 + 4096 + lane * 16;
        const f32x4 a0 = (acc[0][0] + lds_ld<f32x4>(e4)) + (lds_ld<f32x4>(e1) + lds_ld<f32x4>(e5)), a1 = (acc[0][1] + lds_ld<f32x4>(e4 + 1024)) + (lds_ld<f32x4>(e1 + 1024) + lds_ld<f32x4>(e5 + 1024));
        float y0[4], y1[4];
#pragma unroll
        for (int r = 0; r < 4; ++r) {
            const unsigned ug0 = g0[r >> 1], ug1 = g1[r >> 1], ub0 = bb0[r >> 1], ub1 = bb1[r >> 1], ux0 = x1p0[r >> 1], ux1 = x1p1[r >> 1];
            y0[r] = fmaf((r & 1) ? bf_hi(ug0) : bf_lo(ug0), a0[r] + ((r & 1) ? bf_hi(ub0) : bf_lo(ub0)), (r & 1) ? bf_hi(ux0) : bf_lo(ux0));
            y1[r] = fmaf((r & 1) ? bf_hi(ug1) : bf_lo(ug1), a1[r] + ((r & 1) ? bf_hi(ub1) : bf_lo(ub1)), (r & 1) ? bf_hi(ux1) : bf_lo(ux1));
        }
        *(u32x2*)(out + tokP * 512 + colP) = (u32x2){pk2(y0[0], y0[1]), pk2(y0[2], y0[3])};
        *(u32x2*)(out + tokP * 512 + colP + 16) = (u32x2){pk2(y1[0], y1[1]), pk2(y1[2], y1[3])};
    }
    if (b == 0 && w == 0) {
        for (unsigned sp = 0; sp < SPIN_CAP; ++sp) { asm volatile("" ::: "memory"); const u32x4 v = __builtin_amdgcn_raw_buffer_load_b128(rws, WS_HELLO + lane * 16, 0, 16); if (__all(v[0] == epoch && v[1] == epoch && v[2] == epoch && v[3] == epoch)) break; }
        if (lane == 0) stg_sc1(ws, WS_E, epoch);
    }
    STAMP(18); STAMPC(21);
    STAMP_OUT();
    asm volatile("fb_code_end:");
}

extern "C" void kernel_launch(void* const* d_in, const int* in_sizes, int n_in, void* d_out, int out_size, void* d_ws, size_t ws_size, hipStream_t stream) {
    (void)hipFuncSetAttribute((const void*)fused_block, hipFuncAttributeMaxDynamicSharedMemorySize, LDS_BYTES);
    fused_block<<<256, 576, LDS_BYTES, stream>>>((const u16*)d_in[0], (const u16*)d_in[1], (const u16*)d_in[2], (const u16*)d_in[4], (const u16*)d_in[5], (const u16*)d_in[6], (const u16*)d_in[7],
                                                 (const u16*)d_in[8], (const u16*)d_in[9], (u16*)d_out, (char*)d_ws);
}
```

```cpp
#include <hip/hip_runtime.h>
#include <hip/hip_bf16.h>
#include <stdint.h>

typedef unsigned short u16;
typedef short bf16x8 __attribute__((ext_vector_type(8)));
typedef short s16x4 __attribute__((ext_vector_type(4)));
typedef float f32x4 __attribute__((ext_vector_type(4)));
typedef float f32x2 __attribute__((ext_vector_type(2)));
typedef float f32x16 __attribute__((ext_vector_type(16)));
typedef unsigned u32x4 __attribute__((ext_vector_type(4)));
typedef unsigned u32x2 __attribute__((ext_vector_type(2)));
typedef __bf16 bf2 __attribute__((ext_vector_type(2)));
#define LAS __attribute__((address_space(3)))
#define GAS __attribute__((address_space(1)))
#define DI __device__ __forceinline__

#define TS 0

constexpr unsigned WS_E = 0, WS_HELLO = 4096, WS_FLAG = 8192;
constexpr unsigned WS_MODU = 0x50000, WS_MODN = 0x60000;
constexpr unsigned WS_PART = 0x80000;
constexpr unsigned WS_KV = 0x100000, WS_O = 0x500000, WS_XG = 0x700000, WS_ST = 0x900000, WS_H = 0xA00000, WS_TS = 0x2000000;
constexpr unsigned WS_BYTES = 0x2100000;
constexpr unsigned L_X = 0, L_WQKV = 57344, L_RED = 155648, L_SAG = 156672, L_STAT = 160768;
constexpr unsigned L_QS = 65536, L_KIMG = 98304, L_VIMG = 122880, L_WO = 0, L_W1 = 44032;
constexpr unsigned L_GB16 = 158720, L_SB16 = 159744, L_UVW = 160768, L_CONST = 162560, L_GAO = 162592, L_GBO = 162720, L_ONES = 162848, L_ZERO2 = 162864, L_DUMMY = 162880;
constexpr unsigned L_TAGM = L_RED + 512, L_TAGL = L_RED + 544, L_TAGC = L_RED + 560;
constexpr unsigned L_RED2 = 81920;
constexpr unsigned L_SCR = 32768;
constexpr unsigned LDS_BYTES = 163840;
constexpr unsigned SPIN_CAP = 1u << 16;

DI float bf_lo(unsigned u) { return __uint_as_float(u << 16); }
DI float bf_hi(unsigned u) { return __uint_as_float(u & 0xffff0000u); }
DI unsigned pk2(float a, float b) { bf2 v = {(__bf16)a, (__bf16)b}; return __builtin_bit_cast(unsigned, v); }
DI float rbf(float a) { return bf_lo(pk2(a, a)); }
DI void dma16(unsigned voff, const void* sbase, unsigned lds) { asm volatile("s_mov_b32 m0, %1\n\ts_nop 0\n\tglobal_load_lds_dwordx4 %0, %2" ::"v"(voff), "s"(lds), "s"(sbase) : "memory"); }
DI void dma16c(unsigned voff, const void* sbase, unsigned lds) { asm volatile("s_mov_b32 m0, %1\n\ts_nop 0\n\tglobal_load_lds_dwordx4 %0, %2 sc1" ::"v"(voff), "s"(lds), "s"(sbase) : "memory"); }
DI void dma4(unsigned v0, unsigned v1, unsigned v2, unsigned v3, const void* sbase, unsigned lds) {
    asm volatile("s_mov_b32 m0, %5\n\ts_nop 0\n\tglobal_load_lds_dwordx4 %0, %4\n\tglobal_load_lds_dwordx4 %1, %4 offset:1024\n\tglobal_load_lds_dwordx4 %2, %4 offset:2048\n\tglobal_load_lds_dwordx4 %3, %4 offset:3072"
                 ::"v"(v0), "v"(v1), "v"(v2), "v"(v3), "s"(sbase), "s"(lds) : "memory"); }
DI void dma3(unsigned v0, unsigned v1, unsigned v2, const void* sbase, unsigned lds) {
    asm volatile("s_mov_b32 m0, %4\n\ts_nop 0\n\tglobal_load_lds_dwordx4 %0, %3\n\tglobal_load_lds_dwordx4 %1, %3 offset:1024\n\tglobal_load_lds_dwordx4 %2, %3 offset:2048"
                 ::"v"(v0), "v"(v1), "v"(v2), "s"(sbase), "s"(lds) : "memory"); }
DI void dma4lin_c(unsigned v, const void* sbase, unsigned lds) {
    asm volatile("s_mov_b32 m0, %2\n\ts_nop 0\n\tglobal_load_lds_dwordx4 %0, %1 sc1\n\tglobal_load_lds_dwordx4 %0, %1 offset:1024 sc1\n\tglobal_load_lds_dwordx4 %0, %1 offset:2048 sc1\n\tglobal_load_lds_dwordx4 %0, %1 offset:3072 sc1"
                 ::"v"(v), "s"(sbase), "s"(lds) : "memory"); }
DI void lds_wait_tags(unsigned a, int n, unsigned tag) {
    for (unsigned sp = 0; sp < SPIN_CAP; ++sp) { asm volatile("" ::: "memory"); const unsigned t0 = *(const volatile LAS unsigned*)(size_t)a, t1 = *(const volatile LAS unsigned*)(size_t)(a + 4 * (n - 1));
        if (__builtin_amdgcn_readfirstlane(t0) == tag && __builtin_amdgcn_readfirstlane(t1) == tag) break; __builtin_amdgcn_s_sleep(1); }
    asm volatile("" ::: "memory"); }
DI void warm4(unsigned voff, const void* sbase) { asm volatile("s_mov_b32 m0, %1\n\ts_nop 0\n\tglobal_load_lds_dword %0, %2" ::"v"(voff), "s"(L_DUMMY), "s"(sbase) : "memory"); }
#define K_W1B 21
#define K_W1BS 22
#define K_W1C 0
#define K_C01 0
#define K_P17 0
#define P5WIN 4
#define NO_A 0
#define NO_B 0
#define NO_C 0
#define K_NU 2
#define JROT 0
#define MAP4 1
#define K_SLOW 1
#define K_POLL 1
#define WSP 4
#define WS1(i) do { if (WSP == 1) WSTAMP(i); } while (0)
#define WS2(i) do { if (WSP == 2) WSTAMP(i); } while (0)
#define WS3(i) do { if (WSP == 3) WSTAMP(i); } while (0)
#define WS4(i) do { if (WSP == 4) WSTAMP(i); } while (0)
#define WS5(i) do { if (WSP == 5) WSTAMPG(i); } while (0)
#define WS6(i) do { if (WSP == 6) WSTAMP(i); } while (0)
#define FRESH(v) int v = threadIdx.x & 63; asm volatile("" : "+v"(v))
#define SBAR() __builtin_amdgcn_sched_barrier(0)
#define PIN(x) asm volatile("" : "+v"(x))
#define BAR() asm volatile("s_waitcnt lgkmcnt(0)\n\ts_barrier" ::: "memory")
#define RBAR() asm volatile("s_barrier" ::: "memory")
#define VMWAIT(n) asm volatile("s_waitcnt vmcnt(" #n ")" ::: "memory")
DI unsigned ldg_sc1(const char* ws, unsigned off) { return __hip_atomic_load((GAS unsigned*)(ws + off), __ATOMIC_RELAXED, __HIP_MEMORY_SCOPE_AGENT); }
DI unsigned long long ldg64_sc1(const char* ws, unsigned off) { return __hip_atomic_load((GAS unsigned long long*)(ws + off), __ATOMIC_RELAXED, __HIP_MEMORY_SCOPE_AGENT); }
DI void stg_sc1(char* ws, unsigned off, unsigned v) { __hip_atomic_store((GAS unsigned*)(ws + off), v, __ATOMIC_RELAXED, __HIP_MEMORY_SCOPE_AGENT); }
DI void stg64_sc1(char* ws, unsigned off, unsigned long long v) { __hip_atomic_store((GAS unsigned long long*)(ws + off), v, __ATOMIC_RELAXED, __HIP_MEMORY_SCOPE_AGENT); }
template <class T> DI T lds_ld(unsigned a) { return *(const LAS T*)(size_t)a; }
template <class T> DI void lds_st(unsigned a, T v) { *(LAS T*)(size_t)a = v; }
DI s16x4 lds_tr(unsigned a) { return __builtin_amdgcn_ds_read_tr16_b64_v4i16((LAS s16x4*)(size_t)a); }
DI bf16x8 trfrag(unsigned a0, unsigned a1) { s16x4 lo = lds_tr(a0), hi = lds_tr(a1); return __builtin_shufflevector(lo, hi, 0, 1, 2, 3, 4, 5, 6, 7); }
#define MFMA32(a, b, c) __builtin_amdgcn_mfma_f32_32x32x16_bf16((a), (b), (c), 0, 0, 0)
#define MFMA16(a, b, c) __builtin_amdgcn_mfma_f32_16x16x32_bf16((a), (b), (c), 0, 0, 0)
template <int N> DI float bcast(float v) { return __builtin_bit_cast(float, __builtin_amdgcn_mov_dpp(__builtin_bit_cast(int, v), 0x150 + N, 0xf, 0xf, true)); }
template <int N> DI float modu(float t, float gv, float sv) {
#pragma clang fp contract(off)
    const float p = bcast<N>(gv) * t; return bcast<N>(sv) + p; }
template <int N> DI unsigned hdw(unsigned x, float r, float n, float gv, float c, float d) {
    unsigned o; float a, b2;
    asm("v_lshlrev_b32 %1, 16, %5\n\tv_and_b32 %2, 0xffff0000, %5\n\tv_fma_f32 %1, %1, %6, %7\n\tv_fma_f32 %2, %2, %6, %7\n\t"
        "v_fmac_f32_dpp %3, %8, %1 row_newbcast:%c9 row_mask:0xf bank_mask:0xf\n\tv_fmac_f32_dpp %4, %8, %2 row_newbcast:%c10 row_mask:0xf bank_mask:0xf\n\t"
        "v_cvt_pk_bf16_f32 %0, %3, %4"
        : "=v"(o), "=&v"(a), "=&v"(b2), "+v"(c), "+v"(d) : "v"(x), "v"(r), "v"(n), "v"(gv), "i"(N), "i"(N + 1));
    return o; }
template <int C> DI float dppf(float v) { return __builtin_bit_cast(float, __builtin_amdgcn_update_dpp(0, __builtin_bit_cast(int, v), C, 0xf, 0xf, true)); }
DI float rowsum16(float v) { v += dppf<0xB1>(v); v += dppf<0x4E>(v); v += dppf<0x141>(v); v += dppf<0x140>(v); return v; }
DI float xsum32(float v) { auto r = __builtin_amdgcn_permlane32_swap(__float_as_uint(v), __float_as_uint(v), false, false); return __uint_as_float(r[0]) + __uint_as_float(r[1]); }
DI float xmax32(float v) { auto r = __builtin_amdgcn_permlane32_swap(__float_as_uint(v), __float_as_uint(v), false, false); return fmaxf(__uint_as_float(r[0]), __uint_as_float(r[1])); }
DI float xsum16(float v) { auto r = __builtin_amdgcn_permlane16_swap(__float_as_uint(v), __float_as_uint(v), false, false); return __uint_as_float(r[0]) + __uint_as_float(r[1]); }
DI unsigned long long mt() { unsigned long long t; asm volatile("s_memtime %0\n\ts_waitcnt lgkmcnt(0)" : "=s"(t)::"memory"); return t; }
DI unsigned long long rt() { unsigned long long t; asm volatile("s_memrealtime %0\n\ts_waitcnt lgkmcnt(0)" : "=s"(t)::"memory"); return t; }
#define STAMP(i) do {} while (0)
#define STAMP_OUT() do {} while (0)
#define STAMPG(i) do {} while (0)
#define STAMPGC(i) do {} while (0)
#define WSTAMPG(i) do {} while (0)
#define STAMPC(i) do {} while (0)
#define WSTAMP(i) do {} while (0)

DI void poll_flags(const char* ws, unsigned off, int n, unsigned epoch, int lane) {
    const unsigned o = off + (unsigned)(lane < n ? lane : 0) * 256u;
    for (unsigned sp = 0; sp < SPIN_CAP; ++sp) { unsigned f = ldg_sc1(ws, o); if (__all(f == epoch)) break; }
}

DI void mod_part(const u16* __restrict__ cvec, const u16* __restrict__ wmod, char* ws, unsigned epoch, int lane, int slice, int kp) {
    const unsigned row0 = 64 * kp + 4 * (lane >> 2), o = row0 * 6144u + (unsigned)slice * 64u + (lane & 3) * 16u;
    const u32x2 cv = *(const u32x2*)(cvec + row0);
    u32x4 wm[4];
#pragma unroll
    for (int i = 0; i < 4; ++i) wm[i] = *(const u32x4*)((const char*)wmod + o + i * 6144u);
    SBAR();
    unsigned sp[2];
#pragma unroll
    for (int r = 0; r < 2; ++r) { const unsigned cu = cv[r]; const float c0 = bf_lo(cu), c1 = bf_hi(cu);
        sp[r] = pk2(c0 * __builtin_amdgcn_rcpf(1.f + __builtin_amdgcn_exp2f(-1.44269504089f * c0)), c1 * __builtin_amdgcn_rcpf(1.f + __builtin_amdgcn_exp2f(-1.44269504089f * c1))); }
    float a[8] = {0.f, 0.f, 0.f, 0.f, 0.f, 0.f, 0.f, 0.f};
#pragma unroll
    for (int r = 0; r < 2; ++r)
#pragma unroll
        for (int i = 0; i < 4; ++i) { const unsigned u0 = wm[2 * r][i], u1 = wm[2 * r + 1][i], s_ = sp[r];
            const unsigned lo = __builtin_amdgcn_perm(u1, u0, 0x05040100u), hi = __builtin_amdgcn_perm(u1, u0, 0x07060302u);
            a[2 * i] = __builtin_amdgcn_fdot2_f32_bf16(__builtin_bit_cast(bf2, lo), __builtin_bit_cast(bf2, s_), a[2 * i], false);
            a[2 * i + 1] = __builtin_amdgcn_fdot2_f32_bf16(__builtin_bit_cast(bf2, hi), __builtin_bit_cast(bf2, s_), a[2 * i + 1], false); }
    float b4[4], b2[2];
#pragma unroll
    for (int i = 0; i < 4; ++i) { auto r = __builtin_amdgcn_permlane32_swap(__float_as_uint(a[i]), __float_as_uint(a[i + 4]), false, false); b4[i] = __uint_as_float(r[0]) + __uint_as_float(r[1]); }
#pragma unroll
    for (int i = 0; i < 2; ++i) { auto r = __builtin_amdgcn_permlane16_swap(__float_as_uint(b4[i]), __float_as_uint(b4[i + 2]), false, false); float v = __uint_as_float(r[0]) + __uint_as_float(r[1]);
        v += dppf<0x124>(v); v += dppf<0x128>(v); b2[i] = v; }
    const __amdgpu_buffer_rsrc_t rws = __builtin_amdgcn_make_buffer_rsrc(ws, 0, WS_BYTES, 0x00020000);
    if ((lane & 12) == 0) __builtin_amdgcn_raw_buffer_store_b128((u32x4){__float_as_uint(b2[0]), epoch, __float_as_uint(b2[1]), epoch}, rws,
                                                                 WS_PART + (slice * 8 + kp) * 256 + ((lane & 3) * 4 + (lane >> 5) * 2 + ((lane >> 4) & 1)) * 16, 0, 16);
}
struct PartSum { u32x4 v0, v1; };
DI void mod_sum_load(PartSum& p, char* ws, int lane, int slice) {
    const __amdgpu_buffer_rsrc_t rws = __builtin_amdgcn_make_buffer_rsrc(ws, 0, WS_BYTES, 0x00020000);
    const unsigned o = WS_PART + (slice * 8 + (lane >> 3)) * 256 + (lane & 7) * 16;
    p.v0 = __builtin_amdgcn_raw_buffer_load_b128(rws, o, 0, 16); p.v1 = __builtin_amdgcn_raw_buffer_load_b128(rws, o + 128, 0, 16); }
DI bool mod_sum_ready(const PartSum& p, unsigned epoch) { return __all(p.v0[1] == epoch && p.v0[3] == epoch && p.v1[1] == epoch && p.v1[3] == epoch); }
DI void mod_sum_finish(PartSum& p, char* ws, unsigned epoch, int lane, int slice) {
    for (unsigned sp = 0; sp < SPIN_CAP; ++sp) { if (mod_sum_ready(p, epoch)) break; asm volatile("" ::: "memory"); mod_sum_load(p, ws, lane, slice); }
    float s[4] = {__uint_as_float(p.v0[0]), __uint_as_float(p.v0[2]), __uint_as_float(p.v1[0]), __uint_as_float(p.v1[2])};
#pragma unroll
    for (int i = 0; i < 4; ++i) { float v = s[i]; v += dppf<0x128>(v); s[i] = xsum32(xsum16(v)); }
    const unsigned long long g0 = ((unsigned long long)epoch << 32) | pk2(s[0], s[1]), g1 = ((unsigned long long)epoch << 32) | pk2(s[2], s[3]);
    const unsigned base = slice < 32 ? WS_MODU + slice * 128 : WS_MODN + (slice - 32) * 128, rs = slice < 32 ? 4096 : 8192, rep = lane >> 3, o = base + (lane & 7) * 8;
    stg64_sc1(ws, o + rep * rs, g0); stg64_sc1(ws, o + 64 + rep * rs, g1); stg64_sc1(ws, o + (rep + 8) * rs, g0); stg64_sc1(ws, o + 64 + (rep + 8) * rs, g1);
}

__global__ __launch_bounds__(576, 3) void fused_block(const u16* __restrict__ x, const u16* __restrict__ wqkv, const u16* __restrict__ wo, const u16* __restrict__ w1, const u16* __restrict__ b1,
                                                      const u16* __restrict__ w2, const u16* __restrict__ b2, const u16* __restrict__ cvec, const u16* __restrict__ wmod, u16* __restrict__ out, char* __restrict__ ws) {
    unsigned long long tm0 = 0, tm1 = 0;
    const unsigned epoch = *(const unsigned*)(ws + WS_E) + 1u;
    const int tid = threadIdx.x, lane = tid & 63, w = __builtin_amdgcn_readfirstlane(tid >> 6);
    const int b = blockIdx.x, xl = b & 7, idx = b >> 3;
    const int g = 4 * (xl >> 1) + (idx >> 3), j = 8 * (xl & 1) + (idx & 7);
    const int ct = j & 7, rh = j >> 3;
    unsigned long long code_pc; unsigned code_len;
    asm volatile("s_getpc_b64 %0\nfb_code_begin:\n\ts_mov_b32 %1, fb_code_end-fb_code_begin" : "=s"(code_pc), "=s"(code_len));
    STAMP(0); STAMPC(20);
    if (w == 8) {
        if (!NO_C) {
            const unsigned o = ((unsigned)idx * 32u + (lane & 31)) * 64u;
            warm4((int)o < (int)code_len ? o : 0u, (const void*)code_pc);
        }
        if (K_SLOW) {
            const unsigned ls = (3u - (unsigned)((size_t)wqkv >> 7)) & 7u;
            if ((ls >> 2) == (unsigned)(xl & 1)) { const unsigned l_ = lane < 48 ? lane : 0; warm4((16 * idx + l_ / 3) * 3072u + (l_ % 3) * 1024u + ls * 128u, wqkv); } }
        if (!NO_B) {
            const char* px_ = (const char*)x + b * 8192; const char* pw_ = (const char*)wqkv + b * 6144;
            warm4(lane * 64u, px_); warm4(lane * 64u + 4096u, px_); warm4(lane * 64u, pw_); warm4(lane < 32 ? lane * 64u + 4096u : 0u, pw_); }
        if (!NO_A) {
            unsigned o_ = lane < 4 ? WS_FLAG + (lane * 256 + b) * 256 : lane < 10 ? WS_MODU + b * 768 + (lane - 4) * 128 : lane < 22 ? WS_ST + b * 1536 + (lane - 10) * 128 : lane < 28 ? WS_PART + b * 768 + (lane - 22) * 128 : WS_FLAG + b * 256;
            warm4(o_, ws); }
        if (lane == 0) stg_sc1(ws, WS_HELLO + b * 4, epoch);
        __builtin_amdgcn_s_setprio(3);
        const unsigned sw0 = (lane >> 3) * 1024u + ct * 128u + (((lane & 7) >> 1) ^ ((lane >> 4) & 3)) * 32u + (lane & 1) * 16u, sw1 = sw0 + 7168, sw2 = sw0 + 14336, sw3 = sw0 + 21504;
        unsigned w1o[7], wqo[3];
#pragma unroll
        for (int r = 0; r < 7; ++r) { const unsigned u = r * 64 + lane, row = u / 14, c = u % 14; w1o[r] = row * 3584u + 224u * j + c * 16u - (r & 3) * 1024u; }
#pragma unroll
        for (int r = 0; r < 3; ++r) { const unsigned u = r * 64 + lane, row = u / 12, c = u % 12; wqo[r] = row * 3072u + (c >> 2) * 1024u + 64u * ((j + JROT) & 15) + (c & 3) * 16u - r * 1024u; }
#define RUN128(base, first, n4, lds0) { const char* p_ = (const char*)(base) + (size_t)(first) * 8192; unsigned l_ = (lds0); _Pragma("unroll 2") for (int i_ = 0; i_ < (n4); ++i_) { dma4(sw0, sw1, sw2, sw3, p_, l_); p_ += 32768; l_ += 4096; } }
#define W1GRPS(k0, k1) { const char* p_ = (const char*)w1 + (size_t)(k0) * 114688; unsigned l_ = L_W1 + 7 * (k0) * 1024; _Pragma("unroll 1") for (int k_ = (k0); k_ < (k1); ++k_) { \
            dma4(w1o[0], w1o[1], w1o[2], w1o[3], p_, l_); dma3(w1o[4], w1o[5], w1o[6], p_, l_ + 4096); p_ += 114688; l_ += 7168; } }
#define W2CHUNKS(c0, c1, slot0) { _Pragma("unroll 1") for (int c_ = (c0); c_ < (c1); ++c_) { const char* p_ = (const char*)w2 + (size_t)c_ * 32768; const unsigned l_ = ((slot0) + c_ - (c0)) * 16384; \
            dma4(sw0, sw1, sw2, sw3, p_, l_); dma4(sw0, sw1, sw2, sw3, p_ + 56 * 8192, l_ + 4096); dma4(sw0, sw1, sw2, sw3, p_ + 112 * 8192, l_ + 8192); dma4(sw0, sw1, sw2, sw3, p_ + 168 * 8192, l_ + 12288); } }
#define WQGRPS(k0, k1) { const char* p_ = (const char*)wqkv + (size_t)(k0) * 49152; unsigned l_ = L_WQKV + 3 * (k0) * 1024; _Pragma("unroll 2") for (int k_ = (k0); k_ < (k1); ++k_) { dma3(wqo[0], wqo[1], wqo[2], p_, l_); p_ += 49152; l_ += 3072; } }
#define WARM128(base, row0, n, GR) { const char* p_ = (const char*)(base) + (size_t)(row0) * 1024; const unsigned v_ = GR == 64 ? (lane >> 1) * 1024u + ct * 128u + (lane & 1) * 64u : lane * 1024u + ct * 128u; \
            _Pragma("unroll 1") for (int i_ = 0; i_ < (n); ++i_) { warm4(v_, p_); p_ += (GR == 64 ? 32768 : 65536); } }
#define WARMW1(row0, n16) { const char* p_ = (const char*)w1 + (size_t)(row0) * 3584; const unsigned c_ = (224u * j) & ~63u, v_ = (lane >> 2) * 3584u + c_ + (lane & 3) * 64u; \
            _Pragma("unroll 1") for (int i_ = 0; i_ < (n16); ++i_) { warm4(v_, p_); p_ += 16 * 3584; } }
        __builtin_amdgcn_s_barrier();
        __builtin_amdgcn_s_barrier();
        WQGRPS(14, 24)
        __builtin_amdgcn_s_barrier();
        WQGRPS(0, 14)
        VMWAIT(0);
        __builtin_amdgcn_s_barrier();
        {
            const char* p_ = b < 32 ? (const char*)wo + b * 16384 : b < 144 ? (const char*)w1 + (b - 32) * 16384 : (const char*)w2 + (b - 144) * 16384;
#pragma unroll
            for (int i = 0; i < 4; ++i) warm4(lane * 64u + i * 4096u, p_); }
        if (K_NU == 0) {
        mod_part(cvec, wmod, ws, epoch, lane, 32 + 8 * xl + (idx & 3), idx >> 2); mod_part(cvec, wmod, ws, epoch, lane, 36 + 8 * xl + (idx & 3), idx >> 2);
        if (idx < 4) { PartSum p; mod_sum_load(p, ws, lane, 32 + 8 * xl + idx); mod_sum_finish(p, ws, epoch, lane, 32 + 8 * xl + idx); mod_sum_load(p, ws, lane, 36 + 8 * xl + idx); mod_sum_finish(p, ws, epoch, lane, 36 + 8 * xl + idx); }
        }
        if (K_NU == 2) { mod_part(cvec, wmod, ws, epoch, lane, 32 + 8 * xl + (idx & 3), idx >> 2); mod_part(cvec, wmod, ws, epoch, lane, 36 + 8 * xl + (idx & 3), idx >> 2); }
        __builtin_amdgcn_s_barrier();
        __builtin_amdgcn_s_barrier();
        if (g >= 1) {
            const unsigned fo = WS_FLAG + (((g >= 2 ? g - 2 + (lane & 1) : 0) * 16 + j) * 256);
            for (unsigned sp = 0; sp < SPIN_CAP; ++sp) { unsigned f = ldg_sc1(ws, fo); if (__all(f == epoch)) break; }
            const unsigned lo16 = lane * 16;
#pragma unroll 1
            for (int src = (g >= 2 ? 0 : 1); src < 2; ++src) {
                const char* p_ = ws + WS_KV + (size_t)((g - 2 + src) * 16 + j) * 16384;
                dma4lin_c(lo16, p_, L_KIMG + src * 8192); dma4lin_c(lo16, p_ + 4096, L_KIMG + src * 8192 + 4096);
                dma4lin_c(lo16, p_ + 8192, L_VIMG + src * 8192); dma4lin_c(lo16, p_ + 12288, L_VIMG + src * 8192 + 4096);
            }
        }
        VMWAIT(0);
        __builtin_amdgcn_s_barrier();
        if (K_NU == 2 && (idx & 3) == 2) { PartSum p; mod_sum_load(p, ws, lane, 32 + 8 * xl + (idx >> 2)); mod_sum_finish(p, ws, epoch, lane, 32 + 8 * xl + (idx >> 2)); }
        RUN128(wo, 0, 16, L_WO)
        W1GRPS(3, 7)
        W1GRPS(15, 16)
        if (K_NU == 1) {
        mod_part(cvec, wmod, ws, epoch, lane, 32 + 8 * xl + (idx & 3), idx >> 2); mod_part(cvec, wmod, ws, epoch, lane, 36 + 8 * xl + (idx & 3), idx >> 2);
        if (idx < 4) { PartSum p; mod_sum_load(p, ws, lane, 32 + 8 * xl + idx); mod_sum_finish(p, ws, epoch, lane, 32 + 8 * xl + idx); mod_sum_load(p, ws, lane, 36 + 8 * xl + idx); mod_sum_finish(p, ws, epoch, lane, 36 + 8 * xl + idx); }
        }
        WARM128(w2, 448 * (MAP4 ? (g & 3) : rh + 2 * (g & 1)), 7, 128)
        __builtin_amdgcn_s_barrier();
        __builtin_amdgcn_s_sleep(2);
        W1GRPS(7, 10)
        VMWAIT(21);
        __builtin_amdgcn_s_barrier();
        __builtin_amdgcn_s_sleep(K_W1BS);
        W1GRPS(10, 15)
        __builtin_amdgcn_s_barrier();
        __builtin_amdgcn_s_barrier();
        __builtin_amdgcn_s_barrier();
        W1GRPS(0, 3)
        __builtin_amdgcn_s_barrier();
        W2CHUNKS(0, 2, 0)
        VMWAIT(32);
        __builtin_amdgcn_s_barrier();
        __builtin_amdgcn_s_barrier();
        __builtin_amdgcn_s_barrier();
        __builtin_amdgcn_s_barrier();
        W2CHUNKS(2, 6, 2)
        __builtin_amdgcn_s_barrier();
        W2CHUNKS(6, 9, 6)
        __builtin_amdgcn_s_barrier();
        __builtin_amdgcn_s_sleep(2);
        W2CHUNKS(9, 10, 9)
        VMWAIT(16);
        __builtin_amdgcn_s_barrier();
        __builtin_amdgcn_s_barrier();
        VMWAIT(0);
        __builtin_amdgcn_s_barrier();
        __builtin_amdgcn_s_barrier();
        __builtin_amdgcn_s_barrier();
        return;
    }
    const __amdgpu_buffer_rsrc_t rws = __builtin_amdgcn_make_buffer_rsrc(ws, 0, WS_BYTES, 0x00020000);
    const int m = w & 3, kh = w >> 2;
#define XTILE(t) { const unsigned xo = (unsigned)(128 * g + 32 * (t)) * 1024u + ((unsigned)lane << 4); \
        _Pragma("unroll 2") for (int i = 0; i < 32; i += 4) dma4(xo ^ (unsigned)((i & 15) << 4), xo ^ (unsigned)(((i & 15) + 1) << 4), xo ^ (unsigned)(((i & 15) + 2) << 4), xo ^ (unsigned)(((i & 15) + 3) << 4), \
                                                                 (const char*)x + i * 1024, L_X + (3 - (t)) * 32768 + i * 1024); }
    PartSum psum;
    if (w >= 4 && w < 7) XTILE(w - 4)
    if (w == 0) { lds_st<u32x4>(L_ONES, (u32x4){0x3F803F80u, 0x3F803F80u, 0x3F803F80u, 0x3F803F80u}); lds_st<u32x4>(L_ZERO2, (u32x4){0u, 0u, 0u, 0u}); }
    if (w == 3) { __builtin_amdgcn_s_setprio(2); mod_part(cvec, wmod, ws, epoch, lane, 4 * xl + (idx & 3), idx >> 2); __builtin_amdgcn_s_setprio(0); }
    STAMP(1); WS6(0);
    bf16x8 qf; u32x4 kpk, vpk; float qown[8], rstq;
    {
    const int h = lane >> 5, gi = lane >> 4, q4 = (lane & 15) >> 2, p4 = lane & 3;
    u32x4 xr[16];
#define XFRAGS() { const unsigned tok = lane & 31, rb = L_X + (3 - m) * 32768 + tok * 1024 + kh * 512; \
        _Pragma("unroll") for (int s = 0; s < 16; ++s) xr[s] = lds_ld<u32x4>(rb + (s >> 3) * 256 + ((((2 * (s & 7) + h) ^ (tok & 15))) << 4)); \
        _Pragma("unroll") for (int s = 0; s < 16; ++s) PIN(xr[s]); }
    if (w == 4) VMWAIT(0);
    BAR();
    WS6(1);
    if (w == 4) XTILE(3)
    if (w == 7) {
        unsigned wq[3];
#pragma unroll
        for (int r = 0; r < 3; ++r) { const unsigned u = r * 64 + lane, row = u / 12, c = u % 12; wq[r] = row * 3072u + (c >> 2) * 1024u + 64u * ((j + JROT) & 15) + (c & 3) * 16u - r * 1024u; }
#pragma unroll 2
        for (int k = 24; k < 32; ++k) dma3(wq[0], wq[1], wq[2], (const char*)wqkv + (size_t)k * 49152, L_WQKV + 3 * k * 1024); }
    if (w == 3 && (idx & 7) == 0) mod_sum_load(psum, ws, lane, 4 * xl + (idx >> 3));
    if (m == 0) XFRAGS()
    if (w == 5 || w == 6) VMWAIT(0);
    BAR();
    WS6(2);
    bool sum_open = w == 3 && (idx & 7) == 0;
    if (sum_open) {
        for (int k = 0; k < K_POLL; ++k) { if (mod_sum_ready(psum, epoch)) break; asm volatile("" ::: "memory"); mod_sum_load(psum, ws, lane, 4 * xl + (idx >> 3)); }
        if (mod_sum_ready(psum, epoch)) { mod_sum_finish(psum, ws, epoch, lane, 4 * xl + (idx >> 3)); sum_open = false; } else { asm volatile("" ::: "memory"); mod_sum_load(psum, ws, lane, 4 * xl + (idx >> 3)); } }
    if (m == 1 || m == 2) XFRAGS()
    if (w == 4) VMWAIT(0);
    BAR();
    if (sum_open) mod_sum_finish(psum, ws, epoch, lane, 4 * xl + (idx >> 3));
    if (m == 3) XFRAGS()
#undef XFRAGS
#undef XTILE
    STAMP(2);
    const unsigned gmo = WS_MODU + g * 4096 + (tid & 255) * 16; u32x4 gmv;
    if (w < 4) gmv = __builtin_amdgcn_raw_buffer_load_b128(rws, gmo, 0, 16);
    SBAR();
    {
        float s0 = 0.f, s1 = 0.f, s2 = 0.f, s3 = 0.f; const bf2 one = {(__bf16)1.0f, (__bf16)1.0f};
#pragma unroll
        for (int s = 0; s < 16; ++s)
#pragma unroll
            for (int i = 0; i < 4; i += 2) { const unsigned ua = xr[s][i], ub = xr[s][i + 1]; const bf2 va = __builtin_bit_cast(bf2, ua), vb = __builtin_bit_cast(bf2, ub);
                s0 = __builtin_amdgcn_fdot2_f32_bf16(va, one, s0, false); s1 = __builtin_amdgcn_fdot2_f32_bf16(va, va, s1, false);
                s2 = __builtin_amdgcn_fdot2_f32_bf16(vb, one, s2, false); s3 = __builtin_amdgcn_fdot2_f32_bf16(vb, vb, s3, false); }
        s0 = xsum32(s0 + s2); s1 = xsum32(s1 + s3);
        if (lane < 32) lds_st<u32x2>(L_STAT + (kh * 128 + 32 * m + lane) * 8, (u32x2){__float_as_uint(s0), __float_as_uint(s1)});
    }
    u32x4 m1, m2;
    { const int kr = lane & 31, j1 = kr - 8 * h, j2 = kr - 16 - 8 * h; const unsigned hm = (kr & 1) ? 0xFFFF0000u : 0x0000FFFFu;
#pragma unroll
      for (int d = 0; d < 4; ++d) { m1[d] = (j1 >= 0 && j1 < 8 && (j1 >> 1) == d) ? hm : 0u; m2[d] = (j2 >= 0 && j2 < 8 && (j2 >> 1) == d) ? hm : 0u; }
      PIN(m1); PIN(m2); }
    WS6(3);
    if (w < 4) {
        for (unsigned sp = 0; sp < SPIN_CAP; ++sp) { if (__all(gmv[1] == epoch && gmv[3] == epoch)) break; asm volatile("" ::: "memory"); gmv = __builtin_amdgcn_raw_buffer_load_b128(rws, gmo, 0, 16); }
        unsigned u0 = gmv[0], u2 = gmv[2];
        if (tid >= 128) { u0 = pk2(1.f + bf_lo(u0), 1.f + bf_hi(u0)); u2 = pk2(1.f + bf_lo(u2), 1.f + bf_hi(u2)); }
        lds_st<u32x4>(L_SAG + tid * 16, (u32x4){__builtin_amdgcn_perm(u0, u0, 0x01000100u), __builtin_amdgcn_perm(u0, u0, 0x03020302u), __builtin_amdgcn_perm(u2, u2, 0x01000100u), __builtin_amdgcn_perm(u2, u2, 0x03020302u)});
    }
    STAMP(3);
    VMWAIT(0);
    BAR();
    STAMP(4); STAMPC(22); WS1(0);
    float rstd;
    u32x4 b3;
    { const u32x2 a = lds_ld<u32x2>(L_STAT + (32 * m + (lane & 31)) * 8), c2 = lds_ld<u32x2>(L_STAT + (128 + 32 * m + (lane & 31)) * 8);
      const float s0 = __uint_as_float(a[0]) + __uint_as_float(c2[0]), s1 = __uint_as_float(a[1]) + __uint_as_float(c2[1]);
      const float mean = s0 * (1.f / 512.f), var = fmaxf(s1 * (1.f / 512.f) - mean * mean, 0.f) + 1e-5f;
      rstd = __builtin_amdgcn_rsqf(var); const float ri = var * rstd;
      const float mh = rbf(-mean), rh_ = rbf(ri);
      b3[0] = h ? 0u : pk2(mh, -mean - mh); b3[1] = h ? 0u : pk2(rh_, ri - rh_); b3[2] = 0u; b3[3] = 0u; }
    f32x16 aQ, aK, aV;
#pragma unroll
    for (int i = 0; i < 16; ++i) { aQ[i] = 0.f; aK[i] = 0.f; aV[i] = 0.f; }
    {
        const unsigned rowa = L_WQKV + (256 * kh + 4 * (gi >> 1) + q4) * 192 + p4 * 8;
        const unsigned wqa = rowa + (((gi & 1) ^ kh) * 32), wva = rowa + 128 + (gi & 1) * 32;
        const unsigned hz = h ? 0u : 0xFFFFFFFFu, ga = L_SAG + 2048 + (256 * kh + (lane & 31)) * 4;
        const f32x16 z0 = {0.f, 0.f, 0.f, 0.f, 0.f, 0.f, 0.f, 0.f, 0.f, 0.f, 0.f, 0.f, 0.f, 0.f, 0.f, 0.f};
        unsigned gg = lds_ld<unsigned>(ga), ss = lds_ld<unsigned>(ga - 2048);
        f32x16 dA, dB; bf16x8 fk, fv;
#define DIAG(D, p) { const u32x4 a1 = {gg & m1[0], gg & m1[1], gg & m1[2], gg & m1[3]}, a2 = {gg & m2[0], gg & m2[1], gg & m2[2], gg & m2[3]}, a3 = {gg & hz, ss & hz, 0u, 0u}; \
            if ((p) + 1 < 8) { gg = lds_ld<unsigned>(ga + ((p) + 1) * 128); ss = lds_ld<unsigned>(ga - 2048 + ((p) + 1) * 128); } \
            D = MFMA32(__builtin_bit_cast(bf16x8, a3), __builtin_bit_cast(bf16x8, b3), z0); D = MFMA32(__builtin_bit_cast(bf16x8, a1), __builtin_bit_cast(bf16x8, xr[2 * (p)]), D); \
            D = MFMA32(__builtin_bit_cast(bf16x8, a2), __builtin_bit_cast(bf16x8, xr[2 * (p) + 1]), D); PIN(D); }
#define PACK(D, p) { xr[2 * (p)] = (u32x4){pk2(D[0], D[1]), pk2(D[2], D[3]), pk2(D[4], D[5]), pk2(D[6], D[7])}; xr[2 * (p) + 1] = (u32x4){pk2(D[8], D[9]), pk2(D[10], D[11]), pk2(D[12], D[13]), pk2(D[14], D[15])}; \
            PIN(xr[2 * (p)]); PIN(xr[2 * (p) + 1]); }
        DIAG(dA, 0) SBAR(); DIAG(dB, 1) SBAR();
        fk = trfrag(wqa + 64, wqa + 64 + 1536); fv = trfrag(wva, wva + 1536);
        PACK(dA, 0) SBAR();
#define QKV_PAIR(p, DN, DP) { \
            aK = MFMA32(fk, __builtin_bit_cast(bf16x8, xr[2 * (p)]), aK); PIN(aK); SBAR(); fk = trfrag(wqa + (2 * (p) + 1) * 3072 + 64, wqa + (2 * (p) + 1) * 3072 + 64 + 1536); SBAR(); \
            aV = MFMA32(__builtin_bit_cast(bf16x8, xr[2 * (p)]), fv, aV); PIN(aV); SBAR(); fv = trfrag(wva + (2 * (p) + 1) * 3072, wva + (2 * (p) + 1) * 3072 + 1536); \
            if ((p) + 1 < 8) PACK(DP, (p) + 1) SBAR(); \
            aK = MFMA32(fk, __builtin_bit_cast(bf16x8, xr[2 * (p) + 1]), aK); PIN(aK); SBAR(); if ((p) + 1 < 8) fk = trfrag(wqa + (2 * (p) + 2) * 3072 + 64, wqa + (2 * (p) + 2) * 3072 + 64 + 1536); SBAR(); \
            aV = MFMA32(__builtin_bit_cast(bf16x8, xr[2 * (p) + 1]), fv, aV); PIN(aV); SBAR(); if ((p) + 1 < 8) fv = trfrag(wva + (2 * (p) + 2) * 3072, wva + (2 * (p) + 2) * 3072 + 1536); SBAR(); \
            if ((p) + 2 < 8) DIAG(DN, (p) + 2) SBAR(); }
        WS1(1);
        QKV_PAIR(0, dA, dB) QKV_PAIR(1, dB, dA) QKV_PAIR(2, dA, dB) QKV_PAIR(3, dB, dA) QKV_PAIR(4, dA, dB) QKV_PAIR(5, dB, dA) QKV_PAIR(6, dA, dB) QKV_PAIR(7, dB, dA)
#undef QKV_PAIR
#undef PACK
#undef DIAG
        STAMP(5); STAMPC(23); WS1(2);
        float vs[8], vk[8];
#pragma unroll
        for (int i = 0; i < 8; ++i) { float lo = aV[i], hi = aV[8 + i]; PIN(lo); PIN(hi); vs[i] = kh ? lo : hi; vk[i] = kh ? hi : lo; }
        const unsigned ex = w * 6144 + lane * 16;
        lds_st<f32x4>(ex, (f32x4){aK[8], aK[9], aK[10], aK[11]}); lds_st<f32x4>(ex + 1024, (f32x4){aK[12], aK[13], aK[14], aK[15]});
        lds_st<f32x4>(ex + 2048, (f32x4){vs[0], vs[1], vs[2], vs[3]}); lds_st<f32x4>(ex + 3072, (f32x4){vs[4], vs[5], vs[6], vs[7]});
        bf16x8 fq = trfrag(wqa, wqa + 1536), fq1 = trfrag(wqa + 3072, wqa + 3072 + 1536);
        const unsigned rn = 49152 + w * 128;
        if (lane < 32) lds_st<float>(rn + lane * 4, rstd);
        asm volatile("s_waitcnt lgkmcnt(0)" ::: "memory");
        const f32x4 ra = lds_ld<f32x4>(rn + (16 * kh + 4 * h) * 4), rb = lds_ld<f32x4>(rn + (16 * kh + 8 + 4 * h) * 4);
        BAR();
        WS1(3);
        const unsigned ep = (w ^ 4) * 6144 + lane * 16;
        { const f32x4 k0 = lds_ld<f32x4>(ep), k1 = lds_ld<f32x4>(ep + 1024), v0 = lds_ld<f32x4>(ep + 2048), v1 = lds_ld<f32x4>(ep + 3072);
          u32x4 kp, vp;
          kp[0] = pk2((aK[0] + k0[0]) * rstd, (aK[1] + k0[1]) * rstd); kp[1] = pk2((aK[2] + k0[2]) * rstd, (aK[3] + k0[3]) * rstd); kp[2] = pk2((aK[4] + k1[0]) * rstd, (aK[5] + k1[1]) * rstd); kp[3] = pk2((aK[6] + k1[2]) * rstd, (aK[7] + k1[3]) * rstd);
          vp[0] = pk2((vk[0] + v0[0]) * ra[0], (vk[1] + v0[1]) * ra[1]); vp[1] = pk2((vk[2] + v0[2]) * ra[2], (vk[3] + v0[3]) * ra[3]); vp[2] = pk2((vk[4] + v1[0]) * rb[0], (vk[5] + v1[1]) * rb[1]); vp[3] = pk2((vk[6] + v1[2]) * rb[2], (vk[7] + v1[3]) * rb[3]);
          const unsigned kvo = WS_KV + (g * 16 + j) * 16384 + (m * 2 + kh) * 1024 + lane * 16;
          __builtin_amdgcn_raw_buffer_store_b128(kp, rws, kvo, 0, 16);
          __builtin_amdgcn_raw_buffer_store_b128(vp, rws, kvo + 8192, 0, 16);
          PIN(kp); PIN(vp); kpk = kp; vpk = vp; }
        SBAR();
#pragma unroll
        for (int s = 0; s < 16; s += 2) {
            aQ = MFMA32(fq, __builtin_bit_cast(bf16x8, xr[s]), aQ); PIN(aQ); SBAR(); if (s + 2 < 16) fq = trfrag(wqa + (s + 2) * 3072, wqa + (s + 2) * 3072 + 1536); SBAR();
            aQ = MFMA32(fq1, __builtin_bit_cast(bf16x8, xr[s + 1]), aQ); PIN(aQ); SBAR(); if (s + 3 < 16) fq1 = trfrag(wqa + (s + 3) * 3072, wqa + (s + 3) * 3072 + 1536); SBAR(); }
        lds_st<f32x4>(ex + 4096, (f32x4){aQ[8], aQ[9], aQ[10], aQ[11]}); lds_st<f32x4>(ex + 5120, (f32x4){aQ[12], aQ[13], aQ[14], aQ[15]});
#pragma unroll
        for (int i = 0; i < 8; ++i) qown[i] = aQ[i];
        rstq = rstd;
    }
    }
    WS1(4);
    VMWAIT(0); WS1(5);
    BAR();
    if (tid == 0) stg_sc1(ws, WS_FLAG + (0 * 256 + g * 16 + j) * 256, epoch);
    STAMP(6);
    { FRESH(lane);
      const unsigned ep = (w ^ 4) * 6144 + 4096 + lane * 16; const f32x4 q0 = lds_ld<f32x4>(ep), q1 = lds_ld<f32x4>(ep + 1024);
      lds_st<u32x4>(L_KIMG + (8 + m) * 2048 + kh * 1024 + lane * 16, kpk);
      lds_st<u32x4>(L_VIMG + (8 + m) * 2048 + kh * 1024 + lane * 16, vpk);
      const float qs = 0.25f * 1.44269504089f * rstq; u32x4 qp;
      qp[0] = pk2((qown[0] + q0[0]) * qs, (qown[1] + q0[1]) * qs); qp[1] = pk2((qown[2] + q0[2]) * qs, (qown[3] + q0[3]) * qs);
      qp[2] = pk2((qown[4] + q1[0]) * qs, (qown[5] + q1[1]) * qs); qp[3] = pk2((qown[6] + q1[2]) * qs, (qown[7] + q1[3]) * qs);
      qf = __builtin_bit_cast(bf16x8, qp); PIN(qf); }
    const int tt = w & 3;
    {
    FRESH(lane);
    const int hh = kh, qq = lane & 31, h = lane >> 5;
    const float NEG = -__builtin_inff(), fq0 = (float)(4 * h - qq) - 0.5f;
    f32x16 c0, c8;
#pragma unroll
    for (int r = 0; r < 16; ++r) {
        const float t = fmaf(fq0, 1e30f, (float)((r & 3) + 8 * (r >> 2)) * 1e30f); c0[r] = fminf(t, 0.f); c8[r] = fminf(-t, 0.f); }
    PIN(c0); PIN(c8);
    BAR();
    STAMP(8); WS2(0);
    {
        const unsigned ka = L_KIMG + m * 2048 + hh * 1024 + lane * 16;
        const bool isd = ((lane >> 4) & 1) == hh;
        const unsigned va = isd ? L_VIMG + m * 2048 + lane * 16 : L_ONES, vst = isd ? 2048u : 0u, vs1 = isd ? 1024u : 0u;
        f32x16 o;
#pragma unroll
        for (int r = 0; r < 16; ++r) o[r] = 0.f;
#define MAX16(mx, s) { _Pragma("unroll") for (int r = 0; r < 16; r += 2) mx = fmaxf(fmaxf(mx, s[r]), s[r + 1]); }
#define EXPH(P, S, b) { P[0] = pk2(__builtin_amdgcn_exp2f(S[b]), __builtin_amdgcn_exp2f(S[b + 1])); P[1] = pk2(__builtin_amdgcn_exp2f(S[b + 2]), __builtin_amdgcn_exp2f(S[b + 3])); \
                        P[2] = pk2(__builtin_amdgcn_exp2f(S[b + 4]), __builtin_amdgcn_exp2f(S[b + 5])); P[3] = pk2(__builtin_amdgcn_exp2f(S[b + 6]), __builtin_amdgcn_exp2f(S[b + 7])); }
        if (g >= 2) {
            f32x16 z; const f32x16 z0 = {0.f, 0.f, 0.f, 0.f, 0.f, 0.f, 0.f, 0.f, 0.f, 0.f, 0.f, 0.f, 0.f, 0.f, 0.f, 0.f};
            float mx = NEG, mx2 = NEG; f32x16 sA, sB;
#define KF(i) lds_ld<bf16x8>(ka + (i) * 2048)
            bf16x8 k0 = KF(0), k1 = KF(8), k2 = KF(1), k3 = KF(2);
            SBAR();
            sA = MFMA32(k0, qf, c0); PIN(sA); k0 = KF(3); SBAR();
            sB = MFMA32(k1, qf, c8); PIN(sB); k1 = KF(4); SBAR(); MAX16(mx, sA); PIN(mx); SBAR();
            sA = MFMA32(k2, qf, z0); PIN(sA); k2 = KF(5); SBAR(); MAX16(mx2, sB); PIN(mx2); SBAR();
            sB = MFMA32(k3, qf, z0); PIN(sB); k3 = KF(6); SBAR(); MAX16(mx, sA); PIN(mx); SBAR();
            sA = MFMA32(k0, qf, z0); PIN(sA); k0 = KF(7); SBAR(); MAX16(mx2, sB); PIN(mx2); SBAR();
            sB = MFMA32(k1, qf, z0); PIN(sB); k1 = KF(0); SBAR(); MAX16(mx, sA); PIN(mx); SBAR();
            sA = MFMA32(k2, qf, z0); PIN(sA); k2 = KF(1); SBAR(); MAX16(mx2, sB); PIN(mx2); SBAR();
            sB = MFMA32(k3, qf, z0); PIN(sB); SBAR(); MAX16(mx, sA); PIN(mx); SBAR();
            sA = MFMA32(k0, qf, z0); PIN(sA); SBAR(); MAX16(mx2, sB); PIN(mx2); SBAR();
            WS2(1);
            bf16x8 v0 = lds_ld<bf16x8>(va), v1 = lds_ld<bf16x8>(va + vs1);
            MAX16(mx, sA);
            mx = xmax32(fmaxf(mx, mx2));
#pragma unroll
            for (int r = 0; r < 16; ++r) { z[r] = -mx; c0[r] -= mx; c8[r] -= mx; }
            PIN(z); PIN(c0); PIN(c8); SBAR();
            WS2(2);
            sA = MFMA32(k1, qf, c0); SBAR();
            u32x4 pa, pb; bf16x8 v0n, v1n, kn = k2; unsigned vp = va;
#define ABLK(I, SC, SN, CN) { bf16x8 kc = kn; if ((I) < 8) { vp += vst; v0n = lds_ld<bf16x8>(vp); v1n = lds_ld<bf16x8>(vp + vs1); } if ((I) < 7) kn = KF((I) + 2); SBAR(); \
                if ((I) < 8) { SN = MFMA32(kc, qf, CN); PIN(SN); } SBAR(); EXPH(pa, SC, 0); PIN(pa); SBAR(); \
                o = MFMA32(v0, __builtin_bit_cast(bf16x8, pa), o); PIN(o); SBAR(); EXPH(pb, SC, 8); PIN(pb); SBAR(); \
                o = MFMA32(v1, __builtin_bit_cast(bf16x8, pb), o); PIN(o); SBAR(); if ((I) < 8) { v0 = v0n; v1 = v1n; } }
            ABLK(0, sA, sB, z) ABLK(1, sB, sA, z) ABLK(2, sA, sB, z) ABLK(3, sB, sA, z)
            ABLK(4, sA, sB, z) ABLK(5, sB, sA, z) ABLK(6, sA, sB, z) ABLK(7, sB, sA, c8) ABLK(8, sA, sB, c8)
#undef KF
#undef ABLK
        } else {
            const int i0 = 8 - 4 * g - m;
            f32x16 z;
#pragma unroll
            for (int r = 0; r < 16; ++r) z[r] = 0.f;
            float mx = NEG;
            f32x16 s = MFMA32(lds_ld<bf16x8>(ka + 8 * 2048), qf, c8); MAX16(mx, s);
#pragma unroll 1
            for (int i = i0; i < 8; ++i) { s = MFMA32(lds_ld<bf16x8>(ka + i * 2048), qf, z); MAX16(mx, s); }
            mx = xmax32(mx);
#pragma unroll
            for (int r = 0; r < 16; ++r) { z[r] = -mx; c8[r] -= mx; }
#pragma unroll 1
            for (int i = i0; i <= 8; ++i) { u32x4 pa, pb;
                if (i < 8) s = MFMA32(lds_ld<bf16x8>(ka + i * 2048), qf, z); else s = MFMA32(lds_ld<bf16x8>(ka + i * 2048), qf, c8);
                EXPH(pa, s, 0); EXPH(pb, s, 8);
                o = MFMA32(lds_ld<bf16x8>(va + i * vst), __builtin_bit_cast(bf16x8, pa), o); o = MFMA32(lds_ld<bf16x8>(va + i * vst + vs1), __builtin_bit_cast(bf16x8, pb), o); }
        }
#undef EXPH
#undef MAX16
        WS2(3);
        float ov[8], l;
        { float lo = o[0], hi = o[8]; PIN(lo); PIN(hi); l = hh ? lo : hi; }
#pragma unroll
        for (int r = 0; r < 8; ++r) { float lo = o[r], hi = o[8 + r]; PIN(lo); PIN(hi); ov[r] = hh ? hi : lo; }
        const float inv = __builtin_amdgcn_rcpf(l);
        const u32x4 op = {pk2(ov[0] * inv, ov[1] * inv), pk2(ov[2] * inv, ov[3] * inv), pk2(ov[4] * inv, ov[5] * inv), pk2(ov[6] * inv, ov[7] * inv)};
        __builtin_amdgcn_raw_buffer_store_b128(op, rws, WS_O + ((g * 8 + 2 * m + (qq >> 4)) * 16 + j) * 1024 + ((qq & 15) + 16 * (2 * hh + h)) * 16, 0, 16);
    }
    }
    WS2(4);
    VMWAIT(0); WS2(5);
    BAR();
    if (tid == 0) stg_sc1(ws, WS_FLAG + (1 * 256 + g * 16 + j) * 256, epoch);
    STAMP(9);
    if (w >= 4) {
        const int t = tid - 256; const unsigned o = WS_MODN + g * 8192 + t * 32; u32x4 v, v2;
        for (unsigned sp = 0; sp < SPIN_CAP; ++sp) { asm volatile("" ::: "memory"); v = __builtin_amdgcn_raw_buffer_load_b128(rws, o, 0, 16); v2 = __builtin_amdgcn_raw_buffer_load_b128(rws, o + 16, 0, 16);
            if (__all(v[1] == epoch && v[3] == epoch && v2[1] == epoch && v2[3] == epoch)) break; }
        u32x4 val = {v[0], v[2], v2[0], v2[2]};
        const int vec = t >> 6, c8 = (t & 63) * 8;
        if (vec == 1) lds_st<u32x4>(L_SB16 + c8 * 2, val);
        else if (vec == 2) {
#pragma unroll
            for (int i = 0; i < 4; ++i) { const unsigned u_ = val[i]; val[i] = pk2(1.f + bf_lo(u_), 1.f + bf_hi(u_)); }
            lds_st<u32x4>(L_GB16 + c8 * 2, val);
        } else if ((c8 >> 6) == ct) lds_st<u32x4>((vec == 0 ? L_GAO : L_GBO) + (c8 & 63) * 2, val);
    }
    if (w == 0) poll_flags(ws, WS_FLAG + (1 * 256 + g * 16) * 256, 16, epoch, lane);
    BAR();
    STAMP(10); WS3(0);
    u32x2 x1p0, x1p1;
    {
        FRESH(lane);
        const int gi = lane >> 4, q4 = (lane & 15) >> 2, p4 = lane & 3;
        const unsigned tokP = 128 * g + 64 * rh + 16 * tt + (lane & 15), colP = 64 * ct + 32 * kh + 4 * gi;
        u32x4 of[8];
#pragma unroll
        for (int s = 0; s < 8; ++s) of[s] = __builtin_amdgcn_raw_buffer_load_b128(rws, WS_O + ((g * 8 + 4 * rh + tt) * 16 + 8 * kh + s) * 1024 + lane * 16, 0, 16);
        const u32x2 xres0 = *(const u32x2*)(x + tokP * 512 + colP), xres1 = *(const u32x2*)(x + tokP * 512 + colP + 16);
        SBAR();
        f32x4 acc[4];
#pragma unroll
        for (int n = 0; n < 4; ++n) acc[n] = (f32x4){0.f, 0.f, 0.f, 0.f};
        const unsigned xv = 2 * (gi & 1) + (q4 >> 1), rowa = L_WO + (256 * kh + 16 * (gi >> 1) + 4 * (gi & 1) + q4) * 128 + p4 * 8;
        unsigned an[4]; bf16x8 wf[4];
#pragma unroll
        for (int n = 0; n < 4; ++n) { an[n] = rowa + ((n ^ xv) * 32); wf[n] = trfrag(an[n], an[n] + 1024); }
        const unsigned cl = (32 * kh + 4 * gi) * 2, cg = colP * 2;
        const u32x2 ga0 = lds_ld<u32x2>(L_GAO + cl), ga1 = lds_ld<u32x2>(L_GAO + cl + 32);
        const u32x2 gb0 = lds_ld<u32x2>(L_GB16 + cg), gb1 = lds_ld<u32x2>(L_GB16 + cg + 32), sb0 = lds_ld<u32x2>(L_SB16 + cg), sb1 = lds_ld<u32x2>(L_SB16 + cg + 32);
        SBAR();
#pragma unroll
        for (int s = 0; s < 8; ++s)
#pragma unroll
            for (int n = 0; n < 4; ++n) { acc[n] = MFMA16(wf[n], __builtin_bit_cast(bf16x8, of[s]), acc[n]); PIN(acc[n]); SBAR(); if (s < 7) wf[n] = trfrag(an[n] + (s + 1) * 4096, an[n] + (s + 1) * 4096 + 1024); SBAR(); }
        WS3(1);
        BAR();
        STAMP(11);
        const unsigned ex = w * 2048 + lane * 16;
        { f32x4 s0 = acc[0], s1 = acc[1], s2 = acc[2], s3 = acc[3]; PIN(s0); PIN(s1); PIN(s2); PIN(s3);
          lds_st<f32x4>(ex, kh ? s0 : s2); lds_st<f32x4>(ex + 1024, kh ? s1 : s3); acc[0] = kh ? s2 : s0; acc[1] = kh ? s3 : s1; }
        BAR();
        WS3(2);
        const unsigned ep = (w ^ 4) * 2048 + lane * 16;
        const f32x4 a0 = lds_ld<f32x4>(ep) + acc[0], a1 = lds_ld<f32x4>(ep + 1024) + acc[1];
        float xv1[8], gg[8], ss[8];
#pragma unroll
        for (int r = 0; r < 4; ++r) {
            const unsigned ua0 = ga0[r >> 1], ua1 = ga1[r >> 1], ux0 = xres0[r >> 1], ux1 = xres1[r >> 1], ug0 = gb0[r >> 1], ug1 = gb1[r >> 1], us0 = sb0[r >> 1], us1 = sb1[r >> 1];
            xv1[r] = rbf(fmaf((r & 1) ? bf_hi(ua0) : bf_lo(ua0), a0[r], (r & 1) ? bf_hi(ux0) : bf_lo(ux0))); xv1[4 + r] = rbf(fmaf((r & 1) ? bf_hi(ua1) : bf_lo(ua1), a1[r], (r & 1) ? bf_hi(ux1) : bf_lo(ux1)));
            gg[r] = (r & 1) ? bf_hi(ug0) : bf_lo(ug0); gg[4 + r] = (r & 1) ? bf_hi(ug1) : bf_lo(ug1);
            ss[r] = (r & 1) ? bf_hi(us0) : bf_lo(us0); ss[4 + r] = (r & 1) ? bf_hi(us1) : bf_lo(us1);
        }
        x1p0 = (u32x2){pk2(xv1[0], xv1[1]), pk2(xv1[2], xv1[3])}; x1p1 = (u32x2){pk2(xv1[4], xv1[5]), pk2(xv1[6], xv1[7])};
        float st[6] = {0.f, 0.f, 0.f, 0.f, 0.f, 0.f}, pg[8];
#pragma unroll
        for (int r = 0; r < 8; ++r) pg[r] = xv1[r] * gg[r];
        const u32x4 xg = {pk2(pg[0], pg[1]), pk2(pg[2], pg[3]), pk2(pg[4], pg[5]), pk2(pg[6], pg[7])};
        __builtin_amdgcn_raw_buffer_store_b128(xg, rws, WS_XG + ((g * 8 + 4 * rh + tt) * 16 + 2 * ct + kh) * 1024 + lane * 16, 0, 16);
#pragma unroll
        for (int r = 0; r < 8; ++r) { const float p = pg[r]; st[0] += xv1[r]; st[1] = fmaf(xv1[r], xv1[r], st[1]); st[2] += p; st[3] = fmaf(p, p, st[3]); st[4] = fmaf(p, gg[r], st[4]); st[5] = fmaf(p, ss[r], st[5]); }
#pragma unroll
        for (int i = 0; i < 6; ++i) st[i] = xsum32(xsum16(st[i]));
        if (kh == 1 && lane < 16) { lds_st<f32x4>(L_SCR + (tt * 16 + lane) * 32, (f32x4){st[0], st[1], st[2], st[3]}); lds_st<u32x2>(L_SCR + (tt * 16 + lane) * 32 + 16, (u32x2){__float_as_uint(st[4]), __float_as_uint(st[5])}); }
        WS3(3);
        BAR();
        if (kh == 0 && lane < 16) {
            const f32x4 o4 = lds_ld<f32x4>(L_SCR + (tt * 16 + lane) * 32); const u32x2 o2 = lds_ld<u32x2>(L_SCR + (tt * 16 + lane) * 32 + 16);
            const unsigned so = WS_ST + g * 24576, T = 4 * rh + tt;
            const u32x4 sa4 = {__float_as_uint(st[0] + o4[0]), __float_as_uint(st[1] + o4[1]), __float_as_uint(st[2] + o4[2]), __float_as_uint(st[3] + o4[3])};
            const u32x2 sb2 = {__float_as_uint(st[4] + __uint_as_float(o2[0])), __float_as_uint(st[5] + __uint_as_float(o2[1]))};
            __builtin_amdgcn_raw_buffer_store_b128(sa4, rws, so + ((T * 8 + ct) * 16 + lane) * 16, 0, 16);
            __builtin_amdgcn_raw_buffer_store_b64(sb2, rws, so + 16384 + ((T * 8 + ct) * 16 + lane) * 8, 0, 16);
        }
    }
    WS3(4);
    VMWAIT(0); WS3(5);
    BAR();
    if (tid == 0) stg_sc1(ws, WS_FLAG + (2 * 256 + g * 16 + j) * 256, epoch);
    STAMP(12);
    const int odd = j & 1;
    {
    FRESH(lane);
    const int gi = lane >> 4, q4 = (lane & 15) >> 2, p4 = lane & 3;
    const u32x2 b1v = *(const u32x2*)(b1 + 112 * j + (w > 0 ? w - 1 : 0) * 16 + 4 * gi);
    BAR();
    if (w > 0) {
        const int nt = w - 1; f32x4 acc = {0.f, 0.f, 0.f, 0.f}, acc2 = acc;
        const unsigned ra = L_W1 + (4 * gi + q4) * 224 + p4 * 8 + nt * 32; const int tk = lane & 15;
        const unsigned va = tk == 0 ? L_GB16 + 8 * gi : tk == 1 ? L_SB16 + 8 * gi : tk == 2 ? L_ONES : L_ZERO2, vst = tk < 2 ? 64u : 0u, vhi = tk < 2 ? 32u : 0u;
        u32x2 blo[16], bhi[16];
        { unsigned a_ = va;
#pragma unroll
          for (int s = 0; s < 16; ++s) { blo[s] = lds_ld<u32x2>(a_); bhi[s] = lds_ld<u32x2>(a_ + vhi); a_ += vst; } }
        bf16x8 f0 = trfrag(ra, ra + 3584), f1 = trfrag(ra + 7168, ra + 7168 + 3584);
#pragma unroll
        for (int s = 0; s < 16; s += 2) {
            acc = MFMA16(f0, __builtin_bit_cast(bf16x8, ((u32x4){blo[s][0], blo[s][1], bhi[s][0], bhi[s][1]})), acc); PIN(acc); SBAR(); if (s + 2 < 16) f0 = trfrag(ra + (s + 2) * 7168, ra + (s + 2) * 7168 + 3584); SBAR();
            acc2 = MFMA16(f1, __builtin_bit_cast(bf16x8, ((u32x4){blo[s + 1][0], blo[s + 1][1], bhi[s + 1][0], bhi[s + 1][1]})), acc2); PIN(acc2); SBAR(); if (s + 3 < 16) f1 = trfrag(ra + (s + 3) * 7168, ra + (s + 3) * 7168 + 3584); SBAR();
        }
        acc += acc2;
        if (tk < 3) lds_st<f32x4>(L_UVW + (tk * 112 + nt * 16 + 4 * gi) * 4, acc);
        if (tk == 3) lds_st<f32x4>(L_UVW + (3 * 112 + nt * 16 + 4 * gi) * 4, (f32x4){bf_lo(b1v[0]), bf_hi(b1v[0]), bf_lo(b1v[1]), bf_hi(b1v[1])});
    } else {
        const u32x4 gq = lds_ld<u32x4>(L_GB16 + lane * 16), sq = lds_ld<u32x4>(L_SB16 + lane * 16);
        float c5[5] = {0.f, 0.f, 0.f, 0.f, 0.f};
#pragma unroll
        for (int i = 0; i < 4; ++i) { const unsigned ug = gq[i], us = sq[i]; const float g0 = bf_lo(ug), g1 = bf_hi(ug), s0 = bf_lo(us), s1 = bf_hi(us);
            c5[0] += g0 + g1; c5[1] += s0 + s1; c5[2] += g0 * g0 + g1 * g1; c5[3] += g0 * s0 + g1 * s1; c5[4] += s0 * s0 + s1 * s1; }
#pragma unroll
        for (int i = 0; i < 5; ++i) c5[i] = xsum32(xsum16(rowsum16(c5[i])));
        if (lane == 0) { lds_st<f32x4>(L_CONST, (f32x4){c5[0], c5[1], c5[2], c5[3]}); lds_st<float>(L_CONST + 16, c5[4]); }
        poll_flags(ws, WS_FLAG + (2 * 256 + g * 16) * 256, 16, epoch, lane);
    }
    }
    BAR();
    STAMP(13);
    {
        FRESH(lane);
        const int gi = lane >> 4, q4 = (lane & 15) >> 2, p4 = lane & 3;
        const int Tw = 2 * m + kh;
        u32x4 xf[2][8];
        const unsigned xo0 = WS_XG + (g * 8 + Tw) * 16384 + kh * 8192 + lane * 16, xo1 = WS_XG + (g * 8 + (Tw ^ 1)) * 16384 + kh * 8192 + lane * 16;
        constexpr int WIN = 5;
#pragma unroll
        for (int s = 0; s < WIN; ++s) { xf[0][s] = __builtin_amdgcn_raw_buffer_load_b128(rws, xo0 + s * 1024, 0, 16); xf[1][s] = __builtin_amdgcn_raw_buffer_load_b128(rws, xo1 + s * 1024, 0, 16); }
        const unsigned so = WS_ST + g * 24576 + Tw * 2048, sob = WS_ST + g * 24576 + 16384 + Tw * 1024;
        u32x4 sA0, sA1; u32x2 sB0, sB1;
        SBAR(); WS4(0);
        f32x4 acc[7], acb[7]; bf16x8 wf[7];
        const unsigned ra = L_W1 + (256 * kh + 4 * gi + q4) * 224 + p4 * 8, raA = ra + 32 * odd, raB = ra + (odd ? 0 : 192);
#define W1A(s, u) (((u) < 6 ? raA : raB) + (s) * 7168 + ((u) < 6 ? (u) * 32 : 0))
#pragma unroll
        for (int u = 0; u < 7; ++u) { acc[u] = (f32x4){0.f, 0.f, 0.f, 0.f}; acb[u] = acc[u]; wf[u] = trfrag(W1A(0, u), W1A(0, u) + 3584); }
        SBAR();
#pragma unroll
        for (int s = 0; s < 8; ++s) {
            if (s + WIN < 8) { xf[0][s + WIN] = __builtin_amdgcn_raw_buffer_load_b128(rws, xo0 + (s + WIN) * 1024, 0, 16); xf[1][s + WIN] = __builtin_amdgcn_raw_buffer_load_b128(rws, xo1 + (s + WIN) * 1024, 0, 16); }
            if (s == 4) { sA0 = __builtin_amdgcn_raw_buffer_load_b128(rws, so + ((2 * gi) * 16 + (lane & 15)) * 16, 0, 16); sA1 = __builtin_amdgcn_raw_buffer_load_b128(rws, so + ((2 * gi + 1) * 16 + (lane & 15)) * 16, 0, 16); }
            if (s == 5) { sB0 = __builtin_amdgcn_raw_buffer_load_b64(rws, sob + ((2 * gi) * 16 + (lane & 15)) * 8, 0, 16); sB1 = __builtin_amdgcn_raw_buffer_load_b64(rws, sob + ((2 * gi + 1) * 16 + (lane & 15)) * 8, 0, 16); }
            SBAR();
#pragma unroll
            for (int u = 0; u < 7; ++u) { acc[u] = MFMA16(wf[u], __builtin_bit_cast(bf16x8, xf[0][s]), acc[u]); PIN(acc[u]); SBAR();
                acb[u] = MFMA16(wf[u], __builtin_bit_cast(bf16x8, xf[1][s]), acb[u]); PIN(acb[u]); SBAR();
                if (s < 7) wf[u] = trfrag(W1A(s + 1, u), W1A(s + 1, u) + 3584); SBAR(); }
            if (s == 7) WS4(1);
        }
#undef W1A
        float ca, cb, cc, r2;
        { float S[6];
#pragma unroll
          for (int i = 0; i < 4; ++i) S[i] = xsum32(xsum16(__uint_as_float(sA0[i]) + __uint_as_float(sA1[i])));
#pragma unroll
          for (int i = 0; i < 2; ++i) S[4 + i] = xsum32(xsum16(__uint_as_float(sB0[i]) + __uint_as_float(sB1[i])));
          const f32x4 cc4 = lds_ld<f32x4>(L_CONST); const float SB2 = lds_ld<float>(L_CONST + 16);
          const float iD = 1.f / 512.f, m1 = S[0] * iD, r1 = __builtin_amdgcn_rsqf(fmaxf(S[1] * iD - m1 * m1, 0.f) + 1e-5f);
          const float m2 = (r1 * (S[2] - m1 * cc4[0]) + cc4[1]) * iD;
          const float ez2 = (r1 * r1 * (S[3] - 2.f * m1 * S[4] + m1 * m1 * cc4[2]) + 2.f * r1 * (S[5] - m1 * cc4[3]) + SB2) * iD;
          r2 = __builtin_amdgcn_rsqf(fmaxf(ez2 - m2 * m2, 0.f) + 1e-5f);
          ca = r1 * r2; cb = -ca * m1; cc = -r2 * m2; PIN(ca); PIN(cb); PIN(cc); PIN(r2); }
        const unsigned uo = L_UVW + 16 * gi + 64 * odd, uo6 = L_UVW + 16 * gi + (odd ? 0 : 384);
        f32x4 uu = lds_ld<f32x4>(uo), vv = lds_ld<f32x4>(uo + 448), ww = lds_ld<f32x4>(uo + 896), bb = lds_ld<f32x4>(uo + 1344);
        f32x4 u2 = lds_ld<f32x4>(uo + 64), v2 = lds_ld<f32x4>(uo + 64 + 448), w2v = lds_ld<f32x4>(uo + 64 + 896), b2v = lds_ld<f32x4>(uo + 64 + 1344);
        BAR();
        WS4(2);
        { const unsigned ex = 101376 + w * 7168 + lane * 16;
#pragma unroll
          for (int u = 0; u < 7; ++u) lds_st<f32x4>(ex + u * 1024, acb[u]); }
        WS4(3);
        BAR();
        WS4(4);
        { const unsigned ep = 101376 + (w ^ 4) * 7168 + lane * 16;
#pragma unroll
          for (int u = 0; u < 7; ++u) acc[u] += lds_ld<f32x4>(ep + u * 1024); }
#pragma unroll
        for (int u = 0; u < 7; ++u) PIN(acc[u]);
        BAR();
        STAMP(14);
        const unsigned hb = WS_H + (g * 8 + Tw) * 57344, s0 = (7 * j + odd) >> 1;
        const f32x2 kA = {-0.044715f * 2.302208198f, -0.044715f * 2.302208198f}, kB = {-2.302208198f, -2.302208198f}, one2 = {1.f, 1.f};
#define LO2(v) ((f32x2){(v)[0], (v)[1]})
#define HI2(v) ((f32x2){(v)[2], (v)[3]})
#define PRE2(a_, u_, v_, w_, b_) (ca * (a_) + (cb * (u_) + (r2 * (v_) + (cc * (w_) + (b_)))))
#define GELU(N, p, y) { f32x2 z_[N], e_[N]; \
            _Pragma("unroll") for (int r = 0; r < N; ++r) { z_[r] = p[r] * p[r]; PIN(z_[r]); } \
            _Pragma("unroll") for (int r = 0; r < N; ++r) { z_[r] = z_[r] * kA + kB; PIN(z_[r]); } \
            _Pragma("unroll") for (int r = 0; r < N; ++r) { z_[r] = z_[r] * p[r]; PIN(z_[r]); } \
            SBAR(); _Pragma("unroll") for (int r = 0; r < N; ++r) { e_[r] = (f32x2){__builtin_amdgcn_exp2f(z_[r][0]), __builtin_amdgcn_exp2f(z_[r][1])}; PIN(e_[r]); } \
            SBAR(); _Pragma("unroll") for (int r = 0; r < N; ++r) { e_[r] = e_[r] + one2; PIN(e_[r]); } \
            SBAR(); _Pragma("unroll") for (int r = 0; r < N; ++r) { e_[r] = (f32x2){__builtin_amdgcn_rcpf(e_[r][0]), __builtin_amdgcn_rcpf(e_[r][1])}; PIN(e_[r]); } \
            SBAR(); _Pragma("unroll") for (int r = 0; r < N; ++r) y[r] = p[r] * e_[r]; }
#pragma unroll
        for (int u = 0; u < 6; u += 2) {
            f32x2 pre[4], y[4];
            pre[0] = PRE2(LO2(acc[u]), LO2(uu), LO2(vv), LO2(ww), LO2(bb)); pre[1] = PRE2(HI2(acc[u]), HI2(uu), HI2(vv), HI2(ww), HI2(bb));
            pre[2] = PRE2(LO2(acc[u + 1]), LO2(u2), LO2(v2), LO2(w2v), LO2(b2v)); pre[3] = PRE2(HI2(acc[u + 1]), HI2(u2), HI2(v2), HI2(w2v), HI2(b2v));
            PIN(pre[0]); PIN(pre[1]); PIN(pre[2]); PIN(pre[3]);
            SBAR();
            { const unsigned o = u < 4 ? uo + (u + 2) * 64 : uo6; uu = lds_ld<f32x4>(o); vv = lds_ld<f32x4>(o + 448); ww = lds_ld<f32x4>(o + 896); bb = lds_ld<f32x4>(o + 1344); }
            if (u < 4) { const unsigned o = uo + (u + 3) * 64; u2 = lds_ld<f32x4>(o); v2 = lds_ld<f32x4>(o + 448); w2v = lds_ld<f32x4>(o + 896); b2v = lds_ld<f32x4>(o + 1344); }
            SBAR();
            GELU(4, pre, y)
            __builtin_amdgcn_raw_buffer_store_b128((u32x4){pk2(y[0][0], y[0][1]), pk2(y[1][0], y[1][1]), pk2(y[2][0], y[2][1]), pk2(y[3][0], y[3][1])}, rws, hb + (s0 + (u >> 1)) * 1024 + lane * 16, 0, 16);
            SBAR();
        }
        {   f32x2 pre[2], y[2];
            pre[0] = PRE2(LO2(acc[6]), LO2(uu), LO2(vv), LO2(ww), LO2(bb)); pre[1] = PRE2(HI2(acc[6]), HI2(uu), HI2(vv), HI2(ww), HI2(bb));
            GELU(2, pre, y)
            __builtin_amdgcn_raw_buffer_store_b64((u32x2){pk2(y[0][0], y[0][1]), pk2(y[1][0], y[1][1])}, rws, hb + ((7 * j + (odd ? 0 : 6)) >> 1) * 1024 + odd * 512 + lane * 8, 0, 16);
        }
#undef GELU
#undef PRE2
#undef LO2
#undef HI2
        WS4(5);
    }
    u32x2 gbo0, gbo1;
    { FRESH(lane); const unsigned cl = (32 * kh + 4 * (lane >> 4)) * 2; gbo0 = lds_ld<u32x2>(L_GBO + cl); gbo1 = lds_ld<u32x2>(L_GBO + cl + 32); PIN(gbo0); PIN(gbo1); }
    STAMP_OUT();
    VMWAIT(0);
    BAR();
    if (tid == 0) stg_sc1(ws, WS_FLAG + (3 * 256 + g * 16 + j) * 256, epoch);
    STAMPG(15);
    u32x4 park[8];
    { FRESH(lane);
      const unsigned sw = (lane >> 3) * 1024u + ct * 128u + (((lane & 7) >> 1) ^ ((lane >> 4) & 3)) * 32u + (lane & 1) * 16u;
      const char* p0 = (const char*)w2 + (size_t)(56 * (w >> 2) + 40 + (w & 3)) * 8192, * p1 = p0 + (size_t)112 * 8192;
#pragma unroll
      for (int i = 0; i < 4; ++i) { park[2 * i] = *(const u32x4*)(p0 + i * 32768 + sw); park[2 * i + 1] = *(const u32x4*)(p1 + i * 32768 + sw); }
      SBAR();
      if (w == 0) poll_flags(ws, WS_FLAG + (3 * 256 + g * 16) * 256, 16, epoch, lane); }
    SBAR();
    BAR();
    STAMPG(16); WS5(0);
    {
        constexpr int WIN = P5WIN;
        FRESH(lane);
        const int gi = lane >> 4, q4 = (lane & 15) >> 2, p4 = lane & 3;
        const unsigned tokP = 128 * g + 64 * rh + 16 * tt + (lane & 15), colP = 64 * ct + 32 * kh + 4 * gi;
        const int kq = (w & 1) | (kh << 1);
        const unsigned hb0 = WS_H + (g * 8 + 4 * rh + tt) * 57344 + kq * 14 * 1024, hb1 = WS_H + (g * 8 + 4 * rh + (tt ^ 1)) * 57344 + kq * 14 * 1024;
        u32x4 hf[2][14];
#define HLD1(hb_, dst, s) { if ((s) % 7 == 3) { const u32x2 lo = __builtin_amdgcn_raw_buffer_load_b64(rws, hb_ + (s) * 1024 + lane * 8, 0, 16), hi = __builtin_amdgcn_raw_buffer_load_b64(rws, hb_ + (s) * 1024 + 512 + lane * 8, 0, 16); dst = (u32x4){lo[0], lo[1], hi[0], hi[1]}; } \
                    else dst = __builtin_amdgcn_raw_buffer_load_b128(rws, hb_ + (s) * 1024 + lane * 16, 0, 16); }
#define HLOAD(s) { HLD1(hb0, hf[0][s], s) HLD1(hb1, hf[1][s], s) }
#pragma unroll
        for (int s = 0; s < WIN; ++s) HLOAD(s)
        SBAR();
        f32x4 acc[2][4]; bf16x8 wf[4];
        const unsigned xv = (2 * gi + (q4 >> 1)) & 3, rowa = kq * 4096 + (4 * gi + q4) * 128 + p4 * 8;
        unsigned an[4];
#pragma unroll
        for (int n = 0; n < 4; ++n) { an[n] = rowa + (((n ^ (2 * kh)) ^ xv) * 32); acc[0][n] = (f32x4){0.f, 0.f, 0.f, 0.f}; acc[1][n] = acc[0][n]; }
#define W2A(s, n) (an[n] + (((s) % 10) & 3) * 16384)
#pragma unroll
        for (int n = 0; n < 4; ++n) wf[n] = trfrag(W2A(0, n), W2A(0, n) + 2048);
        const u32x2 g0 = gbo0, g1 = gbo1;
        u32x2 bb0, bb1;
        SBAR();
#pragma unroll
        for (int s = 0; s < 14; ++s) {
            if (s == 11) { bb0 = *(const u32x2*)(b2 + colP); bb1 = *(const u32x2*)(b2 + colP + 16); }
            if (s == 1) WS5(1); if (s == 4) WS5(2); if (s == 8) WS5(3);
            if (s == 5) RBAR();
            if (s == 8) BAR();
            if (s == 8) WS5(4);
            if (s + WIN < 14) HLOAD(s + WIN)
            SBAR();
#pragma unroll
            for (int n = 0; n < 4; ++n) { acc[0][n] = MFMA16(wf[n], __builtin_bit_cast(bf16x8, hf[0][s]), acc[0][n]); PIN(acc[0][n]); SBAR();
                acc[1][n] = MFMA16(wf[n], __builtin_bit_cast(bf16x8, hf[1][s]), acc[1][n]); PIN(acc[1][n]); SBAR();
                if (s >= 5 && s <= 6) { const int i = 4 * (s - 5) + n; lds_st<u32x4>((i >> 1) * 16384 + (w + 8 * (i & 1)) * 1024 + lane * 16, park[i]); }
                if (s == 3 || s == 7) { an[n] += 65536; PIN(an[n]); } if (s == 9) { an[n] -= 131072; PIN(an[n]); }
                if (s < 13) wf[n] = trfrag(W2A(s + 1, n), W2A(s + 1, n) + 2048); SBAR(); }
        }
#undef W2A
#undef HLOAD
#undef HLD1
        WS5(5);
        BAR();
        STAMPG(17);
        { const unsigned ex = w * 6144 + lane * 16;
          lds_st<f32x4>(ex, acc[0][2]); lds_st<f32x4>(ex + 1024, acc[0][3]); lds_st<f32x4>(ex + 2048, acc[1][0]); lds_st<f32x4>(ex + 3072, acc[1][1]); lds_st<f32x4>(ex + 4096, acc[1][2]); lds_st<f32x4>(ex + 5120, acc[1][3]); }
        BAR();
        const unsigned e4 = (w ^ 4) * 6144 + lane * 16, e1 = (w ^ 1) * 6144 + 2048 + lane * 16, e5 = (w ^ 5) * 6144 + 4096 + lane * 16;
        const f32x4 a0 = (acc[0][0] + lds_ld<f32x4>(e4)) + (lds_ld<f32x4>(e1) + lds_ld<f32x4>(e5)), a1 = (acc[0][1] + lds_ld<f32x4>(e4 + 1024)) + (lds_ld<f32x4>(e1 + 1024) + lds_ld<f32x4>(e5 + 1024));
        float y0[4], y1[4];
#pragma unroll
        for (int r = 0; r < 4; ++r) {
            const unsigned ug0 = g0[r >> 1], ug1 = g1[r >> 1], ub0 = bb0[r >> 1], ub1 = bb1[r >> 1], ux0 = x1p0[r >> 1], ux1 = x1p1[r >> 1];
            y0[r] = fmaf((r & 1) ? bf_hi(ug0) : bf_lo(ug0), a0[r] + ((r & 1) ? bf_hi(ub0) : bf_lo(ub0)), (r & 1) ? bf_hi(ux0) : bf_lo(ux0));
            y1[r] = fmaf((r & 1) ? bf_hi(ug1) : bf_lo(ug1), a1[r] + ((r & 1) ? bf_hi(ub1) : bf_lo(ub1)), (r & 1) ? bf_hi(ux1) : bf_lo(ux1));
        }
        *(u32x2*)(out + tokP * 512 + colP) = (u32x2){pk2(y0[0], y0[1]), pk2(y0[2], y0[3])};
        *(u32x2*)(out + tokP * 512 + colP + 16) = (u32x2){pk2(y1[0], y1[1]), pk2(y1[2], y1[3])};
    }
    if (b == 0 && w == 0) {
        for (unsigned sp = 0; sp < SPIN_CAP; ++sp) { asm volatile("" ::: "memory"); const u32x4 v = __builtin_amdgcn_raw_buffer_load_b128(rws, WS_HELLO + lane * 16, 0, 16); if (__all(v[0] == epoch && v[1] == epoch && v[2] == epoch && v[3] == epoch)) break; }
        if (lane == 0) stg_sc1(ws, WS_E, epoch);
    }
    STAMPG(18); STAMPGC(21);
    asm volatile("fb_code_end:");
}

extern "C" void kernel_launch(void* const* d_in, const int* in_sizes, int n_in, void* d_out, int out_size, void* d_ws, size_t ws_size, hipStream_t stream) {
    (void)hipFuncSetAttribute((const void*)fused_block, hipFuncAttributeMaxDynamicSharedMemorySize, LDS_BYTES);
    fused_block<<<256, 576, LDS_BYTES, stream>>>((const u16*)d_in[0], (const u16*)d_in[1], (const u16*)d_in[2], (const u16*)d_in[4], (const u16*)d_in[5], (const u16*)d_in[6], (const u16*)d_in[7],
                                                 (const u16*)d_in[8], (const u16*)d_in[9], (u16*)d_out, (char*)d_ws);
}
```
